```python
import torch

def _softmax(x, axis=-1):
    return torch.softmax(x, dim=axis)
def _gelu(x):
    return torch.nn.functional.gelu(x, approximate="tanh")
def _silu(x):
    return torch.nn.functional.silu(x)
def _sigmoid(x):
    return torch.sigmoid(x)
def _layernorm(x, axis=-1, eps=1e-5):
    m = x.mean(dim=axis, keepdim=True)
    v = x.var(dim=axis, keepdim=True, unbiased=False)
    return (x - m) / torch.sqrt(v + eps)
def _scatter_add(tgt, idx, src, axis=0):
    return tgt.clone().index_add_(axis, idx, src)
def _segment_sum(src, seg, n):
    out = torch.zeros((n,) + src.shape[1:], dtype=src.dtype)
    return out.index_add_(0, seg, src)
def _pad(x, pw):
    flat = tuple(v for pair in reversed(pw) for v in pair)
    return torch.nn.functional.pad(x, flat)

kernel_name = 'gated_mix__spectral_gfnet__dense_activation_silu__prompt'

def setup_inputs():

    _w = (lambda a: (lambda t: t.to(torch.bfloat16) if t.is_floating_point() else t)(torch.as_tensor(a)))
    def _A_setup():
        B, S, D = 1, 2048, 512
        Df = D // 2 + 1
        return {
            "x": _w((lambda *s: torch.randn(*s, dtype=torch.bfloat16) * 0.05)(B, S, D)),
            "w_r": _w((lambda *s: torch.randn(*s, dtype=torch.bfloat16) * 0.4)(S, Df) * 0.02),
            "w_i": _w((lambda *s: torch.randn(*s, dtype=torch.bfloat16) * 0.4)(S, Df) * 0.02),
        }
    _A_in = _A_setup()

    def _B_setup():
        B, S, D, F = 1, 2048, 512, 1792
        return {
            "x": _w((lambda *s: torch.randn(*s, dtype=torch.bfloat16) * 0.05)(B, S, D)),
            "w1": _w((lambda *s: torch.randn(*s, dtype=torch.bfloat16) * 3.2)(D, F) * (D ** -0.5)),
            "w2": _w((lambda *s: torch.randn(*s, dtype=torch.bfloat16) * 0.05)(F, D) * (F ** -0.5)),
        }
    _B_in = _B_setup()

    return {
        "A_x": _A_in["x"],
        "A_w_r": _A_in["w_r"],
        "A_w_i": _A_in["w_i"],
        "B_x": _B_in["x"],
        "B_w1": _B_in["w1"],
        "B_w2": _B_in["w2"],
        "gate_w": _w((lambda *s: torch.randn(*s, dtype=torch.bfloat16) * 0.05)(512) * 16.0),
    }

def reference(A_x, A_w_r, A_w_i, B_x, B_w1, B_w2, gate_w):
    def _A_ref(x, w_r, w_i):
        B, S, D = x.shape
        X = (lambda x, axes=(-2,-1): torch.fft.rfft2(x.float(), dim=axes))(x, axes=(-2, -1))
        W = w_r + 1j * w_i
        X = X * W
        y = (lambda x, s, axes=(-2,-1): torch.fft.irfft2(x, s=s, dim=axes).to(torch.bfloat16))(X, (S, D), axes=(-2, -1))
        return y

    def _B_ref(x, w1, w2):
        h = torch.einsum("bsd,df->bsf", x, w1)
        h = _silu(h)
        out = torch.einsum("bsf,fd->bsd", h, w2)
        return out

    x = A_x
    g = _sigmoid((x * gate_w).sum(-1, keepdims=True))
    a = _A_ref(x, A_w_r, A_w_i)
    b = _B_ref(x, B_w1, B_w2)
    return g * a + (1 - g) * b
```

```cpp
#include <hip/hip_runtime.h>
#include <hip/hip_bf16.h>
#include <stdint.h>

namespace {

typedef unsigned v4u __attribute__((ext_vector_type(4)));
typedef unsigned v2u __attribute__((ext_vector_type(2)));
typedef float f32x16 __attribute__((ext_vector_type(16)));
typedef float f32x4 __attribute__((ext_vector_type(4)));
typedef float f32x2 __attribute__((ext_vector_type(2)));
typedef __bf16 bf16x8 __attribute__((ext_vector_type(8)));
typedef __bf16 bf16x2 __attribute__((ext_vector_type(2)));
typedef short s16x4 __attribute__((ext_vector_type(4)));
typedef short s16x8 __attribute__((ext_vector_type(8)));
#define LAS __attribute__((address_space(3)))
#define GAS __attribute__((address_space(1)))
#define DI __device__ __forceinline__

constexpr int NS = 2048, ND = 512, NF = 1792;
constexpr unsigned WS_CTL = 0;
constexpr unsigned WS_GATE = 65536;
constexpr unsigned WS_T = 1u << 20;
constexpr unsigned WS_U = 3u << 20;
constexpr unsigned WS_H = 5u << 20;
constexpr unsigned WS_BYTES = 13u << 20;
constexpr int LDS_BYTES = 160 * 1024;

struct Params {
  const void* Ax; const void* wr; const void* wi; const void* Bx; const void* w1; const void* w2; const void* gw;
  void* out; void* ws;
};

DI v4u make_srd(const void* p, unsigned bytes) {
  const unsigned long long a = (unsigned long long)p;
  v4u r; r.x = (unsigned)a; r.y = (unsigned)(a >> 32) & 0xffffu; r.z = bytes; r.w = 0x00020000u;
  return r;
}
DI void dma16(v4u srd, unsigned voff, unsigned soff, unsigned lds) {
  asm volatile("s_mov_b32 m0, %3\n\ts_nop 0\n\tbuffer_load_dwordx4 %0, %1, %2 offen lds"
               :: "v"(voff), "s"(srd), "s"(soff), "s"(lds) : "memory");
}
#define WAIT_VM(n) asm volatile("s_waitcnt vmcnt(" #n ")" ::: "memory")
#define WAIT_ALL() asm volatile("s_waitcnt vmcnt(0) lgkmcnt(0)" ::: "memory")
DI void wait_vm_bar(int n) {}
#define BAR_VM(n) asm volatile("s_waitcnt vmcnt(" #n ") lgkmcnt(0)\n\ts_barrier" ::: "memory")
#define BAR_LGKM() asm volatile("s_waitcnt lgkmcnt(0)\n\ts_barrier" ::: "memory")

DI unsigned pk2(float a, float b) { f32x2 p = {a, b}; return __builtin_bit_cast(unsigned, __builtin_convertvector(p, bf16x2)); }
DI float bflo(unsigned u) { return __builtin_bit_cast(float, u << 16); }
DI float bfhi(unsigned u) { return __builtin_bit_cast(float, u & 0xffff0000u); }
DI f32x16 mfma32(v4u a, v4u b, f32x16 c) {
  return __builtin_amdgcn_mfma_f32_32x32x16_bf16(__builtin_bit_cast(bf16x8, a), __builtin_bit_cast(bf16x8, b), c, 0, 0, 0);
}
DI f32x4 mfma16(v4u a, v4u b, f32x4 c) {
  return __builtin_amdgcn_mfma_f32_16x16x32_bf16(__builtin_bit_cast(bf16x8, a), __builtin_bit_cast(bf16x8, b), c, 0, 0, 0);
}
DI v2u tr_read(LAS char* p) { return __builtin_bit_cast(v2u, __builtin_amdgcn_ds_read_tr16_b64_v4i16((LAS s16x4*)p)); }
DI v4u tr_read2(LAS char* p0, LAS char* p1) { v2u a = tr_read(p0), b = tr_read(p1); v4u r; r.x = a.x; r.y = a.y; r.z = b.x; r.w = b.y; return r; }
DI v4u lds_ld16(LAS char* p) { return *(LAS v4u*)p; }
DI float cosrev(float r) { return __builtin_amdgcn_cosf(r); }
DI float sinrev(float r) { return __builtin_amdgcn_sinf(r); }
DI void st16_sc1(v4u srd, unsigned voff, v4u v) {
  __builtin_amdgcn_raw_buffer_store_b128(v, __builtin_amdgcn_make_buffer_rsrc((void*)(((unsigned long long)srd.y << 32) | srd.x), 0, srd.z, 0x00020000), voff, 0, 16);
}

constexpr unsigned A_XIMG = 64 * 1024, A_FIMG = 128 * 1024;

DI void phaseA_issue(const Params& p, int i, int dt4, int w, int lane) {
  const v4u srd = make_srd(p.Ax, NS * ND * 2);
  const unsigned voff = (unsigned)(lane >> 4) * 8192u + (unsigned)(((lane & 15) ^ ((lane >> 4) << 2)) << 4);
#pragma unroll
  for (int u = 0; u < 8; ++u) {
    const unsigned row = 32 * w + 4 * u;
    dma16(srd, voff, (unsigned)i * 1024u + (unsigned)dt4 * 256u + row * 8192u, A_XIMG + row * 256u);
  }
}
DI void phaseA_genF(LAS char* L, int i, int mt, int w, int lane) {
  const int mh = w >> 2, sg = w & 3, n = lane & 31, hh = lane >> 5;
  const int m1c = 64 * mt + 32 * mh + n, m1 = m1c >> 1, c = m1c & 1;
  const int j0 = 64 * sg + 32 * hh;
  const float ph = c ? 0.25f : 0.f;
  const float k2 = 2.f * cosrev((float)(8 * m1) * (1.f / 2048.f));
  float vm = cosrev((float)(((i + 8 * (j0 - 1)) * m1) & 2047) * (1.f / 2048.f) + ph);
  float v0 = cosrev((float)(((i + 8 * j0) * m1) & 2047) * (1.f / 2048.f) + ph);
#pragma unroll
  for (int s = 0; s < 4; ++s) {
    float e[8];
#pragma unroll
    for (int q = 0; q < 8; ++q) { e[q] = v0; const float vn = k2 * v0 - vm; vm = v0; v0 = vn; }
    v4u f; f.x = pk2(e[0], e[1]); f.y = pk2(e[2], e[3]); f.z = pk2(e[4], e[5]); f.w = pk2(e[6], e[7]);
    if (m1c < 2) { const unsigned cst = c ? 0x3F800000u : 0x00003F80u; f.x = f.y = f.z = f.w = cst; }
    *(LAS v4u*)(L + A_FIMG + (mh * 16 + sg * 4 + s) * 1024 + lane * 16) = f;
  }
}
DI void phaseA_mma_store(const Params& p, LAS char* L, int i, int mt, int dt4, int w, int lane, int tid) {
  const int dq = w & 3, mh = w >> 2, hh = lane >> 5, blk = (lane >> 4) & 1, q4 = (lane & 15) >> 2, p4 = lane & 3;
  LAS char* xa = L + A_XIMG + (32 * hh + q4) * 256 + (((4 * dq + 2 * blk + (p4 >> 1)) ^ (q4 << 2)) << 4) + 8 * (p4 & 1);
  LAS char* fb = L + A_FIMG + mh * 16384 + lane * 16;
  f32x16 acc = {};
#pragma unroll
  for (int s = 0; s < 16; ++s) {
    const int ro = (64 * (s >> 2) + 8 * (s & 3)) * 256;
    const v4u a = tr_read2(xa + ro, xa + ro + 1024);
    const v4u b = lds_ld16(fb + s * 1024);
    acc = mfma32(a, b, acc);
  }
  BAR_LGKM();
  {
    const int row = 32 * mh + (lane & 31);
#pragma unroll
    for (int G = 0; G < 4; ++G) {
      v2u o; o.x = pk2(acc[4 * G], acc[4 * G + 1]); o.y = pk2(acc[4 * G + 2], acc[4 * G + 3]);
      const int chunk = 4 * dq + G;
      *(LAS v2u*)(L + A_FIMG + row * 256 + ((chunk ^ (row & 15)) << 4) + 8 * hh) = o;
    }
  }
  BAR_LGKM();
  const v4u srdT = make_srd((char*)p.ws + WS_T, 2u << 20);
  const int row = tid >> 3;
#pragma unroll
  for (int u = 0; u < 2; ++u) {
    const int chunk = (tid & 7) + 8 * u;
    const v4u v = lds_ld16(L + A_FIMG + row * 256 + ((chunk ^ (row & 15)) << 4));
    st16_sc1(srdT, (unsigned)(((64 * mt + row) * 8 + i) * 1024 + dt4 * 256 + chunk * 16), v);
  }
}

DI void gate_rows(const Params& p, int i, int jrow, int lane) {
  const int s = i + 8 * jrow;
  const v4u xv = *(const GAS v4u*)((const GAS char*)p.Ax + (size_t)s * 1024 + lane * 16);
  const v4u gv = *(const GAS v4u*)((const GAS char*)p.gw + lane * 16);
  float z = 0.f;
#pragma unroll
  for (int q = 0; q < 4; ++q) {
    const unsigned pr = pk2(bflo(xv[q]) * bflo(gv[q]), bfhi(xv[q]) * bfhi(gv[q]));
    z += bflo(pr) + bfhi(pr);
  }
#pragma unroll
  for (int o = 32; o >= 1; o >>= 1) z += __shfl_xor(z, o);
  const float zb = bflo(pk2(z, 0.f));
  const float g = bflo(pk2(1.f / (1.f + __expf(-zb)), 0.f));
  const float og = bflo(pk2(1.f - g, 0.f));
  if (lane == 0) {
    const unsigned long long v = ((unsigned long long)__builtin_bit_cast(unsigned, og) << 32) | __builtin_bit_cast(unsigned, g);
    __hip_atomic_store((GAS unsigned long long*)((GAS char*)p.ws + WS_GATE + (i * 256 + jrow) * 8), v, __ATOMIC_RELAXED, __HIP_MEMORY_SCOPE_AGENT);
  }
}

struct G1Ctx { v4u sx, sw; unsigned vx0, vx1, vw0, vw1; };
DI G1Ctx g1_setup(const Params& p, int i, int rb, int f0, int nf, int w, int lane) {
  G1Ctx c;
  c.sx = make_srd(p.Ax, NS * ND * 2);
  c.sw = make_srd(p.w1, ND * NF * 2);
  {
    const int r0 = 16 * w + (lane >> 3), pos = lane & 7;
    c.vx0 = (unsigned)(i * 1024 + (128 * rb + r0) * 8192 + ((pos ^ ((r0 >> 1) & 7)) << 4));
    const int r1 = r0 + 8;
    c.vx1 = (unsigned)(i * 1024 + (128 * rb + r1) * 8192 + ((pos ^ ((r1 >> 1) & 7)) << 4));
  }
  {
    const int k0 = 8 * w + (lane >> 4), chunk = (lane & 15) ^ ((lane >> 4) << 2);
    const bool ok = chunk * 8 < nf;
    c.vw0 = ok ? (unsigned)(k0 * 3584 + f0 * 2 + chunk * 16) : 0x80000000u;
    c.vw1 = ok ? (unsigned)((k0 + 4) * 3584 + f0 * 2 + chunk * 16) : 0x80000000u;
  }
  return c;
}
DI void g1_issue(const G1Ctx& c, int kt, int w) {
  const unsigned slot = (unsigned)(kt & 3) * 32768u;
  dma16(c.sx, c.vx0, (unsigned)kt * 128u, slot + (unsigned)w * 2048u);
  dma16(c.sx, c.vx1, (unsigned)kt * 128u, slot + (unsigned)w * 2048u + 1024u);
  dma16(c.sw, c.vw0, (unsigned)kt * (64u * 3584u), slot + 16384u + (unsigned)w * 2048u);
  dma16(c.sw, c.vw1, (unsigned)kt * (64u * 3584u), slot + 16384u + (unsigned)w * 2048u + 1024u);
}
DI void g1_step(LAS char* L, int kt, int w, int lane, bool active, f32x16 (&acc)[2]) {
  if (!active) return;
  const int ft = w & 3, rh = w >> 2, hh = lane >> 5, blk = (lane >> 4) & 1, q4 = (lane & 15) >> 2, p4 = lane & 3;
  LAS char* sl = L + (kt & 3) * 32768;
  LAS char* wa = sl + 16384 + (8 * hh + q4) * 256 + (((4 * ft + 2 * blk + (p4 >> 1)) ^ (q4 << 2)) << 4) + 8 * (p4 & 1);
  const int r = 64 * rh + (lane & 31), fz = (r >> 1) & 7;
#pragma unroll
  for (int s = 0; s < 4; ++s) {
    const v4u a = tr_read2(wa + s * 4096, wa + s * 4096 + 1024);
    LAS char* xb = sl + r * 128 + (((2 * s + hh) ^ fz) << 4);
    const v4u b0 = lds_ld16(xb), b1 = lds_ld16(xb + 4096);
    acc[0] = mfma32(a, b0, acc[0]);
    acc[1] = mfma32(a, b1, acc[1]);
  }
}
DI float silu(float h) { return h * __builtin_amdgcn_rcpf(1.f + __expf(-h)); }
DI void g1_store(const Params& p, int i, int rb, int f0, int w, int lane, bool active, const f32x16 (&acc)[2]) {
  if (!active) return;
  const v4u srdH = make_srd((char*)p.ws + WS_H, 8u * 8u * 112u * 1024u);
  const int ft = w & 3, rh = w >> 2;
#pragma unroll
  for (int rt = 0; rt < 2; ++rt)
#pragma unroll
    for (int u = 0; u < 2; ++u) {
      v4u o;
      o.x = pk2(silu(acc[rt][8 * u + 0]), silu(acc[rt][8 * u + 1]));
      o.y = pk2(silu(acc[rt][8 * u + 2]), silu(acc[rt][8 * u + 3]));
      o.z = pk2(silu(acc[rt][8 * u + 4]), silu(acc[rt][8 * u + 5]));
      o.w = pk2(silu(acc[rt][8 * u + 6]), silu(acc[rt][8 * u + 7]));
      const int jb32 = 4 * rb + 2 * rh + rt, fs = (f0 >> 4) + 2 * ft + u;
      st16_sc1(srdH, (unsigned)(((i * 8 + jb32) * 112 + fs) * 1024 + lane * 16), o);
    }
}

constexpr unsigned B_IMG = 128 * 1024;
struct cf { float r, i; };
DI cf cmul(cf a, cf b) { return {a.r * b.r - a.i * b.i, a.r * b.i + a.i * b.r}; }
DI cf cmulc(cf a, cf b) { return {a.r * b.r + a.i * b.i, a.i * b.r - a.r * b.i}; }
DI cf omega(int e, int n) { const float r = (float)(e & (n - 1)) * (1.f / (float)n); return {cosrev(r), -sinrev(r)}; }

DI v4u pack_ri(const f32x4& re, const f32x4& im) { v4u r; r.x = pk2(re[0], re[1]); r.y = pk2(re[2], re[3]); r.z = pk2(im[0], im[1]); r.w = pk2(im[2], im[3]); return r; }
template <bool CONJ> DI v4u twiddle_pack(const f32x4& re, const f32x4& im, const cf (&t)[4]) {
  float a[4], b[4];
#pragma unroll
  for (int q = 0; q < 4; ++q) {
    const float xr = re[q], xi = im[q], tr = t[q].r, ti = CONJ ? -t[q].i : t[q].i;
    a[q] = __builtin_fmaf(xr, tr, -(xi * ti)); b[q] = __builtin_fmaf(xr, ti, xi * tr);
  }
  v4u r; r.x = pk2(a[0], a[1]); r.y = pk2(a[2], a[3]); r.z = pk2(b[0], b[1]); r.w = pk2(b[2], b[3]); return r;
}
struct CFrag { v4u re, im; };
DI CFrag cfrag(cf m0, cf m1, cf m2, cf m3, bool conj) {
  CFrag f;
  const unsigned sg = conj ? 0x80008000u : 0u;
  const unsigned r0 = pk2(m0.r, m1.r), r1 = pk2(m2.r, m3.r), i0 = pk2(m0.i, m1.i) ^ sg, i1 = pk2(m2.i, m3.i) ^ sg;
  f.re.x = r0; f.re.y = r1; f.re.z = i0 ^ 0x80008000u; f.re.w = i1 ^ 0x80008000u;
  f.im.x = i0; f.im.y = i1; f.im.z = r0; f.im.w = r1;
  return f;
}

DI void load_w_pair(const Params& p, int m, int lane, cf (&w0)[4], cf (&w1)[4]) {
  const int k1 = lane & 15, g = lane >> 4, mm = (NS - m) & (NS - 1);
  const GAS char* wr = (const GAS char*)p.wr; const GAS char* wi = (const GAS char*)p.wi;
  typedef unsigned u32u __attribute__((aligned(2)));
#pragma unroll
  for (int r = 0; r < 4; ++r) {
    const int K2 = 2 * k1 + 32 * (4 * g + r);
    const int e = g < 2 ? m * 257 + K2 : mm * 257 + 511 - K2;
    const unsigned a = *(const GAS u32u*)(wr + e * 2), b = *(const GAS u32u*)(wi + e * 2);
    if (g < 2) { w0[r] = {bflo(a), bflo(b)}; w1[r] = {bfhi(a), bfhi(b)}; }
    else { w0[r] = {bfhi(a), -bfhi(b)}; w1[r] = {bflo(a), -bflo(b)}; }
  }
  const int e2 = g < 2 ? mm * 257 : m * 257 + 256;
  const unsigned short a2 = *(const GAS unsigned short*)(wr + e2 * 2), b2 = *(const GAS unsigned short*)(wi + e2 * 2);
  const float xr = bflo(a2), xi = g < 2 ? -bflo(b2) : bflo(b2);
  if ((lane & 31) == 0) { w0[0].r = 0.5f * (w0[0].r + xr); w0[0].i = 0.5f * (w0[0].i + xi); }
}

DI void phaseB(const Params& p, LAS char* L, int m1, int w, int lane, int tid) {
  const bool special = (m1 == 0);
  const int hh = lane >> 5, blk = (lane >> 4) & 1, q4 = (lane & 15) >> 2, p4 = lane & 3, g = lane >> 4, l15 = lane & 15;
  LAS char* I0 = L + B_IMG;
  LAS char* I1 = L + B_IMG + 16384;
  {
    const GAS char* T = (const GAS char*)p.ws + WS_T + (size_t)m1 * 16384;
#pragma unroll
    for (int u = 0; u < 2; ++u) {
      const int e = tid + 512 * u, kap = e >> 5, ch = e & 31;
      const int c = kap >> 4, ii = (kap >> 1) & 7, d0 = kap & 1;
      const int grow = special ? 2 * ii + c : 8 * c + ii;
      const v4u v = *(const GAS v4u*)(T + grow * 1024 + d0 * 512 + ch * 16);
      *(LAS v4u*)(I0 + kap * 512 + ((ch ^ ((kap & 3) << 2)) << 4)) = v;
    }
  }
  v4u M1[2], M5[2];
  {
    const int n = lane & 31, m2 = n >> 2, pp = (n >> 1) & 1, cp = n & 1;
    float v0[8], v1[8];
#pragma unroll
    for (int jj = 0; jj < 8; ++jj) {
      const int ii = (8 * hh + jj) >> 1, d0 = jj & 1;
      cf o = omega(ii * m2, 8);
      if (d0 & pp) { o.r = -o.r; o.i = -o.i; }
      v0[jj] = cp ? o.i : o.r;
      v1[jj] = cp ? o.r : -o.i;
    }
    M1[0].x = pk2(v0[0], v0[1]); M1[0].y = pk2(v0[2], v0[3]); M1[0].z = pk2(v0[4], v0[5]); M1[0].w = pk2(v0[6], v0[7]);
    M1[1].x = pk2(v1[0], v1[1]); M1[1].y = pk2(v1[2], v1[3]); M1[1].z = pk2(v1[4], v1[5]); M1[1].w = pk2(v1[6], v1[7]);
    const int cq = n >> 4, ii = (n >> 1) & 7, d0 = n & 1;
#pragma unroll
    for (int s = 0; s < 2; ++s) {
      float v[8];
#pragma unroll
      for (int jj = 0; jj < 8; ++jj) {
        const int m2k = 4 * s + 2 * hh + (jj >> 2), pk = (jj >> 1) & 1, ck = jj & 1;
        cf o = omega(ii * m2k, 8); o.i = -o.i;
        if (d0 & pk) { o.r = -o.r; o.i = -o.i; }
        v[jj] = cq ? (ck ? o.r : o.i) : (ck ? -o.i : o.r);
      }
      M5[s].x = pk2(v[0], v[1]); M5[s].y = pk2(v[2], v[3]); M5[s].z = pk2(v[4], v[5]); M5[s].w = pk2(v[6], v[7]);
    }
  }
  CFrag F16f, F16i, S2[2], S5[2];
  cf tw2[2][4], tw4[2][4];
  {
    {
      const cf m0 = omega(l15 * (4 * g), 16), m1_ = omega(l15 * (4 * g + 1), 16), m2 = omega(l15 * (4 * g + 2), 16), m3 = omega(l15 * (4 * g + 3), 16);
      F16f = cfrag(m0, m1_, m2, m3, false); F16i = cfrag(m0, m1_, m2, m3, true);
    }
#pragma unroll
    for (int pp = 0; pp < 2; ++pp) {
      const int ka = 2 * l15 + pp;
      S2[pp] = cfrag(omega((4 * g) * ka, 32), omega((4 * g + 1) * ka, 32), omega((4 * g + 2) * ka, 32), omega((4 * g + 3) * ka, 32), false);
      const int kb = 8 * g + pp;
      S5[pp] = cfrag(omega(l15 * kb, 32), omega(l15 * (kb + 2), 32), omega(l15 * (kb + 4), 32), omega(l15 * (kb + 6), 32), true);
#pragma unroll
      for (int q = 0; q < 4; ++q) {
        tw2[pp][q] = omega((4 * g + q) * (2 * l15 + pp), 512);
        tw4[pp][q] = omega(l15 * (2 * (4 * g + q) + pp), 512);
      }
    }
  }
  cf Wc[2][4], Wd[2][4];
  if (!special) load_w_pair(p, m1 + 256 * w, lane, Wc[0], Wc[1]);
  else { load_w_pair(p, 128 * w, lane, Wc[0], Wc[1]); load_w_pair(p, 128 * w + 1024, lane, Wd[0], Wd[1]); }
  BAR_LGKM();
  {
    LAS char* ta = I0 + (8 * hh + q4) * 512 + (((4 * w + 2 * blk + (p4 >> 1)) ^ (q4 << 2)) << 4) + 8 * (p4 & 1);
    f32x16 acc = {};
#pragma unroll
    for (int s = 0; s < 2; ++s) acc = mfma32(tr_read2(ta + s * 8192, ta + s * 8192 + 2048), M1[s], acc);
    const int n = lane & 31;
#pragma unroll
    for (int G = 0; G < 4; ++G) {
      v2u o; o.x = pk2(acc[4 * G], acc[4 * G + 1]); o.y = pk2(acc[4 * G + 2], acc[4 * G + 3]);
      *(LAS v2u*)(I1 + n * 512 + (((4 * w + G) ^ (n & 15)) << 4) + 8 * hh) = o;
    }
  }
  BAR_LGKM();
  f32x4 zr[2], zi[2];
  const f32x4 z4 = {0.f, 0.f, 0.f, 0.f};
#pragma unroll
  for (int pp = 0; pp < 2; ++pp) {
    const int n0 = 4 * w + 2 * pp, n1 = n0 + 1;
    const int ch = 2 * (4 * g + q4) + (p4 >> 1);
    const v4u a1 = tr_read2(I1 + n0 * 512 + ((ch ^ (n0 & 15)) << 4) + 8 * (p4 & 1), I1 + n1 * 512 + ((ch ^ (n1 & 15)) << 4) + 8 * (p4 & 1));
    const f32x4 dr = mfma16(a1, S2[pp].re, z4), di = mfma16(a1, S2[pp].im, z4);
    const v4u b3 = twiddle_pack<false>(dr, di, tw2[pp]);
    zr[pp] = mfma16(F16f.re, b3, z4); zi[pp] = mfma16(F16f.im, b3, z4);
  }
  v4u a4[2];
  if (!special) {
#pragma unroll
    for (int pp = 0; pp < 2; ++pp) a4[pp] = twiddle_pack<false>(zr[pp], zi[pp], Wc[pp]);
  } else {
    BAR_LGKM();
    LAS char* Z = L + B_IMG;
#pragma unroll
    for (int pp = 0; pp < 2; ++pp)
#pragma unroll
      for (int q = 0; q < 4; ++q) { f32x2 v = {zr[pp][q], zi[pp][q]}; *(LAS f32x2*)(Z + ((w * 2 + pp) * 256 + l15 + 16 * (4 * g + q)) * 8) = v; }
    BAR_LGKM();
    const cf om = omega(w, 16);
#pragma unroll
    for (int pp = 0; pp < 2; ++pp) {
      float yr[4], yi[4];
#pragma unroll
      for (int q = 0; q < 4; ++q) {
        const int K = l15 + 16 * (4 * g + q), Kn = pp ? 255 - K : (256 - K) & 255;
        const f32x2 y = *(LAS f32x2*)(Z + ((((8 - w) & 7) * 2 + pp) * 256 + Kn) * 8);
        const cf z = {zr[pp][q], zi[pp][q]}, zc = {y[0], -y[1]};
        const cf E = {0.5f * (z.r + zc.r), 0.5f * (z.i + zc.i)};
        const cf O = {0.5f * (z.i - zc.i), -0.5f * (z.r - zc.r)};
        const cf sp = {Wc[pp][q].r + Wd[pp][q].r, Wc[pp][q].i + Wd[pp][q].i}, sm = {Wc[pp][q].r - Wd[pp][q].r, Wc[pp][q].i - Wd[pp][q].i};
        const cf t1 = cmulc(sm, om);
        const cf cE = {sp.r - t1.i, sp.i + t1.r};
        const cf t2 = cmul(sm, om);
        const cf cO = {t2.r - sp.i, t2.i + sp.r};
        const cf a = cmul(E, cE), b = cmul(O, cO);
        yr[q] = 0.5f * (a.r + b.r); yi[q] = 0.5f * (a.i + b.i);
      }
      a4[pp].x = pk2(yr[0], yr[1]); a4[pp].y = pk2(yr[2], yr[3]); a4[pp].z = pk2(yi[0], yi[1]); a4[pp].w = pk2(yi[2], yi[3]);
    }
    BAR_LGKM();
  }
#pragma unroll
  for (int pp = 0; pp < 2; ++pp) {
    const f32x4 dr = mfma16(a4[pp], F16i.re, z4), di = mfma16(a4[pp], F16i.im, z4);
    const v4u a5 = twiddle_pack<true>(dr, di, tw4[pp]);
    const f32x4 er = mfma16(a5, S5[pp].re, z4), ei = mfma16(a5, S5[pp].im, z4);
    const int n0 = 4 * w + 2 * pp, n1 = n0 + 1;
    const int ch = 2 * l15 + (g >> 1);
    v2u o; o.x = pk2(er[0], er[1]); o.y = pk2(er[2], er[3]);
    *(LAS v2u*)(I0 + n0 * 512 + ((ch ^ ((n0 & 3) << 2)) << 4) + 8 * (g & 1)) = o;
    o.x = pk2(ei[0], ei[1]); o.y = pk2(ei[2], ei[3]);
    *(LAS v2u*)(I0 + n1 * 512 + ((ch ^ ((n1 & 3) << 2)) << 4) + 8 * (g & 1)) = o;
  }
  BAR_LGKM();
  {
    LAS char* ta = I0 + (8 * hh + q4) * 512 + (((4 * w + 2 * blk + (p4 >> 1)) ^ (q4 << 2)) << 4) + 8 * (p4 & 1);
    f32x16 acc = {};
#pragma unroll
    for (int s = 0; s < 2; ++s) acc = mfma32(tr_read2(ta + s * 8192, ta + s * 8192 + 2048), M5[s], acc);
    const int n = lane & 31;
#pragma unroll
    for (int G = 0; G < 4; ++G) {
      v2u o; o.x = pk2(acc[4 * G], acc[4 * G + 1]); o.y = pk2(acc[4 * G + 2], acc[4 * G + 3]);
      *(LAS v2u*)(I1 + n * 512 + (((4 * w + G) ^ (n & 15)) << 4) + 8 * hh) = o;
    }
  }
  BAR_LGKM();
  {
    const v4u srdU = make_srd((char*)p.ws + WS_U, 2u << 20);
#pragma unroll
    for (int u = 0; u < 2; ++u) {
      const int e = tid + 512 * u, n = e >> 5, ch = e & 31;
      const int c = n >> 4, ii = (n >> 1) & 7, d0 = n & 1;
      const int grow = special ? 2 * ii + c : 8 * c + ii;
      const v4u v = lds_ld16(I1 + n * 512 + ((ch ^ (n & 15)) << 4));
      st16_sc1(srdU, (unsigned)(m1 * 16384 + grow * 1024 + d0 * 512 + ch * 16), v);
    }
  }
}

constexpr int G2_NSLOT = 8, G2_D = 7;
#define HLOAD(dst, voffv, srd, soffv) asm volatile("buffer_load_dwordx4 %0, %1, %2, %3 offen sc1" : "=v"(dst) : "v"(voffv), "s"(srd), "s"(soffv) : "memory")

DI void phaseG2(const Params& p, LAS char* L, int i, int jb, int dt, int w, int lane, int tid) {
  const int rh = w & 1, q = w >> 1;
  const int hh = lane >> 5, blk = (lane >> 4) & 1, q4 = (lane & 15) >> 2, p4 = lane & 3;
  const v4u sw2 = make_srd(p.w2, NF * ND * 2);
  const v4u sH = make_srd((char*)p.ws + WS_H, 8u * 8u * 112u * 1024u);
  const v4u sU = make_srd((char*)p.ws + WS_U, 2u << 20);
  const int rho = 16 * rh + (lane >> 3), pos = lane & 7, chs = pos ^ (4 * ((lane >> 4) & 1));
  const unsigned vw0 = (unsigned)((448 * q + rho) * 1024 + dt * 128 + chs * 16), vw1 = vw0 + 8 * 1024;
  const unsigned ldsw = (unsigned)(q * 4096 + rh * 2048);
  const unsigned vh = (unsigned)(lane * 16);
  const unsigned hbase = (unsigned)(((i * 8 + 2 * jb + rh) * 112 + 28 * q) * 1024);
  v4u hf[14][2];
  auto issue = [&](int t) {
    const unsigned slot = (unsigned)(t % G2_NSLOT) * 16384u;
    dma16(sw2, vw0, (unsigned)t * 32768u, slot + ldsw);
    dma16(sw2, vw1, (unsigned)t * 32768u, slot + ldsw + 1024u);
    HLOAD(hf[t][0], vh, sH, hbase + (unsigned)(2 * t) * 1024u);
    HLOAD(hf[t][1], vh, sH, hbase + (unsigned)(2 * t + 1) * 1024u);
  };
#pragma unroll
  for (int t = 0; t < G2_D; ++t) issue(t);
  const int j = 64 * jb + 32 * rh + (lane & 31), srow = i + 8 * j;
  f32x16 acc[2] = {};
  const int sz = (q4 >> 1) & 1;
  const unsigned ra0 = (unsigned)(q * 4096 + (4 * hh + q4) * 128 + (((0 ^ (4 * sz)) + 2 * blk + (p4 >> 1)) << 4) + 8 * (p4 & 1));
  const unsigned ra1 = (unsigned)(q * 4096 + (4 * hh + q4) * 128 + (((4 ^ (4 * sz)) + 2 * blk + (p4 >> 1)) << 4) + 8 * (p4 & 1));
  auto step = [&](int slot, v4u b0, v4u b1) {
    LAS char* s0 = L + slot * 16384 + ra0; LAS char* s1 = L + slot * 16384 + ra1;
    const v4u a00 = tr_read2(s0, s0 + 1024), a01 = tr_read2(s1, s1 + 1024);
    const v4u a10 = tr_read2(s0 + 2048, s0 + 3072), a11 = tr_read2(s1 + 2048, s1 + 3072);
    acc[0] = mfma32(a00, b0, acc[0]); acc[1] = mfma32(a01, b0, acc[1]);
    acc[0] = mfma32(a10, b1, acc[0]); acc[1] = mfma32(a11, b1, acc[1]);
  };
#define G2_WAIT(n, t) asm volatile("s_waitcnt vmcnt(" #n ") lgkmcnt(0)\n\ts_barrier" : "+v"(hf[t][0]), "+v"(hf[t][1]) :: "memory")
#define G2_STEP_R(t) { G2_WAIT(24, t); issue((t) + 7); step((t) % G2_NSLOT, hf[t][0], hf[t][1]); }
  G2_STEP_R(0) G2_STEP_R(1) G2_STEP_R(2) G2_STEP_R(3) G2_STEP_R(4) G2_STEP_R(5) G2_STEP_R(6)
  G2_WAIT(24, 7); step(7, hf[7][0], hf[7][1]);
  v4u ub[4];
  const unsigned vu = (unsigned)(((64 * q + rho) * 8 + i) * 1024 + dt * 128 + chs * 16);
  G2_WAIT(20, 8);
  HLOAD(ub[0], vu, sU, 0u); HLOAD(ub[1], vu, sU, 8u * 8192u); HLOAD(ub[2], vu, sU, 32u * 8192u); HLOAD(ub[3], vu, sU, 40u * 8192u);
  step(0, hf[8][0], hf[8][1]);
  G2_WAIT(20, 9); step(1, hf[9][0], hf[9][1]);
  G2_WAIT(16, 10); step(2, hf[10][0], hf[10][1]);
  G2_WAIT(12, 11); step(3, hf[11][0], hf[11][1]);
  G2_WAIT(8, 12); step(4, hf[12][0], hf[12][1]);
  G2_WAIT(4, 13); step(5, hf[13][0], hf[13][1]);
  const unsigned long long gg = __hip_atomic_load((GAS unsigned long long*)((GAS char*)p.ws + WS_GATE + (i * 256 + j) * 8), __ATOMIC_RELAXED, __HIP_MEMORY_SCOPE_AGENT);
  const float g = __builtin_bit_cast(float, (unsigned)gg), og = __builtin_bit_cast(float, (unsigned)(gg >> 32));
  asm volatile("s_waitcnt vmcnt(0)" : "+v"(ub[0]), "+v"(ub[1]), "+v"(ub[2]), "+v"(ub[3]) :: "memory");
  *(LAS v4u*)(L + 6 * 16384 + ldsw + lane * 16) = ub[0];
  *(LAS v4u*)(L + 6 * 16384 + ldsw + 1024 + lane * 16) = ub[1];
  *(LAS v4u*)(L + 7 * 16384 + ldsw + lane * 16) = ub[2];
  *(LAS v4u*)(L + 7 * 16384 + ldsw + 1024 + lane * 16) = ub[3];
#pragma unroll
  for (int e = 0; e < 16; ++e) { acc[0][e] *= og; acc[1][e] *= og; }
  v4u gf[4];
  const float gs = g * (1.f / 524288.f);
#pragma unroll
  for (int s = 0; s < 4; ++s) {
    float v[8];
#pragma unroll
    for (int u = 0; u < 4; ++u) {
      const int m1 = 32 * q + 8 * s + 4 * (u >> 1) + 2 * hh + (u & 1);
      const float r = (float)((srow * m1) & 2047) * (1.f / 2048.f);
      v[2 * u] = gs * cosrev(r); v[2 * u + 1] = -gs * sinrev(r);
    }
    if (s == 0 && q == 0 && hh == 0) { v[0] = (j & 1) ? 0.f : gs; v[1] = (j & 1) ? gs : 0.f; }
    gf[s].x = pk2(v[0], v[1]); gf[s].y = pk2(v[2], v[3]); gf[s].z = pk2(v[4], v[5]); gf[s].w = pk2(v[6], v[7]);
  }
  BAR_LGKM();
  step(6, gf[0], gf[1]);
  step(7, gf[2], gf[3]);
  BAR_LGKM();
  {
    const int row = 32 * rh + (lane & 31);
#pragma unroll
    for (int dh = 0; dh < 2; ++dh)
#pragma unroll
      for (int G = 0; G < 4; ++G) {
        f32x4 v = {acc[dh][4 * G], acc[dh][4 * G + 1], acc[dh][4 * G + 2], acc[dh][4 * G + 3]};
        const int chunk = 8 * dh + 2 * G + hh;
        *(LAS f32x4*)(L + q * 16384 + row * 256 + ((chunk ^ (row & 7)) << 4)) = v;
      }
  }
  BAR_LGKM();
  {
    const int row = tid >> 3, c8 = tid & 7;
    f32x4 s0 = {0.f, 0.f, 0.f, 0.f}, s1 = s0;
#pragma unroll
    for (int qq = 0; qq < 4; ++qq) {
      s0 += *(LAS f32x4*)(L + qq * 16384 + row * 256 + (((2 * c8) ^ (row & 7)) << 4));
      s1 += *(LAS f32x4*)(L + qq * 16384 + row * 256 + (((2 * c8 + 1) ^ (row & 7)) << 4));
    }
    v4u o; o.x = pk2(s0[0], s0[1]); o.y = pk2(s0[2], s0[3]); o.z = pk2(s1[0], s1[1]); o.w = pk2(s1[2], s1[3]);
    *(GAS v4u*)((GAS char*)p.out + (size_t)(i + 8 * (64 * jb + row)) * 1024 + dt * 128 + c8 * 16) = o;
  }
}

__global__ __launch_bounds__(512, 2) void k_phase1(Params p) {
  extern __shared__ __attribute__((aligned(16))) char smem[];
  LAS char* L = (LAS char*)smem;
  const int tid = threadIdx.x, lane = tid & 63, w = __builtin_amdgcn_readfirstlane(tid >> 6);
  const int b = blockIdx.x, i = b & 7, Lx = b >> 3, rb = Lx >> 4, fc = Lx & 15;
  const bool isA = fc < 8;
  const int f0 = isA ? 128 * fc : 1024 + 96 * (fc - 8), nf = isA ? 128 : 96;
  const G1Ctx c = g1_setup(p, i, rb, f0, nf, w, lane);
  const bool active = (w & 3) * 32 < nf;
  f32x16 acc[2] = {};
  if (isA) {
    const int a = rb * 8 + fc, mt = a >> 2, dt4 = a & 3;
    phaseA_issue(p, i, dt4, w, lane);
    g1_issue(c, 0, w); g1_issue(c, 1, w);
    phaseA_genF(L, i, mt, w, lane);
    BAR_VM(8);
    phaseA_mma_store(p, L, i, mt, dt4, w, lane, tid);
    gate_rows(p, i, 128 * rb + 8 * fc + w, lane);
    BAR_VM(0);
    g1_issue(c, 2, w);
  } else {
    g1_issue(c, 0, w); g1_issue(c, 1, w); g1_issue(c, 2, w);
    gate_rows(p, i, 128 * rb + 8 * fc + w, lane);
  }
#define G1_STEP(kt, n) { BAR_VM(n); if ((kt) + 3 < 8) g1_issue(c, (kt) + 3, w); g1_step(L, kt, w, lane, active, acc); }
  G1_STEP(0, 8) G1_STEP(1, 8) G1_STEP(2, 8) G1_STEP(3, 8) G1_STEP(4, 8) G1_STEP(5, 8) G1_STEP(6, 4) G1_STEP(7, 0)
  g1_store(p, i, rb, f0, w, lane, active, acc);
}
__global__ __launch_bounds__(512, 2) void k_phase2(Params p) {
  extern __shared__ __attribute__((aligned(16))) char smem[];
  LAS char* L = (LAS char*)smem;
  const int tid = threadIdx.x, lane = tid & 63, w = __builtin_amdgcn_readfirstlane(tid >> 6);
  phaseB(p, L, blockIdx.x, w, lane, tid);
}
__global__ __launch_bounds__(512, 2) void k_phase3(Params p) {
  extern __shared__ __attribute__((aligned(16))) char smem[];
  LAS char* L = (LAS char*)smem;
  const int tid = threadIdx.x, lane = tid & 63, w = __builtin_amdgcn_readfirstlane(tid >> 6);
  const int b = blockIdx.x, i = b & 7, Lx = b >> 3;
  phaseG2(p, L, i, Lx >> 3, Lx & 7, w, lane, tid);
}

}

extern "C" void kernel_launch(void* const* d_in, const int* in_sizes, int n_in, void* d_out, int out_size, void* d_ws, size_t ws_size,
                              hipStream_t stream) {
  Params p;
  p.Ax = d_in[0]; p.wr = d_in[1]; p.wi = d_in[2]; p.Bx = d_in[3]; p.w1 = d_in[4]; p.w2 = d_in[5]; p.gw = d_in[6];
  p.out = d_out; p.ws = d_ws;
  (void)hipFuncSetAttribute((const void*)k_phase1, hipFuncAttributeMaxDynamicSharedMemorySize, LDS_BYTES);
  (void)hipFuncSetAttribute((const void*)k_phase2, hipFuncAttributeMaxDynamicSharedMemorySize, LDS_BYTES);
  (void)hipFuncSetAttribute((const void*)k_phase3, hipFuncAttributeMaxDynamicSharedMemorySize, LDS_BYTES);
  k_phase1<<<256, 512, LDS_BYTES, stream>>>(p);
  k_phase2<<<128, 512, LDS_BYTES, stream>>>(p);
  k_phase3<<<256, 512, LDS_BYTES, stream>>>(p);
}
```

```cpp
#include <hip/hip_runtime.h>
#include <hip/hip_bf16.h>
#include <stdint.h>

namespace {

typedef unsigned v4u __attribute__((ext_vector_type(4)));
typedef unsigned v2u __attribute__((ext_vector_type(2)));
typedef float f32x16 __attribute__((ext_vector_type(16)));
typedef float f32x4 __attribute__((ext_vector_type(4)));
typedef float f32x2 __attribute__((ext_vector_type(2)));
typedef __bf16 bf16x8 __attribute__((ext_vector_type(8)));
typedef __bf16 bf16x2 __attribute__((ext_vector_type(2)));
typedef short s16x4 __attribute__((ext_vector_type(4)));
#define LAS __attribute__((address_space(3)))
#define GAS __attribute__((address_space(1)))
#define DI __device__ __forceinline__

constexpr int NS = 2048, ND = 512, NF = 1792;
constexpr unsigned WS_EPOCH = 0;
constexpr unsigned FS = 64;
constexpr unsigned WS_FLA = 4096;
constexpr unsigned WS_FLH = 16384;
constexpr unsigned WS_FLB = 32768;
constexpr unsigned WS_GATE = 65536;
constexpr unsigned WS_T = 1u << 20;
constexpr unsigned WS_U = 3u << 20;
constexpr unsigned WS_H = 5u << 20;
constexpr unsigned H_BYTES = 8u * 8u * 112u * 1024u;
constexpr int LDS_BYTES = 160 * 1024;
constexpr unsigned kSpinCap = 1u << 20;

struct Params {
  const void* x; const void* wr; const void* wi; const void* w1; const void* w2; const void* gw;
  void* out; void* ws;
};

constexpr double kPi = 3.14159265358979323846;
constexpr double ccos(double x) { double t = 1, s = 1; for (int n = 1; n < 16; ++n) { t *= -x * x / ((2 * n - 1) * (2 * n)); s += t; } return s; }
constexpr double cosn(int e, int n) { e = ((e % n) + n) % n; double x = 2 * kPi * e / n; if (x > kPi) x -= 2 * kPi; return ccos(x); }
constexpr double wre(int e, int n) { return cosn(e, n); }
constexpr double wim(int e, int n) { return -cosn(4 * e - n, 4 * n); }
constexpr unsigned bfb(double v) { float f = (float)v; unsigned u = __builtin_bit_cast(unsigned, f); u += 0x7fffu + ((u >> 16) & 1u); return u >> 16; }
constexpr unsigned bf2(double a, double b) { return bfb(a) | (bfb(b) << 16); }
constexpr unsigned fbits(double v) { return __builtin_bit_cast(unsigned, (float)v); }
struct Tab { unsigned v[16][64][4]; };
constexpr Tab make_tab() {
  Tab t{};
  for (int l = 0; l < 64; ++l) {
    const int g = l >> 4, l15 = l & 15, hh = l >> 5, n = l & 31;
    for (int mtx = 0; mtx < 3; ++mtx) {
      double r[4] = {}, im[4] = {};
      for (int q = 0; q < 4; ++q) {
        const int a = 4 * g + q;
        if (mtx == 0) { r[q] = wre(l15 * a, 16); im[q] = wim(l15 * a, 16); }
        else if (mtx == 1) { r[q] = wre(a * (2 * l15 + 1), 32); im[q] = wim(a * (2 * l15 + 1), 32); }
        else { r[q] = wre(l15 * (2 * a + 1), 32); im[q] = -wim(l15 * (2 * a + 1), 32); }
      }
      t.v[2 * mtx][l][0] = bf2(r[0], r[1]); t.v[2 * mtx][l][1] = bf2(r[2], r[3]); t.v[2 * mtx][l][2] = bf2(-im[0], -im[1]); t.v[2 * mtx][l][3] = bf2(-im[2], -im[3]);
      t.v[2 * mtx + 1][l][0] = bf2(im[0], im[1]); t.v[2 * mtx + 1][l][1] = bf2(im[2], im[3]); t.v[2 * mtx + 1][l][2] = bf2(r[0], r[1]); t.v[2 * mtx + 1][l][3] = bf2(r[2], r[3]);
    }
    {
      const int m2 = n >> 2, pp = (n >> 1) & 1, cp = n & 1;
      double v0[8] = {}, v1[8] = {};
      for (int jj = 0; jj < 8; ++jj) {
        const int ii = (8 * hh + jj) >> 1, d0 = jj & 1;
        const double sg = (d0 & pp) ? -1.0 : 1.0, orr = sg * wre(ii * m2, 8), oi = sg * wim(ii * m2, 8);
        v0[jj] = cp ? oi : orr; v1[jj] = cp ? orr : -oi;
      }
      for (int q = 0; q < 4; ++q) { t.v[6][l][q] = bf2(v0[2 * q], v0[2 * q + 1]); t.v[7][l][q] = bf2(v1[2 * q], v1[2 * q + 1]); }
    }
    {
      const int cq = n >> 4, ii = (n >> 1) & 7, d0 = n & 1;
      for (int s = 0; s < 2; ++s) {
        double v[8] = {};
        for (int jj = 0; jj < 8; ++jj) {
          const int m2k = 4 * s + 2 * hh + (jj >> 2), pk = (jj >> 1) & 1, ck = jj & 1;
          const double sg = (d0 & pk) ? -1.0 : 1.0, orr = sg * wre(ii * m2k, 8), oi = -sg * wim(ii * m2k, 8);
          v[jj] = cq ? (ck ? orr : oi) : (ck ? -oi : orr);
        }
        for (int q = 0; q < 4; ++q) t.v[8 + s][l][q] = bf2(v[2 * q], v[2 * q + 1]);
      }
    }
    for (int q = 0; q < 4; ++q) {
      const int a = 4 * g + q, o = (q & 1) * 2, f = q >> 1;
      t.v[10 + f][l][o] = fbits(wre(a * (2 * l15), 512));      t.v[10 + f][l][o + 1] = fbits(wim(a * (2 * l15), 512));
      t.v[12 + f][l][o] = fbits(wre(a * (2 * l15 + 1), 512));  t.v[12 + f][l][o + 1] = fbits(wim(a * (2 * l15 + 1), 512));
      t.v[14 + f][l][o] = fbits(wre(l15 * (2 * a + 1), 512));  t.v[14 + f][l][o + 1] = fbits(wim(l15 * (2 * a + 1), 512));
    }
  }
  return t;
}
__device__ const Tab kTab = make_tab();

constexpr unsigned STAMP_OFF = 512 * 1024;
#define STAMP(k) do {} while (0)
#define FT(k) do {} while (0)

DI v4u make_srd(const void* p, unsigned bytes) {
  const unsigned long long a = (unsigned long long)p;
  v4u r; r.x = (unsigned)a; r.y = (unsigned)(a >> 32) & 0xffffu; r.z = bytes; r.w = 0x00020000u;
  return r;
}
DI void dma16(v4u srd, unsigned voff, unsigned soff, unsigned lds) {
  asm volatile("s_mov_b32 m0, %3\n\ts_nop 0\n\tbuffer_load_dwordx4 %0, %1, %2 offen lds"
               :: "v"(voff), "s"(srd), "s"(soff), "s"(lds) : "memory");
}
#define LD16(dst, voffv, srd, soffv, flags) asm volatile("buffer_load_dwordx4 %0, %1, %2, %3 offen " flags : "=v"(dst) : "v"(voffv), "s"(srd), "s"(soffv) : "memory")
#define LD8(dst, voffv, srd, soffv, flags) asm volatile("buffer_load_dwordx2 %0, %1, %2, %3 offen " flags : "=v"(dst) : "v"(voffv), "s"(srd), "s"(soffv) : "memory")
#define LD4(dst, voffv, srd, soffv, flags) asm volatile("buffer_load_dword %0, %1, %2, %3 offen " flags : "=v"(dst) : "v"(voffv), "s"(srd), "s"(soffv) : "memory")
#define LD2(dst, voffv, srd, soffv, flags) asm volatile("buffer_load_ushort %0, %1, %2, %3 offen " flags : "=v"(dst) : "v"(voffv), "s"(srd), "s"(soffv) : "memory")
#define XSTR(x) #x
#define STR(x) XSTR(x)
#define BAR_VM(n) asm volatile("s_waitcnt vmcnt(" STR(n) ") lgkmcnt(0)\n\ts_barrier" ::: "memory")
#define BAR_LGKM() asm volatile("s_waitcnt lgkmcnt(0)\n\ts_barrier" ::: "memory")
#define SCHED() __builtin_amdgcn_sched_barrier(0)
template <int N> DI void bar_vm() { asm volatile("s_waitcnt vmcnt(%0) lgkmcnt(0)\n\ts_barrier" :: "i"(N) : "memory"); }
template <int N> DI void wait_vm() { asm volatile("s_waitcnt vmcnt(%0)" :: "i"(N) : "memory"); }

DI unsigned pk2(float a, float b) { f32x2 p = {a, b}; return __builtin_bit_cast(unsigned, __builtin_convertvector(p, bf16x2)); }
DI float bflo(unsigned u) { return __builtin_bit_cast(float, u << 16); }
DI float bfhi(unsigned u) { return __builtin_bit_cast(float, u & 0xffff0000u); }
DI float asf(unsigned u) { return __builtin_bit_cast(float, u); }
DI f32x16 mfma32(v4u a, v4u b, f32x16 c) {
  return __builtin_amdgcn_mfma_f32_32x32x16_bf16(__builtin_bit_cast(bf16x8, a), __builtin_bit_cast(bf16x8, b), c, 0, 0, 0);
}
DI f32x4 mfma16(v4u a, v4u b, f32x4 c) {
  return __builtin_amdgcn_mfma_f32_16x16x32_bf16(__builtin_bit_cast(bf16x8, a), __builtin_bit_cast(bf16x8, b), c, 0, 0, 0);
}
DI v2u tr_read(LAS char* p) { return __builtin_bit_cast(v2u, __builtin_amdgcn_ds_read_tr16_b64_v4i16((LAS s16x4*)p)); }
DI v4u tr_read2(LAS char* p0, LAS char* p1) { v2u a = tr_read(p0), b = tr_read(p1); v4u r; r.x = a.x; r.y = a.y; r.z = b.x; r.w = b.y; return r; }
DI v4u lds_ld16(LAS char* p) { return *(LAS v4u*)p; }
DI float cosrev(float r) { return __builtin_amdgcn_cosf(r); }
DI float sinrev(float r) { return __builtin_amdgcn_sinf(r); }
DI __amdgpu_buffer_rsrc_t as_rsrc(v4u srd) { return __builtin_amdgcn_make_buffer_rsrc((void*)(((unsigned long long)srd.y << 32) | srd.x), 0, srd.z, 0x00020000); }
DI void st16_sc1(v4u srd, unsigned voff, v4u v) { __builtin_amdgcn_raw_buffer_store_b128(v, as_rsrc(srd), voff, 0, 16); }
DI void flag_store(void* ws, unsigned off, unsigned tag) { __hip_atomic_store((GAS unsigned*)((GAS char*)ws + off), tag, __ATOMIC_RELAXED, __HIP_MEMORY_SCOPE_AGENT); }
template <int CTRL, int RMASK> DI float dpp_add(float v) { return v + asf((unsigned)__builtin_amdgcn_update_dpp(0, (int)__builtin_bit_cast(unsigned, v), CTRL, RMASK, 0xf, true)); }
DI void poll_words(v4u sws, unsigned off, int n, unsigned tag, int lane) {
  const unsigned vo = off + FS * (unsigned)(lane < n ? lane : 0);
#pragma unroll 1
  for (unsigned spins = 0; spins < kSpinCap; ++spins) {
    unsigned f;
    asm volatile("buffer_load_dword %0, %1, %2, 0 offen sc1\n\ts_waitcnt vmcnt(0)" : "=&v"(f) : "v"(vo), "s"(sws) : "memory");
    if (__all(f == tag)) return;
    __builtin_amdgcn_s_sleep(4);
  }
}

constexpr unsigned A_XIMG = 64 * 1024, A_FIMG = 128 * 1024;
DI void phaseA_genF(LAS char* L, int i, int mt, int w, int lane) {
  const int mh = w >> 2, sg = w & 3, n = lane & 31, hh = lane >> 5;
  const int m1c = 64 * mt + 32 * mh + n, m1 = m1c >> 1, c = m1c & 1;
  const int j0 = 64 * sg + 32 * hh;
  const float ph = c ? 0.25f : 0.f;
  const float k2 = 2.f * cosrev((float)(8 * m1) * (1.f / 2048.f));
  float vm = cosrev((float)(((i + 8 * (j0 - 1)) * m1) & 2047) * (1.f / 2048.f) + ph);
  float v0 = cosrev((float)(((i + 8 * j0) * m1) & 2047) * (1.f / 2048.f) + ph);
  const unsigned cst = c ? 0x3F800000u : 0x00003F80u;
  const bool sp = m1c < 2;
#pragma unroll
  for (int s = 0; s < 4; ++s) {
    float e[8];
#pragma unroll
    for (int q = 0; q < 8; ++q) { e[q] = v0; const float vn = __builtin_fmaf(k2, v0, -vm); vm = v0; v0 = vn; }
    v4u f; f.x = pk2(e[0], e[1]); f.y = pk2(e[2], e[3]); f.z = pk2(e[4], e[5]); f.w = pk2(e[6], e[7]);
    if (mt == 0 && w < 4) { f.x = sp ? cst : f.x; f.y = sp ? cst : f.y; f.z = sp ? cst : f.z; f.w = sp ? cst : f.w; }
    *(LAS v4u*)(L + A_FIMG + (mh * 16 + sg * 4 + s) * 1024 + lane * 16) = f;
  }
}

struct Frag1 { v4u a[4], b0[4], b1[4]; };
DI void g1_read(Frag1& f, LAS char* wa, LAS char* xb0, LAS char* xb1, LAS char* xb2, LAS char* xb3, int slot) {
  const int so = slot * 32768;
  f.a[0] = tr_read2(wa + so, wa + so + 1024);              f.b0[0] = lds_ld16(xb0 + so); f.b1[0] = lds_ld16(xb0 + so + 4096);
  f.a[1] = tr_read2(wa + so + 4096, wa + so + 5120);       f.b0[1] = lds_ld16(xb1 + so); f.b1[1] = lds_ld16(xb1 + so + 4096);
  f.a[2] = tr_read2(wa + so + 8192, wa + so + 9216);       f.b0[2] = lds_ld16(xb2 + so); f.b1[2] = lds_ld16(xb2 + so + 4096);
  f.a[3] = tr_read2(wa + so + 12288, wa + so + 13312);     f.b0[3] = lds_ld16(xb3 + so); f.b1[3] = lds_ld16(xb3 + so + 4096);
}
DI float silu(float h) { return h * __builtin_amdgcn_rcpf(1.f + __builtin_amdgcn_exp2f(h * -1.44269504088896341f)); }

struct cf { float r, i; };
DI cf cmul(cf a, cf b) { return {a.r * b.r - a.i * b.i, a.r * b.i + a.i * b.r}; }
DI cf cmulc(cf a, cf b) { return {a.r * b.r + a.i * b.i, a.i * b.r - a.r * b.i}; }
template <bool CONJ> DI v4u twiddle_pack(const f32x4& re, const f32x4& im, float t0r, float t0i, float t1r, float t1i, float t2r, float t2i, float t3r, float t3i) {
  const float tr[4] = {t0r, t1r, t2r, t3r}, ti[4] = {t0i, t1i, t2i, t3i};
  float a[4], b[4];
#pragma unroll
  for (int q = 0; q < 4; ++q) {
    const float xr = re[q], xi = im[q], s = CONJ ? -ti[q] : ti[q];
    a[q] = __builtin_fmaf(xr, tr[q], -(xi * s)); b[q] = __builtin_fmaf(xr, s, xi * tr[q]);
  }
  v4u r; r.x = pk2(a[0], a[1]); r.y = pk2(a[2], a[3]); r.z = pk2(b[0], b[1]); r.w = pk2(b[2], b[3]); return r;
}
template <bool CONJ> DI v4u twiddle_pack(const f32x4& re, const f32x4& im, v4u t0, v4u t1) {
  return twiddle_pack<CONJ>(re, im, asf(t0.x), asf(t0.y), asf(t0.z), asf(t0.w), asf(t1.x), asf(t1.y), asf(t1.z), asf(t1.w));
}
struct WRaw { unsigned r0, r1, r2, r3, i0, i1, i2, i3, xr, xi; };
DI void w_issue(WRaw& o, v4u swr, v4u swi, int m, int lane) {
  const int k1 = lane & 15, g = lane >> 4, mm = (NS - m) & (NS - 1);
  const unsigned e0 = g < 2 ? (unsigned)(m * 257 + 2 * k1 + 128 * g) * 2u : (unsigned)(mm * 257 + 511 - 2 * k1 - 128 * g) * 2u;
  const unsigned e1 = g < 2 ? e0 + 64u : e0 - 64u, e2 = g < 2 ? e0 + 128u : e0 - 128u, e3 = g < 2 ? e0 + 192u : e0 - 192u;
  const unsigned ex = (unsigned)(g < 2 ? mm * 257 : m * 257 + 256) * 2u;
  LD4(o.r0, e0, swr, 0u, ""); LD4(o.r1, e1, swr, 0u, ""); LD4(o.r2, e2, swr, 0u, ""); LD4(o.r3, e3, swr, 0u, "");
  LD4(o.i0, e0, swi, 0u, ""); LD4(o.i1, e1, swi, 0u, ""); LD4(o.i2, e2, swi, 0u, ""); LD4(o.i3, e3, swi, 0u, "");
  LD2(o.xr, ex, swr, 0u, ""); LD2(o.xi, ex, swi, 0u, "");
}
#define W_PIN(o) "+v"(o.r0), "+v"(o.r1), "+v"(o.r2), "+v"(o.r3), "+v"(o.i0), "+v"(o.i1), "+v"(o.i2), "+v"(o.i3), "+v"(o.xr), "+v"(o.xi)
DI void w_unpack(const WRaw& o, int lane, float (&w0)[8], float (&w1)[8]) {
  const bool lo = lane < 32;
  const unsigned rr[4] = {o.r0, o.r1, o.r2, o.r3}, ii[4] = {o.i0, o.i1, o.i2, o.i3};
  const unsigned sg = lo ? 0u : 0x80000000u;
#pragma unroll
  for (int r = 0; r < 4; ++r) {
    const unsigned a0 = lo ? rr[r] << 16 : rr[r] & 0xffff0000u, a1 = lo ? rr[r] & 0xffff0000u : rr[r] << 16;
    const unsigned b0 = lo ? ii[r] << 16 : ii[r] & 0xffff0000u, b1 = lo ? ii[r] & 0xffff0000u : ii[r] << 16;
    w0[2 * r] = asf(a0); w0[2 * r + 1] = asf(b0 ^ sg); w1[2 * r] = asf(a1); w1[2 * r + 1] = asf(b1 ^ sg);
  }
  if ((lane & 31) == 0) {
    w0[0] = 0.5f * (w0[0] + bflo(o.xr));
    w0[1] = 0.5f * (w0[1] + asf((o.xi << 16) ^ (sg ^ 0x80000000u)));
  }
}

#define G1_MMA true
__global__ __launch_bounds__(512, 2) void gm_kernel(Params p) {
  extern __shared__ __attribute__((aligned(16))) char smem[];
  LAS char* L = (LAS char*)smem;
  const int tid = threadIdx.x, lane = tid & 63, w = __builtin_amdgcn_readfirstlane(tid >> 6);
  const int b = blockIdx.x, i = b & 7, Lx = b >> 3, rb = Lx >> 4, fc = Lx & 15;
  const bool isA = fc < 8;
  const int hh = lane >> 5, blk = (lane >> 4) & 1, q4 = (lane & 15) >> 2, p4 = lane & 3;
  STAMP(0);
  FT(0);
  const v4u sx = make_srd(p.x, NS * ND * 2), sw1 = make_srd(p.w1, ND * NF * 2), sws = make_srd(p.ws, 16u << 20);
  unsigned ep; v4u gx, gv;
  const int jg = 128 * rb + 8 * fc + w;
  LD16(gx, (unsigned)lane * 16u, sx, (unsigned)(i + 8 * jg) * 1024u, "");
  LD16(gv, (unsigned)lane * 16u, make_srd(p.gw, ND * 2), 0u, "");
  LD4(ep, 0u, sws, WS_EPOCH, "sc1");
  const int f0 = isA ? 96 * fc : 768 + 128 * (fc - 8), nf = isA ? 96 : 128;
  const int a_t = rb * 8 + fc, mt = a_t >> 2, dt4 = a_t & 3;
  const int m1 = ((rb * 8 + (fc - 8)) << 3) | i;
  if (isA) {
    const unsigned voff = (unsigned)(lane >> 4) * 8192u + (unsigned)(((lane & 15) ^ ((lane >> 4) << 2)) << 4);
#pragma unroll
    for (int u = 0; u < 8; ++u) {
      const unsigned row = 32 * w + 4 * u;
      dma16(sx, voff, (unsigned)i * 1024u + (unsigned)dt4 * 256u + row * 8192u, A_XIMG + row * 256u);
    }
  }
  unsigned vx0, vx1, vw0, vw1;
  {
    const int r0 = 16 * w + (lane >> 3), pos = lane & 7, r1 = r0 + 8;
    vx0 = (unsigned)(i * 1024 + (128 * rb + r0) * 8192 + ((pos ^ ((r0 >> 1) & 7)) << 4));
    vx1 = (unsigned)(i * 1024 + (128 * rb + r1) * 8192 + ((pos ^ ((r1 >> 1) & 7)) << 4));
    const int k0 = 8 * w + (lane >> 4), chunk = (lane & 15) ^ ((lane >> 4) << 2);
    const bool ok = chunk * 8 < nf;
    vw0 = ok ? (unsigned)(k0 * 3584 + f0 * 2 + chunk * 16) : 0x80000000u;
    vw1 = ok ? vw0 + 4u * 3584u : 0x80000000u;
  }
  auto g1_issue = [&](int kt) {
    const unsigned slot = (unsigned)(kt & 3) * 32768u + (unsigned)w * 2048u;
    dma16(sx, vx0, (unsigned)kt * 128u, slot);
    dma16(sx, vx1, (unsigned)kt * 128u, slot + 1024u);
    dma16(sw1, vw0, (unsigned)kt * (64u * 3584u), slot + 16384u);
    dma16(sw1, vw1, (unsigned)kt * (64u * 3584u), slot + 17408u);
  };
  if (!isA) {
    g1_issue(0); asm volatile("s_barrier" ::: "memory");
    g1_issue(1); asm volatile("s_barrier" ::: "memory");
    g1_issue(2); asm volatile("s_barrier" ::: "memory");
    g1_issue(3);
  } else {
    phaseA_genF(L, i, mt, w, lane);
  }
  FT(1);
  if (isA) wait_vm<8>(); else wait_vm<16>();
  asm volatile("" : "+v"(ep), "+v"(gx), "+v"(gv) :: "memory");
  const unsigned tag = (unsigned)__builtin_amdgcn_readfirstlane((int)ep) + 1u;
  float gate_g, gate_og;
  {
    float z = 0.f;
#pragma unroll
    for (int q = 0; q < 4; ++q) {
      const unsigned pr = pk2(bflo(gx[q]) * bflo(gv[q]), bfhi(gx[q]) * bfhi(gv[q]));
      z += bflo(pr) + bfhi(pr);
    }
    z = dpp_add<0x111, 0xf>(z); z = dpp_add<0x112, 0xf>(z); z = dpp_add<0x114, 0xf>(z); z = dpp_add<0x118, 0xf>(z);
    z = dpp_add<0x142, 0xa>(z); z = dpp_add<0x143, 0xc>(z);
    const float zb = bflo(pk2(z, 0.f));
    const float g = bflo(pk2(__builtin_amdgcn_rcpf(1.f + __builtin_amdgcn_exp2f(zb * -1.44269504088896341f)), 0.f));
    gate_g = asf((unsigned)__builtin_amdgcn_readlane((int)__builtin_bit_cast(unsigned, g), 63));
    gate_og = bflo(pk2(1.f - gate_g, 0.f));
  }
  FT(2);
  if (isA) {
    const int dq = w & 3, mh = w >> 2;
    LAS char* xa = L + A_XIMG + (32 * hh + q4) * 256 + (((4 * dq + 2 * blk + (p4 >> 1)) ^ (q4 << 2)) << 4) + 8 * (p4 & 1);
    LAS char* fb = L + A_FIMG + mh * 16384 + lane * 16;
    const int srow = 32 * mh + (lane & 31);
    LAS char* stw = L + A_FIMG + srow * 256 + 8 * hh;
    const int rrow = tid >> 3;
    LAS char* str = L + A_FIMG + rrow * 256;
    const unsigned tdst = WS_T + (unsigned)(((64 * mt + rrow) * 8 + i) * 1024 + dt4 * 256 + (tid & 7) * 16);
    BAR_VM(0);
    FT(3);
    f32x16 acc = {};
    v4u fa[2][4], fbv[2][4];
#pragma unroll
    for (int s = 0; s < 4; ++s) { fa[0][s] = tr_read2(xa + 8 * s * 256, xa + 8 * s * 256 + 1024); fbv[0][s] = lds_ld16(fb + s * 1024); }
#pragma unroll
    for (int g = 0; g < 4; ++g) {
      acc = mfma32(fa[g & 1][0], fbv[g & 1][0], acc);
      SCHED();
      if (g < 3) {
#pragma unroll
        for (int s = 0; s < 4; ++s) {
          const int ro = (64 * (g + 1) + 8 * s) * 256;
          fa[(g + 1) & 1][s] = tr_read2(xa + ro, xa + ro + 1024); fbv[(g + 1) & 1][s] = lds_ld16(fb + (4 * (g + 1) + s) * 1024);
        }
      }
      SCHED();
#pragma unroll
      for (int s = 1; s < 4; ++s) acc = mfma32(fa[g & 1][s], fbv[g & 1][s], acc);
      SCHED();
      if (g == 0) g1_issue(0);
      if (g == 1) g1_issue(1);
    }
    FT(4);
    BAR_LGKM();
#pragma unroll
    for (int G = 0; G < 4; ++G) {
      v2u o; o.x = pk2(acc[4 * G], acc[4 * G + 1]); o.y = pk2(acc[4 * G + 2], acc[4 * G + 3]);
      *(LAS v2u*)(stw + (((4 * dq + G) ^ (srow & 15)) << 4)) = o;
    }
    BAR_LGKM();
    {
      const v4u v0 = lds_ld16(str + (((tid & 7) ^ (rrow & 15)) << 4)), v1 = lds_ld16(str + ((((tid & 7) + 8) ^ (rrow & 15)) << 4));
      st16_sc1(sws, tdst, v0);
      st16_sc1(sws, tdst + 128u, v1);
    }
    SCHED();
    FT(5);
    g1_issue(2); g1_issue(3);
  }
  FT(6);
  const int jb = Lx >> 3, dt = Lx & 7, rh = w & 1, q = w >> 1;
  const v4u sw2 = make_srd(p.w2, NF * ND * 2);
  const int rho = 16 * rh + (lane >> 3), chs = (lane & 7) ^ (4 * ((lane >> 4) & 1));
  const unsigned vw20 = (unsigned)(rho * 1024 + dt * 128 + chs * 16), vw21 = vw20 + 8 * 1024;
  const unsigned ldsw = (unsigned)(q * 4096 + rh * 2048);
  const unsigned fq0 = (unsigned)(768 + 256 * q), fq1 = (unsigned)(192 * q - 256);
  auto w2_issue1 = [&](int t, int u) {
    const unsigned slot = (unsigned)(t & 7) * 16384u + ldsw + (unsigned)u * 1024u;
    dma16(sw2, u ? vw21 : vw20, ((t < 8 ? fq0 : fq1) + 32u * (unsigned)t) * 1024u, slot);
  };
  const int ft = w & 3, rh1 = w >> 2;
  const bool active = ft * 32 < nf;
  f32x16 acc1[2] = {};
  v4u tq0, tq1;
  WRaw Wc;
  {
    LAS char* wa = L + 16384 + (8 * hh + q4) * 256 + (((4 * ft + 2 * blk + (p4 >> 1)) ^ (q4 << 2)) << 4) + 8 * (p4 & 1);
    const int r = 64 * rh1 + (lane & 31), fz = (r >> 1) & 7;
    LAS char* xb0 = L + r * 128 + (((0 + hh) ^ fz) << 4);
    LAS char* xb1 = L + r * 128 + (((2 + hh) ^ fz) << 4);
    LAS char* xb2 = L + r * 128 + (((4 + hh) ^ fz) << 4);
    LAS char* xb3 = L + r * 128 + (((6 + hh) ^ fz) << 4);
    Frag1 fr[2];
    FT(7);
    if (isA) bar_vm<14>(); else bar_vm<12>();
    FT(8);
    g1_read(fr[0], wa, xb0, xb1, xb2, xb3, 0);
    FT(9);
    unsigned fla;
#define PIN1(c) asm volatile("" : "+v"(c.a[0]), "+v"(c.a[1]), "+v"(c.a[2]), "+v"(c.a[3]), "+v"(c.b0[0]), "+v"(c.b0[1]), "+v"(c.b0[2]), "+v"(c.b0[3]), \
                             "+v"(c.b1[0]), "+v"(c.b1[1]), "+v"(c.b1[2]), "+v"(c.b1[3]))
#define G1_DMA(kt, u)                                                                                                 \
    {                                                                                                                   \
      if ((kt) < 4) {                                                                                                   \
        const unsigned slot_ = (unsigned)((kt) & 3) * 32768u + (unsigned)w * 2048u;                                     \
        if ((u) == 0) dma16(sx, vx0, (unsigned)((kt) + 4) * 128u, slot_);                                               \
        if ((u) == 1) dma16(sx, vx1, (unsigned)((kt) + 4) * 128u, slot_ + 1024u);                                       \
        if ((u) == 2) dma16(sw1, vw0, (unsigned)((kt) + 4) * (64u * 3584u), slot_ + 16384u);                            \
        if ((u) == 3) dma16(sw1, vw1, (unsigned)((kt) + 4) * (64u * 3584u), slot_ + 17408u);                            \
      } else {                                                                                                          \
        w2_issue1(2 * ((kt) - 4) + ((u) >> 1), (u) & 1);                                                                \
      }                                                                                                                 \
    }
#define G1_STEP(kt)                                                                                                   \
    {                                                                                                                   \
      FT(10 + (kt));                                                                                                    \
      Frag1& c = fr[(kt) & 1];                                                                                          \
      Frag1& n = fr[((kt) + 1) & 1];                                                                                    \
      const int so = (((kt) + 1) & 3) * 32768;                                                                          \
      PIN1(c);                                                                                                          \
      SCHED();                                                                                                          \
      acc1[0] = mfma32(c.a[0], c.b0[0], acc1[0]); if ((kt) < 7) n.a[0] = tr_read2(wa + so, wa + so + 1024); SCHED();   \
      acc1[1] = mfma32(c.a[0], c.b1[0], acc1[1]); if ((kt) < 7) { n.b0[0] = lds_ld16(xb0 + so); n.b1[0] = lds_ld16(xb0 + so + 4096); } SCHED(); \
      acc1[0] = mfma32(c.a[1], c.b0[1], acc1[0]); if ((kt) < 7) n.a[1] = tr_read2(wa + so + 4096, wa + so + 5120); SCHED(); \
      acc1[1] = mfma32(c.a[1], c.b1[1], acc1[1]); if ((kt) < 7) { n.b0[1] = lds_ld16(xb1 + so); n.b1[1] = lds_ld16(xb1 + so + 4096); } SCHED(); \
      acc1[0] = mfma32(c.a[2], c.b0[2], acc1[0]); if ((kt) < 7) n.a[2] = tr_read2(wa + so + 8192, wa + so + 9216); G1_DMA(kt, 0) SCHED(); \
      acc1[1] = mfma32(c.a[2], c.b1[2], acc1[1]); if ((kt) < 7) { n.b0[2] = lds_ld16(xb2 + so); n.b1[2] = lds_ld16(xb2 + so + 4096); } G1_DMA(kt, 1) SCHED(); \
      acc1[0] = mfma32(c.a[3], c.b0[3], acc1[0]); if ((kt) < 7) n.a[3] = tr_read2(wa + so + 12288, wa + so + 13312); G1_DMA(kt, 2) SCHED(); \
      acc1[1] = mfma32(c.a[3], c.b1[3], acc1[1]); if ((kt) < 7) { n.b0[3] = lds_ld16(xb3 + so); n.b1[3] = lds_ld16(xb3 + so + 4096); } G1_DMA(kt, 3) SCHED(); \
    }
    if (isA) bar_vm<10>(); else bar_vm<8>();
    G1_STEP(0)
    bar_vm<8>();
    if (isA && tid == 0) flag_store(p.ws, WS_FLA + (unsigned)(mt * 32 + i * 4 + dt4) * FS, tag);
    G1_STEP(1)
    bar_vm<8>();
    G1_STEP(2)
    if (isA && w == 0) bar_vm<9>(); else bar_vm<8>();
    G1_STEP(3)
    if (!isA) {
      const v4u stab = make_srd(&kTab, sizeof(Tab));
      dma16(stab, (unsigned)lane * 16u, (unsigned)w * 2048u, 128u * 1024u + 16384u + (unsigned)w * 2048u);
      dma16(stab, (unsigned)lane * 16u, (unsigned)w * 2048u + 1024u, 128u * 1024u + 16384u + (unsigned)w * 2048u + 1024u);
      w_issue(Wc, make_srd(p.wr, NS * 257 * 2), make_srd(p.wi, NS * 257 * 2), m1 == 0 ? 128 * w : m1 + 256 * w, lane);
      bar_vm<20>();
    } else {
      bar_vm<8>();
    }
    G1_STEP(4)
    if (!isA) {
      if (w == 0) LD4(fla, WS_FLA + (unsigned)((m1 >> 5) * 32 + (lane & 31)) * FS, sws, 0u, "sc1");
      bar_vm<20>();
    } else {
      bar_vm<8>();
    }
    G1_STEP(5)
    if (!isA) {
      if (w == 0) {
        asm volatile("s_waitcnt vmcnt(4)" : "+v"(fla) :: "memory");
        if (!__all(fla == tag)) poll_words(sws, WS_FLA + (unsigned)(m1 >> 5) * 32u * FS, 32, tag, lane);
      }
      bar_vm<20>();
      const int kap = tid >> 5, ii = (kap >> 1) & 7, d0 = kap & 1;
      const int g0 = m1 == 0 ? 2 * ii : ii, g1 = m1 == 0 ? 2 * ii + 1 : 8 + ii;
      const unsigned tb = WS_T + (unsigned)m1 * 16384u + (unsigned)(d0 * 512 + (tid & 31) * 16);
      LD16(tq0, tb + (unsigned)g0 * 1024u, sws, 0u, "sc1");
      LD16(tq1, tb + (unsigned)g1 * 1024u, sws, 0u, "sc1");
    } else {
      bar_vm<8>();
    }
    G1_STEP(6)
    if (isA) BAR_LGKM(); else bar_vm<24>();
    G1_STEP(7)
  }
  STAMP(5);
  FT(18);
  if (active) {
#pragma unroll
    for (int rt = 0; rt < 2; ++rt)
#pragma unroll
      for (int u = 0; u < 2; ++u) {
        v4u o;
        o.x = pk2(silu(acc1[rt][8 * u + 0]), silu(acc1[rt][8 * u + 1]));
        o.y = pk2(silu(acc1[rt][8 * u + 2]), silu(acc1[rt][8 * u + 3]));
        o.z = pk2(silu(acc1[rt][8 * u + 4]), silu(acc1[rt][8 * u + 5]));
        o.w = pk2(silu(acc1[rt][8 * u + 6]), silu(acc1[rt][8 * u + 7]));
        const int jb32 = 4 * rb + 2 * rh1 + rt, fs = (f0 >> 4) + 2 * ft + u;
        st16_sc1(sws, WS_H + (unsigned)(((i * 8 + jb32) * 112 + fs) * 1024 + lane * 16), o);
      }
  }
  if (lane == 0) {
    const unsigned long long v = ((unsigned long long)__builtin_bit_cast(unsigned, gate_og) << 32) | __builtin_bit_cast(unsigned, gate_g);
    __hip_atomic_store((GAS unsigned long long*)((GAS char*)p.ws + WS_GATE + (i * 256 + jg) * 8), v, __ATOMIC_RELAXED, __HIP_MEMORY_SCOPE_AGENT);
  }
  SCHED();
  STAMP(6);
  FT(20);
  const int j = 64 * jb + 32 * rh + (lane & 31);
  v4u gf[4];
  auto gen_g = [&]() {
    const int srow = i + 8 * j;
    const int mb = 32 * q + 2 * hh;
    const float r1 = (float)(srow & 2047) * (1.f / 2048.f), r4 = (float)((4 * srow) & 2047) * (1.f / 2048.f), r0 = (float)((mb * srow) & 2047) * (1.f / 2048.f);
    const float c1 = cosrev(r1), s1 = sinrev(r1), c4 = cosrev(r4), s4 = sinrev(r4);
    float c = cosrev(r0), s = sinrev(r0);
#pragma unroll
    for (int e = 0; e < 8; ++e) {
      const float cb = __builtin_fmaf(c, c1, -(s * s1)), sb = __builtin_fmaf(s, c1, c * s1);
      unsigned w0 = pk2(c, -s), w1 = pk2(cb, -sb);
      if (e == 0 && q == 0 && hh == 0) w0 = (j & 1) ? 0x3F800000u : 0x00003F80u;
      if (e & 1) { gf[e >> 1].z = w0; gf[e >> 1].w = w1; } else { gf[e >> 1].x = w0; gf[e >> 1].y = w1; }
      const float cn = __builtin_fmaf(c, c4, -(s * s4)), sn = __builtin_fmaf(s, c4, c * s4);
      c = cn; s = sn;
    }
    asm volatile("" : "+v"(gf[0]), "+v"(gf[1]), "+v"(gf[2]), "+v"(gf[3]));
  };
  if (isA) {
    gen_g();
    bar_vm<0>();
    if (tid == 0) flag_store(p.ws, WS_FLH + (unsigned)((i * 2 + rb) * 16 + fc) * FS, tag);
  } else {
    LAS char* I0 = L + 128 * 1024;
    LAS char* I1 = L + 128 * 1024 + 16384;
    const int g = lane >> 4, l15 = lane & 15;
    const bool special = (m1 == 0);
    asm volatile("s_waitcnt vmcnt(12)" : "+v"(tq0), "+v"(tq1), W_PIN(Wc) :: "memory");
    {
      const int kap = tid >> 5, ch = tid & 31;
      *(LAS v4u*)(I0 + kap * 512 + ((ch ^ ((kap & 3) << 2)) << 4)) = tq0;
      *(LAS v4u*)(I0 + (kap + 16) * 512 + ((ch ^ ((kap & 3) << 2)) << 4)) = tq1;
    }
    v4u tb[16];
#pragma unroll
    for (int f = 0; f < 16; ++f) tb[f] = lds_ld16(I1 + f * 1024 + lane * 16);
    float W0[8], W1[8];
    w_unpack(Wc, lane, W0, W1);
    WRaw Wd;
    if (special) w_issue(Wd, make_srd(p.wr, NS * 257 * 2), make_srd(p.wi, NS * 257 * 2), 128 * w + 1024, lane);
    BAR_LGKM();
    FT(21);
    LAS char* ta = I0 + (8 * hh + q4) * 512 + (((4 * w + 2 * blk + (p4 >> 1)) ^ (q4 << 2)) << 4) + 8 * (p4 & 1);
    const int n = lane & 31;
    LAS char* so = I1 + n * 512 + 8 * hh;
    {
      f32x16 acc = {};
      acc = mfma32(tr_read2(ta, ta + 2048), tb[6], acc);
      acc = mfma32(tr_read2(ta + 8192, ta + 10240), tb[7], acc);
#pragma unroll
      for (int G = 0; G < 4; ++G) {
        v2u o; o.x = pk2(acc[4 * G], acc[4 * G + 1]); o.y = pk2(acc[4 * G + 2], acc[4 * G + 3]);
        *(LAS v2u*)(so + (((4 * w + G) ^ (n & 15)) << 4)) = o;
      }
    }
    bar_vm<0>();
    if (tid == 0) flag_store(p.ws, WS_FLH + (unsigned)((i * 2 + rb) * 16 + fc) * FS, tag);
    FT(22);
    const f32x4 z4 = {0.f, 0.f, 0.f, 0.f};
    const v4u F16fr = tb[0], F16fi = tb[1];
    v4u F16ir, F16ii;
    F16ir.x = F16fr.x; F16ir.y = F16fr.y; F16ir.z = F16fr.z ^ 0x80008000u; F16ir.w = F16fr.w ^ 0x80008000u;
    F16ii.x = F16fi.x ^ 0x80008000u; F16ii.y = F16fi.y ^ 0x80008000u; F16ii.z = F16fi.z; F16ii.w = F16fi.w;
    f32x4 zr[2], zi[2];
#pragma unroll
    for (int pp = 0; pp < 2; ++pp) {
      const int n0 = 4 * w + 2 * pp, n1 = n0 + 1;
      const int ch = 2 * (4 * g + q4) + (p4 >> 1);
      const v4u a1 = tr_read2(I1 + n0 * 512 + ((ch ^ (n0 & 15)) << 4) + 8 * (p4 & 1), I1 + n1 * 512 + ((ch ^ (n1 & 15)) << 4) + 8 * (p4 & 1));
      const f32x4 dr = mfma16(a1, pp ? tb[2] : F16fr, z4), di = mfma16(a1, pp ? tb[3] : F16fi, z4);
      const v4u b3 = twiddle_pack<false>(dr, di, tb[10 + 2 * pp], tb[11 + 2 * pp]);
      zr[pp] = mfma16(F16fr, b3, z4); zi[pp] = mfma16(F16fi, b3, z4);
    }
    v4u a4[2];
    if (!special) {
      a4[0] = twiddle_pack<false>(zr[0], zi[0], W0[0], W0[1], W0[2], W0[3], W0[4], W0[5], W0[6], W0[7]);
      a4[1] = twiddle_pack<false>(zr[1], zi[1], W1[0], W1[1], W1[2], W1[3], W1[4], W1[5], W1[6], W1[7]);
    } else {
      asm volatile("s_waitcnt vmcnt(0) lgkmcnt(0)\n\ts_barrier" : W_PIN(Wd) :: "memory");
      float X0[8], X1[8];
      w_unpack(Wd, lane, X0, X1);
      LAS char* Z = I0;
#pragma unroll
      for (int pp = 0; pp < 2; ++pp)
#pragma unroll
        for (int r = 0; r < 4; ++r) { f32x2 v = {zr[pp][r], zi[pp][r]}; *(LAS f32x2*)(Z + ((w * 2 + pp) * 256 + l15 + 16 * (4 * g + r)) * 8) = v; }
      BAR_LGKM();
      const cf om = {cosrev((float)w * 0.0625f), -sinrev((float)w * 0.0625f)};
#pragma unroll
      for (int pp = 0; pp < 2; ++pp) {
        float yr[4], yi[4];
#pragma unroll
        for (int r = 0; r < 4; ++r) {
          const int K = l15 + 16 * (4 * g + r), Kn = pp ? 255 - K : (256 - K) & 255;
          const f32x2 y = *(LAS f32x2*)(Z + ((((8 - w) & 7) * 2 + pp) * 256 + Kn) * 8);
          const float zre = zr[pp][r], zim = zi[pp][r], cr = y[0], ci = -y[1];
          const cf E = {0.5f * (zre + cr), 0.5f * (zim + ci)};
          const cf O = {0.5f * (zim - ci), -0.5f * (zre - cr)};
          const float war = pp ? W1[2 * r] : W0[2 * r], wai = pp ? W1[2 * r + 1] : W0[2 * r + 1];
          const float wbr = pp ? X1[2 * r] : X0[2 * r], wbi = pp ? X1[2 * r + 1] : X0[2 * r + 1];
          const cf sp = {war + wbr, wai + wbi}, sm = {war - wbr, wai - wbi};
          const cf t1 = cmulc(sm, om);
          const cf cE = {sp.r - t1.i, sp.i + t1.r};
          const cf t2 = cmul(sm, om);
          const cf cO = {t2.r - sp.i, t2.i + sp.r};
          const cf a = cmul(E, cE), bq = cmul(O, cO);
          yr[r] = 0.5f * (a.r + bq.r); yi[r] = 0.5f * (a.i + bq.i);
        }
        a4[pp].x = pk2(yr[0], yr[1]); a4[pp].y = pk2(yr[2], yr[3]); a4[pp].z = pk2(yi[0], yi[1]); a4[pp].w = pk2(yi[2], yi[3]);
      }
      BAR_LGKM();
    }
#pragma unroll
    for (int pp = 0; pp < 2; ++pp) {
      const f32x4 dr = mfma16(a4[pp], F16ir, z4), di = mfma16(a4[pp], F16ii, z4);
      const v4u a5 = twiddle_pack<true>(dr, di, tb[pp ? 14 : 10], tb[pp ? 15 : 11]);
      const f32x4 er = mfma16(a5, pp ? tb[4] : F16ir, z4), ei = mfma16(a5, pp ? tb[5] : F16ii, z4);
      const int n0 = 4 * w + 2 * pp, n1 = n0 + 1;
      const int ch = 2 * l15 + (g >> 1);
      v2u o; o.x = pk2(er[0], er[1]); o.y = pk2(er[2], er[3]);
      *(LAS v2u*)(I0 + n0 * 512 + ((ch ^ ((n0 & 3) << 2)) << 4) + 8 * (g & 1)) = o;
      o.x = pk2(ei[0], ei[1]); o.y = pk2(ei[2], ei[3]);
      *(LAS v2u*)(I0 + n1 * 512 + ((ch ^ ((n1 & 3) << 2)) << 4) + 8 * (g & 1)) = o;
    }
    BAR_LGKM();
    FT(23);
    {
      f32x16 acc = {};
      acc = mfma32(tr_read2(ta, ta + 2048), tb[8], acc);
      acc = mfma32(tr_read2(ta + 8192, ta + 10240), tb[9], acc);
#pragma unroll
      for (int G = 0; G < 4; ++G) {
        v2u o; o.x = pk2(acc[4 * G], acc[4 * G + 1]); o.y = pk2(acc[4 * G + 2], acc[4 * G + 3]);
        *(LAS v2u*)(so + (((4 * w + G) ^ (n & 15)) << 4)) = o;
      }
    }
    BAR_LGKM();
    {
      const int nn = tid >> 5, ch = tid & 31, ii = (nn >> 1) & 7, d0 = nn & 1;
      const int g0 = special ? 2 * ii : ii, g1 = special ? 2 * ii + 1 : 8 + ii;
      const v4u v0 = lds_ld16(I1 + nn * 512 + ((ch ^ (nn & 15)) << 4));
      const v4u v1 = lds_ld16(I1 + (nn + 16) * 512 + ((ch ^ (nn & 15)) << 4));
      const unsigned ub = WS_U + (unsigned)m1 * 16384u + (unsigned)(d0 * 512 + ch * 16);
      st16_sc1(sws, ub + (unsigned)g0 * 1024u, v0);
      st16_sc1(sws, ub + (unsigned)g1 * 1024u, v1);
    }
    SCHED();
    FT(24);
  }
  STAMP(7);
  FT(25);
  const unsigned flh = WS_FLH + (unsigned)((i * 2 + (jb >> 1)) * 16) * FS;
  if (w == 0) poll_words(sws, flh + 8u * FS, 8, tag, lane);
  asm volatile("s_barrier" ::: "memory");
  STAMP(8);
  FT(26);
  const unsigned vh = (unsigned)lane * 16u;
  const unsigned hbase = WS_H + (unsigned)((i * 8 + 2 * jb + rh) * 112) * 1024u;
  const unsigned hb0 = hbase + (fq0 >> 4) * 1024u, hb1 = hbase + (unsigned)((int)fq1 >> 4) * 1024u;
  v2u gg;
  v4u hf[14][2];
#define H_ISSUE(t) do { LD16(hf[t][0], vh, sws, ((t) < 8 ? hb0 : hb1) + (unsigned)(2 * (t)) * 1024u, "sc1"); LD16(hf[t][1], vh, sws, ((t) < 8 ? hb0 : hb1) + (unsigned)(2 * (t) + 1) * 1024u, "sc1"); } while (0)
  H_ISSUE(0); H_ISSUE(1);
  asm volatile("s_barrier" ::: "memory");
  H_ISSUE(2); H_ISSUE(3); H_ISSUE(4); H_ISSUE(5); H_ISSUE(6);
  if (!isA) gen_g();
  SCHED();
  f32x16 accb[2] = {}, acca[2] = {};
  const int sz = (q4 >> 1) & 1;
  LAS char* ra0 = L + q * 4096 + (4 * hh + q4) * 128 + (((0 ^ (4 * sz)) + 2 * blk + (p4 >> 1)) << 4) + 8 * (p4 & 1);
  LAS char* ra1 = L + q * 4096 + (4 * hh + q4) * 128 + (((4 ^ (4 * sz)) + 2 * blk + (p4 >> 1)) << 4) + 8 * (p4 & 1);
  v4u wf[2][4];
#define G2_BAR(n, t) asm volatile("s_waitcnt vmcnt(" STR(n) ") lgkmcnt(0)\n\ts_barrier" : "+v"(hf[t][0]), "+v"(hf[t][1]) :: "memory")
  G2_BAR(12, 0);
  if (!isA && tid == 0) flag_store(p.ws, WS_FLB + (unsigned)m1 * FS, tag);
  wf[0][0] = tr_read2(ra0, ra0 + 1024); wf[0][1] = tr_read2(ra1, ra1 + 1024);
  wf[0][2] = tr_read2(ra0 + 2048, ra0 + 3072); wf[0][3] = tr_read2(ra1 + 2048, ra1 + 3072);
  unsigned fl0, fl1, flb;
  v4u ub0, ub1, ub2, ub3;
  const unsigned vu = WS_U + (unsigned)(((64 * q + rho) * 8 + i) * 1024 + dt * 128 + chs * 16);
#define G2_MMA(t, acc, b0, b1, X0, X1, X2)                                                        \
  {                                                                                                 \
    FT(30 + (t));                                                                                   \
    v4u (&c)[4] = wf[(t) & 1];                                                                     \
    v4u (&n)[4] = wf[((t) + 1) & 1];                                                               \
    LAS char* s0 = ra0 + (((t) + 1) & 7) * 16384; LAS char* s1 = ra1 + (((t) + 1) & 7) * 16384;    \
    asm volatile("" : "+v"(c[0]), "+v"(c[1]), "+v"(c[2]), "+v"(c[3]));                             \
    SCHED();                                                                                        \
    acc[0] = mfma32(c[0], b0, acc[0]); if ((t) < 15) n[0] = tr_read2(s0, s0 + 1024); SCHED();      \
    acc[1] = mfma32(c[1], b0, acc[1]); if ((t) < 15) n[1] = tr_read2(s1, s1 + 1024); X0; SCHED();  \
    acc[0] = mfma32(c[2], b1, acc[0]); if ((t) < 15) n[2] = tr_read2(s0 + 2048, s0 + 3072); X1; SCHED(); \
    acc[1] = mfma32(c[3], b1, acc[1]); if ((t) < 15) n[3] = tr_read2(s1 + 2048, s1 + 3072); X2; SCHED(); \
  }
#define NOP do {} while (0)
#define G2_STEP(t, X2) G2_MMA(t, accb, hf[t][0], hf[t][1], if ((t) <= 5) w2_issue1((t) + 8, 0), if ((t) <= 5) w2_issue1((t) + 8, 1), X2)
  BAR_LGKM();    G2_STEP(0, H_ISSUE(7))
  G2_BAR(14, 1); G2_STEP(1, NOP)
  G2_BAR(14, 2); G2_STEP(2, NOP)
  if (w == 0) LD4(flb, flh + (unsigned)(lane & 7) * FS, sws, 0u, "sc1");
  G2_BAR(14, 3); G2_STEP(3, NOP)
  if (w == 0) {
    asm volatile("s_waitcnt vmcnt(2)" : "+v"(flb) :: "memory");
    if (!__all(flb == tag)) poll_words(sws, flh, 8, tag, lane);
  }
  G2_BAR(14, 4); G2_STEP(4, { H_ISSUE(8); H_ISSUE(9); LD8(gg, WS_GATE + (unsigned)(i * 256 + j) * 8u, sws, 0u, "sc1"); })
  G2_BAR(19, 5); G2_STEP(5, { H_ISSUE(10); H_ISSUE(11); })
  if (w == 0) { LD4(fl0, WS_FLB + (unsigned)lane * FS, sws, 0u, "sc1"); LD4(fl1, WS_FLB + (64u + (unsigned)lane) * FS, sws, 0u, "sc1"); }
  G2_BAR(23, 6); G2_STEP(6, { H_ISSUE(12); H_ISSUE(13); })
  STAMP(9);
  G2_BAR(23, 7); G2_STEP(7, NOP)
  if (w == 0) {
    asm volatile("s_waitcnt vmcnt(4)" : "+v"(fl0), "+v"(fl1) :: "memory");
    if (!__all(fl0 == tag && fl1 == tag)) { poll_words(sws, WS_FLB, 64, tag, lane); poll_words(sws, WS_FLB + 64u * FS, 64, tag, lane); }
  }
  G2_BAR(13, 8);
  LD16(ub0, vu, sws, 0u, "sc1"); LD16(ub1, vu, sws, 8u * 8192u, "sc1"); LD16(ub2, vu, sws, 32u * 8192u, "sc1"); LD16(ub3, vu, sws, 40u * 8192u, "sc1");
  G2_STEP(8, NOP)
  G2_BAR(15, 9); G2_STEP(9, NOP)
  G2_BAR(10, 10); G2_STEP(10, NOP)
  G2_BAR(8, 11); G2_STEP(11, NOP)
  G2_BAR(6, 12);
  G2_STEP(12, NOP)
  asm volatile("s_waitcnt vmcnt(0)" : "+v"(ub0), "+v"(ub1), "+v"(ub2), "+v"(ub3), "+v"(gg) :: "memory");
  *(LAS v4u*)(L + 6 * 16384 + ldsw + lane * 16) = ub0;
  *(LAS v4u*)(L + 6 * 16384 + ldsw + 1024 + lane * 16) = ub1;
  *(LAS v4u*)(L + 7 * 16384 + ldsw + lane * 16) = ub2;
  *(LAS v4u*)(L + 7 * 16384 + ldsw + 1024 + lane * 16) = ub3;
  G2_BAR(0, 13); G2_STEP(13, NOP)
  STAMP(10);
  BAR_LGKM(); G2_MMA(14, acca, gf[0], gf[1], NOP, NOP, NOP)
  BAR_LGKM(); G2_MMA(15, acca, gf[2], gf[3], NOP, NOP, NOP)
  FT(50);
  {
    const float og = asf(gg.y), gs = asf(gg.x) * (1.f / 524288.f);
    const int row = 32 * rh + (lane & 31);
#pragma unroll
    for (int dh = 0; dh < 2; ++dh)
#pragma unroll
      for (int G = 0; G < 4; ++G) {
        f32x4 v;
#pragma unroll
        for (int e = 0; e < 4; ++e) v[e] = __builtin_fmaf(og, accb[dh][4 * G + e], gs * acca[dh][4 * G + e]);
        *(LAS f32x4*)(L + q * 16384 + row * 256 + (((8 * dh + 2 * G + hh) ^ (row & 7)) << 4)) = v;
      }
  }
  BAR_LGKM();
  FT(51);
  {
    const int row = tid >> 3, c8 = tid & 7;
    f32x4 s0 = {0.f, 0.f, 0.f, 0.f}, s1 = s0;
#pragma unroll
    for (int qq = 0; qq < 4; ++qq) {
      s0 += *(LAS f32x4*)(L + qq * 16384 + row * 256 + (((2 * c8) ^ (row & 7)) << 4));
      s1 += *(LAS f32x4*)(L + qq * 16384 + row * 256 + (((2 * c8 + 1) ^ (row & 7)) << 4));
    }
    v4u o; o.x = pk2(s0[0], s0[1]); o.y = pk2(s0[2], s0[3]); o.z = pk2(s1[0], s1[1]); o.w = pk2(s1[2], s1[3]);
    *(GAS v4u*)((GAS char*)p.out + (size_t)(i + 8 * (64 * jb + row)) * 1024 + dt * 128 + c8 * 16) = o;
  }
  STAMP(11);
  if (b == 0 && w == 0) {
#pragma unroll 1
    for (int u = 0; u < 4; ++u) poll_words(sws, WS_FLH + 64u * FS * u, 64, tag, lane);
    if (lane == 0) flag_store(p.ws, WS_EPOCH, tag);
  }
}

}

extern "C" void kernel_launch(void* const* d_in, const int* in_sizes, int n_in, void* d_out, int out_size, void* d_ws, size_t ws_size,
                              hipStream_t stream) {
  Params p;
  p.x = d_in[0]; p.wr = d_in[1]; p.wi = d_in[2]; p.w1 = d_in[4]; p.w2 = d_in[5]; p.gw = d_in[6];
  p.out = d_out; p.ws = d_ws;
  (void)hipFuncSetAttribute((const void*)gm_kernel, hipFuncAttributeMaxDynamicSharedMemorySize, LDS_BYTES);
  gm_kernel<<<256, 512, LDS_BYTES, stream>>>(p);
}
```

```cpp
#include <hip/hip_runtime.h>
#include <hip/hip_bf16.h>
#include <stdint.h>

namespace {

typedef unsigned v4u __attribute__((ext_vector_type(4)));
typedef unsigned v2u __attribute__((ext_vector_type(2)));
typedef float f32x16 __attribute__((ext_vector_type(16)));
typedef float f32x4 __attribute__((ext_vector_type(4)));
typedef float f32x2 __attribute__((ext_vector_type(2)));
typedef __bf16 bf16x8 __attribute__((ext_vector_type(8)));
typedef __bf16 bf16x2 __attribute__((ext_vector_type(2)));
typedef short s16x4 __attribute__((ext_vector_type(4)));
#define LAS __attribute__((address_space(3)))
#define GAS __attribute__((address_space(1)))
#define DI __device__ __forceinline__

constexpr int NS = 2048, ND = 512, NF = 1792;
constexpr unsigned WS_EPOCH = 0;
constexpr unsigned FS = 64;
constexpr unsigned WS_FLA = 4096;
constexpr unsigned WS_FLH = 16384;
constexpr unsigned WS_FLB = 32768;
constexpr unsigned WS_GATE = 65536;
constexpr unsigned WS_T = 1u << 20;
constexpr unsigned WS_U = 3u << 20;
constexpr unsigned WS_H = 5u << 20;
constexpr unsigned H_BYTES = 8u * 8u * 112u * 1024u;
constexpr int LDS_BYTES = 160 * 1024;
constexpr unsigned kSpinCap = 1u << 20;

struct Params {
  const void* x; const void* wr; const void* wi; const void* w1; const void* w2; const void* gw;
  void* out; void* ws;
};

constexpr double kPi = 3.14159265358979323846;
constexpr double ccos(double x) { double t = 1, s = 1; for (int n = 1; n < 16; ++n) { t *= -x * x / ((2 * n - 1) * (2 * n)); s += t; } return s; }
constexpr double cosn(int e, int n) { e = ((e % n) + n) % n; double x = 2 * kPi * e / n; if (x > kPi) x -= 2 * kPi; return ccos(x); }
constexpr double wre(int e, int n) { return cosn(e, n); }
constexpr double wim(int e, int n) { return -cosn(4 * e - n, 4 * n); }
constexpr unsigned bfb(double v) { float f = (float)v; unsigned u = __builtin_bit_cast(unsigned, f); u += 0x7fffu + ((u >> 16) & 1u); return u >> 16; }
constexpr unsigned bf2(double a, double b) { return bfb(a) | (bfb(b) << 16); }
constexpr unsigned fbits(double v) { return __builtin_bit_cast(unsigned, (float)v); }
struct Tab { unsigned v[16][64][4]; };
constexpr Tab make_tab() {
  Tab t{};
  for (int l = 0; l < 64; ++l) {
    const int g = l >> 4, l15 = l & 15, hh = l >> 5, n = l & 31;
    for (int mtx = 0; mtx < 3; ++mtx) {
      double r[4] = {}, im[4] = {};
      for (int q = 0; q < 4; ++q) {
        const int a = 4 * g + q;
        if (mtx == 0) { r[q] = wre(l15 * a, 16); im[q] = wim(l15 * a, 16); }
        else if (mtx == 1) { r[q] = wre(a * (2 * l15 + 1), 32); im[q] = wim(a * (2 * l15 + 1), 32); }
        else { r[q] = wre(l15 * (2 * a + 1), 32); im[q] = -wim(l15 * (2 * a + 1), 32); }
      }
      t.v[2 * mtx][l][0] = bf2(r[0], r[1]); t.v[2 * mtx][l][1] = bf2(r[2], r[3]); t.v[2 * mtx][l][2] = bf2(-im[0], -im[1]); t.v[2 * mtx][l][3] = bf2(-im[2], -im[3]);
      t.v[2 * mtx + 1][l][0] = bf2(im[0], im[1]); t.v[2 * mtx + 1][l][1] = bf2(im[2], im[3]); t.v[2 * mtx + 1][l][2] = bf2(r[0], r[1]); t.v[2 * mtx + 1][l][3] = bf2(r[2], r[3]);
    }
    {
      const int m2 = n >> 2, pp = (n >> 1) & 1, cp = n & 1;
      double v0[8] = {}, v1[8] = {};
      for (int jj = 0; jj < 8; ++jj) {
        const int ii = (8 * hh + jj) >> 1, d0 = jj & 1;
        const double sg = (d0 & pp) ? -1.0 : 1.0, orr = sg * wre(ii * m2, 8), oi = sg * wim(ii * m2, 8);
        v0[jj] = cp ? oi : orr; v1[jj] = cp ? orr : -oi;
      }
      for (int q = 0; q < 4; ++q) { t.v[6][l][q] = bf2(v0[2 * q], v0[2 * q + 1]); t.v[7][l][q] = bf2(v1[2 * q], v1[2 * q + 1]); }
    }
    {
      const int cq = n >> 4, ii = (n >> 1) & 7, d0 = n & 1;
      for (int s = 0; s < 2; ++s) {
        double v[8] = {};
        for (int jj = 0; jj < 8; ++jj) {
          const int m2k = 4 * s + 2 * hh + (jj >> 2), pk = (jj >> 1) & 1, ck = jj & 1;
          const double sg = (d0 & pk) ? -1.0 : 1.0, orr = sg * wre(ii * m2k, 8), oi = -sg * wim(ii * m2k, 8);
          v[jj] = cq ? (ck ? orr : oi) : (ck ? -oi : orr);
        }
        for (int q = 0; q < 4; ++q) t.v[8 + s][l][q] = bf2(v[2 * q], v[2 * q + 1]);
      }
    }
    for (int q = 0; q < 4; ++q) {
      const int a = 4 * g + q, o = (q & 1) * 2, f = q >> 1;
      t.v[10 + f][l][o] = fbits(wre(a * (2 * l15), 512));      t.v[10 + f][l][o + 1] = fbits(wim(a * (2 * l15), 512));
      t.v[12 + f][l][o] = fbits(wre(a * (2 * l15 + 1), 512));  t.v[12 + f][l][o + 1] = fbits(wim(a * (2 * l15 + 1), 512));
      t.v[14 + f][l][o] = fbits(wre(l15 * (2 * a + 1), 512));  t.v[14 + f][l][o + 1] = fbits(wim(l15 * (2 * a + 1), 512));
    }
  }
  return t;
}
__device__ const Tab kTab = make_tab();

constexpr unsigned STAMP_OFF = 512 * 1024;
#define STAMP(k) do {} while (0)
#define FT(k) do {} while (0)

DI v4u make_srd(const void* p, unsigned bytes) {
  const unsigned long long a = (unsigned long long)p;
  v4u r; r.x = (unsigned)a; r.y = (unsigned)(a >> 32) & 0xffffu; r.z = bytes; r.w = 0x00020000u;
  return r;
}
DI void dma16(v4u srd, unsigned voff, unsigned soff, unsigned lds) {
  asm volatile("s_mov_b32 m0, %3\n\ts_nop 0\n\tbuffer_load_dwordx4 %0, %1, %2 offen lds"
               :: "v"(voff), "s"(srd), "s"(soff), "s"(lds) : "memory");
}
#define LD16(dst, voffv, srd, soffv, flags) asm volatile("buffer_load_dwordx4 %0, %1, %2, %3 offen " flags : "=v"(dst) : "v"(voffv), "s"(srd), "s"(soffv) : "memory")
#define LD8(dst, voffv, srd, soffv, flags) asm volatile("buffer_load_dwordx2 %0, %1, %2, %3 offen " flags : "=v"(dst) : "v"(voffv), "s"(srd), "s"(soffv) : "memory")
#define LD4(dst, voffv, srd, soffv, flags) asm volatile("buffer_load_dword %0, %1, %2, %3 offen " flags : "=v"(dst) : "v"(voffv), "s"(srd), "s"(soffv) : "memory")
#define LD2(dst, voffv, srd, soffv, flags) asm volatile("buffer_load_ushort %0, %1, %2, %3 offen " flags : "=v"(dst) : "v"(voffv), "s"(srd), "s"(soffv) : "memory")
#define XSTR(x) #x
#define STR(x) XSTR(x)
#define BAR_VM(n) asm volatile("s_waitcnt vmcnt(" STR(n) ") lgkmcnt(0)\n\ts_barrier" ::: "memory")
#define BAR_LGKM() asm volatile("s_waitcnt lgkmcnt(0)\n\ts_barrier" ::: "memory")
#define SCHED() __builtin_amdgcn_sched_barrier(0)
template <int N> DI void bar_vm() { asm volatile("s_waitcnt vmcnt(%0) lgkmcnt(0)\n\ts_barrier" :: "i"(N) : "memory"); }
template <int N> DI void wait_vm() { asm volatile("s_waitcnt vmcnt(%0)" :: "i"(N) : "memory"); }

DI unsigned pk2(float a, float b) { f32x2 p = {a, b}; return __builtin_bit_cast(unsigned, __builtin_convertvector(p, bf16x2)); }
DI float bflo(unsigned u) { return __builtin_bit_cast(float, u << 16); }
DI float bfhi(unsigned u) { return __builtin_bit_cast(float, u & 0xffff0000u); }
DI float asf(unsigned u) { return __builtin_bit_cast(float, u); }
DI f32x16 mfma32(v4u a, v4u b, f32x16 c) {
  return __builtin_amdgcn_mfma_f32_32x32x16_bf16(__builtin_bit_cast(bf16x8, a), __builtin_bit_cast(bf16x8, b), c, 0, 0, 0);
}
DI f32x4 mfma16(v4u a, v4u b, f32x4 c) {
  return __builtin_amdgcn_mfma_f32_16x16x32_bf16(__builtin_bit_cast(bf16x8, a), __builtin_bit_cast(bf16x8, b), c, 0, 0, 0);
}
DI v2u tr_read(LAS char* p) { return __builtin_bit_cast(v2u, __builtin_amdgcn_ds_read_tr16_b64_v4i16((LAS s16x4*)p)); }
DI v4u tr_read2(LAS char* p0, LAS char* p1) { v2u a = tr_read(p0), b = tr_read(p1); v4u r; r.x = a.x; r.y = a.y; r.z = b.x; r.w = b.y; return r; }
DI v4u lds_ld16(LAS char* p) { return *(LAS v4u*)p; }
DI float cosrev(float r) { return __builtin_amdgcn_cosf(r); }
DI float sinrev(float r) { return __builtin_amdgcn_sinf(r); }
DI __amdgpu_buffer_rsrc_t as_rsrc(v4u srd) { return __builtin_amdgcn_make_buffer_rsrc((void*)(((unsigned long long)srd.y << 32) | srd.x), 0, srd.z, 0x00020000); }
DI void st16_sc1(v4u srd, unsigned voff, v4u v) { __builtin_amdgcn_raw_buffer_store_b128(v, as_rsrc(srd), voff, 0, 16); }
DI void flag_store(void* ws, unsigned off, unsigned tag) { __hip_atomic_store((GAS unsigned*)((GAS char*)ws + off), tag, __ATOMIC_RELAXED, __HIP_MEMORY_SCOPE_AGENT); }
template <int CTRL, int RMASK> DI float dpp_add(float v) { return v + asf((unsigned)__builtin_amdgcn_update_dpp(0, (int)__builtin_bit_cast(unsigned, v), CTRL, RMASK, 0xf, true)); }
DI void poll_words(v4u sws, unsigned off, int n, unsigned tag, int lane) {
  const unsigned vo = off + FS * (unsigned)(lane < n ? lane : 0);
#pragma unroll 1
  for (unsigned spins = 0; spins < kSpinCap; ++spins) {
    unsigned f;
    asm volatile("buffer_load_dword %0, %1, %2, 0 offen sc1\n\ts_waitcnt vmcnt(0)" : "=&v"(f) : "v"(vo), "s"(sws) : "memory");
    if (__all(f == tag)) return;
    __builtin_amdgcn_s_sleep(4);
  }
}

constexpr unsigned A_XIMG = 64 * 1024, A_FIMG = 128 * 1024;
DI void phaseA_genF(LAS char* L, int i, int mt, int w, int lane) {
  const int mh = w >> 2, sg = w & 3, n = lane & 31, hh = lane >> 5;
  const int m1c = 64 * mt + 32 * mh + n, m1 = m1c >> 1, c = m1c & 1;
  const int j0 = 64 * sg + 32 * hh;
  const float ph = c ? 0.25f : 0.f;
  const float k2 = 2.f * cosrev((float)(8 * m1) * (1.f / 2048.f));
  float vm = cosrev((float)(((i + 8 * (j0 - 1)) * m1) & 2047) * (1.f / 2048.f) + ph);
  float v0 = cosrev((float)(((i + 8 * j0) * m1) & 2047) * (1.f / 2048.f) + ph);
  const unsigned cst = c ? 0x3F800000u : 0x00003F80u;
  const bool sp = m1c < 2;
#pragma unroll
  for (int s = 0; s < 4; ++s) {
    float e[8];
#pragma unroll
    for (int q = 0; q < 8; ++q) { e[q] = v0; const float vn = __builtin_fmaf(k2, v0, -vm); vm = v0; v0 = vn; }
    v4u f; f.x = pk2(e[0], e[1]); f.y = pk2(e[2], e[3]); f.z = pk2(e[4], e[5]); f.w = pk2(e[6], e[7]);
    if (mt == 0 && w < 4) { f.x = sp ? cst : f.x; f.y = sp ? cst : f.y; f.z = sp ? cst : f.z; f.w = sp ? cst : f.w; }
    *(LAS v4u*)(L + A_FIMG + (mh * 16 + sg * 4 + s) * 1024 + lane * 16) = f;
  }
}

struct Frag1 { v4u a[4], b0[4], b1[4]; };
DI void g1_read(Frag1& f, LAS char* wa, LAS char* xb0, LAS char* xb1, LAS char* xb2, LAS char* xb3, int slot) {
  const int so = slot * 32768;
  f.a[0] = tr_read2(wa + so, wa + so + 1024);              f.b0[0] = lds_ld16(xb0 + so); f.b1[0] = lds_ld16(xb0 + so + 4096);
  f.a[1] = tr_read2(wa + so + 4096, wa + so + 5120);       f.b0[1] = lds_ld16(xb1 + so); f.b1[1] = lds_ld16(xb1 + so + 4096);
  f.a[2] = tr_read2(wa + so + 8192, wa + so + 9216);       f.b0[2] = lds_ld16(xb2 + so); f.b1[2] = lds_ld16(xb2 + so + 4096);
  f.a[3] = tr_read2(wa + so + 12288, wa + so + 13312);     f.b0[3] = lds_ld16(xb3 + so); f.b1[3] = lds_ld16(xb3 + so + 4096);
}
DI float silu(float h) { return h * __builtin_amdgcn_rcpf(1.f + __builtin_amdgcn_exp2f(h * -1.44269504088896341f)); }

struct cf { float r, i; };
DI cf cmul(cf a, cf b) { return {a.r * b.r - a.i * b.i, a.r * b.i + a.i * b.r}; }
DI cf cmulc(cf a, cf b) { return {a.r * b.r + a.i * b.i, a.i * b.r - a.r * b.i}; }
template <bool CONJ> DI v4u twiddle_pack(const f32x4& re, const f32x4& im, float t0r, float t0i, float t1r, float t1i, float t2r, float t2i, float t3r, float t3i) {
  const float tr[4] = {t0r, t1r, t2r, t3r}, ti[4] = {t0i, t1i, t2i, t3i};
  float a[4], b[4];
#pragma unroll
  for (int q = 0; q < 4; ++q) {
    const float xr = re[q], xi = im[q], s = CONJ ? -ti[q] : ti[q];
    a[q] = __builtin_fmaf(xr, tr[q], -(xi * s)); b[q] = __builtin_fmaf(xr, s, xi * tr[q]);
  }
  v4u r; r.x = pk2(a[0], a[1]); r.y = pk2(a[2], a[3]); r.z = pk2(b[0], b[1]); r.w = pk2(b[2], b[3]); return r;
}
template <bool CONJ> DI v4u twiddle_pack(const f32x4& re, const f32x4& im, v4u t0, v4u t1) {
  return twiddle_pack<CONJ>(re, im, asf(t0.x), asf(t0.y), asf(t0.z), asf(t0.w), asf(t1.x), asf(t1.y), asf(t1.z), asf(t1.w));
}
struct WRaw { unsigned r0, r1, r2, r3, i0, i1, i2, i3, xr, xi; };
struct WAddr { unsigned e0, e1, e2, e3, ex; };
DI WAddr w_addr(int m, int lane) {
  const int k1 = lane & 15, g = lane >> 4, mm = (NS - m) & (NS - 1);
  WAddr a;
  a.e0 = g < 2 ? (unsigned)(m * 257 + 2 * k1 + 128 * g) * 2u : (unsigned)(mm * 257 + 511 - 2 * k1 - 128 * g) * 2u;
  a.e1 = g < 2 ? a.e0 + 64u : a.e0 - 64u; a.e2 = g < 2 ? a.e0 + 128u : a.e0 - 128u; a.e3 = g < 2 ? a.e0 + 192u : a.e0 - 192u;
  a.ex = (unsigned)(g < 2 ? mm * 257 : m * 257 + 256) * 2u;
  return a;
}
DI void w_issue(WRaw& o, v4u swr, v4u swi, int m, int lane) {
  const WAddr a = w_addr(m, lane);
  LD4(o.r0, a.e0, swr, 0u, ""); LD4(o.r1, a.e1, swr, 0u, ""); LD4(o.r2, a.e2, swr, 0u, ""); LD4(o.r3, a.e3, swr, 0u, "");
  LD4(o.i0, a.e0, swi, 0u, ""); LD4(o.i1, a.e1, swi, 0u, ""); LD4(o.i2, a.e2, swi, 0u, ""); LD4(o.i3, a.e3, swi, 0u, "");
  LD2(o.xr, a.ex, swr, 0u, ""); LD2(o.xi, a.ex, swi, 0u, "");
}
#define W_PIN(o) "+v"(o.r0), "+v"(o.r1), "+v"(o.r2), "+v"(o.r3), "+v"(o.i0), "+v"(o.i1), "+v"(o.i2), "+v"(o.i3), "+v"(o.xr), "+v"(o.xi)
DI void w_unpack(const WRaw& o, int lane, float (&w0)[8], float (&w1)[8]) {
  const bool lo = lane < 32;
  const unsigned rr[4] = {o.r0, o.r1, o.r2, o.r3}, ii[4] = {o.i0, o.i1, o.i2, o.i3};
  const unsigned sg = lo ? 0u : 0x80000000u;
#pragma unroll
  for (int r = 0; r < 4; ++r) {
    const unsigned a0 = lo ? rr[r] << 16 : rr[r] & 0xffff0000u, a1 = lo ? rr[r] & 0xffff0000u : rr[r] << 16;
    const unsigned b0 = lo ? ii[r] << 16 : ii[r] & 0xffff0000u, b1 = lo ? ii[r] & 0xffff0000u : ii[r] << 16;
    w0[2 * r] = asf(a0); w0[2 * r + 1] = asf(b0 ^ sg); w1[2 * r] = asf(a1); w1[2 * r + 1] = asf(b1 ^ sg);
  }
  if ((lane & 31) == 0) {
    w0[0] = 0.5f * (w0[0] + bflo(o.xr));
    w0[1] = 0.5f * (w0[1] + asf((o.xi << 16) ^ (sg ^ 0x80000000u)));
  }
}

#define G1_MMA true
__global__ __launch_bounds__(512, 2) void gm_kernel(Params p) {
  extern __shared__ __attribute__((aligned(16))) char smem[];
  LAS char* L = (LAS char*)smem;
  const int tid = threadIdx.x, lane = tid & 63, w = __builtin_amdgcn_readfirstlane(tid >> 6);
  const int b = blockIdx.x, i = b & 7, Lx = b >> 3, rb = Lx >> 4, fc = Lx & 15;
  const bool isA = fc < 8;
  const int hh = lane >> 5, blk = (lane >> 4) & 1, q4 = (lane & 15) >> 2, p4 = lane & 3;
  STAMP(0);
  const long long pad_t0 = __builtin_amdgcn_s_memrealtime();
  FT(0);
  const v4u sx = make_srd(p.x, NS * ND * 2), sw1 = make_srd(p.w1, ND * NF * 2), sws = make_srd(p.ws, 16u << 20);
  unsigned ep; v4u gx, gv;
  const int jg = 128 * rb + 8 * fc + w;
  LD16(gx, (unsigned)lane * 16u, sx, (unsigned)(i + 8 * jg) * 1024u, "");
  LD16(gv, (unsigned)lane * 16u, make_srd(p.gw, ND * 2), 0u, "");
  LD4(ep, 0u, sws, WS_EPOCH, "sc1");
  const int f0 = isA ? 96 * fc : 768 + 128 * (fc - 8), nf = isA ? 96 : 128;
  const int a_t = rb * 8 + fc, mt = a_t >> 2, dt4 = a_t & 3;
  const int m1 = ((rb * 8 + (fc - 8)) << 3) | i;
  if (isA) {
    const unsigned voff = (unsigned)(lane >> 4) * 8192u + (unsigned)(((lane & 15) ^ ((lane >> 4) << 2)) << 4);
#pragma unroll
    for (int u = 0; u < 8; ++u) {
      const unsigned row = 32 * w + 4 * u;
      dma16(sx, voff, (unsigned)i * 1024u + (unsigned)dt4 * 256u + row * 8192u, A_XIMG + row * 256u);
    }
  }
  unsigned vx0, vx1, vw0, vw1;
  {
    const int r0 = 16 * w + (lane >> 3), pos = lane & 7, r1 = r0 + 8;
    vx0 = (unsigned)(i * 1024 + (128 * rb + r0) * 8192 + ((pos ^ ((r0 >> 1) & 7)) << 4));
    vx1 = (unsigned)(i * 1024 + (128 * rb + r1) * 8192 + ((pos ^ ((r1 >> 1) & 7)) << 4));
    const int k0 = 8 * w + (lane >> 4), chunk = (lane & 15) ^ ((lane >> 4) << 2);
    const bool ok = chunk * 8 < nf;
    vw0 = ok ? (unsigned)(k0 * 3584 + f0 * 2 + chunk * 16) : 0x80000000u;
    vw1 = ok ? vw0 + 4u * 3584u : 0x80000000u;
  }
  auto g1_issue = [&](int kt) {
    const unsigned slot = (unsigned)(kt & 3) * 32768u + (unsigned)w * 2048u;
    dma16(sx, vx0, (unsigned)kt * 128u, slot);
    dma16(sx, vx1, (unsigned)kt * 128u, slot + 1024u);
    dma16(sw1, vw0, (unsigned)kt * (64u * 3584u), slot + 16384u);
    dma16(sw1, vw1, (unsigned)kt * (64u * 3584u), slot + 17408u);
  };
  if (!isA) {
    g1_issue(0); asm volatile("s_barrier" ::: "memory");
    g1_issue(1); asm volatile("s_barrier" ::: "memory");
    g1_issue(2); asm volatile("s_barrier" ::: "memory");
    g1_issue(3);
  } else {
    phaseA_genF(L, i, mt, w, lane);
    g1_issue(0); g1_issue(1);
  }
  FT(1);
  wait_vm<16>();
  asm volatile("" : "+v"(ep), "+v"(gx), "+v"(gv) :: "memory");
  const unsigned tag = (unsigned)__builtin_amdgcn_readfirstlane((int)ep) + 1u;
  float gate_g, gate_og;
  {
    float z = 0.f;
#pragma unroll
    for (int q = 0; q < 4; ++q) {
      const unsigned pr = pk2(bflo(gx[q]) * bflo(gv[q]), bfhi(gx[q]) * bfhi(gv[q]));
      z += bflo(pr) + bfhi(pr);
    }
    z = dpp_add<0x111, 0xf>(z); z = dpp_add<0x112, 0xf>(z); z = dpp_add<0x114, 0xf>(z); z = dpp_add<0x118, 0xf>(z);
    z = dpp_add<0x142, 0xa>(z); z = dpp_add<0x143, 0xc>(z);
    const float zb = bflo(pk2(z, 0.f));
    const float g = bflo(pk2(__builtin_amdgcn_rcpf(1.f + __builtin_amdgcn_exp2f(zb * -1.44269504088896341f)), 0.f));
    gate_g = asf((unsigned)__builtin_amdgcn_readlane((int)__builtin_bit_cast(unsigned, g), 63));
    gate_og = bflo(pk2(1.f - gate_g, 0.f));
  }
  FT(2);
  if (isA) {
    const int dq = w & 3, mh = w >> 2;
    LAS char* xa = L + A_XIMG + (32 * hh + q4) * 256 + (((4 * dq + 2 * blk + (p4 >> 1)) ^ (q4 << 2)) << 4) + 8 * (p4 & 1);
    LAS char* fb = L + A_FIMG + mh * 16384 + lane * 16;
    const int srow = 32 * mh + (lane & 31);
    LAS char* stw = L + A_FIMG + srow * 256 + 8 * hh;
    const int rrow = tid >> 3;
    LAS char* str = L + A_FIMG + rrow * 256;
    const unsigned tdst = WS_T + (unsigned)(((64 * mt + rrow) * 8 + i) * 1024 + dt4 * 256 + (tid & 7) * 16);
    if (w < 4) bar_vm<8>(); else BAR_LGKM();
    STAMP(1);
    FT(3);
    f32x16 acc = {};
    v4u fa[2][4], fbv[2][4];
#pragma unroll
    for (int s = 0; s < 4; ++s) { fa[0][s] = tr_read2(xa + 8 * s * 256, xa + 8 * s * 256 + 1024); fbv[0][s] = lds_ld16(fb + s * 1024); }
#pragma unroll
    for (int g = 0; g < 4; ++g) {
      acc = mfma32(fa[g & 1][0], fbv[g & 1][0], acc);
      SCHED();
      if (g == 1) { if (w >= 4) bar_vm<8>(); else BAR_LGKM(); }
      if (g < 3) {
#pragma unroll
        for (int s = 0; s < 4; ++s) {
          const int ro = (64 * (g + 1) + 8 * s) * 256;
          fa[(g + 1) & 1][s] = tr_read2(xa + ro, xa + ro + 1024); fbv[(g + 1) & 1][s] = lds_ld16(fb + (4 * (g + 1) + s) * 1024);
        }
      }
      SCHED();
#pragma unroll
      for (int s = 1; s < 4; ++s) acc = mfma32(fa[g & 1][s], fbv[g & 1][s], acc);
      SCHED();
    }
    FT(4);
    BAR_LGKM();   STAMP(2);
    g1_issue(2); g1_issue(3);
#pragma unroll
    for (int G = 0; G < 4; ++G) {
      v2u o; o.x = pk2(acc[4 * G], acc[4 * G + 1]); o.y = pk2(acc[4 * G + 2], acc[4 * G + 3]);
      *(LAS v2u*)(stw + (((4 * dq + G) ^ (srow & 15)) << 4)) = o;
    }
    BAR_LGKM();
    {
      const v4u v0 = lds_ld16(str + (((tid & 7) ^ (rrow & 15)) << 4)), v1 = lds_ld16(str + ((((tid & 7) + 8) ^ (rrow & 15)) << 4));
      st16_sc1(sws, tdst, v0);
      st16_sc1(sws, tdst + 128u, v1);
    }
    SCHED();
    FT(5);
  }
  FT(6);
  const int jb = Lx >> 3, dt = Lx & 7, rh = w & 1, q = w >> 1;
  const v4u sw2 = make_srd(p.w2, NF * ND * 2);
  const int rho = 16 * rh + (lane >> 3), chs = (lane & 7) ^ (4 * ((lane >> 4) & 1));
  const unsigned vw20 = (unsigned)(rho * 1024 + dt * 128 + chs * 16), vw21 = vw20 + 8 * 1024;
  const unsigned ldsw = (unsigned)(q * 4096 + rh * 2048);
  const unsigned fq0 = (unsigned)(768 + 256 * q), fq1 = (unsigned)(192 * q - 256);
  auto w2_issue1 = [&](int t, int u) {
    const unsigned slot = (unsigned)(t & 7) * 16384u + ldsw + (unsigned)u * 1024u;
    dma16(sw2, u ? vw21 : vw20, ((t < 8 ? fq0 : fq1) + 32u * (unsigned)t) * 1024u, slot);
  };
  const int ft = w & 3, rh1 = w >> 2;
  const bool active = ft * 32 < nf;
  f32x16 acc1[2] = {};
  v4u tq0, tq1;
  WRaw Wc;
  {
    LAS char* wa = L + 16384 + (8 * hh + q4) * 256 + (((4 * ft + 2 * blk + (p4 >> 1)) ^ (q4 << 2)) << 4) + 8 * (p4 & 1);
    const int r = 64 * rh1 + (lane & 31), fz = (r >> 1) & 7;
    LAS char* xb0 = L + r * 128 + (((0 + hh) ^ fz) << 4);
    LAS char* xb1 = L + r * 128 + (((2 + hh) ^ fz) << 4);
    LAS char* xb2 = L + r * 128 + (((4 + hh) ^ fz) << 4);
    LAS char* xb3 = L + r * 128 + (((6 + hh) ^ fz) << 4);
    Frag1 fr[2];
    FT(7);
    if (isA) bar_vm<14>(); else bar_vm<12>();
    STAMP(3);
    FT(8);
    g1_read(fr[0], wa, xb0, xb1, xb2, xb3, 0);
    FT(9);
    unsigned fla;
#define PIN1(c) asm volatile("" : "+v"(c.a[0]), "+v"(c.a[1]), "+v"(c.a[2]), "+v"(c.a[3]), "+v"(c.b0[0]), "+v"(c.b0[1]), "+v"(c.b0[2]), "+v"(c.b0[3]), \
                             "+v"(c.b1[0]), "+v"(c.b1[1]), "+v"(c.b1[2]), "+v"(c.b1[3]))
#define G1_DMA(kt, u)                                                                                                 \
    {                                                                                                                   \
      if ((kt) < 4) {                                                                                                   \
        const unsigned slot_ = (unsigned)((kt) & 3) * 32768u + (unsigned)w * 2048u;                                     \
        if ((u) == 0) dma16(sx, vx0, (unsigned)((kt) + 4) * 128u, slot_);                                               \
        if ((u) == 1) dma16(sx, vx1, (unsigned)((kt) + 4) * 128u, slot_ + 1024u);                                       \
        if ((u) == 2) dma16(sw1, vw0, (unsigned)((kt) + 4) * (64u * 3584u), slot_ + 16384u);                            \
        if ((u) == 3) dma16(sw1, vw1, (unsigned)((kt) + 4) * (64u * 3584u), slot_ + 17408u);                            \
      } else {                                                                                                          \
        w2_issue1(2 * ((kt) - 4) + ((u) >> 1), (u) & 1);                                                                \
      }                                                                                                                 \
    }
#define G1_STEP(kt, X0, X1, X2, X3)                                                                                   \
    {                                                                                                                   \
      FT(10 + (kt));                                                                                                    \
      Frag1& c = fr[(kt) & 1];                                                                                          \
      Frag1& n = fr[((kt) + 1) & 1];                                                                                    \
      const int so = (((kt) + 1) & 3) * 32768;                                                                          \
      PIN1(c);                                                                                                          \
      SCHED();                                                                                                          \
      acc1[0] = mfma32(c.a[0], c.b0[0], acc1[0]); if ((kt) < 7) n.a[0] = tr_read2(wa + so, wa + so + 1024); X0; SCHED(); \
      acc1[1] = mfma32(c.a[0], c.b1[0], acc1[1]); if ((kt) < 7) { n.b0[0] = lds_ld16(xb0 + so); n.b1[0] = lds_ld16(xb0 + so + 4096); } X1; SCHED(); \
      acc1[0] = mfma32(c.a[1], c.b0[1], acc1[0]); if ((kt) < 7) n.a[1] = tr_read2(wa + so + 4096, wa + so + 5120); X2; SCHED(); \
      acc1[1] = mfma32(c.a[1], c.b1[1], acc1[1]); if ((kt) < 7) { n.b0[1] = lds_ld16(xb1 + so); n.b1[1] = lds_ld16(xb1 + so + 4096); } X3; SCHED(); \
      acc1[0] = mfma32(c.a[2], c.b0[2], acc1[0]); if ((kt) < 7) n.a[2] = tr_read2(wa + so + 8192, wa + so + 9216); G1_DMA(kt, 0) SCHED(); \
      acc1[1] = mfma32(c.a[2], c.b1[2], acc1[1]); if ((kt) < 7) { n.b0[2] = lds_ld16(xb2 + so); n.b1[2] = lds_ld16(xb2 + so + 4096); } G1_DMA(kt, 1) SCHED(); \
      acc1[0] = mfma32(c.a[3], c.b0[3], acc1[0]); if ((kt) < 7) n.a[3] = tr_read2(wa + so + 12288, wa + so + 13312); G1_DMA(kt, 2) SCHED(); \
      acc1[1] = mfma32(c.a[3], c.b1[3], acc1[1]); if ((kt) < 7) { n.b0[3] = lds_ld16(xb3 + so); n.b1[3] = lds_ld16(xb3 + so + 4096); } G1_DMA(kt, 3) SCHED(); \
    }
#define NOP do {} while (0)
#define IFB(x) if (!isA) { x; }
    const v4u swr = make_srd(p.wr, NS * 257 * 2), swi = make_srd(p.wi, NS * 257 * 2), stab = make_srd(&kTab, sizeof(Tab));
    const WAddr wadr = w_addr(m1 == 0 ? 128 * w : m1 + 256 * w, lane);
    const unsigned tabl = 128u * 1024u + 16384u + (unsigned)w * 2048u;
    unsigned tsrc0, tsrc1;
    {
      const int kap = tid >> 5, ii = (kap >> 1) & 7, d0 = kap & 1;
      const int g0 = m1 == 0 ? 2 * ii : ii, g1 = m1 == 0 ? 2 * ii + 1 : 8 + ii;
      const unsigned tb = WS_T + (unsigned)m1 * 16384u + (unsigned)(d0 * 512 + (tid & 31) * 16);
      tsrc0 = tb + (unsigned)g0 * 1024u; tsrc1 = tb + (unsigned)g1 * 1024u;
    }
    if (isA) bar_vm<10>(); else bar_vm<8>();
    STAMP(4);
    G1_STEP(0, NOP, NOP, NOP, NOP)
    if (isA) bar_vm<10>(); else bar_vm<8>();
    G1_STEP(1, NOP, NOP, NOP, NOP)
    bar_vm<8>();
    if (isA && tid == 0) flag_store(p.ws, WS_FLA + (unsigned)(mt * 32 + i * 4 + dt4) * FS, tag);
    G1_STEP(2, NOP, NOP, NOP, NOP)
    if (isA && w == 0) bar_vm<9>(); else bar_vm<8>();
    G1_STEP(3, NOP, NOP, IFB(dma16(stab, (unsigned)lane * 16u, (unsigned)w * 2048u, tabl)), IFB(dma16(stab, (unsigned)lane * 16u, (unsigned)w * 2048u + 1024u, tabl + 1024u)))
    if (isA) { if (w == 0) bar_vm<9>(); else bar_vm<8>(); } else bar_vm<10>();
    STAMP(12);
    G1_STEP(4, IFB(LD4(Wc.r0, wadr.e0, swr, 0u, "")), IFB(LD4(Wc.r1, wadr.e1, swr, 0u, "")), IFB(LD4(Wc.r2, wadr.e2, swr, 0u, "")), IFB(LD4(Wc.r3, wadr.e3, swr, 0u, "")))
    if (isA) bar_vm<8>(); else bar_vm<14>();
    G1_STEP(5, IFB(LD4(Wc.i0, wadr.e0, swi, 0u, "")), IFB(LD4(Wc.i1, wadr.e1, swi, 0u, "")), IFB(LD4(Wc.i2, wadr.e2, swi, 0u, "")), IFB(LD4(Wc.i3, wadr.e3, swi, 0u, "")))
    if (isA) bar_vm<8>(); else bar_vm<16>();
    G1_STEP(6, IFB(LD2(Wc.xr, wadr.ex, swr, 0u, "")), IFB(LD2(Wc.xi, wadr.ex, swi, 0u, "")),
            IFB(if (w == 0) LD4(fla, WS_FLA + (unsigned)((m1 >> 5) * 32 + (lane & 31)) * FS, sws, 0u, "sc1")), NOP)
    if (!isA && w == 0) {
      asm volatile("s_waitcnt vmcnt(4)" : "+v"(fla) :: "memory");
      if (!__all(fla == tag)) poll_words(sws, WS_FLA + (unsigned)(m1 >> 5) * 32u * FS, 32, tag, lane);
    }
    BAR_LGKM();
    G1_STEP(7, IFB(LD16(tq0, tsrc0, sws, 0u, "sc1")), IFB(LD16(tq1, tsrc1, sws, 0u, "sc1")), NOP, NOP)
  }
  STAMP(5);
  FT(18);
  if (active) {
#pragma unroll
    for (int rt = 0; rt < 2; ++rt)
#pragma unroll
      for (int u = 0; u < 2; ++u) {
        v4u o;
        o.x = pk2(silu(acc1[rt][8 * u + 0]), silu(acc1[rt][8 * u + 1]));
        o.y = pk2(silu(acc1[rt][8 * u + 2]), silu(acc1[rt][8 * u + 3]));
        o.z = pk2(silu(acc1[rt][8 * u + 4]), silu(acc1[rt][8 * u + 5]));
        o.w = pk2(silu(acc1[rt][8 * u + 6]), silu(acc1[rt][8 * u + 7]));
        const int jb32 = 4 * rb + 2 * rh1 + rt, fs = (f0 >> 4) + 2 * ft + u;
        st16_sc1(sws, WS_H + (unsigned)(((i * 8 + jb32) * 112 + fs) * 1024 + lane * 16), o);
      }
  }
  if (lane == 0) {
    const unsigned long long v = ((unsigned long long)__builtin_bit_cast(unsigned, gate_og) << 32) | __builtin_bit_cast(unsigned, gate_g);
    __hip_atomic_store((GAS unsigned long long*)((GAS char*)p.ws + WS_GATE + (i * 256 + jg) * 8), v, __ATOMIC_RELAXED, __HIP_MEMORY_SCOPE_AGENT);
  }
  SCHED();
  STAMP(6);
  FT(20);
  const int j = 64 * jb + 32 * rh + (lane & 31);
  v4u gf[4];
  auto gen_g = [&]() {
    const int srow = i + 8 * j;
    const int mb = 32 * q + 2 * hh;
    const float r1 = (float)(srow & 2047) * (1.f / 2048.f), r4 = (float)((4 * srow) & 2047) * (1.f / 2048.f), r0 = (float)((mb * srow) & 2047) * (1.f / 2048.f);
    const float c1 = cosrev(r1), s1 = sinrev(r1), c4 = cosrev(r4), s4 = sinrev(r4);
    float c = cosrev(r0), s = sinrev(r0);
#pragma unroll
    for (int e = 0; e < 8; ++e) {
      const float cb = __builtin_fmaf(c, c1, -(s * s1)), sb = __builtin_fmaf(s, c1, c * s1);
      unsigned w0 = pk2(c, -s), w1 = pk2(cb, -sb);
      if (e == 0 && q == 0 && hh == 0) w0 = (j & 1) ? 0x3F800000u : 0x00003F80u;
      if (e & 1) { gf[e >> 1].z = w0; gf[e >> 1].w = w1; } else { gf[e >> 1].x = w0; gf[e >> 1].y = w1; }
      const float cn = __builtin_fmaf(c, c4, -(s * s4)), sn = __builtin_fmaf(s, c4, c * s4);
      c = cn; s = sn;
    }
    asm volatile("" : "+v"(gf[0]), "+v"(gf[1]), "+v"(gf[2]), "+v"(gf[3]));
  };
  if (isA) {
    gen_g();
    bar_vm<0>();
    if (tid == 0) flag_store(p.ws, WS_FLH + (unsigned)((i * 2 + rb) * 16 + fc) * FS, tag);
  } else {
    LAS char* I0 = L + 128 * 1024;
    LAS char* I1 = L + 128 * 1024 + 16384;
    const int g = lane >> 4, l15 = lane & 15;
    const bool special = (m1 == 0);
    asm volatile("s_waitcnt vmcnt(8)" : "+v"(tq0), "+v"(tq1), W_PIN(Wc) :: "memory");
    {
      const int kap = tid >> 5, ch = tid & 31;
      *(LAS v4u*)(I0 + kap * 512 + ((ch ^ ((kap & 3) << 2)) << 4)) = tq0;
      *(LAS v4u*)(I0 + (kap + 16) * 512 + ((ch ^ ((kap & 3) << 2)) << 4)) = tq1;
    }
    v4u tb[16];
#pragma unroll
    for (int f = 0; f < 16; ++f) tb[f] = lds_ld16(I1 + f * 1024 + lane * 16);
    float W0[8], W1[8];
    w_unpack(Wc, lane, W0, W1);
    WRaw Wd;
    if (special) w_issue(Wd, make_srd(p.wr, NS * 257 * 2), make_srd(p.wi, NS * 257 * 2), 128 * w + 1024, lane);
    BAR_LGKM();
    FT(21);
    LAS char* ta = I0 + (8 * hh + q4) * 512 + (((4 * w + 2 * blk + (p4 >> 1)) ^ (q4 << 2)) << 4) + 8 * (p4 & 1);
    const int n = lane & 31;
    LAS char* so = I1 + n * 512 + 8 * hh;
    {
      f32x16 acc = {};
      acc = mfma32(tr_read2(ta, ta + 2048), tb[6], acc);
      acc = mfma32(tr_read2(ta + 8192, ta + 10240), tb[7], acc);
#pragma unroll
      for (int G = 0; G < 4; ++G) {
        v2u o; o.x = pk2(acc[4 * G], acc[4 * G + 1]); o.y = pk2(acc[4 * G + 2], acc[4 * G + 3]);
        *(LAS v2u*)(so + (((4 * w + G) ^ (n & 15)) << 4)) = o;
      }
    }
    bar_vm<0>();
    if (tid == 0) flag_store(p.ws, WS_FLH + (unsigned)((i * 2 + rb) * 16 + fc) * FS, tag);
    FT(22);
    const f32x4 z4 = {0.f, 0.f, 0.f, 0.f};
    const v4u F16fr = tb[0], F16fi = tb[1];
    v4u F16ir, F16ii;
    F16ir.x = F16fr.x; F16ir.y = F16fr.y; F16ir.z = F16fr.z ^ 0x80008000u; F16ir.w = F16fr.w ^ 0x80008000u;
    F16ii.x = F16fi.x ^ 0x80008000u; F16ii.y = F16fi.y ^ 0x80008000u; F16ii.z = F16fi.z; F16ii.w = F16fi.w;
    f32x4 zr[2], zi[2];
#pragma unroll
    for (int pp = 0; pp < 2; ++pp) {
      const int n0 = 4 * w + 2 * pp, n1 = n0 + 1;
      const int ch = 2 * (4 * g + q4) + (p4 >> 1);
      const v4u a1 = tr_read2(I1 + n0 * 512 + ((ch ^ (n0 & 15)) << 4) + 8 * (p4 & 1), I1 + n1 * 512 + ((ch ^ (n1 & 15)) << 4) + 8 * (p4 & 1));
      const f32x4 dr = mfma16(a1, pp ? tb[2] : F16fr, z4), di = mfma16(a1, pp ? tb[3] : F16fi, z4);
      const v4u b3 = twiddle_pack<false>(dr, di, tb[10 + 2 * pp], tb[11 + 2 * pp]);
      zr[pp] = mfma16(F16fr, b3, z4); zi[pp] = mfma16(F16fi, b3, z4);
    }
    v4u a4[2];
    if (!special) {
      a4[0] = twiddle_pack<false>(zr[0], zi[0], W0[0], W0[1], W0[2], W0[3], W0[4], W0[5], W0[6], W0[7]);
      a4[1] = twiddle_pack<false>(zr[1], zi[1], W1[0], W1[1], W1[2], W1[3], W1[4], W1[5], W1[6], W1[7]);
    } else {
      asm volatile("s_waitcnt vmcnt(0) lgkmcnt(0)\n\ts_barrier" : W_PIN(Wd) :: "memory");
      float X0[8], X1[8];
      w_unpack(Wd, lane, X0, X1);
      LAS char* Z = I0;
#pragma unroll
      for (int pp = 0; pp < 2; ++pp)
#pragma unroll
        for (int r = 0; r < 4; ++r) { f32x2 v = {zr[pp][r], zi[pp][r]}; *(LAS f32x2*)(Z + ((w * 2 + pp) * 256 + l15 + 16 * (4 * g + r)) * 8) = v; }
      BAR_LGKM();
      const cf om = {cosrev((float)w * 0.0625f), -sinrev((float)w * 0.0625f)};
#pragma unroll
      for (int pp = 0; pp < 2; ++pp) {
        float yr[4], yi[4];
#pragma unroll
        for (int r = 0; r < 4; ++r) {
          const int K = l15 + 16 * (4 * g + r), Kn = pp ? 255 - K : (256 - K) & 255;
          const f32x2 y = *(LAS f32x2*)(Z + ((((8 - w) & 7) * 2 + pp) * 256 + Kn) * 8);
          const float zre = zr[pp][r], zim = zi[pp][r], cr = y[0], ci = -y[1];
          const cf E = {0.5f * (zre + cr), 0.5f * (zim + ci)};
          const cf O = {0.5f * (zim - ci), -0.5f * (zre - cr)};
          const float war = pp ? W1[2 * r] : W0[2 * r], wai = pp ? W1[2 * r + 1] : W0[2 * r + 1];
          const float wbr = pp ? X1[2 * r] : X0[2 * r], wbi = pp ? X1[2 * r + 1] : X0[2 * r + 1];
          const cf sp = {war + wbr, wai + wbi}, sm = {war - wbr, wai - wbi};
          const cf t1 = cmulc(sm, om);
          const cf cE = {sp.r - t1.i, sp.i + t1.r};
          const cf t2 = cmul(sm, om);
          const cf cO = {t2.r - sp.i, t2.i + sp.r};
          const cf a = cmul(E, cE), bq = cmul(O, cO);
          yr[r] = 0.5f * (a.r + bq.r); yi[r] = 0.5f * (a.i + bq.i);
        }
        a4[pp].x = pk2(yr[0], yr[1]); a4[pp].y = pk2(yr[2], yr[3]); a4[pp].z = pk2(yi[0], yi[1]); a4[pp].w = pk2(yi[2], yi[3]);
      }
      BAR_LGKM();
    }
#pragma unroll
    for (int pp = 0; pp < 2; ++pp) {
      const f32x4 dr = mfma16(a4[pp], F16ir, z4), di = mfma16(a4[pp], F16ii, z4);
      const v4u a5 = twiddle_pack<true>(dr, di, tb[pp ? 14 : 10], tb[pp ? 15 : 11]);
      const f32x4 er = mfma16(a5, pp ? tb[4] : F16ir, z4), ei = mfma16(a5, pp ? tb[5] : F16ii, z4);
      const int n0 = 4 * w + 2 * pp, n1 = n0 + 1;
      const int ch = 2 * l15 + (g >> 1);
      v2u o; o.x = pk2(er[0], er[1]); o.y = pk2(er[2], er[3]);
      *(LAS v2u*)(I0 + n0 * 512 + ((ch ^ ((n0 & 3) << 2)) << 4) + 8 * (g & 1)) = o;
      o.x = pk2(ei[0], ei[1]); o.y = pk2(ei[2], ei[3]);
      *(LAS v2u*)(I0 + n1 * 512 + ((ch ^ ((n1 & 3) << 2)) << 4) + 8 * (g & 1)) = o;
    }
    BAR_LGKM();
    FT(23);
    {
      f32x16 acc = {};
      acc = mfma32(tr_read2(ta, ta + 2048), tb[8], acc);
      acc = mfma32(tr_read2(ta + 8192, ta + 10240), tb[9], acc);
#pragma unroll
      for (int G = 0; G < 4; ++G) {
        v2u o; o.x = pk2(acc[4 * G], acc[4 * G + 1]); o.y = pk2(acc[4 * G + 2], acc[4 * G + 3]);
        *(LAS v2u*)(so + (((4 * w + G) ^ (n & 15)) << 4)) = o;
      }
    }
    BAR_LGKM();
    {
      const int nn = tid >> 5, ch = tid & 31, ii = (nn >> 1) & 7, d0 = nn & 1;
      const int g0 = special ? 2 * ii : ii, g1 = special ? 2 * ii + 1 : 8 + ii;
      const v4u v0 = lds_ld16(I1 + nn * 512 + ((ch ^ (nn & 15)) << 4));
      const v4u v1 = lds_ld16(I1 + (nn + 16) * 512 + ((ch ^ (nn & 15)) << 4));
      const unsigned ub = WS_U + (unsigned)m1 * 16384u + (unsigned)(d0 * 512 + ch * 16);
      st16_sc1(sws, ub + (unsigned)g0 * 1024u, v0);
      st16_sc1(sws, ub + (unsigned)g1 * 1024u, v1);
    }
    SCHED();
    FT(24);
  }
  STAMP(7);
  FT(25);
  const unsigned flh = WS_FLH + (unsigned)((i * 2 + (jb >> 1)) * 16) * FS;
  if (w == 0) poll_words(sws, flh + 8u * FS, 8, tag, lane);
  asm volatile("s_barrier" ::: "memory");
  STAMP(8);
  FT(26);
  const unsigned vh = (unsigned)lane * 16u;
  const unsigned hbase = WS_H + (unsigned)((i * 8 + 2 * jb + rh) * 112) * 1024u;
  const unsigned hb0 = hbase + (fq0 >> 4) * 1024u, hb1 = hbase + (unsigned)((int)fq1 >> 4) * 1024u;
  v2u gg;
  v4u hf[14][2];
#define H_ISSUE(t) do { LD16(hf[t][0], vh, sws, ((t) < 8 ? hb0 : hb1) + (unsigned)(2 * (t)) * 1024u, "sc1"); LD16(hf[t][1], vh, sws, ((t) < 8 ? hb0 : hb1) + (unsigned)(2 * (t) + 1) * 1024u, "sc1"); } while (0)
  H_ISSUE(0); H_ISSUE(1);
  asm volatile("s_barrier" ::: "memory");
  H_ISSUE(2); H_ISSUE(3); H_ISSUE(4); H_ISSUE(5); H_ISSUE(6);
  if (!isA) gen_g();
  SCHED();
  f32x16 accb[2] = {}, acca[2] = {};
  const int sz = (q4 >> 1) & 1;
  LAS char* ra0 = L + q * 4096 + (4 * hh + q4) * 128 + (((0 ^ (4 * sz)) + 2 * blk + (p4 >> 1)) << 4) + 8 * (p4 & 1);
  LAS char* ra1 = L + q * 4096 + (4 * hh + q4) * 128 + (((4 ^ (4 * sz)) + 2 * blk + (p4 >> 1)) << 4) + 8 * (p4 & 1);
  v4u wf[2][4];
#define G2_BAR(n, t) asm volatile("s_waitcnt vmcnt(" STR(n) ") lgkmcnt(0)\n\ts_barrier" : "+v"(hf[t][0]), "+v"(hf[t][1]) :: "memory")
  G2_BAR(12, 0);
  if (!isA && tid == 0) flag_store(p.ws, WS_FLB + (unsigned)m1 * FS, tag);
  wf[0][0] = tr_read2(ra0, ra0 + 1024); wf[0][1] = tr_read2(ra1, ra1 + 1024);
  wf[0][2] = tr_read2(ra0 + 2048, ra0 + 3072); wf[0][3] = tr_read2(ra1 + 2048, ra1 + 3072);
  unsigned fl0, fl1, flb;
  v4u ub0, ub1, ub2, ub3;
  const unsigned vu = WS_U + (unsigned)(((64 * q + rho) * 8 + i) * 1024 + dt * 128 + chs * 16);
#define G2_MMA(t, acc, b0, b1, X0, X1, X2)                                                        \
  {                                                                                                 \
    FT(30 + (t));                                                                                   \
    v4u (&c)[4] = wf[(t) & 1];                                                                     \
    v4u (&n)[4] = wf[((t) + 1) & 1];                                                               \
    LAS char* s0 = ra0 + (((t) + 1) & 7) * 16384; LAS char* s1 = ra1 + (((t) + 1) & 7) * 16384;    \
    asm volatile("" : "+v"(c[0]), "+v"(c[1]), "+v"(c[2]), "+v"(c[3]));                             \
    SCHED();                                                                                        \
    acc[0] = mfma32(c[0], b0, acc[0]); if ((t) < 15) n[0] = tr_read2(s0, s0 + 1024); SCHED();      \
    acc[1] = mfma32(c[1], b0, acc[1]); if ((t) < 15) n[1] = tr_read2(s1, s1 + 1024); X0; SCHED();  \
    acc[0] = mfma32(c[2], b1, acc[0]); if ((t) < 15) n[2] = tr_read2(s0 + 2048, s0 + 3072); X1; SCHED(); \
    acc[1] = mfma32(c[3], b1, acc[1]); if ((t) < 15) n[3] = tr_read2(s1 + 2048, s1 + 3072); X2; SCHED(); \
  }
#define G2_STEP(t, X2) G2_MMA(t, accb, hf[t][0], hf[t][1], if ((t) <= 5) w2_issue1((t) + 8, 0), if ((t) <= 5) w2_issue1((t) + 8, 1), X2)
  BAR_LGKM();    G2_STEP(0, H_ISSUE(7))
  G2_BAR(14, 1); G2_STEP(1, NOP)
  G2_BAR(14, 2); G2_STEP(2, NOP)
  if (w == 0) LD4(flb, flh + (unsigned)(lane & 7) * FS, sws, 0u, "sc1");
  G2_BAR(14, 3); G2_STEP(3, NOP)
  if (w == 0) {
    asm volatile("s_waitcnt vmcnt(2)" : "+v"(flb) :: "memory");
    if (!__all(flb == tag)) poll_words(sws, flh, 8, tag, lane);
  }
  G2_BAR(14, 4); G2_STEP(4, { H_ISSUE(8); H_ISSUE(9); LD8(gg, WS_GATE + (unsigned)(i * 256 + j) * 8u, sws, 0u, "sc1"); })
  G2_BAR(19, 5); G2_STEP(5, { H_ISSUE(10); H_ISSUE(11); })
  if (w == 0) { LD4(fl0, WS_FLB + (unsigned)lane * FS, sws, 0u, "sc1"); LD4(fl1, WS_FLB + (64u + (unsigned)lane) * FS, sws, 0u, "sc1"); }
  G2_BAR(23, 6); G2_STEP(6, { H_ISSUE(12); H_ISSUE(13); })
  STAMP(9);
  G2_BAR(23, 7); G2_STEP(7, NOP)
  if (w == 0) {
    asm volatile("s_waitcnt vmcnt(4)" : "+v"(fl0), "+v"(fl1) :: "memory");
    if (!__all(fl0 == tag && fl1 == tag)) { poll_words(sws, WS_FLB, 64, tag, lane); poll_words(sws, WS_FLB + 64u * FS, 64, tag, lane); }
  }
  G2_BAR(13, 8);
  LD16(ub0, vu, sws, 0u, "sc1"); LD16(ub1, vu, sws, 8u * 8192u, "sc1"); LD16(ub2, vu, sws, 32u * 8192u, "sc1"); LD16(ub3, vu, sws, 40u * 8192u, "sc1");
  G2_STEP(8, NOP)
  G2_BAR(15, 9); G2_STEP(9, NOP)
  G2_BAR(10, 10); G2_STEP(10, NOP)
  G2_BAR(8, 11); G2_STEP(11, NOP)
  G2_BAR(6, 12);
  G2_STEP(12, NOP)
  asm volatile("s_waitcnt vmcnt(0)" : "+v"(ub0), "+v"(ub1), "+v"(ub2), "+v"(ub3), "+v"(gg) :: "memory");
  *(LAS v4u*)(L + 6 * 16384 + ldsw + lane * 16) = ub0;
  *(LAS v4u*)(L + 6 * 16384 + ldsw + 1024 + lane * 16) = ub1;
  *(LAS v4u*)(L + 7 * 16384 + ldsw + lane * 16) = ub2;
  *(LAS v4u*)(L + 7 * 16384 + ldsw + 1024 + lane * 16) = ub3;
  G2_BAR(0, 13); G2_STEP(13, NOP)
  STAMP(10);
  BAR_LGKM(); G2_MMA(14, acca, gf[0], gf[1], NOP, NOP, NOP)
  BAR_LGKM(); G2_MMA(15, acca, gf[2], gf[3], NOP, NOP, NOP)
  FT(50);
  {
    const float og = asf(gg.y), gs = asf(gg.x) * (1.f / 524288.f);
    const int row = 32 * rh + (lane & 31);
#pragma unroll
    for (int dh = 0; dh < 2; ++dh)
#pragma unroll
      for (int G = 0; G < 4; ++G) {
        f32x4 v;
#pragma unroll
        for (int e = 0; e < 4; ++e) v[e] = __builtin_fmaf(og, accb[dh][4 * G + e], gs * acca[dh][4 * G + e]);
        *(LAS f32x4*)(L + q * 16384 + row * 256 + (((8 * dh + 2 * G + hh) ^ (row & 7)) << 4)) = v;
      }
  }
  BAR_LGKM();
  FT(51);
  {
    const int row = tid >> 3, c8 = tid & 7;
    f32x4 s0 = {0.f, 0.f, 0.f, 0.f}, s1 = s0;
#pragma unroll
    for (int qq = 0; qq < 4; ++qq) {
      s0 += *(LAS f32x4*)(L + qq * 16384 + row * 256 + (((2 * c8) ^ (row & 7)) << 4));
      s1 += *(LAS f32x4*)(L + qq * 16384 + row * 256 + (((2 * c8 + 1) ^ (row & 7)) << 4));
    }
    v4u o; o.x = pk2(s0[0], s0[1]); o.y = pk2(s0[2], s0[3]); o.z = pk2(s1[0], s1[1]); o.w = pk2(s1[2], s1[3]);
    *(GAS v4u*)((GAS char*)p.out + (size_t)(i + 8 * (64 * jb + row)) * 1024 + dt * 128 + c8 * 16) = o;
  }
  STAMP(11);
  { while (__builtin_amdgcn_s_memrealtime() - pad_t0 < 1800) {} }
  if (b == 0 && w == 0) {
#pragma unroll 1
    for (int u = 0; u < 4; ++u) poll_words(sws, WS_FLH + 64u * FS * u, 64, tag, lane);
    if (lane == 0) flag_store(p.ws, WS_EPOCH, tag);
  }
}

}

extern "C" void kernel_launch(void* const* d_in, const int* in_sizes, int n_in, void* d_out, int out_size, void* d_ws, size_t ws_size,
                              hipStream_t stream) {
  Params p;
  p.x = d_in[0]; p.wr = d_in[1]; p.wi = d_in[2]; p.w1 = d_in[4]; p.w2 = d_in[5]; p.gw = d_in[6];
  p.out = d_out; p.ws = d_ws;
  (void)hipFuncSetAttribute((const void*)gm_kernel, hipFuncAttributeMaxDynamicSharedMemorySize, LDS_BYTES);
  gm_kernel<<<256, 512, LDS_BYTES, stream>>>(p);
}
```

```cpp
#include <hip/hip_runtime.h>
#include <hip/hip_bf16.h>
#include <stdint.h>

namespace {

typedef unsigned v4u __attribute__((ext_vector_type(4)));
typedef unsigned v2u __attribute__((ext_vector_type(2)));
typedef float f32x16 __attribute__((ext_vector_type(16)));
typedef float f32x4 __attribute__((ext_vector_type(4)));
typedef float f32x2 __attribute__((ext_vector_type(2)));
typedef __bf16 bf16x8 __attribute__((ext_vector_type(8)));
typedef __bf16 bf16x2 __attribute__((ext_vector_type(2)));
typedef short s16x4 __attribute__((ext_vector_type(4)));
#define LAS __attribute__((address_space(3)))
#define GAS __attribute__((address_space(1)))
#define DI __device__ __forceinline__

constexpr int NS = 2048, ND = 512, NF = 1792;
constexpr unsigned WS_EPOCH = 0;
constexpr unsigned FS = 64;
constexpr unsigned WS_FLA = 4096;
constexpr unsigned WS_FLH = 16384;
constexpr unsigned WS_FLB = 32768;
constexpr unsigned WS_GATE = 65536;
constexpr unsigned WS_T = 1u << 20;
constexpr unsigned WS_U = 3u << 20;
constexpr unsigned WS_H = 5u << 20;
constexpr unsigned H_BYTES = 8u * 8u * 112u * 1024u;
constexpr int LDS_BYTES = 160 * 1024;
constexpr unsigned kSpinCap = 1u << 20;

struct Params {
  const void* x; const void* wr; const void* wi; const void* w1; const void* w2; const void* gw;
  void* out; void* ws;
};

constexpr double kPi = 3.14159265358979323846;
constexpr double ccos(double x) { double t = 1, s = 1; for (int n = 1; n < 16; ++n) { t *= -x * x / ((2 * n - 1) * (2 * n)); s += t; } return s; }
constexpr double cosn(int e, int n) { e = ((e % n) + n) % n; double x = 2 * kPi * e / n; if (x > kPi) x -= 2 * kPi; return ccos(x); }
constexpr double wre(int e, int n) { return cosn(e, n); }
constexpr double wim(int e, int n) { return -cosn(4 * e - n, 4 * n); }
constexpr unsigned bfb(double v) { float f = (float)v; unsigned u = __builtin_bit_cast(unsigned, f); u += 0x7fffu + ((u >> 16) & 1u); return u >> 16; }
constexpr unsigned bf2(double a, double b) { return bfb(a) | (bfb(b) << 16); }
constexpr unsigned fbits(double v) { return __builtin_bit_cast(unsigned, (float)v); }
struct Tab { unsigned v[16][64][4]; };
constexpr Tab make_tab() {
  Tab t{};
  for (int l = 0; l < 64; ++l) {
    const int g = l >> 4, l15 = l & 15, hh = l >> 5, n = l & 31;
    for (int mtx = 0; mtx < 3; ++mtx) {
      double r[4] = {}, im[4] = {};
      for (int q = 0; q < 4; ++q) {
        const int a = 4 * g + q;
        if (mtx == 0) { r[q] = wre(l15 * a, 16); im[q] = wim(l15 * a, 16); }
        else if (mtx == 1) { r[q] = wre(a * (2 * l15 + 1), 32); im[q] = wim(a * (2 * l15 + 1), 32); }
        else { r[q] = wre(l15 * (2 * a + 1), 32); im[q] = -wim(l15 * (2 * a + 1), 32); }
      }
      t.v[2 * mtx][l][0] = bf2(r[0], r[1]); t.v[2 * mtx][l][1] = bf2(r[2], r[3]); t.v[2 * mtx][l][2] = bf2(-im[0], -im[1]); t.v[2 * mtx][l][3] = bf2(-im[2], -im[3]);
      t.v[2 * mtx + 1][l][0] = bf2(im[0], im[1]); t.v[2 * mtx + 1][l][1] = bf2(im[2], im[3]); t.v[2 * mtx + 1][l][2] = bf2(r[0], r[1]); t.v[2 * mtx + 1][l][3] = bf2(r[2], r[3]);
    }
    {
      const int m2 = n >> 2, pp = (n >> 1) & 1, cp = n & 1;
      double v0[8] = {}, v1[8] = {};
      for (int jj = 0; jj < 8; ++jj) {
        const int ii = (8 * hh + jj) >> 1, d0 = jj & 1;
        const double sg = (d0 & pp) ? -1.0 : 1.0, orr = sg * wre(ii * m2, 8), oi = sg * wim(ii * m2, 8);
        v0[jj] = cp ? oi : orr; v1[jj] = cp ? orr : -oi;
      }
      for (int q = 0; q < 4; ++q) { t.v[6][l][q] = bf2(v0[2 * q], v0[2 * q + 1]); t.v[7][l][q] = bf2(v1[2 * q], v1[2 * q + 1]); }
    }
    {
      const int cq = n >> 4, ii = (n >> 1) & 7, d0 = n & 1;
      for (int s = 0; s < 2; ++s) {
        double v[8] = {};
        for (int jj = 0; jj < 8; ++jj) {
          const int m2k = 4 * s + 2 * hh + (jj >> 2), pk = (jj >> 1) & 1, ck = jj & 1;
          const double sg = (d0 & pk) ? -1.0 : 1.0, orr = sg * wre(ii * m2k, 8), oi = -sg * wim(ii * m2k, 8);
          v[jj] = cq ? (ck ? orr : oi) : (ck ? -oi : orr);
        }
        for (int q = 0; q < 4; ++q) t.v[8 + s][l][q] = bf2(v[2 * q], v[2 * q + 1]);
      }
    }
    for (int q = 0; q < 4; ++q) {
      const int a = 4 * g + q, o = (q & 1) * 2, f = q >> 1;
      t.v[10 + f][l][o] = fbits(wre(a * (2 * l15), 512));      t.v[10 + f][l][o + 1] = fbits(wim(a * (2 * l15), 512));
      t.v[12 + f][l][o] = fbits(wre(a * (2 * l15 + 1), 512));  t.v[12 + f][l][o + 1] = fbits(wim(a * (2 * l15 + 1), 512));
      t.v[14 + f][l][o] = fbits(wre(l15 * (2 * a + 1), 512));  t.v[14 + f][l][o + 1] = fbits(wim(l15 * (2 * a + 1), 512));
    }
  }
  return t;
}
__device__ const Tab kTab = make_tab();

constexpr unsigned STAMP_OFF = 512 * 1024;
#define STAMP(k) do {} while (0)
#define FT(k) do {} while (0)

DI v4u make_srd(const void* p, unsigned bytes) {
  const unsigned long long a = (unsigned long long)p;
  v4u r; r.x = (unsigned)a; r.y = (unsigned)(a >> 32) & 0xffffu; r.z = bytes; r.w = 0x00020000u;
  return r;
}
DI void dma16(v4u srd, unsigned voff, unsigned soff, unsigned lds) {
  asm volatile("s_mov_b32 m0, %3\n\ts_nop 0\n\tbuffer_load_dwordx4 %0, %1, %2 offen lds"
               :: "v"(voff), "s"(srd), "s"(soff), "s"(lds) : "memory");
}
#define LD16(dst, voffv, srd, soffv, flags) asm volatile("buffer_load_dwordx4 %0, %1, %2, %3 offen " flags : "=v"(dst) : "v"(voffv), "s"(srd), "s"(soffv) : "memory")
#define LD8(dst, voffv, srd, soffv, flags) asm volatile("buffer_load_dwordx2 %0, %1, %2, %3 offen " flags : "=v"(dst) : "v"(voffv), "s"(srd), "s"(soffv) : "memory")
#define LD4(dst, voffv, srd, soffv, flags) asm volatile("buffer_load_dword %0, %1, %2, %3 offen " flags : "=v"(dst) : "v"(voffv), "s"(srd), "s"(soffv) : "memory")
#define LD2(dst, voffv, srd, soffv, flags) asm volatile("buffer_load_ushort %0, %1, %2, %3 offen " flags : "=v"(dst) : "v"(voffv), "s"(srd), "s"(soffv) : "memory")
#define XSTR(x) #x
#define STR(x) XSTR(x)
#define BAR_VM(n) asm volatile("s_waitcnt vmcnt(" STR(n) ") lgkmcnt(0)\n\ts_barrier" ::: "memory")
#define BAR_LGKM() asm volatile("s_waitcnt lgkmcnt(0)\n\ts_barrier" ::: "memory")
#define SCHED() __builtin_amdgcn_sched_barrier(0)
template <int N> DI void bar_vm() { asm volatile("s_waitcnt vmcnt(%0) lgkmcnt(0)\n\ts_barrier" :: "i"(N) : "memory"); }
template <int N> DI void wait_vm() { asm volatile("s_waitcnt vmcnt(%0)" :: "i"(N) : "memory"); }

DI unsigned pk2(float a, float b) { f32x2 p = {a, b}; return __builtin_bit_cast(unsigned, __builtin_convertvector(p, bf16x2)); }
DI float bflo(unsigned u) { return __builtin_bit_cast(float, u << 16); }
DI float bfhi(unsigned u) { return __builtin_bit_cast(float, u & 0xffff0000u); }
DI float asf(unsigned u) { return __builtin_bit_cast(float, u); }
DI f32x16 mfma32(v4u a, v4u b, f32x16 c) {
  return __builtin_amdgcn_mfma_f32_32x32x16_bf16(__builtin_bit_cast(bf16x8, a), __builtin_bit_cast(bf16x8, b), c, 0, 0, 0);
}
DI f32x4 mfma16(v4u a, v4u b, f32x4 c) {
  return __builtin_amdgcn_mfma_f32_16x16x32_bf16(__builtin_bit_cast(bf16x8, a), __builtin_bit_cast(bf16x8, b), c, 0, 0, 0);
}
DI v2u tr_read(LAS char* p) { return __builtin_bit_cast(v2u, __builtin_amdgcn_ds_read_tr16_b64_v4i16((LAS s16x4*)p)); }
DI v4u tr_read2(LAS char* p0, LAS char* p1) { v2u a = tr_read(p0), b = tr_read(p1); v4u r; r.x = a.x; r.y = a.y; r.z = b.x; r.w = b.y; return r; }
DI v4u lds_ld16(LAS char* p) { return *(LAS v4u*)p; }
DI float cosrev(float r) { return __builtin_amdgcn_cosf(r); }
DI float sinrev(float r) { return __builtin_amdgcn_sinf(r); }
DI __amdgpu_buffer_rsrc_t as_rsrc(v4u srd) { return __builtin_amdgcn_make_buffer_rsrc((void*)(((unsigned long long)srd.y << 32) | srd.x), 0, srd.z, 0x00020000); }
DI void st16_sc1(v4u srd, unsigned voff, v4u v) { __builtin_amdgcn_raw_buffer_store_b128(v, as_rsrc(srd), voff, 0, 16); }
DI void flag_store(void* ws, unsigned off, unsigned tag) { __hip_atomic_store((GAS unsigned*)((GAS char*)ws + off), tag, __ATOMIC_RELAXED, __HIP_MEMORY_SCOPE_AGENT); }
template <int CTRL, int RMASK> DI float dpp_add(float v) { return v + asf((unsigned)__builtin_amdgcn_update_dpp(0, (int)__builtin_bit_cast(unsigned, v), CTRL, RMASK, 0xf, true)); }
DI void poll_words(v4u sws, unsigned off, int n, unsigned tag, int lane) {
  const unsigned vo = off + FS * (unsigned)(lane < n ? lane : 0);
#pragma unroll 1
  for (unsigned spins = 0; spins < kSpinCap; ++spins) {
    unsigned f;
    asm volatile("buffer_load_dword %0, %1, %2, 0 offen sc1\n\ts_waitcnt vmcnt(0)" : "=&v"(f) : "v"(vo), "s"(sws) : "memory");
    if (__all(f == tag)) return;
    __builtin_amdgcn_s_sleep(4);
  }
}

constexpr unsigned A_XIMG = 64 * 1024, A_FIMG = 128 * 1024;
DI void phaseA_genF(LAS char* L, int i, int mt, int w, int lane) {
  const int mh = w >> 2, sg = w & 3, n = lane & 31, hh = lane >> 5;
  const int m1c = 64 * mt + 32 * mh + n, m1 = m1c >> 1, c = m1c & 1;
  const int j0 = 64 * sg + 32 * hh;
  const float ph = c ? 0.25f : 0.f;
  const float k2 = 2.f * cosrev((float)(8 * m1) * (1.f / 2048.f));
  float vm = cosrev((float)(((i + 8 * (j0 - 1)) * m1) & 2047) * (1.f / 2048.f) + ph);
  float v0 = cosrev((float)(((i + 8 * j0) * m1) & 2047) * (1.f / 2048.f) + ph);
  const unsigned cst = c ? 0x3F800000u : 0x00003F80u;
  const bool sp = m1c < 2;
#pragma unroll
  for (int s = 0; s < 4; ++s) {
    float e[8];
#pragma unroll
    for (int q = 0; q < 8; ++q) { e[q] = v0; const float vn = __builtin_fmaf(k2, v0, -vm); vm = v0; v0 = vn; }
    v4u f; f.x = pk2(e[0], e[1]); f.y = pk2(e[2], e[3]); f.z = pk2(e[4], e[5]); f.w = pk2(e[6], e[7]);
    if (mt == 0 && w < 4) { f.x = sp ? cst : f.x; f.y = sp ? cst : f.y; f.z = sp ? cst : f.z; f.w = sp ? cst : f.w; }
    *(LAS v4u*)(L + A_FIMG + (mh * 16 + sg * 4 + s) * 1024 + lane * 16) = f;
  }
}

struct Frag1 { v4u a[4], b0[4], b1[4]; };
DI void g1_read(Frag1& f, LAS char* wa, LAS char* xb0, LAS char* xb1, LAS char* xb2, LAS char* xb3, int slot) {
  const int so = slot * 32768;
  f.a[0] = tr_read2(wa + so, wa + so + 1024);              f.b0[0] = lds_ld16(xb0 + so); f.b1[0] = lds_ld16(xb0 + so + 4096);
  f.a[1] = tr_read2(wa + so + 4096, wa + so + 5120);       f.b0[1] = lds_ld16(xb1 + so); f.b1[1] = lds_ld16(xb1 + so + 4096);
  f.a[2] = tr_read2(wa + so + 8192, wa + so + 9216);       f.b0[2] = lds_ld16(xb2 + so); f.b1[2] = lds_ld16(xb2 + so + 4096);
  f.a[3] = tr_read2(wa + so + 12288, wa + so + 13312);     f.b0[3] = lds_ld16(xb3 + so); f.b1[3] = lds_ld16(xb3 + so + 4096);
}
DI float silu(float h) { return h * __builtin_amdgcn_rcpf(1.f + __builtin_amdgcn_exp2f(h * -1.44269504088896341f)); }

struct cf { float r, i; };
DI cf cmul(cf a, cf b) { return {a.r * b.r - a.i * b.i, a.r * b.i + a.i * b.r}; }
DI cf cmulc(cf a, cf b) { return {a.r * b.r + a.i * b.i, a.i * b.r - a.r * b.i}; }
template <bool CONJ> DI v4u twiddle_pack(const f32x4& re, const f32x4& im, float t0r, float t0i, float t1r, float t1i, float t2r, float t2i, float t3r, float t3i) {
  const float tr[4] = {t0r, t1r, t2r, t3r}, ti[4] = {t0i, t1i, t2i, t3i};
  float a[4], b[4];
#pragma unroll
  for (int q = 0; q < 4; ++q) {
    const float xr = re[q], xi = im[q], s = CONJ ? -ti[q] : ti[q];
    a[q] = __builtin_fmaf(xr, tr[q], -(xi * s)); b[q] = __builtin_fmaf(xr, s, xi * tr[q]);
  }
  v4u r; r.x = pk2(a[0], a[1]); r.y = pk2(a[2], a[3]); r.z = pk2(b[0], b[1]); r.w = pk2(b[2], b[3]); return r;
}
template <bool CONJ> DI v4u twiddle_pack(const f32x4& re, const f32x4& im, v4u t0, v4u t1) {
  return twiddle_pack<CONJ>(re, im, asf(t0.x), asf(t0.y), asf(t0.z), asf(t0.w), asf(t1.x), asf(t1.y), asf(t1.z), asf(t1.w));
}
struct WRaw { unsigned r0, r1, r2, r3, i0, i1, i2, i3, xr, xi; };
struct WAddr { unsigned e0, e1, e2, e3, ex; };
DI WAddr w_addr(int m, int lane) {
  const int k1 = lane & 15, g = lane >> 4, mm = (NS - m) & (NS - 1);
  WAddr a;
  a.e0 = g < 2 ? (unsigned)(m * 257 + 2 * k1 + 128 * g) * 2u : (unsigned)(mm * 257 + 511 - 2 * k1 - 128 * g) * 2u;
  a.e1 = g < 2 ? a.e0 + 64u : a.e0 - 64u; a.e2 = g < 2 ? a.e0 + 128u : a.e0 - 128u; a.e3 = g < 2 ? a.e0 + 192u : a.e0 - 192u;
  a.ex = (unsigned)(g < 2 ? mm * 257 : m * 257 + 256) * 2u;
  return a;
}
DI void w_issue(WRaw& o, v4u swr, v4u swi, int m, int lane) {
  const WAddr a = w_addr(m, lane);
  LD4(o.r0, a.e0, swr, 0u, ""); LD4(o.r1, a.e1, swr, 0u, ""); LD4(o.r2, a.e2, swr, 0u, ""); LD4(o.r3, a.e3, swr, 0u, "");
  LD4(o.i0, a.e0, swi, 0u, ""); LD4(o.i1, a.e1, swi, 0u, ""); LD4(o.i2, a.e2, swi, 0u, ""); LD4(o.i3, a.e3, swi, 0u, "");
  LD2(o.xr, a.ex, swr, 0u, ""); LD2(o.xi, a.ex, swi, 0u, "");
}
#define W_PIN(o) "+v"(o.r0), "+v"(o.r1), "+v"(o.r2), "+v"(o.r3), "+v"(o.i0), "+v"(o.i1), "+v"(o.i2), "+v"(o.i3), "+v"(o.xr), "+v"(o.xi)
DI void w_unpack(const WRaw& o, int lane, float (&w0)[8], float (&w1)[8]) {
  const bool lo = lane < 32;
  const unsigned rr[4] = {o.r0, o.r1, o.r2, o.r3}, ii[4] = {o.i0, o.i1, o.i2, o.i3};
  const unsigned sg = lo ? 0u : 0x80000000u;
#pragma unroll
  for (int r = 0; r < 4; ++r) {
    const unsigned a0 = lo ? rr[r] << 16 : rr[r] & 0xffff0000u, a1 = lo ? rr[r] & 0xffff0000u : rr[r] << 16;
    const unsigned b0 = lo ? ii[r] << 16 : ii[r] & 0xffff0000u, b1 = lo ? ii[r] & 0xffff0000u : ii[r] << 16;
    w0[2 * r] = asf(a0); w0[2 * r + 1] = asf(b0 ^ sg); w1[2 * r] = asf(a1); w1[2 * r + 1] = asf(b1 ^ sg);
  }
  if ((lane & 31) == 0) {
    w0[0] = 0.5f * (w0[0] + bflo(o.xr));
    w0[1] = 0.5f * (w0[1] + asf((o.xi << 16) ^ (sg ^ 0x80000000u)));
  }
}

#define G1_MMA true
__global__ __launch_bounds__(512, 2) void gm_kernel(Params p) {
  extern __shared__ __attribute__((aligned(16))) char smem[];
  LAS char* L = (LAS char*)smem;
  const int tid = threadIdx.x, lane = tid & 63, w = __builtin_amdgcn_readfirstlane(tid >> 6);
  const int b = blockIdx.x, i = b & 7, Lx = b >> 3, rb = Lx >> 4, fc = Lx & 15;
  const bool isA = fc < 8;
  const int hh = lane >> 5, blk = (lane >> 4) & 1, q4 = (lane & 15) >> 2, p4 = lane & 3;
  STAMP(0);
  FT(0);
  const v4u sx = make_srd(p.x, NS * ND * 2), sw1 = make_srd(p.w1, ND * NF * 2), sws = make_srd(p.ws, 16u << 20);
  unsigned ep; v4u gx, gv;
  const int jg = 128 * rb + 8 * fc + w;
  LD16(gx, (unsigned)lane * 16u, sx, (unsigned)(i + 8 * jg) * 1024u, "");
  LD16(gv, (unsigned)lane * 16u, make_srd(p.gw, ND * 2), 0u, "");
  LD4(ep, 0u, sws, WS_EPOCH, "sc1");
  const int f0 = isA ? 96 * fc : 768 + 128 * (fc - 8), nf = isA ? 96 : 128;
  const int a_t = rb * 8 + fc, mt = a_t >> 2, dt4 = a_t & 3;
  const int m1 = ((rb * 8 + (fc - 8)) << 3) | i;
  if (isA) {
    const unsigned voff = (unsigned)(lane >> 4) * 8192u + (unsigned)(((lane & 15) ^ ((lane >> 4) << 2)) << 4);
#pragma unroll
    for (int u = 0; u < 8; ++u) {
      const unsigned row = 32 * w + 4 * u;
      dma16(sx, voff, (unsigned)i * 1024u + (unsigned)dt4 * 256u + row * 8192u, A_XIMG + row * 256u);
    }
  }
  unsigned vx0, vx1, vw0, vw1;
  {
    const int r0 = 16 * w + (lane >> 3), pos = lane & 7, r1 = r0 + 8;
    vx0 = (unsigned)(i * 1024 + (128 * rb + r0) * 8192 + ((pos ^ ((r0 >> 1) & 7)) << 4));
    vx1 = (unsigned)(i * 1024 + (128 * rb + r1) * 8192 + ((pos ^ ((r1 >> 1) & 7)) << 4));
    const int k0 = 8 * w + (lane >> 4), chunk = (lane & 15) ^ ((lane >> 4) << 2);
    const bool ok = chunk * 8 < nf;
    vw0 = ok ? (unsigned)(k0 * 3584 + f0 * 2 + chunk * 16) : 0x80000000u;
    vw1 = ok ? vw0 + 4u * 3584u : 0x80000000u;
  }
  auto g1_issue = [&](int kt) {
    const unsigned slot = (unsigned)(kt & 3) * 32768u + (unsigned)w * 2048u;
    dma16(sx, vx0, (unsigned)kt * 128u, slot);
    dma16(sx, vx1, (unsigned)kt * 128u, slot + 1024u);
    dma16(sw1, vw0, (unsigned)kt * (64u * 3584u), slot + 16384u);
    dma16(sw1, vw1, (unsigned)kt * (64u * 3584u), slot + 17408u);
  };
  if (!isA) {
    g1_issue(0); asm volatile("s_barrier" ::: "memory");
    g1_issue(1); asm volatile("s_barrier" ::: "memory");
    g1_issue(2); asm volatile("s_barrier" ::: "memory");
    g1_issue(3);
  } else {
    phaseA_genF(L, i, mt, w, lane);
    g1_issue(0); g1_issue(1);
  }
  FT(1);
  wait_vm<16>();
  asm volatile("" : "+v"(ep), "+v"(gx), "+v"(gv) :: "memory");
  const unsigned tag = (unsigned)__builtin_amdgcn_readfirstlane((int)ep) + 1u;
  float gate_g, gate_og;
  {
    float z = 0.f;
#pragma unroll
    for (int q = 0; q < 4; ++q) {
      const unsigned pr = pk2(bflo(gx[q]) * bflo(gv[q]), bfhi(gx[q]) * bfhi(gv[q]));
      z += bflo(pr) + bfhi(pr);
    }
    z = dpp_add<0x111, 0xf>(z); z = dpp_add<0x112, 0xf>(z); z = dpp_add<0x114, 0xf>(z); z = dpp_add<0x118, 0xf>(z);
    z = dpp_add<0x142, 0xa>(z); z = dpp_add<0x143, 0xc>(z);
    const float zb = bflo(pk2(z, 0.f));
    const float g = bflo(pk2(__builtin_amdgcn_rcpf(1.f + __builtin_amdgcn_exp2f(zb * -1.44269504088896341f)), 0.f));
    gate_g = asf((unsigned)__builtin_amdgcn_readlane((int)__builtin_bit_cast(unsigned, g), 63));
    gate_og = bflo(pk2(1.f - gate_g, 0.f));
  }
  FT(2);
  if (isA) {
    const int dq = w & 3, mh = w >> 2;
    LAS char* xa = L + A_XIMG + (32 * hh + q4) * 256 + (((4 * dq + 2 * blk + (p4 >> 1)) ^ (q4 << 2)) << 4) + 8 * (p4 & 1);
    LAS char* fb = L + A_FIMG + mh * 16384 + lane * 16;
    const int srow = 32 * mh + (lane & 31);
    LAS char* stw = L + A_FIMG + srow * 256 + 8 * hh;
    const int rrow = tid >> 3;
    LAS char* str = L + A_FIMG + rrow * 256;
    const unsigned tdst = WS_T + (unsigned)(((64 * mt + rrow) * 8 + i) * 1024 + dt4 * 256 + (tid & 7) * 16);
    if (w < 4) bar_vm<8>(); else BAR_LGKM();
    STAMP(1);
    FT(3);
    f32x16 acc = {};
    v4u fa[2][4], fbv[2][4];
#pragma unroll
    for (int s = 0; s < 4; ++s) { fa[0][s] = tr_read2(xa + 8 * s * 256, xa + 8 * s * 256 + 1024); fbv[0][s] = lds_ld16(fb + s * 1024); }
#pragma unroll
    for (int g = 0; g < 4; ++g) {
      acc = mfma32(fa[g & 1][0], fbv[g & 1][0], acc);
      SCHED();
      if (g == 1) { if (w >= 4) bar_vm<8>(); else BAR_LGKM(); }
      if (g < 3) {
#pragma unroll
        for (int s = 0; s < 4; ++s) {
          const int ro = (64 * (g + 1) + 8 * s) * 256;
          fa[(g + 1) & 1][s] = tr_read2(xa + ro, xa + ro + 1024); fbv[(g + 1) & 1][s] = lds_ld16(fb + (4 * (g + 1) + s) * 1024);
        }
      }
      SCHED();
#pragma unroll
      for (int s = 1; s < 4; ++s) acc = mfma32(fa[g & 1][s], fbv[g & 1][s], acc);
      SCHED();
    }
    FT(4);
    BAR_LGKM();   STAMP(2);
    g1_issue(2); g1_issue(3);
#pragma unroll
    for (int G = 0; G < 4; ++G) {
      v2u o; o.x = pk2(acc[4 * G], acc[4 * G + 1]); o.y = pk2(acc[4 * G + 2], acc[4 * G + 3]);
      *(LAS v2u*)(stw + (((4 * dq + G) ^ (srow & 15)) << 4)) = o;
    }
    BAR_LGKM();
    {
      const v4u v0 = lds_ld16(str + (((tid & 7) ^ (rrow & 15)) << 4)), v1 = lds_ld16(str + ((((tid & 7) + 8) ^ (rrow & 15)) << 4));
      st16_sc1(sws, tdst, v0);
      st16_sc1(sws, tdst + 128u, v1);
    }
    SCHED();
    FT(5);
  }
  FT(6);
  const int jb = Lx >> 3, dt = Lx & 7, rh = w & 1, q = w >> 1;
  const v4u sw2 = make_srd(p.w2, NF * ND * 2);
  const int rho = 16 * rh + (lane >> 3), chs = (lane & 7) ^ (4 * ((lane >> 4) & 1));
  const unsigned vw20 = (unsigned)(rho * 1024 + dt * 128 + chs * 16), vw21 = vw20 + 8 * 1024;
  const unsigned ldsw = (unsigned)(q * 4096 + rh * 2048);
  const unsigned fq0 = (unsigned)(768 + 256 * q), fq1 = (unsigned)(192 * q - 256);
  auto w2_issue1 = [&](int t, int u) {
    const unsigned slot = (unsigned)(t & 7) * 16384u + ldsw + (unsigned)u * 1024u;
    dma16(sw2, u ? vw21 : vw20, ((t < 8 ? fq0 : fq1) + 32u * (unsigned)t) * 1024u, slot);
  };
  const int ft = w & 3, rh1 = w >> 2;
  const bool active = ft * 32 < nf;
  f32x16 acc1[2] = {};
  v4u tq0, tq1;
  WRaw Wc;
  {
    LAS char* wa = L + 16384 + (8 * hh + q4) * 256 + (((4 * ft + 2 * blk + (p4 >> 1)) ^ (q4 << 2)) << 4) + 8 * (p4 & 1);
    const int r = 64 * rh1 + (lane & 31), fz = (r >> 1) & 7;
    LAS char* xb0 = L + r * 128 + (((0 + hh) ^ fz) << 4);
    LAS char* xb1 = L + r * 128 + (((2 + hh) ^ fz) << 4);
    LAS char* xb2 = L + r * 128 + (((4 + hh) ^ fz) << 4);
    LAS char* xb3 = L + r * 128 + (((6 + hh) ^ fz) << 4);
    Frag1 fr[2];
    FT(7);
    if (isA) bar_vm<14>(); else bar_vm<12>();
    STAMP(3);
    FT(8);
    g1_read(fr[0], wa, xb0, xb1, xb2, xb3, 0);
    FT(9);
    unsigned fla;
#define PIN1(c) asm volatile("" : "+v"(c.a[0]), "+v"(c.a[1]), "+v"(c.a[2]), "+v"(c.a[3]), "+v"(c.b0[0]), "+v"(c.b0[1]), "+v"(c.b0[2]), "+v"(c.b0[3]), \
                             "+v"(c.b1[0]), "+v"(c.b1[1]), "+v"(c.b1[2]), "+v"(c.b1[3]))
#define G1_DMA(kt, u)                                                                                                 \
    {                                                                                                                   \
      if ((kt) < 4) {                                                                                                   \
        const unsigned slot_ = (unsigned)((kt) & 3) * 32768u + (unsigned)w * 2048u;                                     \
        if ((u) == 0) dma16(sx, vx0, (unsigned)((kt) + 4) * 128u, slot_);                                               \
        if ((u) == 1) dma16(sx, vx1, (unsigned)((kt) + 4) * 128u, slot_ + 1024u);                                       \
        if ((u) == 2) dma16(sw1, vw0, (unsigned)((kt) + 4) * (64u * 3584u), slot_ + 16384u);                            \
        if ((u) == 3) dma16(sw1, vw1, (unsigned)((kt) + 4) * (64u * 3584u), slot_ + 17408u);                            \
      } else {                                                                                                          \
        w2_issue1(2 * ((kt) - 4) + ((u) >> 1), (u) & 1);                                                                \
      }                                                                                                                 \
    }
#define G1_STEP(kt, X0, X1, X2, X3)                                                                                   \
    {                                                                                                                   \
      FT(10 + (kt));                                                                                                    \
      Frag1& c = fr[(kt) & 1];                                                                                          \
      Frag1& n = fr[((kt) + 1) & 1];                                                                                    \
      const int so = (((kt) + 1) & 3) * 32768;                                                                          \
      PIN1(c);                                                                                                          \
      SCHED();                                                                                                          \
      acc1[0] = mfma32(c.a[0], c.b0[0], acc1[0]); if ((kt) < 7) n.a[0] = tr_read2(wa + so, wa + so + 1024); X0; SCHED(); \
      acc1[1] = mfma32(c.a[0], c.b1[0], acc1[1]); if ((kt) < 7) { n.b0[0] = lds_ld16(xb0 + so); n.b1[0] = lds_ld16(xb0 + so + 4096); } X1; SCHED(); \
      acc1[0] = mfma32(c.a[1], c.b0[1], acc1[0]); if ((kt) < 7) n.a[1] = tr_read2(wa + so + 4096, wa + so + 5120); X2; SCHED(); \
      acc1[1] = mfma32(c.a[1], c.b1[1], acc1[1]); if ((kt) < 7) { n.b0[1] = lds_ld16(xb1 + so); n.b1[1] = lds_ld16(xb1 + so + 4096); } X3; SCHED(); \
      acc1[0] = mfma32(c.a[2], c.b0[2], acc1[0]); if ((kt) < 7) n.a[2] = tr_read2(wa + so + 8192, wa + so + 9216); G1_DMA(kt, 0) SCHED(); \
      acc1[1] = mfma32(c.a[2], c.b1[2], acc1[1]); if ((kt) < 7) { n.b0[2] = lds_ld16(xb2 + so); n.b1[2] = lds_ld16(xb2 + so + 4096); } G1_DMA(kt, 1) SCHED(); \
      acc1[0] = mfma32(c.a[3], c.b0[3], acc1[0]); if ((kt) < 7) n.a[3] = tr_read2(wa + so + 12288, wa + so + 13312); G1_DMA(kt, 2) SCHED(); \
      acc1[1] = mfma32(c.a[3], c.b1[3], acc1[1]); if ((kt) < 7) { n.b0[3] = lds_ld16(xb3 + so); n.b1[3] = lds_ld16(xb3 + so + 4096); } G1_DMA(kt, 3) SCHED(); \
    }
#define NOP do {} while (0)
#define IFB(x) if (!isA) { x; }
    const v4u swr = make_srd(p.wr, NS * 257 * 2), swi = make_srd(p.wi, NS * 257 * 2), stab = make_srd(&kTab, sizeof(Tab));
    const WAddr wadr = w_addr(m1 == 0 ? 128 * w : m1 + 256 * w, lane);
    const unsigned tabl = 128u * 1024u + 16384u + (unsigned)w * 2048u;
    unsigned tsrc0, tsrc1;
    {
      const int kap = tid >> 5, ii = (kap >> 1) & 7, d0 = kap & 1;
      const int g0 = m1 == 0 ? 2 * ii : ii, g1 = m1 == 0 ? 2 * ii + 1 : 8 + ii;
      const unsigned tb = WS_T + (unsigned)m1 * 16384u + (unsigned)(d0 * 512 + (tid & 31) * 16);
      tsrc0 = tb + (unsigned)g0 * 1024u; tsrc1 = tb + (unsigned)g1 * 1024u;
    }
    if (isA) bar_vm<10>(); else bar_vm<8>();
    STAMP(4);
    G1_STEP(0, NOP, NOP, NOP, NOP)
    if (isA) bar_vm<10>(); else bar_vm<8>();
    G1_STEP(1, NOP, NOP, NOP, NOP)
    bar_vm<8>();
    if (isA && tid == 0) flag_store(p.ws, WS_FLA + (unsigned)(mt * 32 + i * 4 + dt4) * FS, tag);
    G1_STEP(2, NOP, NOP, NOP, NOP)
    if (isA && w == 0) bar_vm<9>(); else bar_vm<8>();
    G1_STEP(3, NOP, NOP, IFB(dma16(stab, (unsigned)lane * 16u, (unsigned)w * 2048u, tabl)), IFB(dma16(stab, (unsigned)lane * 16u, (unsigned)w * 2048u + 1024u, tabl + 1024u)))
    if (isA) { if (w == 0) bar_vm<9>(); else bar_vm<8>(); } else bar_vm<10>();
    STAMP(12);
    G1_STEP(4, IFB(LD4(Wc.r0, wadr.e0, swr, 0u, "")), IFB(LD4(Wc.r1, wadr.e1, swr, 0u, "")), IFB(LD4(Wc.r2, wadr.e2, swr, 0u, "")), IFB(LD4(Wc.r3, wadr.e3, swr, 0u, "")))
    if (isA) bar_vm<8>(); else bar_vm<14>();
    G1_STEP(5, IFB(LD4(Wc.i0, wadr.e0, swi, 0u, "")), IFB(LD4(Wc.i1, wadr.e1, swi, 0u, "")), IFB(LD4(Wc.i2, wadr.e2, swi, 0u, "")), IFB(LD4(Wc.i3, wadr.e3, swi, 0u, "")))
    if (isA) bar_vm<8>(); else bar_vm<16>();
    G1_STEP(6, IFB(LD2(Wc.xr, wadr.ex, swr, 0u, "")), IFB(LD2(Wc.xi, wadr.ex, swi, 0u, "")),
            IFB(if (w == 0) LD4(fla, WS_FLA + (unsigned)((m1 >> 5) * 32 + (lane & 31)) * FS, sws, 0u, "sc1")), NOP)
    if (!isA && w == 0) {
      asm volatile("s_waitcnt vmcnt(4)" : "+v"(fla) :: "memory");
      if (!__all(fla == tag)) poll_words(sws, WS_FLA + (unsigned)(m1 >> 5) * 32u * FS, 32, tag, lane);
    }
    BAR_LGKM();
    G1_STEP(7, IFB(LD16(tq0, tsrc0, sws, 0u, "sc1")), IFB(LD16(tq1, tsrc1, sws, 0u, "sc1")), NOP, NOP)
  }
  STAMP(5);
  FT(18);
  if (active) {
#pragma unroll
    for (int rt = 0; rt < 2; ++rt)
#pragma unroll
      for (int u = 0; u < 2; ++u) {
        v4u o;
        o.x = pk2(silu(acc1[rt][8 * u + 0]), silu(acc1[rt][8 * u + 1]));
        o.y = pk2(silu(acc1[rt][8 * u + 2]), silu(acc1[rt][8 * u + 3]));
        o.z = pk2(silu(acc1[rt][8 * u + 4]), silu(acc1[rt][8 * u + 5]));
        o.w = pk2(silu(acc1[rt][8 * u + 6]), silu(acc1[rt][8 * u + 7]));
        const int jb32 = 4 * rb + 2 * rh1 + rt, fs = (f0 >> 4) + 2 * ft + u;
        st16_sc1(sws, WS_H + (unsigned)(((i * 8 + jb32) * 112 + fs) * 1024 + lane * 16), o);
      }
  }
  if (lane == 0) {
    const unsigned long long v = ((unsigned long long)__builtin_bit_cast(unsigned, gate_og) << 32) | __builtin_bit_cast(unsigned, gate_g);
    __hip_atomic_store((GAS unsigned long long*)((GAS char*)p.ws + WS_GATE + (i * 256 + jg) * 8), v, __ATOMIC_RELAXED, __HIP_MEMORY_SCOPE_AGENT);
  }
  SCHED();
  STAMP(6);
  FT(20);
  const int j = 64 * jb + 32 * rh + (lane & 31);
  v4u gf[4];
  auto gen_g = [&]() {
    const int srow = i + 8 * j;
    const int mb = 32 * q + 2 * hh;
    const float r1 = (float)(srow & 2047) * (1.f / 2048.f), r4 = (float)((4 * srow) & 2047) * (1.f / 2048.f), r0 = (float)((mb * srow) & 2047) * (1.f / 2048.f);
    const float c1 = cosrev(r1), s1 = sinrev(r1), c4 = cosrev(r4), s4 = sinrev(r4);
    float c = cosrev(r0), s = sinrev(r0);
#pragma unroll
    for (int e = 0; e < 8; ++e) {
      const float cb = __builtin_fmaf(c, c1, -(s * s1)), sb = __builtin_fmaf(s, c1, c * s1);
      unsigned w0 = pk2(c, -s), w1 = pk2(cb, -sb);
      if (e == 0 && q == 0 && hh == 0) w0 = (j & 1) ? 0x3F800000u : 0x00003F80u;
      if (e & 1) { gf[e >> 1].z = w0; gf[e >> 1].w = w1; } else { gf[e >> 1].x = w0; gf[e >> 1].y = w1; }
      const float cn = __builtin_fmaf(c, c4, -(s * s4)), sn = __builtin_fmaf(s, c4, c * s4);
      c = cn; s = sn;
    }
    asm volatile("" : "+v"(gf[0]), "+v"(gf[1]), "+v"(gf[2]), "+v"(gf[3]));
  };
  if (isA) {
    gen_g();
    bar_vm<0>();
    if (tid == 0) flag_store(p.ws, WS_FLH + (unsigned)((i * 2 + rb) * 16 + fc) * FS, tag);
  } else {
    LAS char* I0 = L + 128 * 1024;
    LAS char* I1 = L + 128 * 1024 + 16384;
    const int g = lane >> 4, l15 = lane & 15;
    const bool special = (m1 == 0);
    asm volatile("s_waitcnt vmcnt(8)" : "+v"(tq0), "+v"(tq1), W_PIN(Wc) :: "memory");
    {
      const int kap = tid >> 5, ch = tid & 31;
      *(LAS v4u*)(I0 + kap * 512 + ((ch ^ ((kap & 3) << 2)) << 4)) = tq0;
      *(LAS v4u*)(I0 + (kap + 16) * 512 + ((ch ^ ((kap & 3) << 2)) << 4)) = tq1;
    }
    v4u tb[16];
#pragma unroll
    for (int f = 0; f < 16; ++f) tb[f] = lds_ld16(I1 + f * 1024 + lane * 16);
    float W0[8], W1[8];
    w_unpack(Wc, lane, W0, W1);
    WRaw Wd;
    if (special) w_issue(Wd, make_srd(p.wr, NS * 257 * 2), make_srd(p.wi, NS * 257 * 2), 128 * w + 1024, lane);
    BAR_LGKM();
    FT(21);
    LAS char* ta = I0 + (8 * hh + q4) * 512 + (((4 * w + 2 * blk + (p4 >> 1)) ^ (q4 << 2)) << 4) + 8 * (p4 & 1);
    const int n = lane & 31;
    LAS char* so = I1 + n * 512 + 8 * hh;
    {
      f32x16 acc = {};
      acc = mfma32(tr_read2(ta, ta + 2048), tb[6], acc);
      acc = mfma32(tr_read2(ta + 8192, ta + 10240), tb[7], acc);
#pragma unroll
      for (int G = 0; G < 4; ++G) {
        v2u o; o.x = pk2(acc[4 * G], acc[4 * G + 1]); o.y = pk2(acc[4 * G + 2], acc[4 * G + 3]);
        *(LAS v2u*)(so + (((4 * w + G) ^ (n & 15)) << 4)) = o;
      }
    }
    bar_vm<0>();
    if (tid == 0) flag_store(p.ws, WS_FLH + (unsigned)((i * 2 + rb) * 16 + fc) * FS, tag);
    FT(22);
    const f32x4 z4 = {0.f, 0.f, 0.f, 0.f};
    const v4u F16fr = tb[0], F16fi = tb[1];
    v4u F16ir, F16ii;
    F16ir.x = F16fr.x; F16ir.y = F16fr.y; F16ir.z = F16fr.z ^ 0x80008000u; F16ir.w = F16fr.w ^ 0x80008000u;
    F16ii.x = F16fi.x ^ 0x80008000u; F16ii.y = F16fi.y ^ 0x80008000u; F16ii.z = F16fi.z; F16ii.w = F16fi.w;
    f32x4 zr[2], zi[2];
#pragma unroll
    for (int pp = 0; pp < 2; ++pp) {
      const int n0 = 4 * w + 2 * pp, n1 = n0 + 1;
      const int ch = 2 * (4 * g + q4) + (p4 >> 1);
      const v4u a1 = tr_read2(I1 + n0 * 512 + ((ch ^ (n0 & 15)) << 4) + 8 * (p4 & 1), I1 + n1 * 512 + ((ch ^ (n1 & 15)) << 4) + 8 * (p4 & 1));
      const f32x4 dr = mfma16(a1, pp ? tb[2] : F16fr, z4), di = mfma16(a1, pp ? tb[3] : F16fi, z4);
      const v4u b3 = twiddle_pack<false>(dr, di, tb[10 + 2 * pp], tb[11 + 2 * pp]);
      zr[pp] = mfma16(F16fr, b3, z4); zi[pp] = mfma16(F16fi, b3, z4);
    }
    v4u a4[2];
    if (!special) {
      a4[0] = twiddle_pack<false>(zr[0], zi[0], W0[0], W0[1], W0[2], W0[3], W0[4], W0[5], W0[6], W0[7]);
      a4[1] = twiddle_pack<false>(zr[1], zi[1], W1[0], W1[1], W1[2], W1[3], W1[4], W1[5], W1[6], W1[7]);
    } else {
      asm volatile("s_waitcnt vmcnt(0) lgkmcnt(0)\n\ts_barrier" : W_PIN(Wd) :: "memory");
      float X0[8], X1[8];
      w_unpack(Wd, lane, X0, X1);
      LAS char* Z = I0;
#pragma unroll
      for (int pp = 0; pp < 2; ++pp)
#pragma unroll
        for (int r = 0; r < 4; ++r) { f32x2 v = {zr[pp][r], zi[pp][r]}; *(LAS f32x2*)(Z + ((w * 2 + pp) * 256 + l15 + 16 * (4 * g + r)) * 8) = v; }
      BAR_LGKM();
      const cf om = {cosrev((float)w * 0.0625f), -sinrev((float)w * 0.0625f)};
#pragma unroll
      for (int pp = 0; pp < 2; ++pp) {
        float yr[4], yi[4];
#pragma unroll
        for (int r = 0; r < 4; ++r) {
          const int K = l15 + 16 * (4 * g + r), Kn = pp ? 255 - K : (256 - K) & 255;
          const f32x2 y = *(LAS f32x2*)(Z + ((((8 - w) & 7) * 2 + pp) * 256 + Kn) * 8);
          const float zre = zr[pp][r], zim = zi[pp][r], cr = y[0], ci = -y[1];
          const cf E = {0.5f * (zre + cr), 0.5f * (zim + ci)};
          const cf O = {0.5f * (zim - ci), -0.5f * (zre - cr)};
          const float war = pp ? W1[2 * r] : W0[2 * r], wai = pp ? W1[2 * r + 1] : W0[2 * r + 1];
          const float wbr = pp ? X1[2 * r] : X0[2 * r], wbi = pp ? X1[2 * r + 1] : X0[2 * r + 1];
          const cf sp = {war + wbr, wai + wbi}, sm = {war - wbr, wai - wbi};
          const cf t1 = cmulc(sm, om);
          const cf cE = {sp.r - t1.i, sp.i + t1.r};
          const cf t2 = cmul(sm, om);
          const cf cO = {t2.r - sp.i, t2.i + sp.r};
          const cf a = cmul(E, cE), bq = cmul(O, cO);
          yr[r] = 0.5f * (a.r + bq.r); yi[r] = 0.5f * (a.i + bq.i);
        }
        a4[pp].x = pk2(yr[0], yr[1]); a4[pp].y = pk2(yr[2], yr[3]); a4[pp].z = pk2(yi[0], yi[1]); a4[pp].w = pk2(yi[2], yi[3]);
      }
      BAR_LGKM();
    }
#pragma unroll
    for (int pp = 0; pp < 2; ++pp) {
      const f32x4 dr = mfma16(a4[pp], F16ir, z4), di = mfma16(a4[pp], F16ii, z4);
      const v4u a5 = twiddle_pack<true>(dr, di, tb[pp ? 14 : 10], tb[pp ? 15 : 11]);
      const f32x4 er = mfma16(a5, pp ? tb[4] : F16ir, z4), ei = mfma16(a5, pp ? tb[5] : F16ii, z4);
      const int n0 = 4 * w + 2 * pp, n1 = n0 + 1;
      const int ch = 2 * l15 + (g >> 1);
      v2u o; o.x = pk2(er[0], er[1]); o.y = pk2(er[2], er[3]);
      *(LAS v2u*)(I0 + n0 * 512 + ((ch ^ ((n0 & 3) << 2)) << 4) + 8 * (g & 1)) = o;
      o.x = pk2(ei[0], ei[1]); o.y = pk2(ei[2], ei[3]);
      *(LAS v2u*)(I0 + n1 * 512 + ((ch ^ ((n1 & 3) << 2)) << 4) + 8 * (g & 1)) = o;
    }
    BAR_LGKM();
    FT(23);
    {
      f32x16 acc = {};
      acc = mfma32(tr_read2(ta, ta + 2048), tb[8], acc);
      acc = mfma32(tr_read2(ta + 8192, ta + 10240), tb[9], acc);
#pragma unroll
      for (int G = 0; G < 4; ++G) {
        v2u o; o.x = pk2(acc[4 * G], acc[4 * G + 1]); o.y = pk2(acc[4 * G + 2], acc[4 * G + 3]);
        *(LAS v2u*)(so + (((4 * w + G) ^ (n & 15)) << 4)) = o;
      }
    }
    BAR_LGKM();
    {
      const int nn = tid >> 5, ch = tid & 31, ii = (nn >> 1) & 7, d0 = nn & 1;
      const int g0 = special ? 2 * ii : ii, g1 = special ? 2 * ii + 1 : 8 + ii;
      const v4u v0 = lds_ld16(I1 + nn * 512 + ((ch ^ (nn & 15)) << 4));
      const v4u v1 = lds_ld16(I1 + (nn + 16) * 512 + ((ch ^ (nn & 15)) << 4));
      const unsigned ub = WS_U + (unsigned)m1 * 16384u + (unsigned)(d0 * 512 + ch * 16);
      st16_sc1(sws, ub + (unsigned)g0 * 1024u, v0);
      st16_sc1(sws, ub + (unsigned)g1 * 1024u, v1);
    }
    SCHED();
    FT(24);
  }
  STAMP(7);
  FT(25);
  const unsigned flh = WS_FLH + (unsigned)((i * 2 + (jb >> 1)) * 16) * FS;
  if (w == 0) poll_words(sws, flh + 8u * FS, 8, tag, lane);
  asm volatile("s_barrier" ::: "memory");
  STAMP(8);
  FT(26);
  const unsigned vh = (unsigned)lane * 16u;
  const unsigned hbase = WS_H + (unsigned)((i * 8 + 2 * jb + rh) * 112) * 1024u;
  const unsigned hb0 = hbase + (fq0 >> 4) * 1024u, hb1 = hbase + (unsigned)((int)fq1 >> 4) * 1024u;
  v2u gg;
  v4u hf[14][2];
#define H_ISSUE(t) do { LD16(hf[t][0], vh, sws, ((t) < 8 ? hb0 : hb1) + (unsigned)(2 * (t)) * 1024u, "sc1"); LD16(hf[t][1], vh, sws, ((t) < 8 ? hb0 : hb1) + (unsigned)(2 * (t) + 1) * 1024u, "sc1"); } while (0)
  H_ISSUE(0); H_ISSUE(1);
  asm volatile("s_barrier" ::: "memory");
  H_ISSUE(2); H_ISSUE(3); H_ISSUE(4); H_ISSUE(5); H_ISSUE(6);
  if (!isA) gen_g();
  SCHED();
  f32x16 accb[2] = {}, acca[2] = {};
  const int sz = (q4 >> 1) & 1;
  LAS char* ra0 = L + q * 4096 + (4 * hh + q4) * 128 + (((0 ^ (4 * sz)) + 2 * blk + (p4 >> 1)) << 4) + 8 * (p4 & 1);
  LAS char* ra1 = L + q * 4096 + (4 * hh + q4) * 128 + (((4 ^ (4 * sz)) + 2 * blk + (p4 >> 1)) << 4) + 8 * (p4 & 1);
  v4u wf[2][4];
#define G2_BAR(n, t) asm volatile("s_waitcnt vmcnt(" STR(n) ") lgkmcnt(0)\n\ts_barrier" : "+v"(hf[t][0]), "+v"(hf[t][1]) :: "memory")
  G2_BAR(12, 0);
  if (!isA && tid == 0) flag_store(p.ws, WS_FLB + (unsigned)m1 * FS, tag);
  wf[0][0] = tr_read2(ra0, ra0 + 1024); wf[0][1] = tr_read2(ra1, ra1 + 1024);
  wf[0][2] = tr_read2(ra0 + 2048, ra0 + 3072); wf[0][3] = tr_read2(ra1 + 2048, ra1 + 3072);
  unsigned fl0, fl1, flb;
  v4u ub0, ub1, ub2, ub3;
  const unsigned vu = WS_U + (unsigned)(((64 * q + rho) * 8 + i) * 1024 + dt * 128 + chs * 16);
#define G2_MMA(t, acc, b0, b1, X0, X1, X2)                                                        \
  {                                                                                                 \
    FT(30 + (t));                                                                                   \
    v4u (&c)[4] = wf[(t) & 1];                                                                     \
    v4u (&n)[4] = wf[((t) + 1) & 1];                                                               \
    LAS char* s0 = ra0 + (((t) + 1) & 7) * 16384; LAS char* s1 = ra1 + (((t) + 1) & 7) * 16384;    \
    asm volatile("" : "+v"(c[0]), "+v"(c[1]), "+v"(c[2]), "+v"(c[3]));                             \
    SCHED();                                                                                        \
    acc[0] = mfma32(c[0], b0, acc[0]); if ((t) < 15) n[0] = tr_read2(s0, s0 + 1024); SCHED();      \
    acc[1] = mfma32(c[1], b0, acc[1]); if ((t) < 15) n[1] = tr_read2(s1, s1 + 1024); X0; SCHED();  \
    acc[0] = mfma32(c[2], b1, acc[0]); if ((t) < 15) n[2] = tr_read2(s0 + 2048, s0 + 3072); X1; SCHED(); \
    acc[1] = mfma32(c[3], b1, acc[1]); if ((t) < 15) n[3] = tr_read2(s1 + 2048, s1 + 3072); X2; SCHED(); \
  }
#define G2_STEP(t, X2) G2_MMA(t, accb, hf[t][0], hf[t][1], if ((t) <= 5) w2_issue1((t) + 8, 0), if ((t) <= 5) w2_issue1((t) + 8, 1), X2)
  BAR_LGKM();    G2_STEP(0, H_ISSUE(7))
  G2_BAR(14, 1); G2_STEP(1, NOP)
  G2_BAR(14, 2); G2_STEP(2, NOP)
  if (w == 0) LD4(flb, flh + (unsigned)(lane & 7) * FS, sws, 0u, "sc1");
  G2_BAR(14, 3); G2_STEP(3, NOP)
  if (w == 0) {
    asm volatile("s_waitcnt vmcnt(2)" : "+v"(flb) :: "memory");
    if (!__all(flb == tag)) poll_words(sws, flh, 8, tag, lane);
  }
  G2_BAR(14, 4); G2_STEP(4, { H_ISSUE(8); H_ISSUE(9); LD8(gg, WS_GATE + (unsigned)(i * 256 + j) * 8u, sws, 0u, "sc1"); })
  G2_BAR(19, 5); G2_STEP(5, { H_ISSUE(10); H_ISSUE(11); })
  if (w == 0) { LD4(fl0, WS_FLB + (unsigned)lane * FS, sws, 0u, "sc1"); LD4(fl1, WS_FLB + (64u + (unsigned)lane) * FS, sws, 0u, "sc1"); }
  G2_BAR(23, 6); G2_STEP(6, { H_ISSUE(12); H_ISSUE(13); })
  STAMP(9);
  G2_BAR(23, 7); G2_STEP(7, NOP)
  if (w == 0) {
    asm volatile("s_waitcnt vmcnt(4)" : "+v"(fl0), "+v"(fl1) :: "memory");
    if (!__all(fl0 == tag && fl1 == tag)) { poll_words(sws, WS_FLB, 64, tag, lane); poll_words(sws, WS_FLB + 64u * FS, 64, tag, lane); }
  }
  G2_BAR(13, 8);
  LD16(ub0, vu, sws, 0u, "sc1"); LD16(ub1, vu, sws, 8u * 8192u, "sc1"); LD16(ub2, vu, sws, 32u * 8192u, "sc1"); LD16(ub3, vu, sws, 40u * 8192u, "sc1");
  G2_STEP(8, NOP)
  G2_BAR(15, 9); G2_STEP(9, NOP)
  G2_BAR(10, 10); G2_STEP(10, NOP)
  G2_BAR(8, 11); G2_STEP(11, NOP)
  G2_BAR(6, 12);
  G2_STEP(12, NOP)
  asm volatile("s_waitcnt vmcnt(0)" : "+v"(ub0), "+v"(ub1), "+v"(ub2), "+v"(ub3), "+v"(gg) :: "memory");
  *(LAS v4u*)(L + 6 * 16384 + ldsw + lane * 16) = ub0;
  *(LAS v4u*)(L + 6 * 16384 + ldsw + 1024 + lane * 16) = ub1;
  *(LAS v4u*)(L + 7 * 16384 + ldsw + lane * 16) = ub2;
  *(LAS v4u*)(L + 7 * 16384 + ldsw + 1024 + lane * 16) = ub3;
  G2_BAR(0, 13); G2_STEP(13, NOP)
  STAMP(10);
  BAR_LGKM(); G2_MMA(14, acca, gf[0], gf[1], NOP, NOP, NOP)
  BAR_LGKM(); G2_MMA(15, acca, gf[2], gf[3], NOP, NOP, NOP)
  FT(50);
  {
    const float og = asf(gg.y), gs = asf(gg.x) * (1.f / 524288.f);
    const int row = 32 * rh + (lane & 31);
#pragma unroll
    for (int dh = 0; dh < 2; ++dh)
#pragma unroll
      for (int G = 0; G < 4; ++G) {
        f32x4 v;
#pragma unroll
        for (int e = 0; e < 4; ++e) v[e] = __builtin_fmaf(og, accb[dh][4 * G + e], gs * acca[dh][4 * G + e]);
        *(LAS f32x4*)(L + q * 16384 + row * 256 + (((8 * dh + 2 * G + hh) ^ (row & 7)) << 4)) = v;
      }
  }
  BAR_LGKM();
  FT(51);
  {
    const int row = tid >> 3, c8 = tid & 7;
    f32x4 s0 = {0.f, 0.f, 0.f, 0.f}, s1 = s0;
#pragma unroll
    for (int qq = 0; qq < 4; ++qq) {
      s0 += *(LAS f32x4*)(L + qq * 16384 + row * 256 + (((2 * c8) ^ (row & 7)) << 4));
      s1 += *(LAS f32x4*)(L + qq * 16384 + row * 256 + (((2 * c8 + 1) ^ (row & 7)) << 4));
    }
    v4u o; o.x = pk2(s0[0], s0[1]); o.y = pk2(s0[2], s0[3]); o.z = pk2(s1[0], s1[1]); o.w = pk2(s1[2], s1[3]);
    *(GAS v4u*)((GAS char*)p.out + (size_t)(i + 8 * (64 * jb + row)) * 1024 + dt * 128 + c8 * 16) = o;
  }
  STAMP(11);
  if (b == 0 && w == 0) {
#pragma unroll 1
    for (int u = 0; u < 4; ++u) poll_words(sws, WS_FLH + 64u * FS * u, 64, tag, lane);
    if (lane == 0) flag_store(p.ws, WS_EPOCH, tag);
  }
  { const long long t0 = (long long)__hip_atomic_load((GAS unsigned long long*)((GAS char*)p.ws + 2048), __ATOMIC_RELAXED, __HIP_MEMORY_SCOPE_AGENT);
    while (__builtin_amdgcn_s_memrealtime() - t0 < 4000) {} }
}
__global__ void k_stamp(void* ws) { if (threadIdx.x == 0) __hip_atomic_store((GAS unsigned long long*)((GAS char*)ws + 2048), (unsigned long long)__builtin_amdgcn_s_memrealtime(), __ATOMIC_RELAXED, __HIP_MEMORY_SCOPE_AGENT); }

}

extern "C" void kernel_launch(void* const* d_in, const int* in_sizes, int n_in, void* d_out, int out_size, void* d_ws, size_t ws_size,
                              hipStream_t stream) {
  Params p;
  p.x = d_in[0]; p.wr = d_in[1]; p.wi = d_in[2]; p.w1 = d_in[4]; p.w2 = d_in[5]; p.gw = d_in[6];
  p.out = d_out; p.ws = d_ws;
  (void)hipFuncSetAttribute((const void*)gm_kernel, hipFuncAttributeMaxDynamicSharedMemorySize, LDS_BYTES);
  k_stamp<<<1, 64, 0, stream>>>(d_ws);
  gm_kernel<<<256, 512, LDS_BYTES, stream>>>(p);
}
```

```cpp
#include <hip/hip_runtime.h>
#include <hip/hip_bf16.h>
#include <stdint.h>

namespace {

typedef unsigned v4u __attribute__((ext_vector_type(4)));
typedef unsigned v2u __attribute__((ext_vector_type(2)));
typedef float f32x16 __attribute__((ext_vector_type(16)));
typedef float f32x4 __attribute__((ext_vector_type(4)));
typedef float f32x2 __attribute__((ext_vector_type(2)));
typedef __bf16 bf16x8 __attribute__((ext_vector_type(8)));
typedef __bf16 bf16x2 __attribute__((ext_vector_type(2)));
typedef short s16x4 __attribute__((ext_vector_type(4)));
#define LAS __attribute__((address_space(3)))
#define GAS __attribute__((address_space(1)))
#define DI __device__ __forceinline__

constexpr int NS = 2048, ND = 512, NF = 1792;
constexpr unsigned WS_EPOCH = 0;
constexpr unsigned FS = 64;
constexpr unsigned WS_FLA = 4096;
constexpr unsigned WS_FLH = 16384;
constexpr unsigned WS_FLB = 32768;
constexpr unsigned WS_GATE = 65536;
constexpr unsigned WS_T = 1u << 20;
constexpr unsigned WS_U = 3u << 20;
constexpr unsigned WS_H = 5u << 20;
constexpr unsigned H_BYTES = 8u * 8u * 112u * 1024u;
constexpr int LDS_BYTES = 160 * 1024;
constexpr unsigned kSpinCap = 1u << 20;

struct Params {
  const void* x; const void* wr; const void* wi; const void* w1; const void* w2; const void* gw;
  void* out; void* ws;
};

constexpr double kPi = 3.14159265358979323846;
constexpr double ccos(double x) { double t = 1, s = 1; for (int n = 1; n < 16; ++n) { t *= -x * x / ((2 * n - 1) * (2 * n)); s += t; } return s; }
constexpr double cosn(int e, int n) { e = ((e % n) + n) % n; double x = 2 * kPi * e / n; if (x > kPi) x -= 2 * kPi; return ccos(x); }
constexpr double wre(int e, int n) { return cosn(e, n); }
constexpr double wim(int e, int n) { return -cosn(4 * e - n, 4 * n); }
constexpr unsigned bfb(double v) { float f = (float)v; unsigned u = __builtin_bit_cast(unsigned, f); u += 0x7fffu + ((u >> 16) & 1u); return u >> 16; }
constexpr unsigned bf2(double a, double b) { return bfb(a) | (bfb(b) << 16); }
constexpr unsigned fbits(double v) { return __builtin_bit_cast(unsigned, (float)v); }
struct Tab { unsigned v[16][64][4]; };
constexpr Tab make_tab() {
  Tab t{};
  for (int l = 0; l < 64; ++l) {
    const int g = l >> 4, l15 = l & 15, hh = l >> 5, n = l & 31;
    for (int mtx = 0; mtx < 3; ++mtx) {
      double r[4] = {}, im[4] = {};
      for (int q = 0; q < 4; ++q) {
        const int a = 4 * g + q;
        if (mtx == 0) { r[q] = wre(l15 * a, 16); im[q] = wim(l15 * a, 16); }
        else if (mtx == 1) { r[q] = wre(a * (2 * l15 + 1), 32); im[q] = wim(a * (2 * l15 + 1), 32); }
        else { r[q] = wre(l15 * (2 * a + 1), 32); im[q] = -wim(l15 * (2 * a + 1), 32); }
      }
      t.v[2 * mtx][l][0] = bf2(r[0], r[1]); t.v[2 * mtx][l][1] = bf2(r[2], r[3]); t.v[2 * mtx][l][2] = bf2(-im[0], -im[1]); t.v[2 * mtx][l][3] = bf2(-im[2], -im[3]);
      t.v[2 * mtx + 1][l][0] = bf2(im[0], im[1]); t.v[2 * mtx + 1][l][1] = bf2(im[2], im[3]); t.v[2 * mtx + 1][l][2] = bf2(r[0], r[1]); t.v[2 * mtx + 1][l][3] = bf2(r[2], r[3]);
    }
    {
      const int m2 = n >> 2, pp = (n >> 1) & 1, cp = n & 1;
      double v0[8] = {}, v1[8] = {};
      for (int jj = 0; jj < 8; ++jj) {
        const int ii = (8 * hh + jj) >> 1, d0 = jj & 1;
        const double sg = (d0 & pp) ? -1.0 : 1.0, orr = sg * wre(ii * m2, 8), oi = sg * wim(ii * m2, 8);
        v0[jj] = cp ? oi : orr; v1[jj] = cp ? orr : -oi;
      }
      for (int q = 0; q < 4; ++q) { t.v[6][l][q] = bf2(v0[2 * q], v0[2 * q + 1]); t.v[7][l][q] = bf2(v1[2 * q], v1[2 * q + 1]); }
    }
    {
      const int cq = n >> 4, ii = (n >> 1) & 7, d0 = n & 1;
      for (int s = 0; s < 2; ++s) {
        double v[8] = {};
        for (int jj = 0; jj < 8; ++jj) {
          const int m2k = 4 * s + 2 * hh + (jj >> 2), pk = (jj >> 1) & 1, ck = jj & 1;
          const double sg = (d0 & pk) ? -1.0 : 1.0, orr = sg * wre(ii * m2k, 8), oi = -sg * wim(ii * m2k, 8);
          v[jj] = cq ? (ck ? orr : oi) : (ck ? -oi : orr);
        }
        for (int q = 0; q < 4; ++q) t.v[8 + s][l][q] = bf2(v[2 * q], v[2 * q + 1]);
      }
    }
    for (int q = 0; q < 4; ++q) {
      const int a = 4 * g + q;
      t.v[10][l][q] = fbits(wre(a * (2 * l15), 512));      t.v[11][l][q] = fbits(wim(a * (2 * l15), 512));
      t.v[12][l][q] = fbits(wre(a * (2 * l15 + 1), 512));  t.v[13][l][q] = fbits(wim(a * (2 * l15 + 1), 512));
      t.v[14][l][q] = fbits(wre(l15 * (2 * a + 1), 512));  t.v[15][l][q] = fbits(wim(l15 * (2 * a + 1), 512));
    }
  }
  return t;
}
__device__ const Tab kTab = make_tab();

constexpr unsigned STAMP_OFF = 512 * 1024;
#define STAMP(k) do {} while (0)
#define FT(k) do {} while (0)

DI v4u make_srd(const void* p, unsigned bytes) {
  const unsigned long long a = (unsigned long long)p;
  v4u r; r.x = (unsigned)a; r.y = (unsigned)(a >> 32) & 0xffffu; r.z = bytes; r.w = 0x00020000u;
  return r;
}
DI void dma16(v4u srd, unsigned voff, unsigned soff, unsigned lds) {
  asm volatile("s_mov_b32 m0, %3\n\ts_nop 0\n\tbuffer_load_dwordx4 %0, %1, %2 offen lds"
               :: "v"(voff), "s"(srd), "s"(soff), "s"(lds) : "memory");
}
#define LD16(dst, voffv, srd, soffv, flags) asm volatile("buffer_load_dwordx4 %0, %1, %2, %3 offen " flags : "=v"(dst) : "v"(voffv), "s"(srd), "s"(soffv) : "memory")
#define LD8(dst, voffv, srd, soffv, flags) asm volatile("buffer_load_dwordx2 %0, %1, %2, %3 offen " flags : "=v"(dst) : "v"(voffv), "s"(srd), "s"(soffv) : "memory")
#define LD4(dst, voffv, srd, soffv, flags) asm volatile("buffer_load_dword %0, %1, %2, %3 offen " flags : "=v"(dst) : "v"(voffv), "s"(srd), "s"(soffv) : "memory")
#define LD2(dst, voffv, srd, soffv, flags) asm volatile("buffer_load_ushort %0, %1, %2, %3 offen " flags : "=v"(dst) : "v"(voffv), "s"(srd), "s"(soffv) : "memory")
#define XSTR(x) #x
#define STR(x) XSTR(x)
#define BAR_VM(n) asm volatile("s_waitcnt vmcnt(" STR(n) ") lgkmcnt(0)\n\ts_barrier" ::: "memory")
#define BAR_LGKM() asm volatile("s_waitcnt lgkmcnt(0)\n\ts_barrier" ::: "memory")
#define SCHED() __builtin_amdgcn_sched_barrier(0)
template <int N> DI void bar_vm() { asm volatile("s_waitcnt vmcnt(%0) lgkmcnt(0)\n\ts_barrier" :: "i"(N) : "memory"); }
template <int N> DI void wait_vm() { asm volatile("s_waitcnt vmcnt(%0)" :: "i"(N) : "memory"); }

DI unsigned pk2(float a, float b) { f32x2 p = {a, b}; return __builtin_bit_cast(unsigned, __builtin_convertvector(p, bf16x2)); }
DI float bflo(unsigned u) { return __builtin_bit_cast(float, u << 16); }
DI float bfhi(unsigned u) { return __builtin_bit_cast(float, u & 0xffff0000u); }
DI float asf(unsigned u) { return __builtin_bit_cast(float, u); }
DI f32x16 mfma32(v4u a, v4u b, f32x16 c) {
  return __builtin_amdgcn_mfma_f32_32x32x16_bf16(__builtin_bit_cast(bf16x8, a), __builtin_bit_cast(bf16x8, b), c, 0, 0, 0);
}
DI f32x4 mfma16(v4u a, v4u b, f32x4 c) {
  return __builtin_amdgcn_mfma_f32_16x16x32_bf16(__builtin_bit_cast(bf16x8, a), __builtin_bit_cast(bf16x8, b), c, 0, 0, 0);
}
DI v2u tr_read(LAS char* p) { return __builtin_bit_cast(v2u, __builtin_amdgcn_ds_read_tr16_b64_v4i16((LAS s16x4*)p)); }
DI v4u tr_read2(LAS char* p0, LAS char* p1) { v2u a = tr_read(p0), b = tr_read(p1); v4u r; r.x = a.x; r.y = a.y; r.z = b.x; r.w = b.y; return r; }
DI v4u lds_ld16(LAS char* p) { return *(LAS v4u*)p; }
DI float cosrev(float r) { return __builtin_amdgcn_cosf(r); }
DI float sinrev(float r) { return __builtin_amdgcn_sinf(r); }
DI __amdgpu_buffer_rsrc_t as_rsrc(v4u srd) { return __builtin_amdgcn_make_buffer_rsrc((void*)(((unsigned long long)srd.y << 32) | srd.x), 0, srd.z, 0x00020000); }
DI void st16_sc1(v4u srd, unsigned voff, v4u v) { __builtin_amdgcn_raw_buffer_store_b128(v, as_rsrc(srd), voff, 0, 16); }
DI void flag_store(void* ws, unsigned off, unsigned tag) { __hip_atomic_store((GAS unsigned*)((GAS char*)ws + off), tag, __ATOMIC_RELAXED, __HIP_MEMORY_SCOPE_AGENT); }
template <int CTRL, int RMASK> DI float dpp_add(float v) { return v + asf((unsigned)__builtin_amdgcn_update_dpp(0, (int)__builtin_bit_cast(unsigned, v), CTRL, RMASK, 0xf, true)); }
DI void poll_words(v4u sws, unsigned off, int n, unsigned tag, int lane) {
  const unsigned vo = off + FS * (unsigned)(lane < n ? lane : 0);
#pragma unroll 1
  for (unsigned spins = 0; spins < kSpinCap; ++spins) {
    unsigned f;
    asm volatile("buffer_load_dword %0, %1, %2, 0 offen sc1\n\ts_waitcnt vmcnt(0)" : "=&v"(f) : "v"(vo), "s"(sws) : "memory");
    if (__all(f == tag)) return;
    __builtin_amdgcn_s_sleep(4);
  }
}

constexpr unsigned A_XIMG = 64 * 1024, A_FIMG = 128 * 1024;
DI void phaseA_genF(LAS char* L, int i, int mt, int w, int lane) {
  const int mh = w >> 2, sg = w & 3, n = lane & 31, hh = lane >> 5;
  const int m1c = 64 * mt + 32 * mh + n, m1 = m1c >> 1, c = m1c & 1;
  const int j0 = 64 * sg + 32 * hh;
  const float ph = c ? 0.25f : 0.f;
  const float k2 = 2.f * cosrev((float)(8 * m1) * (1.f / 2048.f));
  float vm = cosrev((float)(((i + 8 * (j0 - 1)) * m1) & 2047) * (1.f / 2048.f) + ph);
  float v0 = cosrev((float)(((i + 8 * j0) * m1) & 2047) * (1.f / 2048.f) + ph);
  const unsigned cst = c ? 0x3F800000u : 0x00003F80u;
  const bool sp = m1c < 2;
#pragma unroll
  for (int s = 0; s < 4; ++s) {
    float e[8];
#pragma unroll
    for (int q = 0; q < 8; ++q) { e[q] = v0; const float vn = __builtin_fmaf(k2, v0, -vm); vm = v0; v0 = vn; }
    v4u f; f.x = pk2(e[0], e[1]); f.y = pk2(e[2], e[3]); f.z = pk2(e[4], e[5]); f.w = pk2(e[6], e[7]);
    if (mt == 0 && w < 4) { f.x = sp ? cst : f.x; f.y = sp ? cst : f.y; f.z = sp ? cst : f.z; f.w = sp ? cst : f.w; }
    *(LAS v4u*)(L + A_FIMG + (mh * 16 + sg * 4 + s) * 1024 + lane * 16) = f;
  }
}

struct Frag1 { v4u a[4], b0[4], b1[4]; };
DI void g1_read(Frag1& f, LAS char* wa, LAS char* xb0, LAS char* xb1, LAS char* xb2, LAS char* xb3, int slot) {
  const int so = slot * 32768;
  f.a[0] = tr_read2(wa + so, wa + so + 1024);              f.b0[0] = lds_ld16(xb0 + so); f.b1[0] = lds_ld16(xb0 + so + 4096);
  f.a[1] = tr_read2(wa + so + 4096, wa + so + 5120);       f.b0[1] = lds_ld16(xb1 + so); f.b1[1] = lds_ld16(xb1 + so + 4096);
  f.a[2] = tr_read2(wa + so + 8192, wa + so + 9216);       f.b0[2] = lds_ld16(xb2 + so); f.b1[2] = lds_ld16(xb2 + so + 4096);
  f.a[3] = tr_read2(wa + so + 12288, wa + so + 13312);     f.b0[3] = lds_ld16(xb3 + so); f.b1[3] = lds_ld16(xb3 + so + 4096);
}
DI unsigned silu2(float a, float b) {
  const f32x2 h = {a, b}, t = h * -1.44269504088896341f;
  f32x2 e = {__builtin_amdgcn_exp2f(t[0]), __builtin_amdgcn_exp2f(t[1])};
  e = e + 1.f;
  const f32x2 r = {__builtin_amdgcn_rcpf(e[0]), __builtin_amdgcn_rcpf(e[1])};
  return __builtin_bit_cast(unsigned, __builtin_convertvector(h * r, bf16x2));
}

DI f32x2 lo2(const f32x4& v) { return __builtin_shufflevector(v, v, 0, 1); }
DI f32x2 hi2(const f32x4& v) { return __builtin_shufflevector(v, v, 2, 3); }
DI unsigned pk2v(f32x2 p) { return __builtin_bit_cast(unsigned, __builtin_convertvector(p, bf16x2)); }
DI f32x4 asf4(v4u v) { return __builtin_bit_cast(f32x4, v); }
template <bool CONJ> DI v4u cmul_pack(const f32x4& re, const f32x4& im, const f32x4& tr, const f32x4& ti) {
  const f32x2 r0 = lo2(re), r1 = hi2(re), i0 = lo2(im), i1 = hi2(im), c0 = lo2(tr), c1 = hi2(tr), s0 = lo2(ti), s1 = hi2(ti);
  f32x2 a0, a1, b0, b1;
  if (CONJ) {
    a0 = __builtin_elementwise_fma(r0, c0, i0 * s0); a1 = __builtin_elementwise_fma(r1, c1, i1 * s1);
    b0 = __builtin_elementwise_fma(i0, c0, -(r0 * s0)); b1 = __builtin_elementwise_fma(i1, c1, -(r1 * s1));
  } else {
    a0 = __builtin_elementwise_fma(r0, c0, -(i0 * s0)); a1 = __builtin_elementwise_fma(r1, c1, -(i1 * s1));
    b0 = __builtin_elementwise_fma(r0, s0, i0 * c0); b1 = __builtin_elementwise_fma(r1, s1, i1 * c1);
  }
  v4u r; r.x = pk2v(a0); r.y = pk2v(a1); r.z = pk2v(b0); r.w = pk2v(b1); return r;
}
struct cf { float r, i; };
DI cf cmul(cf a, cf b) { return {a.r * b.r - a.i * b.i, a.r * b.i + a.i * b.r}; }
DI cf cmulc(cf a, cf b) { return {a.r * b.r + a.i * b.i, a.i * b.r - a.r * b.i}; }
DI unsigned w_src(int m, int lane) {
  const int mm = (NS - m) & (NS - 1);
  const int e = lane < 32 ? m * 257 - (m & 1) : mm * 257 + (m & 1);
  return (unsigned)(e * 2 + (lane & 31) * 16);
}
DI unsigned w_tail_src(int m, int lane) {
  const int mm = (NS - m) & (NS - 1), l = lane & 3;
  return (unsigned)((l < 2 ? m * 257 + 255 + l : mm * 257 + (l - 2) * 256) * 2);
}
struct WTail { unsigned r255, r256, rm0, rm256, i255, i256, im0, im256; };
DI WTail w_tail(unsigned tr, unsigned ti) {
  return {(unsigned)__builtin_amdgcn_readlane((int)tr, 0), (unsigned)__builtin_amdgcn_readlane((int)tr, 1), (unsigned)__builtin_amdgcn_readlane((int)tr, 2), (unsigned)__builtin_amdgcn_readlane((int)tr, 3),
          (unsigned)__builtin_amdgcn_readlane((int)ti, 0), (unsigned)__builtin_amdgcn_readlane((int)ti, 1), (unsigned)__builtin_amdgcn_readlane((int)ti, 2), (unsigned)__builtin_amdgcn_readlane((int)ti, 3)};
}
struct WVal { f32x4 r0, i0, r1, i1; };
struct WRawL { unsigned r[4][2], i[4][2]; };
DI void w_put(LAS char* scr, v4u qr, v4u qi, int lane) { *(LAS v4u*)(scr + lane * 16) = qr; *(LAS v4u*)(scr + 1024 + lane * 16) = qi; }
DI int w_off(int m, int lane) { const int k1 = lane & 15, g = lane >> 4, odd = m & 1; return lane < 32 ? (2 * k1 + 128 * g + odd) * 2 : 512 + (511 - 2 * k1 - 128 * g - odd) * 2; }
DI void w_get(WRawL& o, LAS char* scr, int off, int lane, int r) {
  LAS unsigned* a = (LAS unsigned*)(scr + ((off + (lane < 32 ? 64 : -64) * r) & ~3));
  o.r[r][0] = a[0]; o.r[r][1] = a[1]; o.i[r][0] = a[256]; o.i[r][1] = a[257];
}
DI WVal w_unpack(const WRawL& x, const WTail& t, int off, int m, int lane) {
  const bool lo = lane < 32;
  const unsigned sh = (off & 2) * 8;
  unsigned rr[4], ii[4];
#pragma unroll
  for (int r = 0; r < 4; ++r) { rr[r] = __builtin_amdgcn_alignbit(x.r[r][1], x.r[r][0], sh); ii[r] = __builtin_amdgcn_alignbit(x.i[r][1], x.i[r][0], sh); }
  if (m & 1) { rr[3] = lane == 31 ? (rr[3] & 0xffffu) | (t.r255 << 16) : rr[3]; ii[3] = lane == 31 ? (ii[3] & 0xffffu) | (t.i255 << 16) : ii[3]; }
  else { rr[0] = lane == 32 ? (rr[0] & 0xffffu) | (t.rm256 << 16) : rr[0]; ii[0] = lane == 32 ? (ii[0] & 0xffffu) | (t.im256 << 16) : ii[0]; }
  const unsigned s0 = lo ? 16u : 0u, s1 = lo ? 0u : 16u, sg = lo ? 0u : 0x80000000u;
  WVal o;
#pragma unroll
  for (int r = 0; r < 4; ++r) {
    o.r0[r] = asf((rr[r] << s0) & 0xffff0000u); o.r1[r] = asf((rr[r] << s1) & 0xffff0000u);
    o.i0[r] = asf(((ii[r] << s0) & 0xffff0000u) ^ sg); o.i1[r] = asf(((ii[r] << s1) & 0xffff0000u) ^ sg);
  }
  const bool av = (lane & 31) == 0;
  const float xr = bflo(lo ? t.rm0 : t.r256), xi = asf(((lo ? t.im0 : t.i256) << 16) ^ (sg ^ 0x80000000u));
  o.r0[0] = av ? 0.5f * (o.r0[0] + xr) : o.r0[0];
  o.i0[0] = av ? 0.5f * (o.i0[0] + xi) : o.i0[0];
  return o;
}

struct Geo {
  int tid, lane, w, b, i, rb, fc, hh, blk, q4, p4;
  int jb, dt, rh, q, j;
  v4u sx, sws, sw2;
  unsigned vw20, vw21, ldsw, fq0, fq1;
};
DI void w2_dma(const v4u& sw2, unsigned vw20, unsigned vw21, unsigned ldsw, unsigned fq0, unsigned fq1, int t, int u) {
  dma16(sw2, u ? vw21 : vw20, ((t < 8 ? fq0 : fq1) + 32u * (unsigned)t) * 1024u, (unsigned)(t & 7) * 16384u + ldsw + (unsigned)u * 1024u);
}
DI void w2_issue1(const Geo& g, int t, int u) { w2_dma(g.sw2, g.vw20, g.vw21, g.ldsw, g.fq0, g.fq1, t, u); }
DI void gen_g(const Geo& g, v4u (&gf)[4]) {
  const int srow = g.i + 8 * g.j;
  const int mb = 32 * g.q + 2 * g.hh;
  const float r1 = (float)(srow & 2047) * (1.f / 2048.f), r4 = (float)((4 * srow) & 2047) * (1.f / 2048.f), r0 = (float)((mb * srow) & 2047) * (1.f / 2048.f);
  const float c1 = cosrev(r1), s1 = sinrev(r1), c4 = cosrev(r4), s4 = sinrev(r4);
  float c = cosrev(r0), s = sinrev(r0);
#pragma unroll
  for (int e = 0; e < 8; ++e) {
    const float cb = __builtin_fmaf(c, c1, -(s * s1)), sb = __builtin_fmaf(s, c1, c * s1);
    unsigned w0 = pk2(c, -s), w1 = pk2(cb, -sb);
    if (e == 0 && g.q == 0 && g.hh == 0) w0 = (g.j & 1) ? 0x3F800000u : 0x00003F80u;
    if (e & 1) { gf[e >> 1].z = w0; gf[e >> 1].w = w1; } else { gf[e >> 1].x = w0; gf[e >> 1].y = w1; }
    const float cn = __builtin_fmaf(c, c4, -(s * s4)), sn = __builtin_fmaf(s, c4, c * s4);
    c = cn; s = sn;
  }
  asm volatile("" : "+v"(gf[0]), "+v"(gf[1]), "+v"(gf[2]), "+v"(gf[3]));
}
template <int N0, int N1, int NR> DI void bar_w(int w) { if (w == 0) bar_vm<N0>(); else if (w == 1) bar_vm<N1>(); else bar_vm<NR>(); }
#define NOP do {} while (0)

template <bool ISA>
DI unsigned phase1(const Params& p, LAS char* L, const Geo& G, v4u (&gf)[4]
) {
  const int tid = G.tid, lane = G.lane, w = G.w, i = G.i, rb = G.rb, fc = G.fc, hh = G.hh, blk = G.blk, q4 = G.q4, p4 = G.p4;
  const v4u sx = G.sx, sws = G.sws, sw1 = make_srd(p.w1, ND * NF * 2);
  unsigned ep; v4u gx, gv;
  const int jg = 128 * rb + 8 * fc + w;
  LD16(gx, (unsigned)lane * 16u, sx, (unsigned)(i + 8 * jg) * 1024u, "");
  LD16(gv, (unsigned)lane * 16u, make_srd(p.gw, ND * 2), 0u, "");
  LD4(ep, 0u, sws, WS_EPOCH, "sc1");
  const int f0 = ISA ? 96 * fc : 768 + 128 * (fc - 8), nf = ISA ? 96 : 128;
  const int a_t = rb * 8 + fc, mt = a_t >> 2, dt4 = a_t & 3;
  const int m1 = ((rb * 8 + (fc - 8)) << 3) | i;
  if (ISA) {
    const unsigned voff = (unsigned)(lane >> 4) * 8192u + (unsigned)(((lane & 15) ^ ((lane >> 4) << 2)) << 4);
#pragma unroll
    for (int u = 0; u < 8; ++u) {
      const unsigned row = 32 * w + 4 * u;
      dma16(sx, voff, (unsigned)i * 1024u + (unsigned)dt4 * 256u + row * 8192u, A_XIMG + row * 256u);
    }
  }
  unsigned vx0, vx1, vw0, vw1;
  {
    const int r0 = 16 * w + (lane >> 3), pos = lane & 7, r1 = r0 + 8;
    vx0 = (unsigned)(i * 1024 + (128 * rb + r0) * 8192 + ((pos ^ ((r0 >> 1) & 7)) << 4));
    vx1 = (unsigned)(i * 1024 + (128 * rb + r1) * 8192 + ((pos ^ ((r1 >> 1) & 7)) << 4));
    const int k0 = 8 * w + (lane >> 4), chunk = (lane & 15) ^ ((lane >> 4) << 2);
    const bool ok = chunk * 8 < nf;
    vw0 = ok ? (unsigned)(k0 * 3584 + f0 * 2 + chunk * 16) : 0x80000000u;
    vw1 = ok ? vw0 + 4u * 3584u : 0x80000000u;
  }
  auto g1_dma = [&](int kt, int u) {
    const unsigned slot = (unsigned)(kt & 3) * 32768u + (unsigned)w * 2048u;
    if (u == 0) dma16(sx, vx0, (unsigned)kt * 128u, slot);
    if (u == 1) dma16(sx, vx1, (unsigned)kt * 128u, slot + 1024u);
    if (u == 2) dma16(sw1, vw0, (unsigned)kt * (64u * 3584u), slot + 16384u);
    if (u == 3) dma16(sw1, vw1, (unsigned)kt * (64u * 3584u), slot + 17408u);
  };
  auto g1_issue = [&](int kt) { g1_dma(kt, 0); g1_dma(kt, 1); g1_dma(kt, 2); g1_dma(kt, 3); };
  if (ISA) {
    phaseA_genF(L, i, mt, w, lane);
    g1_issue(0); g1_issue(1);
  } else {
    g1_issue(0); asm volatile("s_barrier" ::: "memory");
    g1_issue(1); asm volatile("s_barrier" ::: "memory");
    g1_issue(2); asm volatile("s_barrier" ::: "memory");
    g1_issue(3);
  }
  asm volatile("s_waitcnt vmcnt(16)" : "+v"(ep), "+v"(gx), "+v"(gv) :: "memory");
  const unsigned tag = (unsigned)__builtin_amdgcn_readfirstlane((int)ep) + 1u;
  float gate_g, gate_og;
  {
    float z = 0.f;
#pragma unroll
    for (int q = 0; q < 4; ++q) {
      const unsigned pr = pk2(bflo(gx[q]) * bflo(gv[q]), bfhi(gx[q]) * bfhi(gv[q]));
      z += bflo(pr) + bfhi(pr);
    }
    z = dpp_add<0x111, 0xf>(z); z = dpp_add<0x112, 0xf>(z); z = dpp_add<0x114, 0xf>(z); z = dpp_add<0x118, 0xf>(z);
    z = dpp_add<0x142, 0xa>(z); z = dpp_add<0x143, 0xc>(z);
    const float zb = bflo(pk2(z, 0.f));
    const float g = bflo(pk2(__builtin_amdgcn_rcpf(1.f + __builtin_amdgcn_exp2f(zb * -1.44269504088896341f)), 0.f));
    gate_g = asf((unsigned)__builtin_amdgcn_readlane((int)__builtin_bit_cast(unsigned, g), 63));
    gate_og = bflo(pk2(1.f - gate_g, 0.f));
  }
  if (ISA) {
    const int dq = w & 3, mh = w >> 2;
    LAS char* xa = L + A_XIMG + (32 * hh + q4) * 256 + (((4 * dq + 2 * blk + (p4 >> 1)) ^ (q4 << 2)) << 4) + 8 * (p4 & 1);
    LAS char* fb = L + A_FIMG + mh * 16384 + lane * 16;
    const int srow = 32 * mh + (lane & 31);
    LAS char* stw = L + A_FIMG + srow * 256 + 8 * hh;
    const int rrow = tid >> 3;
    LAS char* str = L + A_FIMG + rrow * 256;
    const unsigned tdst = WS_T + (unsigned)(((64 * mt + rrow) * 8 + i) * 1024 + dt4 * 256 + (tid & 7) * 16);
    if (w < 4) bar_vm<8>(); else BAR_LGKM();
    STAMP(1);
    f32x16 acc = {};
    v4u fa[2][4], fbv[2][4];
#pragma unroll
    for (int s = 0; s < 4; ++s) { fa[0][s] = tr_read2(xa + 8 * s * 256, xa + 8 * s * 256 + 1024); fbv[0][s] = lds_ld16(fb + s * 1024); }
#pragma unroll
    for (int g = 0; g < 4; ++g) {
      acc = mfma32(fa[g & 1][0], fbv[g & 1][0], acc);
      SCHED();
      if (g == 1) { if (w >= 4) bar_vm<8>(); else BAR_LGKM(); }
      if (g < 3) {
#pragma unroll
        for (int s = 0; s < 4; ++s) {
          const int ro = (64 * (g + 1) + 8 * s) * 256;
          fa[(g + 1) & 1][s] = tr_read2(xa + ro, xa + ro + 1024); fbv[(g + 1) & 1][s] = lds_ld16(fb + (4 * (g + 1) + s) * 1024);
        }
      }
      SCHED();
#pragma unroll
      for (int s = 1; s < 4; ++s) { acc = mfma32(fa[g & 1][s], fbv[g & 1][s], acc); if (g == 1 && s < 3) { g1_dma(2, 2 * s - 2); g1_dma(2, 2 * s - 1); } SCHED(); }
    }
    BAR_LGKM();
    STAMP(2);
    g1_issue(3);
#pragma unroll
    for (int Gq = 0; Gq < 4; ++Gq) {
      v2u o; o.x = pk2(acc[4 * Gq], acc[4 * Gq + 1]); o.y = pk2(acc[4 * Gq + 2], acc[4 * Gq + 3]);
      *(LAS v2u*)(stw + (((4 * dq + Gq) ^ (srow & 15)) << 4)) = o;
    }
    BAR_LGKM();
    {
      const v4u v0 = lds_ld16(str + (((tid & 7) ^ (rrow & 15)) << 4)), v1 = lds_ld16(str + ((((tid & 7) + 8) ^ (rrow & 15)) << 4));
      st16_sc1(sws, tdst, v0);
      st16_sc1(sws, tdst + 128u, v1);
    }
    SCHED();
  }
  const int ft = w & 3, rh1 = w >> 2;
  const bool active = ft * 32 < nf;
  f32x16 acc1[2] = {};
  v4u wqr, wqi, tq0, tq1;
  unsigned wtr, wti;
  WVal Wc;
  v4u tb[16];
  unsigned fla;
  int t_ok = 0, t_first = 0; (void)t_first;
  const v4u swr = make_srd(p.wr, NS * 257 * 2), swi = make_srd(p.wi, NS * 257 * 2);
  const int mW = m1 == 0 ? 128 * w : m1 + 256 * w;
  unsigned tsrc0 = 0, tsrc1 = 0;
  const unsigned flaoff = WS_FLA + (unsigned)(((m1 >> 5) & 3) * 32 + (lane & 31)) * FS;
  LAS char* CTL = L + 128 * 1024;
  {
    LAS char* wa = L + 16384 + (8 * hh + q4) * 256 + (((4 * ft + 2 * blk + (p4 >> 1)) ^ (q4 << 2)) << 4) + 8 * (p4 & 1);
    const int r = 64 * rh1 + (lane & 31), fz = (r >> 1) & 7;
    LAS char* xb0 = L + r * 128 + (((0 + hh) ^ fz) << 4);
    LAS char* xb1 = L + r * 128 + (((2 + hh) ^ fz) << 4);
    LAS char* xb2 = L + r * 128 + (((4 + hh) ^ fz) << 4);
    LAS char* xb3 = L + r * 128 + (((6 + hh) ^ fz) << 4);
    Frag1 fr[2];
    bar_vm<ISA ? 14 : 12>();
    STAMP(3);
    if (lane == 0) { v2u gv2; gv2.x = __builtin_bit_cast(unsigned, gate_g); gv2.y = __builtin_bit_cast(unsigned, gate_og); *(LAS v2u*)(CTL + 64 + w * 8) = gv2; }
    g1_read(fr[0], wa, xb0, xb1, xb2, xb3, 0);
#define PIN1(c) asm volatile("" : "+v"(c.a[0]), "+v"(c.a[1]), "+v"(c.a[2]), "+v"(c.a[3]), "+v"(c.b0[0]), "+v"(c.b0[1]), "+v"(c.b0[2]), "+v"(c.b0[3]), \
                             "+v"(c.b1[0]), "+v"(c.b1[1]), "+v"(c.b1[2]), "+v"(c.b1[3]))
#define G1_STEP(kt, X0, X1, X2, X3, X4, X5, X6, X7)                                                                   \
    {                                                                                                                   \
      FT(10 + (kt));                                                                                                    \
      Frag1& c = fr[(kt) & 1];                                                                                          \
      Frag1& n = fr[((kt) + 1) & 1];                                                                                    \
      const int so = (((kt) + 1) & 3) * 32768;                                                                          \
      PIN1(c);                                                                                                          \
      SCHED();                                                                                                          \
      acc1[0] = mfma32(c.a[0], c.b0[0], acc1[0]); if ((kt) < 7) n.a[0] = tr_read2(wa + so, wa + so + 1024); X0; SCHED(); \
      acc1[1] = mfma32(c.a[0], c.b1[0], acc1[1]); if ((kt) < 7) { n.b0[0] = lds_ld16(xb0 + so); n.b1[0] = lds_ld16(xb0 + so + 4096); } X1; SCHED(); \
      acc1[0] = mfma32(c.a[1], c.b0[1], acc1[0]); if ((kt) < 7) n.a[1] = tr_read2(wa + so + 4096, wa + so + 5120); X2; SCHED(); \
      acc1[1] = mfma32(c.a[1], c.b1[1], acc1[1]); if ((kt) < 7) { n.b0[1] = lds_ld16(xb1 + so); n.b1[1] = lds_ld16(xb1 + so + 4096); } X3; SCHED(); \
      acc1[0] = mfma32(c.a[2], c.b0[2], acc1[0]); if ((kt) < 7) n.a[2] = tr_read2(wa + so + 8192, wa + so + 9216); X4; SCHED(); \
      acc1[1] = mfma32(c.a[2], c.b1[2], acc1[1]); if ((kt) < 7) { n.b0[2] = lds_ld16(xb2 + so); n.b1[2] = lds_ld16(xb2 + so + 4096); } X5; SCHED(); \
      acc1[0] = mfma32(c.a[3], c.b0[3], acc1[0]); if ((kt) < 7) n.a[3] = tr_read2(wa + so + 12288, wa + so + 13312); X6; SCHED(); \
      acc1[1] = mfma32(c.a[3], c.b1[3], acc1[1]); if ((kt) < 7) { n.b0[3] = lds_ld16(xb3 + so); n.b1[3] = lds_ld16(xb3 + so + 4096); } X7; SCHED(); \
    }
#define GATE_STORE() do { if (w == 2 && lane < 8) { const v2u gv2 = *(LAS v2u*)(CTL + 64 + lane * 8);                       \
      __builtin_amdgcn_raw_buffer_store_b64(gv2, as_rsrc(sws), WS_GATE + (unsigned)(i * 256 + 128 * rb + 8 * fc + lane) * 8u, 0, 16); } } while (0)
#define S4(k) g1_dma(k, 0), g1_dma(k, 1), g1_dma(k, 2), g1_dma(k, 3)
#define W4(t) w2_issue1(G, t, 0), w2_issue1(G, t, 1), w2_issue1(G, (t) + 1, 0), w2_issue1(G, (t) + 1, 1)
#define STEP8(kt, ...) G1_STEP(kt, __VA_ARGS__)
    if constexpr (ISA) {
      bar_vm<10>();
      STAMP(4);
      STEP8(0, NOP, NOP, NOP, NOP, NOP, NOP, NOP, NOP)
      bar_vm<0>();
      if (tid == 0) flag_store(p.ws, WS_FLA + (unsigned)(mt * 32 + i * 4 + dt4) * FS, tag);
      STAMP(13);
      STEP8(1, g1_dma(4, 0), g1_dma(4, 1), g1_dma(4, 2), g1_dma(4, 3), g1_dma(5, 0), g1_dma(5, 1), g1_dma(5, 2), g1_dma(5, 3))
      BAR_LGKM();
      GATE_STORE();
      STEP8(2, NOP, NOP, NOP, NOP, g1_dma(6, 0), g1_dma(6, 1), g1_dma(6, 2), g1_dma(6, 3))
      bar_vm<8>();
      STEP8(3, NOP, NOP, NOP, NOP, g1_dma(7, 0), g1_dma(7, 1), g1_dma(7, 2), g1_dma(7, 3))
      bar_vm<8>();
      STAMP(12);
      STEP8(4, NOP, NOP, NOP, NOP, w2_issue1(G, 0, 0), w2_issue1(G, 0, 1), w2_issue1(G, 1, 0), w2_issue1(G, 1, 1))
      bar_vm<8>();
      STEP8(5, NOP, NOP, NOP, NOP, w2_issue1(G, 2, 0), w2_issue1(G, 2, 1), w2_issue1(G, 3, 0), w2_issue1(G, 3, 1))
      bar_vm<8>();
      STEP8(6, NOP, NOP, NOP, NOP, w2_issue1(G, 4, 0), w2_issue1(G, 4, 1), w2_issue1(G, 5, 0), w2_issue1(G, 5, 1))
      BAR_LGKM();
    } else {
      const v4u stab = make_srd(&kTab, sizeof(Tab));
      const unsigned tabl = 128u * 1024u + 16384u + (unsigned)w * 2048u;
      const unsigned wsrc = w_src(mW, lane);
      {
        const int kap = tid >> 5, ii = (kap >> 1) & 7, d0 = kap & 1;
        const int g0 = m1 == 0 ? 2 * ii : ii, g1 = m1 == 0 ? 2 * ii + 1 : 8 + ii;
        const unsigned tb = WS_T + (unsigned)m1 * 16384u + (unsigned)(d0 * 512 + (tid & 31) * 16);
        tsrc0 = tb + (unsigned)g0 * 1024u; tsrc1 = tb + (unsigned)g1 * 1024u;
      }
      const unsigned vw20z = G.vw20 - 16u * 1024u, vw21z = G.vw21 - 16u * 1024u, ldswz = G.ldsw - 2048u;
#define W2B(t, u) do { if (w != 0) w2_issue1(G, t, u); } while (0)
#define W2Z(t, u) do { if (w == 1) w2_dma(G.sw2, vw20z, vw21z, ldswz, G.fq0, G.fq1, t, u); } while (0)
      bar_vm<8>();
      STAMP(4);
      const unsigned wts = w_tail_src(mW, lane);
      STEP8(0, LD16(wqr, wsrc, swr, 0u, ""), LD16(wqi, wsrc, swi, 0u, ""), LD2(wtr, wts, swr, 0u, ""), LD2(wti, wts, swi, 0u, ""), g1_dma(4, 0), g1_dma(4, 1), g1_dma(4, 2), g1_dma(4, 3))
      bar_vm<12>();
      STEP8(1, NOP, NOP, NOP, NOP, g1_dma(5, 0), g1_dma(5, 1), g1_dma(5, 2), g1_dma(5, 3))
      bar_vm<12>();
      GATE_STORE();
      STEP8(2, NOP, NOP, NOP, NOP, g1_dma(6, 0), g1_dma(6, 1), g1_dma(6, 2), g1_dma(6, 3))
      bar_vm<8>();
      STEP8(3, NOP, NOP, dma16(stab, (unsigned)lane * 16u, (unsigned)w * 2048u, tabl), dma16(stab, (unsigned)lane * 16u, (unsigned)w * 2048u + 1024u, tabl + 1024u),
            g1_dma(7, 0), g1_dma(7, 1), g1_dma(7, 2), g1_dma(7, 3))
      bar_vm<10>();
      STAMP(12);
      LAS char* wscr0 = L + 128 * 1024 + w * 2048;
      const int woff = w_off(mW, lane);
      asm volatile("" : "+v"(wqr), "+v"(wqi), "+v"(wtr), "+v"(wti));
      w_put(wscr0, wqr, wqi, lane);
      STEP8(4, W2Z(0, 0), W2Z(0, 1), W2Z(1, 0), W2Z(1, 1), W2B(0, 0), W2B(0, 1), W2B(1, 0), W2B(1, 1))
      bar_w<6, 14, 10>(w);
      WRawL wraw;
      w_get(wraw, wscr0, woff, lane, 0); w_get(wraw, wscr0, woff, lane, 1); w_get(wraw, wscr0, woff, lane, 2); w_get(wraw, wscr0, woff, lane, 3);
      STEP8(5, W2Z(2, 0), W2Z(2, 1), W2Z(3, 0), W2Z(3, 1), W2B(2, 0), W2B(2, 1), W2B(3, 0), W2B(3, 1))
      bar_w<0, 16, 8>(w);
      Wc = w_unpack(wraw, w_tail(wtr, wti), woff, mW, lane);
      STEP8(6, if (w == 0) LD4(fla, flaoff, sws, 0u, "sc1"), NOP, NOP, NOP, NOP, NOP, NOP, NOP)
      if (w == 0) {
        asm volatile("s_waitcnt vmcnt(0)" : "+v"(fla) :: "memory");
        const unsigned ok = __all(fla == tag) ? 1u : 0u;
        if (lane == 0) *(LAS unsigned*)CTL = ok;
      }
      BAR_LGKM();
      t_ok = __builtin_amdgcn_readfirstlane(*(volatile LAS unsigned*)CTL);
      t_first = t_ok;
    }
    if constexpr (ISA) {
      STEP8(7, NOP, NOP, NOP, NOP, w2_issue1(G, 6, 0), w2_issue1(G, 6, 1), w2_issue1(G, 7, 0), w2_issue1(G, 7, 1))
    } else {
      LAS char* TB = L + 128 * 1024 + 16384 + lane * 16;
#define TBR(f) (tb[f] = lds_ld16(TB + (f) * 1024), tb[(f) + 1] = lds_ld16(TB + ((f) + 1) * 1024))
      STEP8(7, { if (t_ok) LD16(tq0, tsrc0, sws, 0u, "sc1"); TBR(6); }, { if (t_ok) LD16(tq1, tsrc1, sws, 0u, "sc1"); TBR(0); },
            { if (!t_ok && w == 0) LD4(fla, flaoff, sws, 0u, "sc1"); TBR(10); }, TBR(2), TBR(12), TBR(14), TBR(4), TBR(8))
      if (!t_ok) {
        if (w == 0) {
          asm volatile("s_waitcnt vmcnt(0)" : "+v"(fla) :: "memory");
          const unsigned ok = __all(fla == tag) ? 1u : 0u;
          if (lane == 0) *(LAS unsigned*)CTL = ok;
        }
        BAR_LGKM();
        t_ok = __builtin_amdgcn_readfirstlane(*(volatile LAS unsigned*)CTL);
        if (t_ok) { LD16(tq0, tsrc0, sws, 0u, "sc1"); LD16(tq1, tsrc1, sws, 0u, "sc1"); }
      }
    }
  }
  STAMP(5);
  FT(18);
  if (active) {
#pragma unroll
    for (int rt = 0; rt < 2; ++rt)
#pragma unroll
      for (int u = 0; u < 2; ++u) {
        v4u o;
        o.x = silu2(acc1[rt][8 * u + 0], acc1[rt][8 * u + 1]);
        o.y = silu2(acc1[rt][8 * u + 2], acc1[rt][8 * u + 3]);
        o.z = silu2(acc1[rt][8 * u + 4], acc1[rt][8 * u + 5]);
        o.w = silu2(acc1[rt][8 * u + 6], acc1[rt][8 * u + 7]);
        const int jb32 = 4 * rb + 2 * rh1 + rt, fs = (f0 >> 4) + 2 * ft + u;
        st16_sc1(sws, WS_H + (unsigned)(((i * 8 + jb32) * 112 + fs) * 1024 + lane * 16), o);
      }
  }
  SCHED();
  STAMP(6);
  FT(20);
  const unsigned flh_own = WS_FLH + (unsigned)((i * 2 + rb) * 16 + fc) * FS;
  if constexpr (ISA) {
    gen_g(G, gf);
    bar_vm<0>();
    if (tid == 0) flag_store(p.ws, flh_own, tag);
  } else {
    LAS char* I0 = L + 128 * 1024;
    LAS char* I1 = L + 128 * 1024 + 16384;
    LAS char* wscr = L + 7 * 16384 + w * 2048;
    LAS unsigned* cnt = (LAS unsigned*)(L + 7 * 16384);
    const int g = lane >> 4, l15 = lane & 15;
    const bool special = (m1 == 0);
    bool published = false;
    if (t_ok) {
      wait_vm<4>();
      asm volatile("s_barrier" ::: "memory");
    } else {
      bar_vm<0>();
      if (tid == 0) flag_store(p.ws, flh_own, tag);
      published = true;
      if (w == 0) poll_words(sws, WS_FLA + (unsigned)(m1 >> 5) * 32u * FS, 32, tag, lane);
      asm volatile("s_barrier" ::: "memory");
      LD16(tq0, tsrc0, sws, 0u, "sc1"); LD16(tq1, tsrc1, sws, 0u, "sc1");
      wait_vm<0>();
    }
    asm volatile("" : "+v"(tq0), "+v"(tq1) :: "memory");
    {
      const int kap = tid >> 5, ch = tid & 31;
      *(LAS v4u*)(I0 + kap * 512 + ((ch ^ ((kap & 3) << 2)) << 4)) = tq0;
      *(LAS v4u*)(I0 + (kap + 16) * 512 + ((ch ^ ((kap & 3) << 2)) << 4)) = tq1;
    }
    WVal Wd;
    if (special) {
      const int m2nd = 128 * w + 1024;
      const unsigned ws2 = w_src(m2nd, lane), wt2 = w_tail_src(m2nd, lane);
      LD16(wqr, ws2, swr, 0u, ""); LD16(wqi, ws2, swi, 0u, ""); LD2(wtr, wt2, swr, 0u, ""); LD2(wti, wt2, swi, 0u, "");
      asm volatile("s_waitcnt vmcnt(0)" : "+v"(wqr), "+v"(wqi), "+v"(wtr), "+v"(wti) :: "memory");
      w_put(wscr, wqr, wqi, lane);
      asm volatile("s_waitcnt lgkmcnt(0)" ::: "memory");
      WRawL raw2; const int off2 = w_off(m2nd, lane);
      w_get(raw2, wscr, off2, lane, 0); w_get(raw2, wscr, off2, lane, 1); w_get(raw2, wscr, off2, lane, 2); w_get(raw2, wscr, off2, lane, 3);
      Wd = w_unpack(raw2, w_tail(wtr, wti), off2, m2nd, lane);
    }
    BAR_LGKM();
    FT(21);
    if (tid == 0) *cnt = 0u;
    LAS char* ta = I0 + (8 * hh + q4) * 512 + (((4 * w + 2 * blk + (p4 >> 1)) ^ (q4 << 2)) << 4) + 8 * (p4 & 1);
    const int n = lane & 31;
    LAS char* so = I1 + n * 512 + 8 * hh;
    {
      f32x16 acc = {};
      acc = mfma32(tr_read2(ta, ta + 2048), tb[6], acc);
      acc = mfma32(tr_read2(ta + 8192, ta + 10240), tb[7], acc);
#pragma unroll
      for (int Gq = 0; Gq < 4; ++Gq) {
        v2u o; o.x = pk2(acc[4 * Gq], acc[4 * Gq + 1]); o.y = pk2(acc[4 * Gq + 2], acc[4 * Gq + 3]);
        *(LAS v2u*)(so + (((4 * w + Gq) ^ (n & 15)) << 4)) = o;
      }
    }
    BAR_LGKM();
    FT(22);
    const f32x4 z4 = {0.f, 0.f, 0.f, 0.f};
    const v4u F16fr = tb[0], F16fi = tb[1];
    v4u F16ir, F16ii;
    F16ir.x = F16fr.x; F16ir.y = F16fr.y; F16ir.z = F16fr.z ^ 0x80008000u; F16ir.w = F16fr.w ^ 0x80008000u;
    F16ii.x = F16fi.x ^ 0x80008000u; F16ii.y = F16fi.y ^ 0x80008000u; F16ii.z = F16fi.z; F16ii.w = F16fi.w;
    f32x4 zr[2], zi[2];
#pragma unroll
    for (int pp = 0; pp < 2; ++pp) {
      const int n0 = 4 * w + 2 * pp, n1 = n0 + 1;
      const int ch = 2 * (4 * g + q4) + (p4 >> 1);
      const v4u a1 = tr_read2(I1 + n0 * 512 + ((ch ^ (n0 & 15)) << 4) + 8 * (p4 & 1), I1 + n1 * 512 + ((ch ^ (n1 & 15)) << 4) + 8 * (p4 & 1));
      const f32x4 dr = mfma16(a1, pp ? tb[2] : F16fr, z4), di = mfma16(a1, pp ? tb[3] : F16fi, z4);
      const v4u b3 = cmul_pack<false>(dr, di, asf4(tb[10 + 2 * pp]), asf4(tb[11 + 2 * pp]));
      zr[pp] = mfma16(F16fr, b3, z4); zi[pp] = mfma16(F16fi, b3, z4);
    }
    v4u a4[2];
    if (!special) {
      a4[0] = cmul_pack<false>(zr[0], zi[0], Wc.r0, Wc.i0);
      a4[1] = cmul_pack<false>(zr[1], zi[1], Wc.r1, Wc.i1);
    } else {
      BAR_LGKM();
      LAS char* Z = I0;
#pragma unroll
      for (int pp = 0; pp < 2; ++pp)
#pragma unroll
        for (int r = 0; r < 4; ++r) { f32x2 v = {zr[pp][r], zi[pp][r]}; *(LAS f32x2*)(Z + ((w * 2 + pp) * 256 + l15 + 16 * (4 * g + r)) * 8) = v; }
      BAR_LGKM();
      const cf om = {cosrev((float)w * 0.0625f), -sinrev((float)w * 0.0625f)};
#pragma unroll
      for (int pp = 0; pp < 2; ++pp) {
        float yr[4], yi[4];
#pragma unroll
        for (int r = 0; r < 4; ++r) {
          const int K = l15 + 16 * (4 * g + r), Kn = pp ? 255 - K : (256 - K) & 255;
          const f32x2 y = *(LAS f32x2*)(Z + ((((8 - w) & 7) * 2 + pp) * 256 + Kn) * 8);
          const float zre = zr[pp][r], zim = zi[pp][r], cr = y[0], ci = -y[1];
          const cf E = {0.5f * (zre + cr), 0.5f * (zim + ci)};
          const cf O = {0.5f * (zim - ci), -0.5f * (zre - cr)};
          const float war = pp ? Wc.r1[r] : Wc.r0[r], wai = pp ? Wc.i1[r] : Wc.i0[r];
          const float wbr = pp ? Wd.r1[r] : Wd.r0[r], wbi = pp ? Wd.i1[r] : Wd.i0[r];
          const cf sp = {war + wbr, wai + wbi}, sm = {war - wbr, wai - wbi};
          const cf t1 = cmulc(sm, om);
          const cf cE = {sp.r - t1.i, sp.i + t1.r};
          const cf t2 = cmul(sm, om);
          const cf cO = {t2.r - sp.i, t2.i + sp.r};
          const cf a = cmul(E, cE), bq = cmul(O, cO);
          yr[r] = 0.5f * (a.r + bq.r); yi[r] = 0.5f * (a.i + bq.i);
        }
        a4[pp].x = pk2(yr[0], yr[1]); a4[pp].y = pk2(yr[2], yr[3]); a4[pp].z = pk2(yi[0], yi[1]); a4[pp].w = pk2(yi[2], yi[3]);
      }
      BAR_LGKM();
    }
    if (!published) {
      wait_vm<0>();
      unsigned old = 0;
      if (lane == 0) old = __hip_atomic_fetch_add(cnt, 1u, __ATOMIC_RELAXED, __HIP_MEMORY_SCOPE_WORKGROUP);
      if (__builtin_amdgcn_readfirstlane(old) == 7 && lane == 0) flag_store(p.ws, flh_own, tag);
    }
    w2_issue1(G, 4, 0); w2_issue1(G, 4, 1); w2_issue1(G, 5, 0); w2_issue1(G, 5, 1); w2_issue1(G, 6, 0); w2_issue1(G, 6, 1);
#pragma unroll
    for (int pp = 0; pp < 2; ++pp) {
      const f32x4 dr = mfma16(a4[pp], F16ir, z4), di = mfma16(a4[pp], F16ii, z4);
      const v4u a5 = cmul_pack<true>(dr, di, asf4(tb[pp ? 14 : 10]), asf4(tb[pp ? 15 : 11]));
      const f32x4 er = mfma16(a5, pp ? tb[4] : F16ir, z4), ei = mfma16(a5, pp ? tb[5] : F16ii, z4);
      const int n0 = 4 * w + 2 * pp, n1 = n0 + 1;
      const int ch = 2 * l15 + (g >> 1);
      v2u o; o.x = pk2(er[0], er[1]); o.y = pk2(er[2], er[3]);
      *(LAS v2u*)(I0 + n0 * 512 + ((ch ^ ((n0 & 3) << 2)) << 4) + 8 * (g & 1)) = o;
      o.x = pk2(ei[0], ei[1]); o.y = pk2(ei[2], ei[3]);
      *(LAS v2u*)(I0 + n1 * 512 + ((ch ^ ((n1 & 3) << 2)) << 4) + 8 * (g & 1)) = o;
    }
    BAR_LGKM();
    FT(23);
    w2_issue1(G, 7, 0); w2_issue1(G, 7, 1);
    {
      f32x16 acc = {};
      acc = mfma32(tr_read2(ta, ta + 2048), tb[8], acc);
      acc = mfma32(tr_read2(ta + 8192, ta + 10240), tb[9], acc);
#pragma unroll
      for (int Gq = 0; Gq < 4; ++Gq) {
        v2u o; o.x = pk2(acc[4 * Gq], acc[4 * Gq + 1]); o.y = pk2(acc[4 * Gq + 2], acc[4 * Gq + 3]);
        *(LAS v2u*)(so + (((4 * w + Gq) ^ (n & 15)) << 4)) = o;
      }
    }
    BAR_LGKM();
    {
      const int nn = tid >> 5, ch = tid & 31, ii = (nn >> 1) & 7, d0 = nn & 1;
      const int g0 = special ? 2 * ii : ii, g1 = special ? 2 * ii + 1 : 8 + ii;
      const v4u v0 = lds_ld16(I1 + nn * 512 + ((ch ^ (nn & 15)) << 4));
      const v4u v1 = lds_ld16(I1 + (nn + 16) * 512 + ((ch ^ (nn & 15)) << 4));
      const unsigned ub = WS_U + (unsigned)m1 * 16384u + (unsigned)(d0 * 512 + ch * 16);
      st16_sc1(sws, ub + (unsigned)g0 * 1024u, v0);
      st16_sc1(sws, ub + (unsigned)g1 * 1024u, v1);
    }
    SCHED();
    FT(24);
  }
  return tag;
}

__global__ __launch_bounds__(512, 2) void gm_kernel(Params p) {
  extern __shared__ __attribute__((aligned(16))) char smem[];
  LAS char* L = (LAS char*)smem;
  Geo G;
  G.tid = threadIdx.x; G.lane = G.tid & 63; G.w = __builtin_amdgcn_readfirstlane(G.tid >> 6);
  G.b = blockIdx.x; G.i = G.b & 7;
  const int Lx = G.b >> 3;
  G.rb = Lx >> 4; G.fc = Lx & 15;
  const bool isA = G.fc < 8;
  G.hh = G.lane >> 5; G.blk = (G.lane >> 4) & 1; G.q4 = (G.lane & 15) >> 2; G.p4 = G.lane & 3;
  STAMP(0);
  FT(0);
  G.sx = make_srd(p.x, NS * ND * 2); G.sws = make_srd(p.ws, 16u << 20); G.sw2 = make_srd(p.w2, NF * ND * 2);
  G.jb = Lx >> 3; G.dt = Lx & 7; G.rh = G.w & 1; G.q = G.w >> 1;
  G.j = 64 * G.jb + 32 * G.rh + (G.lane & 31);
  const int rho = 16 * G.rh + (G.lane >> 3), chs = (G.lane & 7) ^ (4 * ((G.lane >> 4) & 1));
  G.vw20 = (unsigned)(rho * 1024 + G.dt * 128 + chs * 16); G.vw21 = G.vw20 + 8 * 1024;
  G.ldsw = (unsigned)(G.q * 4096 + G.rh * 2048);
  G.fq0 = (unsigned)(768 + 256 * G.q); G.fq1 = (unsigned)(192 * G.q - 256);
  v4u gf[4];
  unsigned tag;
  if (isA) tag = phase1<true>(p, L, G, gf); else tag = phase1<false>(p, L, G, gf);
  const int tid = G.tid, lane = G.lane, w = G.w, b = G.b, i = G.i, jb = G.jb, dt = G.dt, rh = G.rh, q = G.q, j = G.j;
  const int hh = G.hh, blk = G.blk, q4 = G.q4, p4 = G.p4;
  const v4u sws = G.sws;
  const unsigned ldsw = G.ldsw, fq0 = G.fq0, fq1 = G.fq1;
  const int m1 = ((G.rb * 8 + (G.fc - 8)) << 3) | i;
  STAMP(7);
  FT(25);
  const unsigned flh = WS_FLH + (unsigned)((i * 2 + (jb >> 1)) * 16) * FS;
  if (w == 0) poll_words(sws, flh + 8u * FS, 8, tag, lane);
  asm volatile("s_barrier" ::: "memory");
  STAMP(8);
  FT(26);
  const unsigned vh = (unsigned)lane * 16u;
  const unsigned hbase = WS_H + (unsigned)((i * 8 + 2 * jb + rh) * 112) * 1024u;
  const unsigned hb0 = hbase + (fq0 >> 4) * 1024u, hb1 = hbase + (unsigned)((int)fq1 >> 4) * 1024u;
  v2u gg;
  v4u hf[14][2];
#define H_ISSUE(t) do { LD16(hf[t][0], vh, sws, ((t) < 8 ? hb0 : hb1) + (unsigned)(2 * (t)) * 1024u, "sc1"); LD16(hf[t][1], vh, sws, ((t) < 8 ? hb0 : hb1) + (unsigned)(2 * (t) + 1) * 1024u, "sc1"); } while (0)
  H_ISSUE(0); H_ISSUE(1);
  asm volatile("s_barrier" ::: "memory");
  H_ISSUE(2); H_ISSUE(3); H_ISSUE(4); H_ISSUE(5); H_ISSUE(6);
  if (!isA) gen_g(G, gf);
  SCHED();
  f32x16 accb[2] = {}, acca[2] = {};
  const int sz = (q4 >> 1) & 1;
  LAS char* ra0 = L + q * 4096 + (4 * hh + q4) * 128 + (((0 ^ (4 * sz)) + 2 * blk + (p4 >> 1)) << 4) + 8 * (p4 & 1);
  LAS char* ra1 = L + q * 4096 + (4 * hh + q4) * 128 + (((4 ^ (4 * sz)) + 2 * blk + (p4 >> 1)) << 4) + 8 * (p4 & 1);
  v4u wf[2][4];
#define G2_BAR(n, t) asm volatile("s_waitcnt vmcnt(" STR(n) ") lgkmcnt(0)\n\ts_barrier" : "+v"(hf[t][0]), "+v"(hf[t][1]) :: "memory")
  G2_BAR(12, 0);
  if (!isA && tid == 0) flag_store(p.ws, WS_FLB + (unsigned)m1 * FS, tag);
  wf[0][0] = tr_read2(ra0, ra0 + 1024); wf[0][1] = tr_read2(ra1, ra1 + 1024);
  wf[0][2] = tr_read2(ra0 + 2048, ra0 + 3072); wf[0][3] = tr_read2(ra1 + 2048, ra1 + 3072);
  unsigned fl0, fl1, flb;
  v4u ub0, ub1, ub2, ub3;
  const unsigned vu = WS_U + (unsigned)(((64 * q + rho) * 8 + i) * 1024 + dt * 128 + chs * 16);
#define G2_MMA(t, acc, b0, b1, X0, X1, X2)                                                        \
  {                                                                                                 \
    FT(30 + (t));                                                                                   \
    v4u (&c)[4] = wf[(t) & 1];                                                                     \
    v4u (&n)[4] = wf[((t) + 1) & 1];                                                               \
    LAS char* s0 = ra0 + (((t) + 1) & 7) * 16384; LAS char* s1 = ra1 + (((t) + 1) & 7) * 16384;    \
    asm volatile("" : "+v"(c[0]), "+v"(c[1]), "+v"(c[2]), "+v"(c[3]));                             \
    SCHED();                                                                                        \
    acc[0] = mfma32(c[0], b0, acc[0]); if ((t) < 15) n[0] = tr_read2(s0, s0 + 1024); SCHED();      \
    acc[1] = mfma32(c[1], b0, acc[1]); if ((t) < 15) n[1] = tr_read2(s1, s1 + 1024); X0; SCHED();  \
    acc[0] = mfma32(c[2], b1, acc[0]); if ((t) < 15) n[2] = tr_read2(s0 + 2048, s0 + 3072); X1; SCHED(); \
    acc[1] = mfma32(c[3], b1, acc[1]); if ((t) < 15) n[3] = tr_read2(s1 + 2048, s1 + 3072); X2; SCHED(); \
  }
#define G2_STEP(t, X2) G2_MMA(t, accb, hf[t][0], hf[t][1], if ((t) <= 5) w2_issue1(G, (t) + 8, 0), if ((t) <= 5) w2_issue1(G, (t) + 8, 1), X2)
  BAR_LGKM();    G2_STEP(0, H_ISSUE(7))
  G2_BAR(14, 1); G2_STEP(1, NOP)
  G2_BAR(14, 2); G2_STEP(2, NOP)
  if (w == 0) LD4(flb, flh + (unsigned)(lane & 7) * FS, sws, 0u, "sc1");
  G2_BAR(14, 3); G2_STEP(3, NOP)
  if (w == 0) {
    asm volatile("s_waitcnt vmcnt(2)" : "+v"(flb) :: "memory");
    if (!__all(flb == tag)) poll_words(sws, flh, 8, tag, lane);
  }
  G2_BAR(14, 4); G2_STEP(4, { H_ISSUE(8); H_ISSUE(9); LD8(gg, WS_GATE + (unsigned)(i * 256 + j) * 8u, sws, 0u, "sc1"); })
  G2_BAR(19, 5); G2_STEP(5, { H_ISSUE(10); H_ISSUE(11); })
  if (w == 0) { LD4(fl0, WS_FLB + (unsigned)lane * FS, sws, 0u, "sc1"); LD4(fl1, WS_FLB + (64u + (unsigned)lane) * FS, sws, 0u, "sc1"); }
  G2_BAR(23, 6); G2_STEP(6, { H_ISSUE(12); H_ISSUE(13); })
  STAMP(9);
  G2_BAR(23, 7); G2_STEP(7, NOP)
  if (w == 0) {
    asm volatile("s_waitcnt vmcnt(4)" : "+v"(fl0), "+v"(fl1) :: "memory");
    if (!__all(fl0 == tag && fl1 == tag)) { poll_words(sws, WS_FLB, 64, tag, lane); poll_words(sws, WS_FLB + 64u * FS, 64, tag, lane); }
  }
  G2_BAR(13, 8);
  LD16(ub0, vu, sws, 0u, "sc1"); LD16(ub1, vu, sws, 8u * 8192u, "sc1"); LD16(ub2, vu, sws, 32u * 8192u, "sc1"); LD16(ub3, vu, sws, 40u * 8192u, "sc1");
  G2_STEP(8, NOP)
  unsigned e0, e1, e2, e3;
  if (b == 0 && w == 1) {
    LD4(e0, WS_FLH + (unsigned)lane * FS, sws, 0u, "sc1"); LD4(e1, WS_FLH + (64u + (unsigned)lane) * FS, sws, 0u, "sc1");
    LD4(e2, WS_FLH + (128u + (unsigned)lane) * FS, sws, 0u, "sc1"); LD4(e3, WS_FLH + (192u + (unsigned)lane) * FS, sws, 0u, "sc1");
  }
  G2_BAR(15, 9); G2_STEP(9, NOP)
  G2_BAR(10, 10); G2_STEP(10, NOP)
  G2_BAR(8, 11); G2_STEP(11, NOP)
  G2_BAR(6, 12);
  G2_STEP(12, NOP)
  asm volatile("s_waitcnt vmcnt(0)" : "+v"(ub0), "+v"(ub1), "+v"(ub2), "+v"(ub3), "+v"(gg) :: "memory");
  *(LAS v4u*)(L + 6 * 16384 + ldsw + lane * 16) = ub0;
  *(LAS v4u*)(L + 6 * 16384 + ldsw + 1024 + lane * 16) = ub1;
  *(LAS v4u*)(L + 7 * 16384 + ldsw + lane * 16) = ub2;
  *(LAS v4u*)(L + 7 * 16384 + ldsw + 1024 + lane * 16) = ub3;
  G2_BAR(0, 13);
  if (b == 0 && w == 1) {
    asm volatile("" : "+v"(e0), "+v"(e1), "+v"(e2), "+v"(e3));
    if (!__all(e0 == tag && e1 == tag && e2 == tag && e3 == tag)) {
#pragma unroll 1
      for (int u = 0; u < 4; ++u) poll_words(sws, WS_FLH + 64u * FS * u, 64, tag, lane);
    }
    if (lane == 0) flag_store(p.ws, WS_EPOCH, tag);
  }
  G2_STEP(13, NOP)
  STAMP(10);
  BAR_LGKM(); G2_MMA(14, acca, gf[0], gf[1], NOP, NOP, NOP)
  BAR_LGKM(); G2_MMA(15, acca, gf[2], gf[3], NOP, NOP, NOP)
  FT(50);
  {
    const float og = asf(gg.y), gs = asf(gg.x) * (1.f / 524288.f);
    const int row = 32 * rh + (lane & 31);
#pragma unroll
    for (int dh = 0; dh < 2; ++dh)
#pragma unroll
      for (int G = 0; G < 4; ++G) {
        f32x4 v;
#pragma unroll
        for (int e = 0; e < 4; ++e) v[e] = __builtin_fmaf(og, accb[dh][4 * G + e], gs * acca[dh][4 * G + e]);
        *(LAS f32x4*)(L + q * 16384 + row * 256 + (((8 * dh + 2 * G + hh) ^ (row & 7)) << 4)) = v;
      }
  }
  BAR_LGKM();
  FT(51);
  {
    const int row = tid >> 3, c8 = tid & 7;
    f32x4 s0 = {0.f, 0.f, 0.f, 0.f}, s1 = s0;
#pragma unroll
    for (int qq = 0; qq < 4; ++qq) {
      s0 += *(LAS f32x4*)(L + qq * 16384 + row * 256 + (((2 * c8) ^ (row & 7)) << 4));
      s1 += *(LAS f32x4*)(L + qq * 16384 + row * 256 + (((2 * c8 + 1) ^ (row & 7)) << 4));
    }
    v4u o; o.x = pk2(s0[0], s0[1]); o.y = pk2(s0[2], s0[3]); o.z = pk2(s1[0], s1[1]); o.w = pk2(s1[2], s1[3]);
    *(GAS v4u*)((GAS char*)p.out + (size_t)(i + 8 * (64 * jb + row)) * 1024 + dt * 128 + c8 * 16) = o;
  }
  STAMP(11);
}

}

extern "C" void kernel_launch(void* const* d_in, const int* in_sizes, int n_in, void* d_out, int out_size, void* d_ws, size_t ws_size,
                              hipStream_t stream) {
  Params p;
  p.x = d_in[0]; p.wr = d_in[1]; p.wi = d_in[2]; p.w1 = d_in[4]; p.w2 = d_in[5]; p.gw = d_in[6];
  p.out = d_out; p.ws = d_ws;
  (void)hipFuncSetAttribute((const void*)gm_kernel, hipFuncAttributeMaxDynamicSharedMemorySize, LDS_BYTES);
  gm_kernel<<<256, 512, LDS_BYTES, stream>>>(p);
}
```

```cpp
#include <hip/hip_runtime.h>
#include <hip/hip_bf16.h>
#include <stdint.h>

namespace {

typedef unsigned v4u __attribute__((ext_vector_type(4)));
typedef unsigned v2u __attribute__((ext_vector_type(2)));
typedef float f32x16 __attribute__((ext_vector_type(16)));
typedef float f32x4 __attribute__((ext_vector_type(4)));
typedef float f32x2 __attribute__((ext_vector_type(2)));
typedef __bf16 bf16x8 __attribute__((ext_vector_type(8)));
typedef __bf16 bf16x2 __attribute__((ext_vector_type(2)));
typedef short s16x4 __attribute__((ext_vector_type(4)));
#define LAS __attribute__((address_space(3)))
#define GAS __attribute__((address_space(1)))
#define DI __device__ __forceinline__

constexpr int NS = 2048, ND = 512, NF = 1792;
constexpr unsigned WS_EPOCH = 0;
constexpr unsigned FS = 64;
constexpr unsigned WS_FLA = 4096;
constexpr unsigned WS_FLH = 16384;
constexpr unsigned WS_FLB = 32768;
constexpr unsigned WS_GATE = 65536;
constexpr unsigned WS_T = 1u << 20;
constexpr unsigned WS_U = 3u << 20;
constexpr unsigned WS_H = 5u << 20;
constexpr unsigned H_BYTES = 8u * 8u * 112u * 1024u;
constexpr int LDS_BYTES = 160 * 1024;
constexpr unsigned kSpinCap = 1u << 20;

struct Params {
  const void* x; const void* wr; const void* wi; const void* w1; const void* w2; const void* gw;
  void* out; void* ws;
};

constexpr double kPi = 3.14159265358979323846;
constexpr double ccos(double x) { double t = 1, s = 1; for (int n = 1; n < 16; ++n) { t *= -x * x / ((2 * n - 1) * (2 * n)); s += t; } return s; }
constexpr double cosn(int e, int n) { e = ((e % n) + n) % n; double x = 2 * kPi * e / n; if (x > kPi) x -= 2 * kPi; return ccos(x); }
constexpr double wre(int e, int n) { return cosn(e, n); }
constexpr double wim(int e, int n) { return -cosn(4 * e - n, 4 * n); }
constexpr unsigned bfb(double v) { float f = (float)v; unsigned u = __builtin_bit_cast(unsigned, f); u += 0x7fffu + ((u >> 16) & 1u); return u >> 16; }
constexpr unsigned bf2(double a, double b) { return bfb(a) | (bfb(b) << 16); }
constexpr unsigned fbits(double v) { return __builtin_bit_cast(unsigned, (float)v); }
struct Tab { unsigned v[16][64][4]; };
constexpr Tab make_tab() {
  Tab t{};
  for (int l = 0; l < 64; ++l) {
    const int g = l >> 4, l15 = l & 15, hh = l >> 5, n = l & 31;
    for (int mtx = 0; mtx < 3; ++mtx) {
      double r[4] = {}, im[4] = {};
      for (int q = 0; q < 4; ++q) {
        const int a = 4 * g + q;
        if (mtx == 0) { r[q] = wre(l15 * a, 16); im[q] = wim(l15 * a, 16); }
        else if (mtx == 1) { r[q] = wre(a * (2 * l15 + 1), 32); im[q] = wim(a * (2 * l15 + 1), 32); }
        else { r[q] = wre(l15 * (2 * a + 1), 32); im[q] = -wim(l15 * (2 * a + 1), 32); }
      }
      t.v[2 * mtx][l][0] = bf2(r[0], r[1]); t.v[2 * mtx][l][1] = bf2(r[2], r[3]); t.v[2 * mtx][l][2] = bf2(-im[0], -im[1]); t.v[2 * mtx][l][3] = bf2(-im[2], -im[3]);
      t.v[2 * mtx + 1][l][0] = bf2(im[0], im[1]); t.v[2 * mtx + 1][l][1] = bf2(im[2], im[3]); t.v[2 * mtx + 1][l][2] = bf2(r[0], r[1]); t.v[2 * mtx + 1][l][3] = bf2(r[2], r[3]);
    }
    {
      const int m2 = n >> 2, pp = (n >> 1) & 1, cp = n & 1;
      double v0[8] = {}, v1[8] = {};
      for (int jj = 0; jj < 8; ++jj) {
        const int ii = (8 * hh + jj) >> 1, d0 = jj & 1;
        const double sg = (d0 & pp) ? -1.0 : 1.0, orr = sg * wre(ii * m2, 8), oi = sg * wim(ii * m2, 8);
        v0[jj] = cp ? oi : orr; v1[jj] = cp ? orr : -oi;
      }
      for (int q = 0; q < 4; ++q) { t.v[6][l][q] = bf2(v0[2 * q], v0[2 * q + 1]); t.v[7][l][q] = bf2(v1[2 * q], v1[2 * q + 1]); }
    }
    {
      const int cq = n >> 4, ii = (n >> 1) & 7, d0 = n & 1;
      for (int s = 0; s < 2; ++s) {
        double v[8] = {};
        for (int jj = 0; jj < 8; ++jj) {
          const int m2k = 4 * s + 2 * hh + (jj >> 2), pk = (jj >> 1) & 1, ck = jj & 1;
          const double sg = (d0 & pk) ? -1.0 : 1.0, orr = sg * wre(ii * m2k, 8), oi = -sg * wim(ii * m2k, 8);
          v[jj] = cq ? (ck ? orr : oi) : (ck ? -oi : orr);
        }
        for (int q = 0; q < 4; ++q) t.v[8 + s][l][q] = bf2(v[2 * q], v[2 * q + 1]);
      }
    }
    for (int q = 0; q < 4; ++q) {
      const int a = 4 * g + q;
      t.v[10][l][q] = fbits(wre(a * (2 * l15), 512));      t.v[11][l][q] = fbits(wim(a * (2 * l15), 512));
      t.v[12][l][q] = fbits(wre(a * (2 * l15 + 1), 512));  t.v[13][l][q] = fbits(wim(a * (2 * l15 + 1), 512));
      t.v[14][l][q] = fbits(wre(l15 * (2 * a + 1), 512));  t.v[15][l][q] = fbits(wim(l15 * (2 * a + 1), 512));
    }
  }
  return t;
}
__device__ const Tab kTab = make_tab();

constexpr unsigned STAMP_OFF = 512 * 1024;
#define STAMP(k) do {} while (0)
#define FT(k) do {} while (0)

DI v4u make_srd(const void* p, unsigned bytes) {
  const unsigned long long a = (unsigned long long)p;
  v4u r; r.x = (unsigned)a; r.y = (unsigned)(a >> 32) & 0xffffu; r.z = bytes; r.w = 0x00020000u;
  return r;
}
template <unsigned LDSI, unsigned SOI> DI void dma16(v4u srd, unsigned voff, unsigned ldsb, unsigned sob) {
  unsigned tmp;
  asm volatile("s_add_u32 m0, %3, %5\n\ts_add_u32 %0, %4, %6\n\tbuffer_load_dwordx4 %1, %2, %0 offen lds"
               : "=&s"(tmp) : "v"(voff), "s"(srd), "s"(ldsb), "s"(sob), "i"(LDSI), "i"(SOI) : "memory", "scc");
}
template <unsigned LDSI, unsigned SOI> DI void dma16z(v4u srd, unsigned voff, unsigned ldsb) {
  unsigned tmp;
  asm volatile("s_add_u32 m0, %3, %4\n\ts_mov_b32 %0, %5\n\tbuffer_load_dwordx4 %1, %2, %0 offen lds"
               : "=&s"(tmp) : "v"(voff), "s"(srd), "s"(ldsb), "i"(LDSI), "i"(SOI) : "memory", "scc");
}
DI unsigned uni(unsigned v) { return (unsigned)__builtin_amdgcn_readfirstlane((int)v); }
#define LD16(dst, voffv, srd, soffv, flags) asm volatile("buffer_load_dwordx4 %0, %1, %2, %3 offen " flags : "=v"(dst) : "v"(voffv), "s"(srd), "s"(soffv) : "memory")
#define LD16I(dst, voffv, srd, sob, soi, flags) do { unsigned tmp_; asm volatile("s_add_u32 %1, %4, %5\n\tbuffer_load_dwordx4 %0, %2, %3, %1 offen " flags : "=v"(dst), "=&s"(tmp_) : "v"(voffv), "s"(srd), "s"(sob), "i"(soi) : "memory", "scc"); } while (0)
#define LD16Z(dst, voffv, srd, flags) asm volatile("buffer_load_dwordx4 %0, %1, %2, 0 offen " flags : "=v"(dst) : "v"(voffv), "s"(srd) : "memory")
#define LD4Z(dst, voffv, srd, flags) asm volatile("buffer_load_dword %0, %1, %2, 0 offen " flags : "=v"(dst) : "v"(voffv), "s"(srd) : "memory")
#define LD2Z(dst, voffv, srd, flags) asm volatile("buffer_load_ushort %0, %1, %2, 0 offen " flags : "=v"(dst) : "v"(voffv), "s"(srd) : "memory")
#define LD8Z(dst, voffv, srd, flags) asm volatile("buffer_load_dwordx2 %0, %1, %2, 0 offen " flags : "=v"(dst) : "v"(voffv), "s"(srd) : "memory")
#define LD8(dst, voffv, srd, soffv, flags) asm volatile("buffer_load_dwordx2 %0, %1, %2, %3 offen " flags : "=v"(dst) : "v"(voffv), "s"(srd), "s"(soffv) : "memory")
#define LD4(dst, voffv, srd, soffv, flags) asm volatile("buffer_load_dword %0, %1, %2, %3 offen " flags : "=v"(dst) : "v"(voffv), "s"(srd), "s"(soffv) : "memory")
#define LD2(dst, voffv, srd, soffv, flags) asm volatile("buffer_load_ushort %0, %1, %2, %3 offen " flags : "=v"(dst) : "v"(voffv), "s"(srd), "s"(soffv) : "memory")
#define XSTR(x) #x
#define STR(x) XSTR(x)
#define BAR_VM(n) asm volatile("s_waitcnt vmcnt(" STR(n) ") lgkmcnt(0)\n\ts_barrier" ::: "memory")
#define BAR_LGKM() asm volatile("s_waitcnt lgkmcnt(0)\n\ts_barrier" ::: "memory")
#define SCHED() __builtin_amdgcn_sched_barrier(0)
template <int N> DI void bar_vm() { asm volatile("s_waitcnt vmcnt(%0) lgkmcnt(0)\n\ts_barrier" :: "i"(N) : "memory"); }
template <int N> DI void wait_vm() { asm volatile("s_waitcnt vmcnt(%0)" :: "i"(N) : "memory"); }

DI unsigned pk2(float a, float b) { f32x2 p = {a, b}; return __builtin_bit_cast(unsigned, __builtin_convertvector(p, bf16x2)); }
DI float bflo(unsigned u) { return __builtin_bit_cast(float, u << 16); }
DI float bfhi(unsigned u) { return __builtin_bit_cast(float, u & 0xffff0000u); }
DI float asf(unsigned u) { return __builtin_bit_cast(float, u); }
DI f32x16 mfma32(v4u a, v4u b, f32x16 c) {
  return __builtin_amdgcn_mfma_f32_32x32x16_bf16(__builtin_bit_cast(bf16x8, a), __builtin_bit_cast(bf16x8, b), c, 0, 0, 0);
}
DI f32x4 mfma16(v4u a, v4u b, f32x4 c) {
  return __builtin_amdgcn_mfma_f32_16x16x32_bf16(__builtin_bit_cast(bf16x8, a), __builtin_bit_cast(bf16x8, b), c, 0, 0, 0);
}
DI v2u tr_read(LAS char* p) { return __builtin_bit_cast(v2u, __builtin_amdgcn_ds_read_tr16_b64_v4i16((LAS s16x4*)p)); }
DI v4u tr_read2(LAS char* p0, LAS char* p1) { v2u a = tr_read(p0), b = tr_read(p1); v4u r; r.x = a.x; r.y = a.y; r.z = b.x; r.w = b.y; return r; }
DI v4u lds_ld16(LAS char* p) { return *(LAS v4u*)p; }
DI float cosrev(float r) { return __builtin_amdgcn_cosf(r); }
DI float sinrev(float r) { return __builtin_amdgcn_sinf(r); }
DI __amdgpu_buffer_rsrc_t as_rsrc(v4u srd) { return __builtin_amdgcn_make_buffer_rsrc((void*)(((unsigned long long)srd.y << 32) | srd.x), 0, srd.z, 0x00020000); }
DI void st16_sc1(v4u srd, unsigned voff, v4u v) { __builtin_amdgcn_raw_buffer_store_b128(v, as_rsrc(srd), voff, 0, 16); }
DI void flag_store(void* ws, unsigned off, unsigned tag) { __hip_atomic_store((GAS unsigned*)((GAS char*)ws + off), tag, __ATOMIC_RELAXED, __HIP_MEMORY_SCOPE_AGENT); }
template <int CTRL, int RMASK> DI float dpp_add(float v) { return v + asf((unsigned)__builtin_amdgcn_update_dpp(0, (int)__builtin_bit_cast(unsigned, v), CTRL, RMASK, 0xf, true)); }
DI void poll_words(v4u sws, unsigned off, int n, unsigned tag, int lane) {
  const unsigned vo = off + FS * (unsigned)(lane < n ? lane : 0);
#pragma unroll 1
  for (unsigned spins = 0; spins < kSpinCap; ++spins) {
    unsigned f;
    asm volatile("buffer_load_dword %0, %1, %2, 0 offen sc1\n\ts_waitcnt vmcnt(0)" : "=&v"(f) : "v"(vo), "s"(sws) : "memory");
    if (__all(f == tag)) return;
    __builtin_amdgcn_s_sleep(4);
  }
}

constexpr unsigned A_XIMG = 64 * 1024, A_FIMG = 128 * 1024;
DI void phaseA_genF(LAS char* L, int i, int mt, int w, int lane) {
  const int mh = w >> 2, sg = w & 3, n = lane & 31, hh = lane >> 5;
  const int m1c = 64 * mt + 32 * mh + n, m1 = m1c >> 1, c = m1c & 1;
  const int j0 = 64 * sg + 32 * hh;
  const float ph = c ? 0.25f : 0.f;
  const float k2 = 2.f * cosrev((float)(8 * m1) * (1.f / 2048.f));
  float vm = cosrev((float)(((i + 8 * (j0 - 1)) * m1) & 2047) * (1.f / 2048.f) + ph);
  float v0 = cosrev((float)(((i + 8 * j0) * m1) & 2047) * (1.f / 2048.f) + ph);
  const unsigned cst = c ? 0x3F800000u : 0x00003F80u;
  const bool sp = m1c < 2;
#pragma unroll
  for (int s = 0; s < 4; ++s) {
    float e[8];
#pragma unroll
    for (int q = 0; q < 8; ++q) { e[q] = v0; const float vn = __builtin_fmaf(k2, v0, -vm); vm = v0; v0 = vn; }
    v4u f; f.x = pk2(e[0], e[1]); f.y = pk2(e[2], e[3]); f.z = pk2(e[4], e[5]); f.w = pk2(e[6], e[7]);
    if (mt == 0 && w < 4) { f.x = sp ? cst : f.x; f.y = sp ? cst : f.y; f.z = sp ? cst : f.z; f.w = sp ? cst : f.w; }
    *(LAS v4u*)(L + A_FIMG + (mh * 16 + sg * 4 + s) * 1024 + lane * 16) = f;
  }
}

struct Frag1 { v4u a[4], b0[4], b1[4]; };
DI void g1_read(Frag1& f, LAS char* wa, LAS char* xb0, LAS char* xb1, LAS char* xb2, LAS char* xb3, int slot) {
  const int so = slot * 32768;
  f.a[0] = tr_read2(wa + so, wa + so + 1024);              f.b0[0] = lds_ld16(xb0 + so); f.b1[0] = lds_ld16(xb0 + so + 4096);
  f.a[1] = tr_read2(wa + so + 4096, wa + so + 5120);       f.b0[1] = lds_ld16(xb1 + so); f.b1[1] = lds_ld16(xb1 + so + 4096);
  f.a[2] = tr_read2(wa + so + 8192, wa + so + 9216);       f.b0[2] = lds_ld16(xb2 + so); f.b1[2] = lds_ld16(xb2 + so + 4096);
  f.a[3] = tr_read2(wa + so + 12288, wa + so + 13312);     f.b0[3] = lds_ld16(xb3 + so); f.b1[3] = lds_ld16(xb3 + so + 4096);
}
DI unsigned silu2(float a, float b) {
  const f32x2 h = {a, b}, t = h * -1.44269504088896341f;
  f32x2 e = {__builtin_amdgcn_exp2f(t[0]), __builtin_amdgcn_exp2f(t[1])};
  e = e + 1.f;
  const f32x2 r = {__builtin_amdgcn_rcpf(e[0]), __builtin_amdgcn_rcpf(e[1])};
  return __builtin_bit_cast(unsigned, __builtin_convertvector(h * r, bf16x2));
}

DI f32x2 lo2(const f32x4& v) { return __builtin_shufflevector(v, v, 0, 1); }
DI f32x2 hi2(const f32x4& v) { return __builtin_shufflevector(v, v, 2, 3); }
DI unsigned pk2v(f32x2 p) { return __builtin_bit_cast(unsigned, __builtin_convertvector(p, bf16x2)); }
DI f32x4 asf4(v4u v) { return __builtin_bit_cast(f32x4, v); }
template <bool CONJ> DI v4u cmul_pack(const f32x4& re, const f32x4& im, const f32x4& tr, const f32x4& ti) {
  const f32x2 r0 = lo2(re), r1 = hi2(re), i0 = lo2(im), i1 = hi2(im), c0 = lo2(tr), c1 = hi2(tr), s0 = lo2(ti), s1 = hi2(ti);
  f32x2 a0, a1, b0, b1;
  if (CONJ) {
    a0 = __builtin_elementwise_fma(r0, c0, i0 * s0); a1 = __builtin_elementwise_fma(r1, c1, i1 * s1);
    b0 = __builtin_elementwise_fma(i0, c0, -(r0 * s0)); b1 = __builtin_elementwise_fma(i1, c1, -(r1 * s1));
  } else {
    a0 = __builtin_elementwise_fma(r0, c0, -(i0 * s0)); a1 = __builtin_elementwise_fma(r1, c1, -(i1 * s1));
    b0 = __builtin_elementwise_fma(r0, s0, i0 * c0); b1 = __builtin_elementwise_fma(r1, s1, i1 * c1);
  }
  v4u r; r.x = pk2v(a0); r.y = pk2v(a1); r.z = pk2v(b0); r.w = pk2v(b1); return r;
}
struct cf { float r, i; };
DI cf cmul(cf a, cf b) { return {a.r * b.r - a.i * b.i, a.r * b.i + a.i * b.r}; }
DI cf cmulc(cf a, cf b) { return {a.r * b.r + a.i * b.i, a.i * b.r - a.r * b.i}; }
DI unsigned w_src(int m, int lane) {
  const int mm = (NS - m) & (NS - 1);
  const int e = lane < 32 ? m * 257 - (m & 1) : mm * 257 + (m & 1);
  return (unsigned)(e * 2 + (lane & 31) * 16);
}
DI unsigned w_tail_src(int m, int lane) {
  const int mm = (NS - m) & (NS - 1), l = lane & 3;
  return (unsigned)((l < 2 ? m * 257 + 255 + l : mm * 257 + (l - 2) * 256) * 2);
}
struct WTail { unsigned r255, r256, rm0, rm256, i255, i256, im0, im256; };
DI WTail w_tail(unsigned tr, unsigned ti) {
  return {(unsigned)__builtin_amdgcn_readlane((int)tr, 0), (unsigned)__builtin_amdgcn_readlane((int)tr, 1), (unsigned)__builtin_amdgcn_readlane((int)tr, 2), (unsigned)__builtin_amdgcn_readlane((int)tr, 3),
          (unsigned)__builtin_amdgcn_readlane((int)ti, 0), (unsigned)__builtin_amdgcn_readlane((int)ti, 1), (unsigned)__builtin_amdgcn_readlane((int)ti, 2), (unsigned)__builtin_amdgcn_readlane((int)ti, 3)};
}
struct WVal { f32x4 r0, i0, r1, i1; };
struct WRawL { unsigned r[4][2], i[4][2]; };
DI void w_put(LAS char* scr, v4u qr, v4u qi, int lane) { *(LAS v4u*)(scr + lane * 16) = qr; *(LAS v4u*)(scr + 1024 + lane * 16) = qi; }
DI int w_off(int m, int lane) { const int k1 = lane & 15, g = lane >> 4, odd = m & 1; return lane < 32 ? (2 * k1 + 128 * g + odd) * 2 : 512 + (511 - 2 * k1 - 128 * g - odd) * 2; }
DI void w_get(WRawL& o, LAS char* scr, int off, int lane, int r) {
  LAS unsigned* a = (LAS unsigned*)(scr + ((off + (lane < 32 ? 64 : -64) * r) & ~3));
  o.r[r][0] = a[0]; o.r[r][1] = a[1]; o.i[r][0] = a[256]; o.i[r][1] = a[257];
}
DI WVal w_unpack(const WRawL& x, const WTail& t, int off, int m, int lane) {
  const bool lo = lane < 32;
  const unsigned sh = (off & 2) * 8;
  unsigned rr[4], ii[4];
#pragma unroll
  for (int r = 0; r < 4; ++r) { rr[r] = __builtin_amdgcn_alignbit(x.r[r][1], x.r[r][0], sh); ii[r] = __builtin_amdgcn_alignbit(x.i[r][1], x.i[r][0], sh); }
  if (m & 1) { rr[3] = lane == 31 ? (rr[3] & 0xffffu) | (t.r255 << 16) : rr[3]; ii[3] = lane == 31 ? (ii[3] & 0xffffu) | (t.i255 << 16) : ii[3]; }
  else { rr[0] = lane == 32 ? (rr[0] & 0xffffu) | (t.rm256 << 16) : rr[0]; ii[0] = lane == 32 ? (ii[0] & 0xffffu) | (t.im256 << 16) : ii[0]; }
  const unsigned s0 = lo ? 16u : 0u, s1 = lo ? 0u : 16u, sg = lo ? 0u : 0x80000000u;
  WVal o;
#pragma unroll
  for (int r = 0; r < 4; ++r) {
    o.r0[r] = asf((rr[r] << s0) & 0xffff0000u); o.r1[r] = asf((rr[r] << s1) & 0xffff0000u);
    o.i0[r] = asf(((ii[r] << s0) & 0xffff0000u) ^ sg); o.i1[r] = asf(((ii[r] << s1) & 0xffff0000u) ^ sg);
  }
  const bool av = (lane & 31) == 0;
  const float xr = bflo(lo ? t.rm0 : t.r256), xi = asf(((lo ? t.im0 : t.i256) << 16) ^ (sg ^ 0x80000000u));
  o.r0[0] = av ? 0.5f * (o.r0[0] + xr) : o.r0[0];
  o.i0[0] = av ? 0.5f * (o.i0[0] + xi) : o.i0[0];
  return o;
}

struct Geo {
  int tid, lane, w, b, i, i1, at, m1, rb, fc, hh, blk, q4, p4;
  int jb, dt, rh, q, j;
  v4u sx, sws, sw2;
  unsigned vw20, vw21, ldsw, fq0b, fq1b;
};
template <int T, int U> DI void w2_dma(const v4u& sw2, unsigned vw20, unsigned vw21, unsigned ldsw, unsigned fq0, unsigned fq1) {
  dma16<(T & 7) * 16384u + U * 1024u, (T < 8 ? T : T - 8) * 32768u>(sw2, U ? vw21 : vw20, ldsw, T < 8 ? fq0 : fq1);
}
#define w2_issue1(g, t, u) (w2_dma<t, u>((g).sw2, (g).vw20, (g).vw21, (g).ldsw, (g).fq0b, (g).fq1b))
struct GRot { float c1, s1, c4, s4, c, s; };
DI void gen_g_init(const Geo& g, GRot& r) {
  const int srow = g.i + 8 * g.j;
  const int mb = 32 * g.q + 2 * g.hh;
  const float r1 = (float)(srow & 2047) * (1.f / 2048.f), r4 = (float)((4 * srow) & 2047) * (1.f / 2048.f), r0 = (float)((mb * srow) & 2047) * (1.f / 2048.f);
  r.c1 = cosrev(r1); r.s1 = sinrev(r1); r.c4 = cosrev(r4); r.s4 = sinrev(r4); r.c = cosrev(r0); r.s = sinrev(r0);
}
template <int S> DI void gen_g_frag(const Geo& g, GRot& r, v4u& f) {
#pragma unroll
  for (int h = 0; h < 2; ++h) {
    const float cb = __builtin_fmaf(r.c, r.c1, -(r.s * r.s1)), sb = __builtin_fmaf(r.s, r.c1, r.c * r.s1);
    unsigned w0 = pk2(r.c, -r.s), w1 = pk2(cb, -sb);
    if (S == 0 && h == 0) w0 = (g.q == 0 && g.hh == 0) ? ((g.j & 1) ? 0x3F800000u : 0x00003F80u) : w0;
    if (h) { f.z = w0; f.w = w1; } else { f.x = w0; f.y = w1; }
    const float cn = __builtin_fmaf(r.c, r.c4, -(r.s * r.s4)), sn = __builtin_fmaf(r.s, r.c4, r.c * r.s4);
    r.c = cn; r.s = sn;
  }
  asm volatile("" : "+v"(f));
}
template <int N0, int N1, int NR> DI void bar_w(int w) { if (w == 0) bar_vm<N0>(); else if (w == 1) bar_vm<N1>(); else bar_vm<NR>(); }
#define NOP do {} while (0)

#define TOUCH() do {                                                                                                      \
    const int gl = G.m1 * 512 + w * 64 + lane;                                                                              \
    const char* base_ = gl < 7168 ? (const char*)p.w1 + 7168 * 128 : gl < 21504 ? (const char*)p.w2 - 7168 * 128 : gl < 29728 ? (const char*)p.wr - 21504 * 128 : (const char*)p.wi - 29728 * 128; \
    const char* ad_ = base_ + (size_t)(gl < 37952 ? gl : 29728) * 128;                                                      \
    asm volatile("global_load_dword %0, %1, off" : "=v"(touch) : "v"(ad_) : "memory");                                     \
  } while (0)
template <bool ISA>
DI unsigned phase1(const Params& p, LAS char* L, const Geo& G, v4u& m5a, v4u& m5b
) {
  const int tid = G.tid, lane = G.lane, w = G.w, i = G.i, i1 = G.i1, rb = G.rb, fc = G.fc, hh = G.hh, blk = G.blk, q4 = G.q4, p4 = G.p4;
  const v4u sx = G.sx, sws = G.sws, sw1 = make_srd(p.w1, ND * NF * 2);
  unsigned touch;
  unsigned ep; v4u gx, gv;
  const int jg = 128 * rb + 8 * fc + w;
  LD16Z(gx, (unsigned)(lane * 16 + (i1 + 8 * jg) * 1024), sx, "");
  LD16Z(gv, (unsigned)lane * 16u, make_srd(p.gw, ND * 2), "");
  LD4Z(ep, WS_EPOCH, sws, "sc1");
  const int f0 = ISA ? 96 * fc : 768 + 128 * (fc - 8), nf = ISA ? 96 : 128;
  const int mt = G.at >> 2, dt4 = G.at & 3;
  const int m1 = G.m1;
  if (ISA) {
    const unsigned voff = (unsigned)(lane >> 4) * 8192u + (unsigned)(((lane & 15) ^ ((lane >> 4) << 2)) << 4);
    const unsigned xl = uni(A_XIMG + (unsigned)w * 8192u), xs = uni((unsigned)i * 1024u + (unsigned)dt4 * 256u + (unsigned)w * 262144u);
    dma16<0, 0>(sx, voff, xl, xs); dma16<1024, 32768>(sx, voff, xl, xs); dma16<2048, 65536>(sx, voff, xl, xs); dma16<3072, 98304>(sx, voff, xl, xs);
    dma16<4096, 131072>(sx, voff, xl, xs); dma16<5120, 163840>(sx, voff, xl, xs); dma16<6144, 196608>(sx, voff, xl, xs); dma16<7168, 229376>(sx, voff, xl, xs);
  }
  unsigned vx0, vx1, vw0, vw1;
  {
    const int r0 = 16 * w + (lane >> 3), pos = lane & 7, r1 = r0 + 8;
    vx0 = (unsigned)(i1 * 1024 + (128 * rb + r0) * 8192 + ((pos ^ ((r0 >> 1) & 7)) << 4));
    vx1 = (unsigned)(i1 * 1024 + (128 * rb + r1) * 8192 + ((pos ^ ((r1 >> 1) & 7)) << 4));
    const int k0 = 8 * w + (lane >> 4), chunk = (lane & 15) ^ ((lane >> 4) << 2);
    const bool ok = chunk * 8 < nf;
    vw0 = ok ? (unsigned)(k0 * 3584 + f0 * 2 + chunk * 16) : 0x80000000u;
    vw1 = ok ? vw0 + 4u * 3584u : 0x80000000u;
  }
  const unsigned g1l = uni((unsigned)w * 2048u);
#define g1_dma(kt, u) do {                                                                                         \
    if ((u) == 0) dma16z<((kt) & 3) * 32768u, (kt) * 128u>(sx, vx0, g1l);                                           \
    if ((u) == 1) dma16z<((kt) & 3) * 32768u + 1024u, (kt) * 128u>(sx, vx1, g1l);                                   \
    if ((u) == 2) dma16z<((kt) & 3) * 32768u + 16384u, (kt) * (64u * 3584u)>(sw1, vw0, g1l);                        \
    if ((u) == 3) dma16z<((kt) & 3) * 32768u + 17408u, (kt) * (64u * 3584u)>(sw1, vw1, g1l); } while (0)
#define g1_issue(kt) do { g1_dma(kt, 0); g1_dma(kt, 1); g1_dma(kt, 2); g1_dma(kt, 3); } while (0)
  if (ISA) {
    phaseA_genF(L, i, mt, w, lane);
    g1_issue(0); g1_issue(1);
  } else {
    g1_issue(0); asm volatile("s_barrier" ::: "memory");
    g1_issue(1); asm volatile("s_barrier" ::: "memory");
    g1_issue(2); asm volatile("s_barrier" ::: "memory");
    g1_issue(3);
  }
  asm volatile("s_waitcnt vmcnt(16)" : "+v"(ep), "+v"(gx), "+v"(gv) :: "memory");
  const unsigned tag = (unsigned)__builtin_amdgcn_readfirstlane((int)ep) + 1u;
  float gate_g, gate_og;
  {
    float z = 0.f;
#pragma unroll
    for (int q = 0; q < 4; ++q) {
      const unsigned pr = pk2(bflo(gx[q]) * bflo(gv[q]), bfhi(gx[q]) * bfhi(gv[q]));
      z += bflo(pr) + bfhi(pr);
    }
    z = dpp_add<0x111, 0xf>(z); z = dpp_add<0x112, 0xf>(z); z = dpp_add<0x114, 0xf>(z); z = dpp_add<0x118, 0xf>(z);
    z = dpp_add<0x142, 0xa>(z); z = dpp_add<0x143, 0xc>(z);
    const float zb = bflo(pk2(z, 0.f));
    const float g = bflo(pk2(__builtin_amdgcn_rcpf(1.f + __builtin_amdgcn_exp2f(zb * -1.44269504088896341f)), 0.f));
    gate_g = asf((unsigned)__builtin_amdgcn_readlane((int)__builtin_bit_cast(unsigned, g), 63));
    gate_og = bflo(pk2(1.f - gate_g, 0.f));
  }
  if (ISA) {
    const int dq = w & 3, mh = w >> 2;
    LAS char* xa = L + A_XIMG + (32 * hh + q4) * 256 + (((4 * dq + 2 * blk + (p4 >> 1)) ^ (q4 << 2)) << 4) + 8 * (p4 & 1);
    LAS char* fb = L + A_FIMG + mh * 16384 + lane * 16;
    const int srow = 32 * mh + (lane & 31);
    LAS char* stw = L + A_FIMG + srow * 256 + 8 * hh;
    const int rrow = tid >> 3;
    LAS char* str = L + A_FIMG + rrow * 256;
    const unsigned tdst = WS_T + (unsigned)(((64 * mt + rrow) * 8 + i) * 1024 + dt4 * 256 + (tid & 7) * 16);
    if (w < 4) bar_vm<8>(); else BAR_LGKM();
    STAMP(1);
    f32x16 acc = {};
    v4u fa[2][4], fbv[2][4];
#pragma unroll
    for (int s = 0; s < 4; ++s) { fa[0][s] = tr_read2(xa + 8 * s * 256, xa + 8 * s * 256 + 1024); fbv[0][s] = lds_ld16(fb + s * 1024); }
#pragma unroll
    for (int g = 0; g < 4; ++g) {
      acc = mfma32(fa[g & 1][0], fbv[g & 1][0], acc);
      SCHED();
      if (g == 1) { if (w >= 4) bar_vm<8>(); else BAR_LGKM(); }
      if (g < 3) {
#pragma unroll
        for (int s = 0; s < 4; ++s) {
          const int ro = (64 * (g + 1) + 8 * s) * 256;
          fa[(g + 1) & 1][s] = tr_read2(xa + ro, xa + ro + 1024); fbv[(g + 1) & 1][s] = lds_ld16(fb + (4 * (g + 1) + s) * 1024);
        }
      }
      SCHED();
#pragma unroll
      for (int s = 1; s < 4; ++s) { acc = mfma32(fa[g & 1][s], fbv[g & 1][s], acc); if (g == 1 && s == 1) { g1_dma(2, 0); g1_dma(2, 1); } if (g == 1 && s == 2) { g1_dma(2, 2); g1_dma(2, 3); } SCHED(); }
    }
    BAR_LGKM();
    STAMP(2);
    g1_issue(3);
#pragma unroll
    for (int Gq = 0; Gq < 4; ++Gq) {
      v2u o; o.x = pk2(acc[4 * Gq], acc[4 * Gq + 1]); o.y = pk2(acc[4 * Gq + 2], acc[4 * Gq + 3]);
      *(LAS v2u*)(stw + (((4 * dq + Gq) ^ (srow & 15)) << 4)) = o;
    }
    BAR_LGKM();
    {
      const v4u v0 = lds_ld16(str + (((tid & 7) ^ (rrow & 15)) << 4)), v1 = lds_ld16(str + ((((tid & 7) + 8) ^ (rrow & 15)) << 4));
      st16_sc1(sws, tdst, v0);
      st16_sc1(sws, tdst + 128u, v1);
    }
    SCHED();
  }
  const int ft = w & 3, rh1 = w >> 2;
  const bool active = ft * 32 < nf;
  f32x16 acc1[2] = {};
  v4u wqr, wqi, tq0, tq1;
  unsigned wtr, wti, xtr, xti;
  v4u xqr, xqi;
  WVal Wc;
  v4u tb[16];
  unsigned fla;
  int t_ok = 0, t_first = 0; (void)t_first;
  const v4u swr = make_srd(p.wr, NS * 257 * 2), swi = make_srd(p.wi, NS * 257 * 2);
  const int mW = m1 == 0 ? 128 * w : m1 + 256 * w;
  unsigned tsrc0 = 0, tsrc1 = 0;
  const unsigned flaoff = WS_FLA + (unsigned)(((m1 >> 5) & 3) * 32 + (lane & 31)) * FS;
  LAS char* CTL = L + 128 * 1024;
  {
    LAS char* wa = L + 16384 + (8 * hh + q4) * 256 + (((4 * ft + 2 * blk + (p4 >> 1)) ^ (q4 << 2)) << 4) + 8 * (p4 & 1);
    const int r = 64 * rh1 + (lane & 31), fz = (r >> 1) & 7;
    LAS char* xb0 = L + r * 128 + (((0 + hh) ^ fz) << 4);
    LAS char* xb1 = L + r * 128 + (((2 + hh) ^ fz) << 4);
    LAS char* xb2 = L + r * 128 + (((4 + hh) ^ fz) << 4);
    LAS char* xb3 = L + r * 128 + (((6 + hh) ^ fz) << 4);
    Frag1 fr[2];
    bar_vm<ISA ? 14 : 12>();
    if (!ISA) TOUCH();
    STAMP(3);
    if (lane == 0) { v2u gv2; gv2.x = __builtin_bit_cast(unsigned, gate_g); gv2.y = __builtin_bit_cast(unsigned, gate_og); *(LAS v2u*)(CTL + 64 + w * 8) = gv2; }
    g1_read(fr[0], wa, xb0, xb1, xb2, xb3, 0);
#define PIN1(c) asm volatile("" : "+v"(c.a[0]), "+v"(c.a[1]), "+v"(c.a[2]), "+v"(c.a[3]), "+v"(c.b0[0]), "+v"(c.b0[1]), "+v"(c.b0[2]), "+v"(c.b0[3]), \
                             "+v"(c.b1[0]), "+v"(c.b1[1]), "+v"(c.b1[2]), "+v"(c.b1[3]))
#define G1_STEP(kt, X0, X1, X2, X3, X4, X5, X6, X7)                                                                   \
    {                                                                                                                   \
      FT(10 + (kt));                                                                                                    \
      Frag1& c = fr[(kt) & 1];                                                                                          \
      Frag1& n = fr[((kt) + 1) & 1];                                                                                    \
      const int so = (((kt) + 1) & 3) * 32768;                                                                          \
      PIN1(c);                                                                                                          \
      asm volatile("" :: "v"(acc1[0]), "v"(acc1[1]));                                                                   \
      SCHED();                                                                                                          \
      acc1[0] = mfma32(c.a[0], c.b0[0], acc1[0]); if ((kt) < 7) n.a[0] = tr_read2(wa + so, wa + so + 1024); X0; SCHED(); \
      acc1[1] = mfma32(c.a[0], c.b1[0], acc1[1]); if ((kt) < 7) { n.b0[0] = lds_ld16(xb0 + so); n.b1[0] = lds_ld16(xb0 + so + 4096); } X1; SCHED(); \
      acc1[0] = mfma32(c.a[1], c.b0[1], acc1[0]); if ((kt) < 7) n.a[1] = tr_read2(wa + so + 4096, wa + so + 5120); X2; SCHED(); \
      acc1[1] = mfma32(c.a[1], c.b1[1], acc1[1]); if ((kt) < 7) { n.b0[1] = lds_ld16(xb1 + so); n.b1[1] = lds_ld16(xb1 + so + 4096); } X3; SCHED(); \
      acc1[0] = mfma32(c.a[2], c.b0[2], acc1[0]); if ((kt) < 7) n.a[2] = tr_read2(wa + so + 8192, wa + so + 9216); X4; SCHED(); \
      acc1[1] = mfma32(c.a[2], c.b1[2], acc1[1]); if ((kt) < 7) { n.b0[2] = lds_ld16(xb2 + so); n.b1[2] = lds_ld16(xb2 + so + 4096); } X5; SCHED(); \
      acc1[0] = mfma32(c.a[3], c.b0[3], acc1[0]); if ((kt) < 7) n.a[3] = tr_read2(wa + so + 12288, wa + so + 13312); X6; SCHED(); \
      acc1[1] = mfma32(c.a[3], c.b1[3], acc1[1]); if ((kt) < 7) { n.b0[3] = lds_ld16(xb3 + so); n.b1[3] = lds_ld16(xb3 + so + 4096); } X7; SCHED(); \
    }
#define GATE_STORE() do { if (w == 2 && lane < 8) { const v2u gv2 = *(LAS v2u*)(CTL + 64 + lane * 8);                       \
      __builtin_amdgcn_raw_buffer_store_b64(gv2, as_rsrc(sws), WS_GATE + (unsigned)(i1 * 256 + 128 * rb + 8 * fc + lane) * 8u, 0, 16); } } while (0)
#define STEP8 G1_STEP
    if constexpr (ISA) {
      bar_vm<10>();
      STAMP(4);
      STEP8(0, NOP, NOP, NOP, NOP, NOP, NOP, NOP, NOP)
      bar_vm<0>();
      if (tid == 0) flag_store(p.ws, WS_FLA + (unsigned)(mt * 32 + i * 4 + dt4) * FS, tag);
      STAMP(13);
      STEP8(1, g1_dma(4, 0), g1_dma(4, 1), g1_dma(4, 2), g1_dma(4, 3), g1_dma(5, 0), g1_dma(5, 1), g1_dma(5, 2), g1_dma(5, 3))
      BAR_LGKM();
      GATE_STORE();
      STEP8(2, NOP, NOP, NOP, NOP, g1_dma(6, 0), g1_dma(6, 1), g1_dma(6, 2), g1_dma(6, 3))
      bar_vm<8>();
      STEP8(3, NOP, NOP, NOP, NOP, g1_dma(7, 0), g1_dma(7, 1), g1_dma(7, 2), g1_dma(7, 3))
      bar_vm<8>();
      STAMP(12);
      STEP8(4, NOP, NOP, NOP, NOP, w2_issue1(G, 0, 0), w2_issue1(G, 0, 1), w2_issue1(G, 1, 0), w2_issue1(G, 1, 1))
      bar_vm<8>();
      STEP8(5, NOP, NOP, NOP, NOP, w2_issue1(G, 2, 0), w2_issue1(G, 2, 1), w2_issue1(G, 3, 0), w2_issue1(G, 3, 1))
      bar_vm<8>();
      STEP8(6, NOP, NOP, NOP, NOP, w2_issue1(G, 4, 0), w2_issue1(G, 4, 1), w2_issue1(G, 5, 0), w2_issue1(G, 5, 1))
      BAR_LGKM();
    } else {
      const v4u stab = make_srd(&kTab, sizeof(Tab));
      const unsigned wsrc = w_src(mW, lane);
      {
        const int kap = tid >> 5, ii = (kap >> 1) & 7, d0 = kap & 1;
        const int g0 = m1 == 0 ? 2 * ii : ii, g1 = m1 == 0 ? 2 * ii + 1 : 8 + ii;
        const unsigned tb = WS_T + (unsigned)m1 * 16384u + (unsigned)(d0 * 512 + (tid & 31) * 16);
        tsrc0 = tb + (unsigned)g0 * 1024u; tsrc1 = tb + (unsigned)g1 * 1024u;
      }
      const unsigned vw20z = G.vw20 - 16u * 1024u, vw21z = G.vw21 - 16u * 1024u, ldswz = uni(G.ldsw - 2048u);
#define W2B(t, u) do { if (w != 0) w2_issue1(G, t, u); } while (0)
#define W2Z(t, u) do { if (w == 1) w2_dma<t, u>(G.sw2, vw20z, vw21z, ldswz, G.fq0b, G.fq1b); } while (0)
      bar_vm<9>();
      STAMP(4);
      const unsigned wts = w_tail_src(mW, lane);
      if (m1 == 0) {
        const unsigned ws2 = w_src(128 * w + 1024, lane), wt2 = w_tail_src(128 * w + 1024, lane);
        LD16Z(xqr, ws2, swr, ""); LD16Z(xqi, ws2, swi, ""); LD2Z(xtr, wt2, swr, ""); LD2Z(xti, wt2, swi, "");
      }
      STEP8(0, LD16Z(wqr, wsrc, swr, ""), LD16Z(wqi, wsrc, swi, ""), LD2Z(wtr, wts, swr, ""), LD2Z(wti, wts, swi, ""), g1_dma(4, 0), g1_dma(4, 1), g1_dma(4, 2), g1_dma(4, 3))
      bar_vm<13>();
      STEP8(1, NOP, NOP, NOP, NOP, g1_dma(5, 0), g1_dma(5, 1), g1_dma(5, 2), g1_dma(5, 3))
      bar_vm<13>();
      GATE_STORE();
      STEP8(2, NOP, NOP, NOP, NOP, g1_dma(6, 0), g1_dma(6, 1), g1_dma(6, 2), g1_dma(6, 3))
      bar_vm<8>();
      STEP8(3, NOP, NOP, (dma16<128u * 1024u + 16384u, 0>(stab, (unsigned)lane * 16u, g1l, g1l)), (dma16<128u * 1024u + 17408u, 1024>(stab, (unsigned)lane * 16u, g1l, g1l)),
            g1_dma(7, 0), g1_dma(7, 1), g1_dma(7, 2), g1_dma(7, 3))
      bar_vm<10>();
      STAMP(12);
      LAS char* wscr0 = L + 128 * 1024 + w * 2048;
      const int woff = w_off(mW, lane);
      asm volatile("" : "+v"(wqr), "+v"(wqi), "+v"(wtr), "+v"(wti));
      w_put(wscr0, wqr, wqi, lane);
      STEP8(4, W2Z(0, 0), W2Z(0, 1), W2Z(1, 0), W2Z(1, 1), W2B(0, 0), W2B(0, 1), W2B(1, 0), W2B(1, 1))
      bar_w<6, 14, 10>(w);
      WRawL wraw;
      w_get(wraw, wscr0, woff, lane, 0); w_get(wraw, wscr0, woff, lane, 1); w_get(wraw, wscr0, woff, lane, 2); w_get(wraw, wscr0, woff, lane, 3);
      STEP8(5, W2Z(2, 0), W2Z(2, 1), W2Z(3, 0), W2Z(3, 1), W2B(2, 0), W2B(2, 1), W2B(3, 0), W2B(3, 1))
      bar_w<0, 16, 8>(w);
      Wc = w_unpack(wraw, w_tail(wtr, wti), woff, mW, lane);
      STEP8(6, if (w == 0) LD4Z(fla, flaoff, sws, "sc1"), NOP, NOP, NOP, NOP, NOP, NOP, NOP)
      if (w == 0) {
        asm volatile("s_waitcnt vmcnt(0)" : "+v"(fla) :: "memory");
        const unsigned ok = __all(fla == tag) ? 1u : 0u;
        if (lane == 0) *(LAS unsigned*)CTL = ok;
      }
      BAR_LGKM();
      t_ok = __builtin_amdgcn_readfirstlane(*(volatile LAS unsigned*)CTL);
      t_first = t_ok;
    }
    if constexpr (ISA) {
      STEP8(7, NOP, NOP, NOP, NOP, w2_issue1(G, 6, 0), w2_issue1(G, 6, 1), w2_issue1(G, 7, 0), w2_issue1(G, 7, 1))
    } else {
      LAS char* TB = L + 128 * 1024 + 16384 + lane * 16;
#define TBR(f) (tb[f] = lds_ld16(TB + (f) * 1024), tb[(f) + 1] = lds_ld16(TB + ((f) + 1) * 1024))
      STEP8(7, { if (t_ok) LD16Z(tq0, tsrc0, sws, "sc1"); TBR(6); }, { if (t_ok) LD16Z(tq1, tsrc1, sws, "sc1"); TBR(0); },
            { if (!t_ok && w == 0) LD4Z(fla, flaoff, sws, "sc1"); TBR(10); }, TBR(2), TBR(12), TBR(14), TBR(4), TBR(8))
      if (!t_ok) {
        if (w == 0) {
          asm volatile("s_waitcnt vmcnt(0)" : "+v"(fla) :: "memory");
          const unsigned ok = __all(fla == tag) ? 1u : 0u;
          if (lane == 0) *(LAS unsigned*)CTL = ok;
        }
        BAR_LGKM();
        t_ok = __builtin_amdgcn_readfirstlane(*(volatile LAS unsigned*)CTL);
        if (t_ok) { LD16Z(tq0, tsrc0, sws, "sc1"); LD16Z(tq1, tsrc1, sws, "sc1"); }
      }
    }
  }
  if (!ISA) asm volatile("" :: "v"(touch));
  STAMP(5);
  FT(18);
  if (active) {
#pragma unroll
    for (int rt = 0; rt < 2; ++rt)
#pragma unroll
      for (int u = 0; u < 2; ++u) {
        v4u o;
        o.x = silu2(acc1[rt][8 * u + 0], acc1[rt][8 * u + 1]);
        o.y = silu2(acc1[rt][8 * u + 2], acc1[rt][8 * u + 3]);
        o.z = silu2(acc1[rt][8 * u + 4], acc1[rt][8 * u + 5]);
        o.w = silu2(acc1[rt][8 * u + 6], acc1[rt][8 * u + 7]);
        const int jb32 = 4 * rb + 2 * rh1 + rt, fs = (f0 >> 4) + 2 * ft + u;
        st16_sc1(sws, WS_H + (unsigned)(((i1 * 8 + jb32) * 112 + fs) * 1024 + lane * 16), o);
      }
  }
  SCHED();
  STAMP(6);
  FT(20);
  const unsigned flh_own = WS_FLH + (unsigned)((i1 * 2 + rb) * 16 + fc) * FS;
  if constexpr (ISA) {
    bar_vm<0>();
    if (tid == 0) flag_store(p.ws, flh_own, tag);
  } else {
    LAS char* I0 = L + 128 * 1024;
    LAS char* I1 = L + 128 * 1024 + 16384;
    LAS char* wscr = L + 7 * 16384 + w * 2048;
    const int g = lane >> 4, l15 = lane & 15;
    const bool special = (m1 == 0);
    bool published = false;
    if (t_ok) {
      wait_vm<4>();
      asm volatile("s_barrier" ::: "memory");
    } else {
      bar_vm<0>();
      if (tid == 0) flag_store(p.ws, flh_own, tag);
      published = true;
      if (w == 0) poll_words(sws, WS_FLA + (unsigned)(m1 >> 5) * 32u * FS, 32, tag, lane);
      asm volatile("s_barrier" ::: "memory");
      LD16Z(tq0, tsrc0, sws, "sc1"); LD16Z(tq1, tsrc1, sws, "sc1");
      wait_vm<0>();
    }
    asm volatile("" : "+v"(tq0), "+v"(tq1) :: "memory");
    {
      const int kap = tid >> 5, ch = tid & 31;
      *(LAS v4u*)(I0 + kap * 512 + ((ch ^ ((kap & 3) << 2)) << 4)) = tq0;
      *(LAS v4u*)(I0 + (kap + 16) * 512 + ((ch ^ ((kap & 3) << 2)) << 4)) = tq1;
    }
    WVal Wd;
    if (special) {
      const int m2nd = 128 * w + 1024;
      asm volatile("" : "+v"(xqr), "+v"(xqi), "+v"(xtr), "+v"(xti));
      w_put(wscr, xqr, xqi, lane);
      asm volatile("s_waitcnt lgkmcnt(0)" ::: "memory");
      WRawL raw2; const int off2 = w_off(m2nd, lane);
      w_get(raw2, wscr, off2, lane, 0); w_get(raw2, wscr, off2, lane, 1); w_get(raw2, wscr, off2, lane, 2); w_get(raw2, wscr, off2, lane, 3);
      Wd = w_unpack(raw2, w_tail(xtr, xti), off2, m2nd, lane);
    }
    BAR_LGKM();
    FT(21);
    LAS char* ta = I0 + (8 * hh + q4) * 512 + (((4 * w + 2 * blk + (p4 >> 1)) ^ (q4 << 2)) << 4) + 8 * (p4 & 1);
    const int n = lane & 31;
    LAS char* so = I1 + n * 512 + 8 * hh;
    {
      f32x16 acc = {};
      acc = mfma32(tr_read2(ta, ta + 2048), tb[6], acc);
      acc = mfma32(tr_read2(ta + 8192, ta + 10240), tb[7], acc);
#pragma unroll
      for (int Gq = 0; Gq < 4; ++Gq) {
        v2u o; o.x = pk2(acc[4 * Gq], acc[4 * Gq + 1]); o.y = pk2(acc[4 * Gq + 2], acc[4 * Gq + 3]);
        *(LAS v2u*)(so + (((4 * w + Gq) ^ (n & 15)) << 4)) = o;
      }
    }
    bar_vm<0>();
    if (!published && tid == 0) flag_store(p.ws, flh_own, tag);
    w2_issue1(G, 4, 0); w2_issue1(G, 4, 1); w2_issue1(G, 5, 0); w2_issue1(G, 5, 1);
    FT(22);
    const f32x4 z4 = {0.f, 0.f, 0.f, 0.f};
    const v4u F16fr = tb[0], F16fi = tb[1];
    v4u F16ir, F16ii;
    F16ir.x = F16fr.x; F16ir.y = F16fr.y; F16ir.z = F16fr.z ^ 0x80008000u; F16ir.w = F16fr.w ^ 0x80008000u;
    F16ii.x = F16fi.x ^ 0x80008000u; F16ii.y = F16fi.y ^ 0x80008000u; F16ii.z = F16fi.z; F16ii.w = F16fi.w;
    f32x4 zr[2], zi[2];
#pragma unroll
    for (int pp = 0; pp < 2; ++pp) {
      const int n0 = 4 * w + 2 * pp, n1 = n0 + 1;
      const int ch = 2 * (4 * g + q4) + (p4 >> 1);
      const v4u a1 = tr_read2(I1 + n0 * 512 + ((ch ^ (n0 & 15)) << 4) + 8 * (p4 & 1), I1 + n1 * 512 + ((ch ^ (n1 & 15)) << 4) + 8 * (p4 & 1));
      const f32x4 dr = mfma16(a1, pp ? tb[2] : F16fr, z4), di = mfma16(a1, pp ? tb[3] : F16fi, z4);
      const v4u b3 = cmul_pack<false>(dr, di, asf4(tb[10 + 2 * pp]), asf4(tb[11 + 2 * pp]));
      zr[pp] = mfma16(F16fr, b3, z4); zi[pp] = mfma16(F16fi, b3, z4);
    }
    v4u a4[2];
    if (!special) {
      a4[0] = cmul_pack<false>(zr[0], zi[0], Wc.r0, Wc.i0);
      a4[1] = cmul_pack<false>(zr[1], zi[1], Wc.r1, Wc.i1);
    } else {
      BAR_LGKM();
      LAS char* Z = I0;
#pragma unroll
      for (int pp = 0; pp < 2; ++pp)
#pragma unroll
        for (int r = 0; r < 4; ++r) { f32x2 v = {zr[pp][r], zi[pp][r]}; *(LAS f32x2*)(Z + ((w * 2 + pp) * 256 + l15 + 16 * (4 * g + r)) * 8) = v; }
      BAR_LGKM();
      const cf om = {cosrev((float)w * 0.0625f), -sinrev((float)w * 0.0625f)};
#pragma unroll
      for (int pp = 0; pp < 2; ++pp) {
        float yr[4], yi[4];
#pragma unroll
        for (int r = 0; r < 4; ++r) {
          const int K = l15 + 16 * (4 * g + r), Kn = pp ? 255 - K : (256 - K) & 255;
          const f32x2 y = *(LAS f32x2*)(Z + ((((8 - w) & 7) * 2 + pp) * 256 + Kn) * 8);
          const float zre = zr[pp][r], zim = zi[pp][r], cr = y[0], ci = -y[1];
          const cf E = {0.5f * (zre + cr), 0.5f * (zim + ci)};
          const cf O = {0.5f * (zim - ci), -0.5f * (zre - cr)};
          const float war = pp ? Wc.r1[r] : Wc.r0[r], wai = pp ? Wc.i1[r] : Wc.i0[r];
          const float wbr = pp ? Wd.r1[r] : Wd.r0[r], wbi = pp ? Wd.i1[r] : Wd.i0[r];
          const cf sp = {war + wbr, wai + wbi}, sm = {war - wbr, wai - wbi};
          const cf t1 = cmulc(sm, om);
          const cf cE = {sp.r - t1.i, sp.i + t1.r};
          const cf t2 = cmul(sm, om);
          const cf cO = {t2.r - sp.i, t2.i + sp.r};
          const cf a = cmul(E, cE), bq = cmul(O, cO);
          yr[r] = 0.5f * (a.r + bq.r); yi[r] = 0.5f * (a.i + bq.i);
        }
        a4[pp].x = pk2(yr[0], yr[1]); a4[pp].y = pk2(yr[2], yr[3]); a4[pp].z = pk2(yi[0], yi[1]); a4[pp].w = pk2(yi[2], yi[3]);
      }
      BAR_LGKM();
    }
    w2_issue1(G, 6, 0); w2_issue1(G, 6, 1); w2_issue1(G, 7, 0); w2_issue1(G, 7, 1);
#pragma unroll
    for (int pp = 0; pp < 2; ++pp) {
      const f32x4 dr = mfma16(a4[pp], F16ir, z4), di = mfma16(a4[pp], F16ii, z4);
      const v4u a5 = cmul_pack<true>(dr, di, asf4(tb[pp ? 14 : 10]), asf4(tb[pp ? 15 : 11]));
      const f32x4 er = mfma16(a5, pp ? tb[4] : F16ir, z4), ei = mfma16(a5, pp ? tb[5] : F16ii, z4);
      const int n0 = 4 * w + 2 * pp, n1 = n0 + 1;
      const int ch = 2 * l15 + (g >> 1);
      v2u o; o.x = pk2(er[0], er[1]); o.y = pk2(er[2], er[3]);
      *(LAS v2u*)(I0 + n0 * 512 + ((ch ^ ((n0 & 3) << 2)) << 4) + 8 * (g & 1)) = o;
      o.x = pk2(ei[0], ei[1]); o.y = pk2(ei[2], ei[3]);
      *(LAS v2u*)(I0 + n1 * 512 + ((ch ^ ((n1 & 3) << 2)) << 4) + 8 * (g & 1)) = o;
    }
    BAR_LGKM();
    FT(23);
    m5a = tb[8]; m5b = tb[9];
  }
  return tag;
}

DI void make_geo(Geo& G, const Params& p, int tz) {
  G.tid = tz; G.lane = G.tid & 63; G.w = __builtin_amdgcn_readfirstlane(G.tid >> 6);
  G.b = blockIdx.x;
  const int Lx = G.b >> 3, x8 = G.b & 7;
  {
    const int a = x8 & 3, h = x8 >> 2, rbi = Lx >> 3, c = Lx & 7;
    G.i = 2 * a + h; G.i1 = 2 * a + (rbi >> 1); G.rb = rbi & 1;
    G.fc = c < 4 ? 4 * h + c : 4 + 4 * h + c;
    G.at = rbi * 4 + c; G.m1 = ((rbi * 4 + c - 4) << 3) | x8;
  }
  G.hh = G.lane >> 5; G.blk = (G.lane >> 4) & 1; G.q4 = (G.lane & 15) >> 2; G.p4 = G.lane & 3;
  G.sx = make_srd(p.x, NS * ND * 2); G.sws = make_srd(p.ws, 16u << 20); G.sw2 = make_srd(p.w2, NF * ND * 2);
  G.jb = Lx >> 3; G.dt = Lx & 7; G.rh = G.w & 1; G.q = G.w >> 1;
  G.j = 64 * G.jb + 32 * G.rh + (G.lane & 31);
  const int rho = 16 * G.rh + (G.lane >> 3), chs = (G.lane & 7) ^ (4 * ((G.lane >> 4) & 1));
  G.vw20 = (unsigned)(rho * 1024 + G.dt * 128 + chs * 16); G.vw21 = G.vw20 + 8 * 1024;
  G.ldsw = uni((unsigned)(G.q * 4096 + G.rh * 2048));
  G.fq0b = uni((unsigned)(768 + 256 * G.q) * 1024u); G.fq1b = uni((unsigned)(192 * G.q) * 1024u);
}

__global__ __launch_bounds__(512, 2) void gm_kernel(Params p) {
  extern __shared__ __attribute__((aligned(16))) char smem[];
  LAS char* L = (LAS char*)smem;
  STAMP(0);
  FT(0);
  Geo G;
  make_geo(G, p, threadIdx.x);
  const bool isA = G.fc < 8;
  v4u gf[4], m5a, m5b;
  unsigned tag;
  if (isA) tag = phase1<true>(p, L, G, m5a, m5b); else tag = phase1<false>(p, L, G, m5a, m5b);
  { int tz = threadIdx.x; asm volatile("" : "+v"(tz)); make_geo(G, p, tz); }
  const int tid = G.tid, lane = G.lane, w = G.w, b = G.b, i = G.i, jb = G.jb, dt = G.dt, rh = G.rh, q = G.q, j = G.j;
  const int hh = G.hh, blk = G.blk, q4 = G.q4, p4 = G.p4;
  const v4u sws = G.sws;
  const unsigned ldsw = G.ldsw;
  const int m1 = G.m1;
  STAMP(7);
  FT(25);
  if (!isA) {
    LAS char* I0 = L + 128 * 1024;
    LAS char* I1 = L + 128 * 1024 + 16384;
    LAS char* ta = I0 + (8 * hh + q4) * 512 + (((4 * w + 2 * blk + (p4 >> 1)) ^ (q4 << 2)) << 4) + 8 * (p4 & 1);
    const int n = lane & 31;
    LAS char* so = I1 + n * 512 + 8 * hh;
    f32x16 acc = {};
    acc = mfma32(tr_read2(ta, ta + 2048), m5a, acc);
    acc = mfma32(tr_read2(ta + 8192, ta + 10240), m5b, acc);
#pragma unroll
    for (int Gq = 0; Gq < 4; ++Gq) {
      v2u o; o.x = pk2(acc[4 * Gq], acc[4 * Gq + 1]); o.y = pk2(acc[4 * Gq + 2], acc[4 * Gq + 3]);
      *(LAS v2u*)(so + (((4 * w + Gq) ^ (n & 15)) << 4)) = o;
    }
    BAR_LGKM();
    const int nn = tid >> 5, ch = tid & 31, ii = (nn >> 1) & 7, d0 = nn & 1;
    const bool special = (m1 == 0);
    const int g0 = special ? 2 * ii : ii, g1 = special ? 2 * ii + 1 : 8 + ii;
    const v4u v0 = lds_ld16(I1 + nn * 512 + ((ch ^ (nn & 15)) << 4));
    const v4u v1 = lds_ld16(I1 + (nn + 16) * 512 + ((ch ^ (nn & 15)) << 4));
    const unsigned ub = WS_U + (unsigned)m1 * 16384u + (unsigned)(d0 * 512 + ch * 16);
    st16_sc1(sws, ub + (unsigned)g0 * 1024u, v0);
    st16_sc1(sws, ub + (unsigned)g1 * 1024u, v1);
    SCHED();
  }
  const int sz = (q4 >> 1) & 1;
  LAS char* ra0 = L + q * 4096 + (4 * hh + q4) * 128 + (((0 ^ (4 * sz)) + 2 * blk + (p4 >> 1)) << 4) + 8 * (p4 & 1);
  LAS char* ra1 = L + q * 4096 + (4 * hh + q4) * 128 + (((4 ^ (4 * sz)) + 2 * blk + (p4 >> 1)) << 4) + 8 * (p4 & 1);
  v4u wp[4][4];
  BAR_LGKM();
#pragma unroll
  for (int t = 0; t < 4; ++t) {
    LAS char* s0 = ra0 + t * 16384; LAS char* s1 = ra1 + t * 16384;
    wp[t][0] = tr_read2(s0, s0 + 1024); wp[t][1] = tr_read2(s1, s1 + 1024);
    wp[t][2] = tr_read2(s0 + 2048, s0 + 3072); wp[t][3] = tr_read2(s1 + 2048, s1 + 3072);
  }
#define W2_8_11() do { w2_issue1(G, 8, 0); w2_issue1(G, 8, 1); w2_issue1(G, 9, 0); w2_issue1(G, 9, 1); w2_issue1(G, 10, 0); w2_issue1(G, 10, 1); w2_issue1(G, 11, 0); w2_issue1(G, 11, 1); } while (0)
  if (isA) { BAR_LGKM(); W2_8_11(); }
  const unsigned flh = WS_FLH + (unsigned)((i * 2 + (jb >> 1)) * 16) * FS;
  if (w == 0) poll_words(sws, flh, 16, tag, lane);
  BAR_LGKM();
  STAMP(8);
  FT(26);
  const unsigned vh = (unsigned)lane * 16u;
  const unsigned hbase = WS_H + (unsigned)((i * 8 + 2 * jb + rh) * 112) * 1024u;
  const unsigned hb0 = uni(hbase + (G.fq0b >> 4)), hb1 = uni(hbase + (G.fq1b >> 4));
  v2u gg;
  v4u hf[14][2];
#define H_ISSUE(t) do { LD16I(hf[t][0], vh, sws, ((t) < 8 ? hb0 : hb1), (2 * ((t) & 7)) * 1024, "sc1"); LD16I(hf[t][1], vh, sws, ((t) < 8 ? hb0 : hb1), (2 * ((t) & 7) + 1) * 1024, "sc1"); } while (0)
  H_ISSUE(0); H_ISSUE(1);
  asm volatile("s_barrier" ::: "memory");
  H_ISSUE(2); H_ISSUE(3); H_ISSUE(4); H_ISSUE(5); H_ISSUE(6);
  LD8Z(gg, WS_GATE + (unsigned)(i * 256 + j) * 8u, sws, "sc1");
  SCHED();
  f32x16 accb[2] = {}, acca[2] = {};
  v4u wf[2][4];
  unsigned fl0, fl1;
  v4u ub0, ub1, ub2, ub3;
  const unsigned vu = WS_U + (unsigned)(((64 * q + 16 * rh + (lane >> 3)) * 8 + i) * 1024 + dt * 128 + ((lane & 7) ^ (4 * ((lane >> 4) & 1))) * 16);
#define G2_BAR(n, t) asm volatile("s_waitcnt vmcnt(" STR(n) ") lgkmcnt(0)\n\ts_barrier" : "+v"(hf[t][0]), "+v"(hf[t][1]) :: "memory")
#define G2_CORE(t, c, n, RD, acc, b0, b1, X0, X1, X2)                                             \
  {                                                                                                 \
    FT(30 + (t));                                                                                   \
    LAS char* s0 = ra0 + (((t) + 1) & 7) * 16384; LAS char* s1 = ra1 + (((t) + 1) & 7) * 16384;    \
    asm volatile("" : "+v"(c[0]), "+v"(c[1]), "+v"(c[2]), "+v"(c[3]));                             \
    asm volatile("" :: "v"(acc[0]), "v"(acc[1]));                                                   \
    SCHED();                                                                                        \
    acc[0] = mfma32(c[0], b0, acc[0]); if (RD) n[0] = tr_read2(s0, s0 + 1024); SCHED();            \
    acc[1] = mfma32(c[1], b0, acc[1]); if (RD) n[1] = tr_read2(s1, s1 + 1024); X0; SCHED();        \
    acc[0] = mfma32(c[2], b1, acc[0]); if (RD) n[2] = tr_read2(s0 + 2048, s0 + 3072); X1; SCHED(); \
    acc[1] = mfma32(c[3], b1, acc[1]); if (RD) n[3] = tr_read2(s1 + 2048, s1 + 3072); X2; SCHED(); \
  }
#define G2_PRE(t, X2) G2_CORE(t, wp[t], wf[0], (t) == 3, accb, hf[t][0], hf[t][1], if (!isA) w2_issue1(G, (t) + 8, 0), if (!isA) w2_issue1(G, (t) + 8, 1), X2)
#define G2_STEP(t, X0, X1, X2) G2_CORE(t, wf[(t) & 1], wf[((t) + 1) & 1], true, accb, hf[t][0], hf[t][1], X0, X1, X2)
  G2_BAR(13, 0);
  if (!isA) { if (tid == 0) flag_store(p.ws, WS_FLB + (unsigned)m1 * FS, tag); STAMP(14); }
  G2_PRE(0, H_ISSUE(7))
  G2_BAR(13, 1); G2_PRE(1, H_ISSUE(8))
  G2_BAR(13, 2); G2_PRE(2, H_ISSUE(9))
  G2_BAR(13, 3); G2_PRE(3, H_ISSUE(10))
  G2_BAR(13, 4); G2_STEP(4, w2_issue1(G, 12, 0), w2_issue1(G, 12, 1), H_ISSUE(11))
  G2_BAR(15, 5); G2_STEP(5, w2_issue1(G, 13, 0), w2_issue1(G, 13, 1), H_ISSUE(12))
  if (w == 0) { LD4Z(fl0, WS_FLB + (unsigned)lane * FS, sws, "sc1"); LD4Z(fl1, WS_FLB + (64u + (unsigned)lane) * FS, sws, "sc1"); }
  G2_BAR(17, 6); G2_STEP(6, NOP, NOP, H_ISSUE(13))
  STAMP(9);
  G2_BAR(16, 7); G2_STEP(7, NOP, NOP, NOP)
  if (w == 0) {
    asm volatile("s_waitcnt vmcnt(2)" : "+v"(fl0), "+v"(fl1) :: "memory");
    if (!__all(fl0 == tag && fl1 == tag)) { poll_words(sws, WS_FLB, 64, tag, lane); poll_words(sws, WS_FLB + 64u * FS, 64, tag, lane); }
  }
  G2_BAR(14, 8);
  STAMP(15);
  LD16Z(ub0, vu, sws, "sc1"); LD16Z(ub1, vu + 8u * 8192u, sws, "sc1"); LD16Z(ub2, vu + 32u * 8192u, sws, "sc1"); LD16Z(ub3, vu + 40u * 8192u, sws, "sc1");
  GRot grot;
  G2_STEP(8, gen_g_init(G, grot), NOP, gen_g_frag<0>(G, grot, gf[0]))
  unsigned e0, e1, e2, e3;
  if (b == 0 && w == 1) {
    LD4Z(e0, WS_FLH + (unsigned)lane * FS, sws, "sc1"); LD4Z(e1, WS_FLH + (64u + (unsigned)lane) * FS, sws, "sc1");
    LD4Z(e2, WS_FLH + (128u + (unsigned)lane) * FS, sws, "sc1"); LD4Z(e3, WS_FLH + (192u + (unsigned)lane) * FS, sws, "sc1");
  }
  G2_BAR(16, 9); G2_STEP(9, NOP, NOP, gen_g_frag<1>(G, grot, gf[1]))
  G2_BAR(14, 10); G2_STEP(10, NOP, NOP, gen_g_frag<2>(G, grot, gf[2]))
  G2_BAR(10, 11); G2_STEP(11, NOP, NOP, gen_g_frag<3>(G, grot, gf[3]))
  G2_BAR(6, 12);
  G2_STEP(12, NOP, NOP, NOP)
  asm volatile("s_waitcnt vmcnt(0)" : "+v"(ub0), "+v"(ub1), "+v"(ub2), "+v"(ub3), "+v"(gg) :: "memory");
  *(LAS v4u*)(L + 6 * 16384 + ldsw + lane * 16) = ub0;
  *(LAS v4u*)(L + 6 * 16384 + ldsw + 1024 + lane * 16) = ub1;
  *(LAS v4u*)(L + 7 * 16384 + ldsw + lane * 16) = ub2;
  *(LAS v4u*)(L + 7 * 16384 + ldsw + 1024 + lane * 16) = ub3;
  G2_BAR(0, 13);
  if (b == 0 && w == 1) {
    asm volatile("" : "+v"(e0), "+v"(e1), "+v"(e2), "+v"(e3));
    if (!__all(e0 == tag && e1 == tag && e2 == tag && e3 == tag)) {
#pragma unroll 1
      for (int u = 0; u < 4; ++u) poll_words(sws, WS_FLH + 64u * FS * u, 64, tag, lane);
    }
    if (lane == 0) flag_store(p.ws, WS_EPOCH, tag);
  }
  G2_STEP(13, NOP, NOP, NOP)
  STAMP(10);
  BAR_LGKM(); G2_CORE(14, wf[0], wf[1], true, acca, gf[0], gf[1], NOP, NOP, NOP)
  BAR_LGKM(); G2_CORE(15, wf[1], wf[0], false, acca, gf[2], gf[3], NOP, NOP, NOP)
  FT(50);
  {
    const float og = asf(gg.y), gs = asf(gg.x) * (1.f / 524288.f);
    const int row = 32 * rh + (lane & 31);
#pragma unroll
    for (int dh = 0; dh < 2; ++dh)
#pragma unroll
      for (int G = 0; G < 4; ++G) {
        f32x4 v;
#pragma unroll
        for (int e = 0; e < 4; ++e) v[e] = __builtin_fmaf(og, accb[dh][4 * G + e], gs * acca[dh][4 * G + e]);
        *(LAS f32x4*)(L + q * 16384 + row * 256 + (((8 * dh + 2 * G + hh) ^ (row & 7)) << 4)) = v;
      }
  }
  BAR_LGKM();
  FT(51);
  {
    const int row = tid >> 3, c8 = tid & 7;
    f32x4 s0 = {0.f, 0.f, 0.f, 0.f}, s1 = s0;
#pragma unroll
    for (int qq = 0; qq < 4; ++qq) {
      s0 += *(LAS f32x4*)(L + qq * 16384 + row * 256 + (((2 * c8) ^ (row & 7)) << 4));
      s1 += *(LAS f32x4*)(L + qq * 16384 + row * 256 + (((2 * c8 + 1) ^ (row & 7)) << 4));
    }
    v4u o; o.x = pk2(s0[0], s0[1]); o.y = pk2(s0[2], s0[3]); o.z = pk2(s1[0], s1[1]); o.w = pk2(s1[2], s1[3]);
    *(GAS v4u*)((GAS char*)p.out + (size_t)(i + 8 * (64 * jb + row)) * 1024 + dt * 128 + c8 * 16) = o;
  }
  STAMP(11);
}

}

extern "C" void kernel_launch(void* const* d_in, const int* in_sizes, int n_in, void* d_out, int out_size, void* d_ws, size_t ws_size,
                              hipStream_t stream) {
  Params p;
  p.x = d_in[0]; p.wr = d_in[1]; p.wi = d_in[2]; p.w1 = d_in[4]; p.w2 = d_in[5]; p.gw = d_in[6];
  p.out = d_out; p.ws = d_ws;
  (void)hipFuncSetAttribute((const void*)gm_kernel, hipFuncAttributeMaxDynamicSharedMemorySize, LDS_BYTES);
  gm_kernel<<<256, 512, LDS_BYTES, stream>>>(p);
}
```

```cpp
#include <hip/hip_runtime.h>
#include <hip/hip_bf16.h>
#include <stdint.h>

namespace {

typedef unsigned v4u __attribute__((ext_vector_type(4)));
typedef unsigned v2u __attribute__((ext_vector_type(2)));
typedef float f32x16 __attribute__((ext_vector_type(16)));
typedef float f32x4 __attribute__((ext_vector_type(4)));
typedef float f32x2 __attribute__((ext_vector_type(2)));
typedef __bf16 bf16x8 __attribute__((ext_vector_type(8)));
typedef __bf16 bf16x2 __attribute__((ext_vector_type(2)));
typedef short s16x4 __attribute__((ext_vector_type(4)));
#define LAS __attribute__((address_space(3)))
#define GAS __attribute__((address_space(1)))
#define DI __device__ __forceinline__

constexpr int NS = 2048, ND = 512, NF = 1792;
constexpr unsigned WS_EPOCH = 0;
constexpr unsigned FS = 64;
constexpr unsigned WS_FLA = 4096;
constexpr unsigned WS_FLH = 16384;
constexpr unsigned WS_FLB = 32768;
constexpr unsigned WS_GATE = 65536;
constexpr unsigned WS_T = 1u << 20;
constexpr unsigned WS_U = 3u << 20;
constexpr unsigned WS_H = 5u << 20;
constexpr unsigned H_BYTES = 8u * 8u * 112u * 1024u;
constexpr int LDS_BYTES = 160 * 1024;
constexpr unsigned kSpinCap = 1u << 20;

struct Params {
  const void* x; const void* wr; const void* wi; const void* w1; const void* w2; const void* gw;
  void* out; void* ws;
};

constexpr double kPi = 3.14159265358979323846;
constexpr double ccos(double x) { double t = 1, s = 1; for (int n = 1; n < 16; ++n) { t *= -x * x / ((2 * n - 1) * (2 * n)); s += t; } return s; }
constexpr double cosn(int e, int n) { e = ((e % n) + n) % n; double x = 2 * kPi * e / n; if (x > kPi) x -= 2 * kPi; return ccos(x); }
constexpr double wre(int e, int n) { return cosn(e, n); }
constexpr double wim(int e, int n) { return -cosn(4 * e - n, 4 * n); }
constexpr unsigned bfb(double v) { float f = (float)v; unsigned u = __builtin_bit_cast(unsigned, f); u += 0x7fffu + ((u >> 16) & 1u); return u >> 16; }
constexpr unsigned bf2(double a, double b) { return bfb(a) | (bfb(b) << 16); }
constexpr unsigned fbits(double v) { return __builtin_bit_cast(unsigned, (float)v); }
struct Tab { unsigned v[16][64][4]; };
constexpr Tab make_tab() {
  Tab t{};
  for (int l = 0; l < 64; ++l) {
    const int g = l >> 4, l15 = l & 15, hh = l >> 5, n = l & 31;
    for (int mtx = 0; mtx < 3; ++mtx) {
      double r[4] = {}, im[4] = {};
      for (int q = 0; q < 4; ++q) {
        const int a = 4 * g + q;
        if (mtx == 0) { r[q] = wre(l15 * a, 16); im[q] = wim(l15 * a, 16); }
        else if (mtx == 1) { r[q] = wre(a * (2 * l15 + 1), 32); im[q] = wim(a * (2 * l15 + 1), 32); }
        else { r[q] = wre(l15 * (2 * a + 1), 32); im[q] = -wim(l15 * (2 * a + 1), 32); }
      }
      t.v[2 * mtx][l][0] = bf2(r[0], r[1]); t.v[2 * mtx][l][1] = bf2(r[2], r[3]); t.v[2 * mtx][l][2] = bf2(-im[0], -im[1]); t.v[2 * mtx][l][3] = bf2(-im[2], -im[3]);
      t.v[2 * mtx + 1][l][0] = bf2(im[0], im[1]); t.v[2 * mtx + 1][l][1] = bf2(im[2], im[3]); t.v[2 * mtx + 1][l][2] = bf2(r[0], r[1]); t.v[2 * mtx + 1][l][3] = bf2(r[2], r[3]);
    }
    {
      const int m2 = n >> 2, pp = (n >> 1) & 1, cp = n & 1;
      double v0[8] = {}, v1[8] = {};
      for (int jj = 0; jj < 8; ++jj) {
        const int ii = (8 * hh + jj) >> 1, d0 = jj & 1;
        const double sg = (d0 & pp) ? -1.0 : 1.0, orr = sg * wre(ii * m2, 8), oi = sg * wim(ii * m2, 8);
        v0[jj] = cp ? oi : orr; v1[jj] = cp ? orr : -oi;
      }
      for (int q = 0; q < 4; ++q) { t.v[6][l][q] = bf2(v0[2 * q], v0[2 * q + 1]); t.v[7][l][q] = bf2(v1[2 * q], v1[2 * q + 1]); }
    }
    {
      const int cq = n >> 4, ii = (n >> 1) & 7, d0 = n & 1;
      for (int s = 0; s < 2; ++s) {
        double v[8] = {};
        for (int jj = 0; jj < 8; ++jj) {
          const int m2k = 4 * s + 2 * hh + (jj >> 2), pk = (jj >> 1) & 1, ck = jj & 1;
          const double sg = (d0 & pk) ? -1.0 : 1.0, orr = sg * wre(ii * m2k, 8), oi = -sg * wim(ii * m2k, 8);
          v[jj] = cq ? (ck ? orr : oi) : (ck ? -oi : orr);
        }
        for (int q = 0; q < 4; ++q) t.v[8 + s][l][q] = bf2(v[2 * q], v[2 * q + 1]);
      }
    }
    for (int q = 0; q < 4; ++q) {
      const int a = 4 * g + q;
      t.v[10][l][q] = fbits(wre(a * (2 * l15), 512));      t.v[11][l][q] = fbits(wim(a * (2 * l15), 512));
      t.v[12][l][q] = fbits(wre(a * (2 * l15 + 1), 512));  t.v[13][l][q] = fbits(wim(a * (2 * l15 + 1), 512));
      t.v[14][l][q] = fbits(wre(l15 * (2 * a + 1), 512));  t.v[15][l][q] = fbits(wim(l15 * (2 * a + 1), 512));
    }
  }
  return t;
}
__device__ const Tab kTab = make_tab();

constexpr unsigned STAMP_OFF = 512 * 1024;
#define STAMP(k) do { if ((k) == 0) { asm volatile("s_memrealtime %0\n\ts_waitcnt lgkmcnt(0)" : "=s"(pad_t) :: "memory"); } } while (0)
#define FT(k) do {} while (0)

DI v4u make_srd(const void* p, unsigned bytes) {
  const unsigned long long a = (unsigned long long)p;
  v4u r; r.x = (unsigned)a; r.y = (unsigned)(a >> 32) & 0xffffu; r.z = bytes; r.w = 0x00020000u;
  return r;
}
template <unsigned LDSI, unsigned SOI> DI void dma16(v4u srd, unsigned voff, unsigned ldsb, unsigned sob) {
  unsigned tmp;
  asm volatile("s_add_u32 m0, %3, %5\n\ts_add_u32 %0, %4, %6\n\tbuffer_load_dwordx4 %1, %2, %0 offen lds"
               : "=&s"(tmp) : "v"(voff), "s"(srd), "s"(ldsb), "s"(sob), "i"(LDSI), "i"(SOI) : "memory", "scc");
}
template <unsigned LDSI, unsigned SOI> DI void dma16z(v4u srd, unsigned voff, unsigned ldsb) {
  unsigned tmp;
  asm volatile("s_add_u32 m0, %3, %4\n\ts_mov_b32 %0, %5\n\tbuffer_load_dwordx4 %1, %2, %0 offen lds"
               : "=&s"(tmp) : "v"(voff), "s"(srd), "s"(ldsb), "i"(LDSI), "i"(SOI) : "memory", "scc");
}
DI unsigned uni(unsigned v) { return (unsigned)__builtin_amdgcn_readfirstlane((int)v); }
#define LD16(dst, voffv, srd, soffv, flags) asm volatile("buffer_load_dwordx4 %0, %1, %2, %3 offen " flags : "=v"(dst) : "v"(voffv), "s"(srd), "s"(soffv) : "memory")
#define LD16I(dst, voffv, srd, sob, soi, flags) do { unsigned tmp_; asm volatile("s_add_u32 %1, %4, %5\n\tbuffer_load_dwordx4 %0, %2, %3, %1 offen " flags : "=v"(dst), "=&s"(tmp_) : "v"(voffv), "s"(srd), "s"(sob), "i"(soi) : "memory", "scc"); } while (0)
#define LD16Z(dst, voffv, srd, flags) asm volatile("buffer_load_dwordx4 %0, %1, %2, 0 offen " flags : "=v"(dst) : "v"(voffv), "s"(srd) : "memory")
#define LD4Z(dst, voffv, srd, flags) asm volatile("buffer_load_dword %0, %1, %2, 0 offen " flags : "=v"(dst) : "v"(voffv), "s"(srd) : "memory")
#define LD2Z(dst, voffv, srd, flags) asm volatile("buffer_load_ushort %0, %1, %2, 0 offen " flags : "=v"(dst) : "v"(voffv), "s"(srd) : "memory")
#define LD8Z(dst, voffv, srd, flags) asm volatile("buffer_load_dwordx2 %0, %1, %2, 0 offen " flags : "=v"(dst) : "v"(voffv), "s"(srd) : "memory")
#define LD8(dst, voffv, srd, soffv, flags) asm volatile("buffer_load_dwordx2 %0, %1, %2, %3 offen " flags : "=v"(dst) : "v"(voffv), "s"(srd), "s"(soffv) : "memory")
#define LD4(dst, voffv, srd, soffv, flags) asm volatile("buffer_load_dword %0, %1, %2, %3 offen " flags : "=v"(dst) : "v"(voffv), "s"(srd), "s"(soffv) : "memory")
#define LD2(dst, voffv, srd, soffv, flags) asm volatile("buffer_load_ushort %0, %1, %2, %3 offen " flags : "=v"(dst) : "v"(voffv), "s"(srd), "s"(soffv) : "memory")
#define XSTR(x) #x
#define STR(x) XSTR(x)
#define BAR_VM(n) asm volatile("s_waitcnt vmcnt(" STR(n) ") lgkmcnt(0)\n\ts_barrier" ::: "memory")
#define BAR_LGKM() asm volatile("s_waitcnt lgkmcnt(0)\n\ts_barrier" ::: "memory")
#define SCHED() __builtin_amdgcn_sched_barrier(0)
template <int N> DI void bar_vm() { asm volatile("s_waitcnt vmcnt(%0) lgkmcnt(0)\n\ts_barrier" :: "i"(N) : "memory"); }
template <int N> DI void wait_vm() { asm volatile("s_waitcnt vmcnt(%0)" :: "i"(N) : "memory"); }

DI unsigned pk2(float a, float b) { f32x2 p = {a, b}; return __builtin_bit_cast(unsigned, __builtin_convertvector(p, bf16x2)); }
DI float bflo(unsigned u) { return __builtin_bit_cast(float, u << 16); }
DI float bfhi(unsigned u) { return __builtin_bit_cast(float, u & 0xffff0000u); }
DI float asf(unsigned u) { return __builtin_bit_cast(float, u); }
DI f32x16 mfma32(v4u a, v4u b, f32x16 c) {
  return __builtin_amdgcn_mfma_f32_32x32x16_bf16(__builtin_bit_cast(bf16x8, a), __builtin_bit_cast(bf16x8, b), c, 0, 0, 0);
}
DI f32x4 mfma16(v4u a, v4u b, f32x4 c) {
  return __builtin_amdgcn_mfma_f32_16x16x32_bf16(__builtin_bit_cast(bf16x8, a), __builtin_bit_cast(bf16x8, b), c, 0, 0, 0);
}
DI v2u tr_read(LAS char* p) { return __builtin_bit_cast(v2u, __builtin_amdgcn_ds_read_tr16_b64_v4i16((LAS s16x4*)p)); }
DI v4u tr_read2(LAS char* p0, LAS char* p1) { v2u a = tr_read(p0), b = tr_read(p1); v4u r; r.x = a.x; r.y = a.y; r.z = b.x; r.w = b.y; return r; }
DI v4u lds_ld16(LAS char* p) { return *(LAS v4u*)p; }
DI float cosrev(float r) { return __builtin_amdgcn_cosf(r); }
DI float sinrev(float r) { return __builtin_amdgcn_sinf(r); }
DI __amdgpu_buffer_rsrc_t as_rsrc(v4u srd) { return __builtin_amdgcn_make_buffer_rsrc((void*)(((unsigned long long)srd.y << 32) | srd.x), 0, srd.z, 0x00020000); }
DI void st16_sc1(v4u srd, unsigned voff, v4u v) { __builtin_amdgcn_raw_buffer_store_b128(v, as_rsrc(srd), voff, 0, 16); }
DI void flag_store(void* ws, unsigned off, unsigned tag) { __hip_atomic_store((GAS unsigned*)((GAS char*)ws + off), tag, __ATOMIC_RELAXED, __HIP_MEMORY_SCOPE_AGENT); }
template <int CTRL, int RMASK> DI float dpp_add(float v) { return v + asf((unsigned)__builtin_amdgcn_update_dpp(0, (int)__builtin_bit_cast(unsigned, v), CTRL, RMASK, 0xf, true)); }
DI void poll_words(v4u sws, unsigned off, int n, unsigned tag, int lane) {
  const unsigned vo = off + FS * (unsigned)(lane < n ? lane : 0);
#pragma unroll 1
  for (unsigned spins = 0; spins < kSpinCap; ++spins) {
    unsigned f;
    asm volatile("buffer_load_dword %0, %1, %2, 0 offen sc1\n\ts_waitcnt vmcnt(0)" : "=&v"(f) : "v"(vo), "s"(sws) : "memory");
    if (__all(f == tag)) return;
    __builtin_amdgcn_s_sleep(4);
  }
}

constexpr unsigned A_XIMG = 64 * 1024, A_FIMG = 128 * 1024;
DI void phaseA_genF(LAS char* L, int i, int mt, int w, int lane) {
  const int mh = w >> 2, sg = w & 3, n = lane & 31, hh = lane >> 5;
  const int m1c = 64 * mt + 32 * mh + n, m1 = m1c >> 1, c = m1c & 1;
  const int j0 = 64 * sg + 32 * hh;
  const float ph = c ? 0.25f : 0.f;
  const float k2 = 2.f * cosrev((float)(8 * m1) * (1.f / 2048.f));
  float vm = cosrev((float)(((i + 8 * (j0 - 1)) * m1) & 2047) * (1.f / 2048.f) + ph);
  float v0 = cosrev((float)(((i + 8 * j0) * m1) & 2047) * (1.f / 2048.f) + ph);
  const unsigned cst = c ? 0x3F800000u : 0x00003F80u;
  const bool sp = m1c < 2;
#pragma unroll
  for (int s = 0; s < 4; ++s) {
    float e[8];
#pragma unroll
    for (int q = 0; q < 8; ++q) { e[q] = v0; const float vn = __builtin_fmaf(k2, v0, -vm); vm = v0; v0 = vn; }
    v4u f; f.x = pk2(e[0], e[1]); f.y = pk2(e[2], e[3]); f.z = pk2(e[4], e[5]); f.w = pk2(e[6], e[7]);
    if (mt == 0 && w < 4) { f.x = sp ? cst : f.x; f.y = sp ? cst : f.y; f.z = sp ? cst : f.z; f.w = sp ? cst : f.w; }
    *(LAS v4u*)(L + A_FIMG + (mh * 16 + sg * 4 + s) * 1024 + lane * 16) = f;
  }
}

struct Frag1 { v4u a[4], b0[4], b1[4]; };
DI void g1_read(Frag1& f, LAS char* wa, LAS char* xb0, LAS char* xb1, LAS char* xb2, LAS char* xb3, int slot) {
  const int so = slot * 32768;
  f.a[0] = tr_read2(wa + so, wa + so + 1024);              f.b0[0] = lds_ld16(xb0 + so); f.b1[0] = lds_ld16(xb0 + so + 4096);
  f.a[1] = tr_read2(wa + so + 4096, wa + so + 5120);       f.b0[1] = lds_ld16(xb1 + so); f.b1[1] = lds_ld16(xb1 + so + 4096);
  f.a[2] = tr_read2(wa + so + 8192, wa + so + 9216);       f.b0[2] = lds_ld16(xb2 + so); f.b1[2] = lds_ld16(xb2 + so + 4096);
  f.a[3] = tr_read2(wa + so + 12288, wa + so + 13312);     f.b0[3] = lds_ld16(xb3 + so); f.b1[3] = lds_ld16(xb3 + so + 4096);
}
DI unsigned silu2(float a, float b) {
  const f32x2 h = {a, b}, t = h * -1.44269504088896341f;
  f32x2 e = {__builtin_amdgcn_exp2f(t[0]), __builtin_amdgcn_exp2f(t[1])};
  e = e + 1.f;
  const f32x2 r = {__builtin_amdgcn_rcpf(e[0]), __builtin_amdgcn_rcpf(e[1])};
  return __builtin_bit_cast(unsigned, __builtin_convertvector(h * r, bf16x2));
}

DI f32x2 lo2(const f32x4& v) { return __builtin_shufflevector(v, v, 0, 1); }
DI f32x2 hi2(const f32x4& v) { return __builtin_shufflevector(v, v, 2, 3); }
DI unsigned pk2v(f32x2 p) { return __builtin_bit_cast(unsigned, __builtin_convertvector(p, bf16x2)); }
DI f32x4 asf4(v4u v) { return __builtin_bit_cast(f32x4, v); }
template <bool CONJ> DI v4u cmul_pack(const f32x4& re, const f32x4& im, const f32x4& tr, const f32x4& ti) {
  const f32x2 r0 = lo2(re), r1 = hi2(re), i0 = lo2(im), i1 = hi2(im), c0 = lo2(tr), c1 = hi2(tr), s0 = lo2(ti), s1 = hi2(ti);
  f32x2 a0, a1, b0, b1;
  if (CONJ) {
    a0 = __builtin_elementwise_fma(r0, c0, i0 * s0); a1 = __builtin_elementwise_fma(r1, c1, i1 * s1);
    b0 = __builtin_elementwise_fma(i0, c0, -(r0 * s0)); b1 = __builtin_elementwise_fma(i1, c1, -(r1 * s1));
  } else {
    a0 = __builtin_elementwise_fma(r0, c0, -(i0 * s0)); a1 = __builtin_elementwise_fma(r1, c1, -(i1 * s1));
    b0 = __builtin_elementwise_fma(r0, s0, i0 * c0); b1 = __builtin_elementwise_fma(r1, s1, i1 * c1);
  }
  v4u r; r.x = pk2v(a0); r.y = pk2v(a1); r.z = pk2v(b0); r.w = pk2v(b1); return r;
}
struct cf { float r, i; };
DI cf cmul(cf a, cf b) { return {a.r * b.r - a.i * b.i, a.r * b.i + a.i * b.r}; }
DI cf cmulc(cf a, cf b) { return {a.r * b.r + a.i * b.i, a.i * b.r - a.r * b.i}; }
DI unsigned w_src(int m, int lane) {
  const int mm = (NS - m) & (NS - 1);
  const int e = lane < 32 ? m * 257 - (m & 1) : mm * 257 + (m & 1);
  return (unsigned)(e * 2 + (lane & 31) * 16);
}
DI unsigned w_tail_src(int m, int lane) {
  const int mm = (NS - m) & (NS - 1), l = lane & 3;
  return (unsigned)((l < 2 ? m * 257 + 255 + l : mm * 257 + (l - 2) * 256) * 2);
}
struct WTail { unsigned r255, r256, rm0, rm256, i255, i256, im0, im256; };
DI WTail w_tail(unsigned tr, unsigned ti) {
  return {(unsigned)__builtin_amdgcn_readlane((int)tr, 0), (unsigned)__builtin_amdgcn_readlane((int)tr, 1), (unsigned)__builtin_amdgcn_readlane((int)tr, 2), (unsigned)__builtin_amdgcn_readlane((int)tr, 3),
          (unsigned)__builtin_amdgcn_readlane((int)ti, 0), (unsigned)__builtin_amdgcn_readlane((int)ti, 1), (unsigned)__builtin_amdgcn_readlane((int)ti, 2), (unsigned)__builtin_amdgcn_readlane((int)ti, 3)};
}
struct WVal { f32x4 r0, i0, r1, i1; };
struct WRawL { unsigned r[4][2], i[4][2]; };
DI void w_put(LAS char* scr, v4u qr, v4u qi, int lane) { *(LAS v4u*)(scr + lane * 16) = qr; *(LAS v4u*)(scr + 1024 + lane * 16) = qi; }
DI int w_off(int m, int lane) { const int k1 = lane & 15, g = lane >> 4, odd = m & 1; return lane < 32 ? (2 * k1 + 128 * g + odd) * 2 : 512 + (511 - 2 * k1 - 128 * g - odd) * 2; }
DI void w_get(WRawL& o, LAS char* scr, int off, int lane, int r) {
  LAS unsigned* a = (LAS unsigned*)(scr + ((off + (lane < 32 ? 64 : -64) * r) & ~3));
  o.r[r][0] = a[0]; o.r[r][1] = a[1]; o.i[r][0] = a[256]; o.i[r][1] = a[257];
}
DI WVal w_unpack(const WRawL& x, const WTail& t, int off, int m, int lane) {
  const bool lo = lane < 32;
  const unsigned sh = (off & 2) * 8;
  unsigned rr[4], ii[4];
#pragma unroll
  for (int r = 0; r < 4; ++r) { rr[r] = __builtin_amdgcn_alignbit(x.r[r][1], x.r[r][0], sh); ii[r] = __builtin_amdgcn_alignbit(x.i[r][1], x.i[r][0], sh); }
  if (m & 1) { rr[3] = lane == 31 ? (rr[3] & 0xffffu) | (t.r255 << 16) : rr[3]; ii[3] = lane == 31 ? (ii[3] & 0xffffu) | (t.i255 << 16) : ii[3]; }
  else { rr[0] = lane == 32 ? (rr[0] & 0xffffu) | (t.rm256 << 16) : rr[0]; ii[0] = lane == 32 ? (ii[0] & 0xffffu) | (t.im256 << 16) : ii[0]; }
  const unsigned s0 = lo ? 16u : 0u, s1 = lo ? 0u : 16u, sg = lo ? 0u : 0x80000000u;
  WVal o;
#pragma unroll
  for (int r = 0; r < 4; ++r) {
    o.r0[r] = asf((rr[r] << s0) & 0xffff0000u); o.r1[r] = asf((rr[r] << s1) & 0xffff0000u);
    o.i0[r] = asf(((ii[r] << s0) & 0xffff0000u) ^ sg); o.i1[r] = asf(((ii[r] << s1) & 0xffff0000u) ^ sg);
  }
  const bool av = (lane & 31) == 0;
  const float xr = bflo(lo ? t.rm0 : t.r256), xi = asf(((lo ? t.im0 : t.i256) << 16) ^ (sg ^ 0x80000000u));
  o.r0[0] = av ? 0.5f * (o.r0[0] + xr) : o.r0[0];
  o.i0[0] = av ? 0.5f * (o.i0[0] + xi) : o.i0[0];
  return o;
}

struct Geo {
  int tid, lane, w, b, i, i1, at, m1, rb, fc, hh, blk, q4, p4;
  int jb, dt, rh, q, j;
  v4u sx, sws, sw2;
  unsigned vw20, vw21, ldsw, fq0b, fq1b;
};
template <int T, int U> DI void w2_dma(const v4u& sw2, unsigned vw20, unsigned vw21, unsigned ldsw, unsigned fq0, unsigned fq1) {
  dma16<(T & 7) * 16384u + U * 1024u, (T < 8 ? T : T - 8) * 32768u>(sw2, U ? vw21 : vw20, ldsw, T < 8 ? fq0 : fq1);
}
#define w2_issue1(g, t, u) (w2_dma<t, u>((g).sw2, (g).vw20, (g).vw21, (g).ldsw, (g).fq0b, (g).fq1b))
struct GRot { float c1, s1, c4, s4, c, s; };
DI void gen_g_init(const Geo& g, GRot& r) {
  const int srow = g.i + 8 * g.j;
  const int mb = 32 * g.q + 2 * g.hh;
  const float r1 = (float)(srow & 2047) * (1.f / 2048.f), r4 = (float)((4 * srow) & 2047) * (1.f / 2048.f), r0 = (float)((mb * srow) & 2047) * (1.f / 2048.f);
  r.c1 = cosrev(r1); r.s1 = sinrev(r1); r.c4 = cosrev(r4); r.s4 = sinrev(r4); r.c = cosrev(r0); r.s = sinrev(r0);
}
template <int S> DI void gen_g_frag(const Geo& g, GRot& r, v4u& f) {
#pragma unroll
  for (int h = 0; h < 2; ++h) {
    const float cb = __builtin_fmaf(r.c, r.c1, -(r.s * r.s1)), sb = __builtin_fmaf(r.s, r.c1, r.c * r.s1);
    unsigned w0 = pk2(r.c, -r.s), w1 = pk2(cb, -sb);
    if (S == 0 && h == 0) w0 = (g.q == 0 && g.hh == 0) ? ((g.j & 1) ? 0x3F800000u : 0x00003F80u) : w0;
    if (h) { f.z = w0; f.w = w1; } else { f.x = w0; f.y = w1; }
    const float cn = __builtin_fmaf(r.c, r.c4, -(r.s * r.s4)), sn = __builtin_fmaf(r.s, r.c4, r.c * r.s4);
    r.c = cn; r.s = sn;
  }
  asm volatile("" : "+v"(f));
}
template <int N0, int N1, int NR> DI void bar_w(int w) { if (w == 0) bar_vm<N0>(); else if (w == 1) bar_vm<N1>(); else bar_vm<NR>(); }
#define NOP do {} while (0)

#define TOUCH_POS 1
#define TLBW 0
#define TOUCH() do {                                                                                                      \
    const int gl = G.b * 256 + (w & 3) * 64 + lane;                                                                         \
    const char* base_ = gl < 14336 ? (const char*)p.w2 : gl < 28672 ? (const char*)p.w1 - 14336 * 128 : gl < 36896 ? (const char*)p.wr - 28672 * 128           \
                      : gl < 45120 ? (const char*)p.wi - 36896 * 128 : (const char*)p.x - 45120 * 128;                     \
    const char* ad_ = base_ + (size_t)(gl < 61504 ? gl : 45120) * 128;                                                      \
    if (w >= 4) ad_ = TLBW ? (w == 7 ? (const char*)p.out + lane * 32768 : (const char*)p.ws + (size_t)((w - 4) * 64 + lane + 8) * 65536 + 32768 - 128) : (const char*)p.x; \
    asm volatile("global_load_dword %0, %1, off" : "=v"(touch) : "v"(ad_) : "memory");                                     \
  } while (0)
template <bool ISA>
DI unsigned phase1(long long& pad_t, const Params& p, LAS char* L, const Geo& G, v4u& m5a, v4u& m5b
) {
  const int tid = G.tid, lane = G.lane, w = G.w, i = G.i, i1 = G.i1, rb = G.rb, fc = G.fc, hh = G.hh, blk = G.blk, q4 = G.q4, p4 = G.p4;
  const v4u sx = G.sx, sws = G.sws, sw1 = make_srd(p.w1, ND * NF * 2);
  unsigned touch;
  unsigned ep; v4u gx, gv;
  const int jg = 128 * rb + 8 * fc + w;
  LD16Z(gx, (unsigned)(lane * 16 + (i1 + 8 * jg) * 1024), sx, "");
  LD16Z(gv, (unsigned)lane * 16u, make_srd(p.gw, ND * 2), "");
  LD4Z(ep, WS_EPOCH, sws, "sc1");
  const int f0 = ISA ? 96 * fc : 768 + 128 * (fc - 8), nf = ISA ? 96 : 128;
  const int mt = G.at >> 2, dt4 = G.at & 3;
  const int m1 = G.m1;
  if (ISA) {
    const unsigned voff = (unsigned)(lane >> 4) * 8192u + (unsigned)(((lane & 15) ^ ((lane >> 4) << 2)) << 4);
    const unsigned xl = uni(A_XIMG + (unsigned)w * 8192u), xs = uni((unsigned)i * 1024u + (unsigned)dt4 * 256u + (unsigned)w * 262144u);
    dma16<0, 0>(sx, voff, xl, xs); dma16<1024, 32768>(sx, voff, xl, xs); dma16<2048, 65536>(sx, voff, xl, xs); dma16<3072, 98304>(sx, voff, xl, xs);
    dma16<4096, 131072>(sx, voff, xl, xs); dma16<5120, 163840>(sx, voff, xl, xs); dma16<6144, 196608>(sx, voff, xl, xs); dma16<7168, 229376>(sx, voff, xl, xs);
  }
  unsigned vx0, vx1, vw0, vw1;
  {
    const int r0 = 16 * w + (lane >> 3), pos = lane & 7, r1 = r0 + 8;
    vx0 = (unsigned)(i1 * 1024 + (128 * rb + r0) * 8192 + ((pos ^ ((r0 >> 1) & 7)) << 4));
    vx1 = (unsigned)(i1 * 1024 + (128 * rb + r1) * 8192 + ((pos ^ ((r1 >> 1) & 7)) << 4));
    const int k0 = 8 * w + (lane >> 4), chunk = (lane & 15) ^ ((lane >> 4) << 2);
    const bool ok = chunk * 8 < nf;
    vw0 = ok ? (unsigned)(k0 * 3584 + f0 * 2 + chunk * 16) : 0x80000000u;
    vw1 = ok ? vw0 + 4u * 3584u : 0x80000000u;
  }
  const unsigned g1l = uni((unsigned)w * 2048u);
#define g1_dma(kt, u) do {                                                                                         \
    if ((u) == 0) dma16z<((kt) & 3) * 32768u, (kt) * 128u>(sx, vx0, g1l);                                           \
    if ((u) == 1) dma16z<((kt) & 3) * 32768u + 1024u, (kt) * 128u>(sx, vx1, g1l);                                   \
    if ((u) == 2) dma16z<((kt) & 3) * 32768u + 16384u, (kt) * (64u * 3584u)>(sw1, vw0, g1l);                        \
    if ((u) == 3) dma16z<((kt) & 3) * 32768u + 17408u, (kt) * (64u * 3584u)>(sw1, vw1, g1l); } while (0)
#define g1_issue(kt) do { g1_dma(kt, 0); g1_dma(kt, 1); g1_dma(kt, 2); g1_dma(kt, 3); } while (0)
  if (ISA) {
    phaseA_genF(L, i, mt, w, lane);
    g1_issue(0); g1_issue(1);
  } else {
    g1_issue(0); asm volatile("s_barrier" ::: "memory");
    g1_issue(1); asm volatile("s_barrier" ::: "memory");
    g1_issue(2); asm volatile("s_barrier" ::: "memory");
    g1_issue(3);
  }
  TOUCH();
  asm volatile("s_waitcnt vmcnt(17)" : "+v"(ep), "+v"(gx), "+v"(gv) :: "memory");
  const unsigned tag = (unsigned)__builtin_amdgcn_readfirstlane((int)ep) + 1u;
  float gate_g, gate_og;
  {
    float z = 0.f;
#pragma unroll
    for (int q = 0; q < 4; ++q) {
      const unsigned pr = pk2(bflo(gx[q]) * bflo(gv[q]), bfhi(gx[q]) * bfhi(gv[q]));
      z += bflo(pr) + bfhi(pr);
    }
    z = dpp_add<0x111, 0xf>(z); z = dpp_add<0x112, 0xf>(z); z = dpp_add<0x114, 0xf>(z); z = dpp_add<0x118, 0xf>(z);
    z = dpp_add<0x142, 0xa>(z); z = dpp_add<0x143, 0xc>(z);
    const float zb = bflo(pk2(z, 0.f));
    const float g = bflo(pk2(__builtin_amdgcn_rcpf(1.f + __builtin_amdgcn_exp2f(zb * -1.44269504088896341f)), 0.f));
    gate_g = asf((unsigned)__builtin_amdgcn_readlane((int)__builtin_bit_cast(unsigned, g), 63));
    gate_og = bflo(pk2(1.f - gate_g, 0.f));
  }
  if (ISA) {
    const int dq = w & 3, mh = w >> 2;
    LAS char* xa = L + A_XIMG + (32 * hh + q4) * 256 + (((4 * dq + 2 * blk + (p4 >> 1)) ^ (q4 << 2)) << 4) + 8 * (p4 & 1);
    LAS char* fb = L + A_FIMG + mh * 16384 + lane * 16;
    const int srow = 32 * mh + (lane & 31);
    LAS char* stw = L + A_FIMG + srow * 256 + 8 * hh;
    const int rrow = tid >> 3;
    LAS char* str = L + A_FIMG + rrow * 256;
    const unsigned tdst = WS_T + (unsigned)(((64 * mt + rrow) * 8 + i) * 1024 + dt4 * 256 + (tid & 7) * 16);
    if (w < 4) bar_vm<9>(); else BAR_LGKM();
    STAMP(1);
    f32x16 acc = {};
    v4u fa[2][4], fbv[2][4];
#pragma unroll
    for (int s = 0; s < 4; ++s) { fa[0][s] = tr_read2(xa + 8 * s * 256, xa + 8 * s * 256 + 1024); fbv[0][s] = lds_ld16(fb + s * 1024); }
#pragma unroll
    for (int g = 0; g < 4; ++g) {
      acc = mfma32(fa[g & 1][0], fbv[g & 1][0], acc);
      SCHED();
      if (g == 1) { if (w >= 4) bar_vm<9>(); else BAR_LGKM(); }
      if (g < 3) {
#pragma unroll
        for (int s = 0; s < 4; ++s) {
          const int ro = (64 * (g + 1) + 8 * s) * 256;
          fa[(g + 1) & 1][s] = tr_read2(xa + ro, xa + ro + 1024); fbv[(g + 1) & 1][s] = lds_ld16(fb + (4 * (g + 1) + s) * 1024);
        }
      }
      SCHED();
#pragma unroll
      for (int s = 1; s < 4; ++s) { acc = mfma32(fa[g & 1][s], fbv[g & 1][s], acc); if (g == 1 && s == 1) { g1_dma(2, 0); g1_dma(2, 1); } if (g == 1 && s == 2) { g1_dma(2, 2); g1_dma(2, 3); } SCHED(); }
    }
    BAR_LGKM();
    STAMP(2);
    g1_issue(3);
#pragma unroll
    for (int Gq = 0; Gq < 4; ++Gq) {
      v2u o; o.x = pk2(acc[4 * Gq], acc[4 * Gq + 1]); o.y = pk2(acc[4 * Gq + 2], acc[4 * Gq + 3]);
      *(LAS v2u*)(stw + (((4 * dq + Gq) ^ (srow & 15)) << 4)) = o;
    }
    BAR_LGKM();
    {
      const v4u v0 = lds_ld16(str + (((tid & 7) ^ (rrow & 15)) << 4)), v1 = lds_ld16(str + ((((tid & 7) + 8) ^ (rrow & 15)) << 4));
      st16_sc1(sws, tdst, v0);
      st16_sc1(sws, tdst + 128u, v1);
    }
    SCHED();
  }
  const int ft = w & 3, rh1 = w >> 2;
  const bool active = ft * 32 < nf;
  f32x16 acc1[2] = {};
  v4u wqr, wqi, tq0, tq1;
  unsigned wtr, wti, xtr, xti;
  v4u xqr, xqi;
  WVal Wc;
  v4u tb[16];
  unsigned fla;
  int t_ok = 0, t_first = 0; (void)t_first;
  const v4u swr = make_srd(p.wr, NS * 257 * 2), swi = make_srd(p.wi, NS * 257 * 2);
  const int mW = m1 == 0 ? 128 * w : m1 + 256 * w;
  unsigned tsrc0 = 0, tsrc1 = 0;
  const unsigned flaoff = WS_FLA + (unsigned)(((m1 >> 5) & 3) * 32 + (lane & 31)) * FS;
  LAS char* CTL = L + 128 * 1024;
  {
    LAS char* wa = L + 16384 + (8 * hh + q4) * 256 + (((4 * ft + 2 * blk + (p4 >> 1)) ^ (q4 << 2)) << 4) + 8 * (p4 & 1);
    const int r = 64 * rh1 + (lane & 31), fz = (r >> 1) & 7;
    LAS char* xb0 = L + r * 128 + (((0 + hh) ^ fz) << 4);
    LAS char* xb1 = L + r * 128 + (((2 + hh) ^ fz) << 4);
    LAS char* xb2 = L + r * 128 + (((4 + hh) ^ fz) << 4);
    LAS char* xb3 = L + r * 128 + (((6 + hh) ^ fz) << 4);
    Frag1 fr[2];
    bar_vm<ISA ? 15 : 13>();
    STAMP(3);
    if (lane == 0) { v2u gv2; gv2.x = __builtin_bit_cast(unsigned, gate_g); gv2.y = __builtin_bit_cast(unsigned, gate_og); *(LAS v2u*)(CTL + 64 + w * 8) = gv2; }
    g1_read(fr[0], wa, xb0, xb1, xb2, xb3, 0);
#define PIN1(c) asm volatile("" : "+v"(c.a[0]), "+v"(c.a[1]), "+v"(c.a[2]), "+v"(c.a[3]), "+v"(c.b0[0]), "+v"(c.b0[1]), "+v"(c.b0[2]), "+v"(c.b0[3]), \
                             "+v"(c.b1[0]), "+v"(c.b1[1]), "+v"(c.b1[2]), "+v"(c.b1[3]))
#define G1_STEP(kt, X0, X1, X2, X3, X4, X5, X6, X7)                                                                   \
    {                                                                                                                   \
      FT(10 + (kt));                                                                                                    \
      Frag1& c = fr[(kt) & 1];                                                                                          \
      Frag1& n = fr[((kt) + 1) & 1];                                                                                    \
      const int so = (((kt) + 1) & 3) * 32768;                                                                          \
      PIN1(c);                                                                                                          \
      asm volatile("" :: "v"(acc1[0]), "v"(acc1[1]));                                                                   \
      SCHED();                                                                                                          \
      acc1[0] = mfma32(c.a[0], c.b0[0], acc1[0]); if ((kt) < 7) n.a[0] = tr_read2(wa + so, wa + so + 1024); X0; SCHED(); \
      acc1[1] = mfma32(c.a[0], c.b1[0], acc1[1]); if ((kt) < 7) { n.b0[0] = lds_ld16(xb0 + so); n.b1[0] = lds_ld16(xb0 + so + 4096); } X1; SCHED(); \
      acc1[0] = mfma32(c.a[1], c.b0[1], acc1[0]); if ((kt) < 7) n.a[1] = tr_read2(wa + so + 4096, wa + so + 5120); X2; SCHED(); \
      acc1[1] = mfma32(c.a[1], c.b1[1], acc1[1]); if ((kt) < 7) { n.b0[1] = lds_ld16(xb1 + so); n.b1[1] = lds_ld16(xb1 + so + 4096); } X3; SCHED(); \
      acc1[0] = mfma32(c.a[2], c.b0[2], acc1[0]); if ((kt) < 7) n.a[2] = tr_read2(wa + so + 8192, wa + so + 9216); X4; SCHED(); \
      acc1[1] = mfma32(c.a[2], c.b1[2], acc1[1]); if ((kt) < 7) { n.b0[2] = lds_ld16(xb2 + so); n.b1[2] = lds_ld16(xb2 + so + 4096); } X5; SCHED(); \
      acc1[0] = mfma32(c.a[3], c.b0[3], acc1[0]); if ((kt) < 7) n.a[3] = tr_read2(wa + so + 12288, wa + so + 13312); X6; SCHED(); \
      acc1[1] = mfma32(c.a[3], c.b1[3], acc1[1]); if ((kt) < 7) { n.b0[3] = lds_ld16(xb3 + so); n.b1[3] = lds_ld16(xb3 + so + 4096); } X7; SCHED(); \
    }
#define GATE_STORE() do { if (w == 2 && lane < 8) { const v2u gv2 = *(LAS v2u*)(CTL + 64 + lane * 8);                       \
      __builtin_amdgcn_raw_buffer_store_b64(gv2, as_rsrc(sws), WS_GATE + (unsigned)(i1 * 256 + 128 * rb + 8 * fc + lane) * 8u, 0, 16); } } while (0)
#define STEP8 G1_STEP
    if constexpr (ISA) {
      bar_vm<11>();
      STAMP(4);
      STEP8(0, NOP, NOP, NOP, NOP, NOP, NOP, NOP, NOP)
      bar_vm<0>();
      if (tid == 0) flag_store(p.ws, WS_FLA + (unsigned)(mt * 32 + i * 4 + dt4) * FS, tag);
      STAMP(13);
      STEP8(1, g1_dma(4, 0), g1_dma(4, 1), g1_dma(4, 2), g1_dma(4, 3), g1_dma(5, 0), g1_dma(5, 1), g1_dma(5, 2), g1_dma(5, 3))
      BAR_LGKM();
      GATE_STORE();
      STEP8(2, NOP, NOP, NOP, NOP, g1_dma(6, 0), g1_dma(6, 1), g1_dma(6, 2), g1_dma(6, 3))
      bar_vm<8>();
      STEP8(3, NOP, NOP, NOP, NOP, g1_dma(7, 0), g1_dma(7, 1), g1_dma(7, 2), g1_dma(7, 3))
      bar_vm<8>();
      STAMP(12);
      STEP8(4, NOP, NOP, NOP, NOP, w2_issue1(G, 0, 0), w2_issue1(G, 0, 1), w2_issue1(G, 1, 0), w2_issue1(G, 1, 1))
      bar_vm<8>();
      STEP8(5, NOP, NOP, NOP, NOP, w2_issue1(G, 2, 0), w2_issue1(G, 2, 1), w2_issue1(G, 3, 0), w2_issue1(G, 3, 1))
      bar_vm<8>();
      STEP8(6, NOP, NOP, NOP, NOP, w2_issue1(G, 4, 0), w2_issue1(G, 4, 1), w2_issue1(G, 5, 0), w2_issue1(G, 5, 1))
      BAR_LGKM();
    } else {
      const v4u stab = make_srd(&kTab, sizeof(Tab));
      const unsigned wsrc = w_src(mW, lane);
      {
        const int kap = tid >> 5, ii = (kap >> 1) & 7, d0 = kap & 1;
        const int g0 = m1 == 0 ? 2 * ii : ii, g1 = m1 == 0 ? 2 * ii + 1 : 8 + ii;
        const unsigned tb = WS_T + (unsigned)m1 * 16384u + (unsigned)(d0 * 512 + (tid & 31) * 16);
        tsrc0 = tb + (unsigned)g0 * 1024u; tsrc1 = tb + (unsigned)g1 * 1024u;
      }
      const unsigned vw20z = G.vw20 - 16u * 1024u, vw21z = G.vw21 - 16u * 1024u, ldswz = uni(G.ldsw - 2048u);
#define W2B(t, u) do { if (w != 0) w2_issue1(G, t, u); } while (0)
#define W2Z(t, u) do { if (w == 1) w2_dma<t, u>(G.sw2, vw20z, vw21z, ldswz, G.fq0b, G.fq1b); } while (0)
      bar_vm<9>();
      STAMP(4);
      const unsigned wts = w_tail_src(mW, lane);
      if (m1 == 0) {
        const unsigned ws2 = w_src(128 * w + 1024, lane), wt2 = w_tail_src(128 * w + 1024, lane);
        LD16Z(xqr, ws2, swr, ""); LD16Z(xqi, ws2, swi, ""); LD2Z(xtr, wt2, swr, ""); LD2Z(xti, wt2, swi, "");
      }
      STEP8(0, LD16Z(wqr, wsrc, swr, ""), LD16Z(wqi, wsrc, swi, ""), LD2Z(wtr, wts, swr, ""), LD2Z(wti, wts, swi, ""), g1_dma(4, 0), g1_dma(4, 1), g1_dma(4, 2), g1_dma(4, 3))
      bar_vm<13>();
      STEP8(1, NOP, NOP, NOP, NOP, g1_dma(5, 0), g1_dma(5, 1), g1_dma(5, 2), g1_dma(5, 3))
      bar_vm<13>();
      GATE_STORE();
      STEP8(2, NOP, NOP, NOP, NOP, g1_dma(6, 0), g1_dma(6, 1), g1_dma(6, 2), g1_dma(6, 3))
      bar_vm<8>();
      STEP8(3, NOP, NOP, (dma16<128u * 1024u + 16384u, 0>(stab, (unsigned)lane * 16u, g1l, g1l)), (dma16<128u * 1024u + 17408u, 1024>(stab, (unsigned)lane * 16u, g1l, g1l)),
            g1_dma(7, 0), g1_dma(7, 1), g1_dma(7, 2), g1_dma(7, 3))
      bar_vm<10>();
      STAMP(12);
      LAS char* wscr0 = L + 128 * 1024 + w * 2048;
      const int woff = w_off(mW, lane);
      asm volatile("" : "+v"(wqr), "+v"(wqi), "+v"(wtr), "+v"(wti));
      w_put(wscr0, wqr, wqi, lane);
      STEP8(4, W2Z(0, 0), W2Z(0, 1), W2Z(1, 0), W2Z(1, 1), W2B(0, 0), W2B(0, 1), W2B(1, 0), W2B(1, 1))
      bar_w<6, 14, 10>(w);
      WRawL wraw;
      w_get(wraw, wscr0, woff, lane, 0); w_get(wraw, wscr0, woff, lane, 1); w_get(wraw, wscr0, woff, lane, 2); w_get(wraw, wscr0, woff, lane, 3);
      STEP8(5, W2Z(2, 0), W2Z(2, 1), W2Z(3, 0), W2Z(3, 1), W2B(2, 0), W2B(2, 1), W2B(3, 0), W2B(3, 1))
      bar_w<0, 16, 8>(w);
      Wc = w_unpack(wraw, w_tail(wtr, wti), woff, mW, lane);
      STEP8(6, if (w == 0) LD4Z(fla, flaoff, sws, "sc1"), NOP, NOP, NOP, NOP, NOP, NOP, NOP)
      if (w == 0) {
        asm volatile("s_waitcnt vmcnt(0)" : "+v"(fla) :: "memory");
        const unsigned ok = __all(fla == tag) ? 1u : 0u;
        if (lane == 0) *(LAS unsigned*)CTL = ok;
      }
      BAR_LGKM();
      t_ok = __builtin_amdgcn_readfirstlane(*(volatile LAS unsigned*)CTL);
      t_first = t_ok;
    }
    if constexpr (ISA) {
      STEP8(7, NOP, NOP, NOP, NOP, w2_issue1(G, 6, 0), w2_issue1(G, 6, 1), w2_issue1(G, 7, 0), w2_issue1(G, 7, 1))
    } else {
      LAS char* TB = L + 128 * 1024 + 16384 + lane * 16;
#define TBR(f) (tb[f] = lds_ld16(TB + (f) * 1024), tb[(f) + 1] = lds_ld16(TB + ((f) + 1) * 1024))
      STEP8(7, { if (t_ok) LD16Z(tq0, tsrc0, sws, "sc1"); TBR(6); }, { if (t_ok) LD16Z(tq1, tsrc1, sws, "sc1"); TBR(0); },
            { if (!t_ok && w == 0) LD4Z(fla, flaoff, sws, "sc1"); TBR(10); }, TBR(2), TBR(12), TBR(14), TBR(4), TBR(8))
      if (!t_ok) {
        if (w == 0) {
          asm volatile("s_waitcnt vmcnt(0)" : "+v"(fla) :: "memory");
          const unsigned ok = __all(fla == tag) ? 1u : 0u;
          if (lane == 0) *(LAS unsigned*)CTL = ok;
        }
        BAR_LGKM();
        t_ok = __builtin_amdgcn_readfirstlane(*(volatile LAS unsigned*)CTL);
        if (t_ok) { LD16Z(tq0, tsrc0, sws, "sc1"); LD16Z(tq1, tsrc1, sws, "sc1"); }
      }
    }
  }
  asm volatile("" :: "v"(touch));
  STAMP(5);
  FT(18);
  if (active) {
#pragma unroll
    for (int rt = 0; rt < 2; ++rt)
#pragma unroll
      for (int u = 0; u < 2; ++u) {
        v4u o;
        o.x = silu2(acc1[rt][8 * u + 0], acc1[rt][8 * u + 1]);
        o.y = silu2(acc1[rt][8 * u + 2], acc1[rt][8 * u + 3]);
        o.z = silu2(acc1[rt][8 * u + 4], acc1[rt][8 * u + 5]);
        o.w = silu2(acc1[rt][8 * u + 6], acc1[rt][8 * u + 7]);
        const int jb32 = 4 * rb + 2 * rh1 + rt, fs = (f0 >> 4) + 2 * ft + u;
        st16_sc1(sws, WS_H + (unsigned)(((i1 * 8 + jb32) * 112 + fs) * 1024 + lane * 16), o);
      }
  }
  SCHED();
  STAMP(6);
  FT(20);
  const unsigned flh_own = WS_FLH + (unsigned)((i1 * 2 + rb) * 16 + fc) * FS;
  if constexpr (ISA) {
    bar_vm<0>();
    if (tid == 0) flag_store(p.ws, flh_own, tag);
  } else {
    LAS char* I0 = L + 128 * 1024;
    LAS char* I1 = L + 128 * 1024 + 16384;
    LAS char* wscr = L + 7 * 16384 + w * 2048;
    const int g = lane >> 4, l15 = lane & 15;
    const bool special = (m1 == 0);
    bool published = false;
    if (t_ok) {
      wait_vm<4>();
      asm volatile("s_barrier" ::: "memory");
    } else {
      bar_vm<0>();
      if (tid == 0) flag_store(p.ws, flh_own, tag);
      published = true;
      if (w == 0) poll_words(sws, WS_FLA + (unsigned)(m1 >> 5) * 32u * FS, 32, tag, lane);
      asm volatile("s_barrier" ::: "memory");
      LD16Z(tq0, tsrc0, sws, "sc1"); LD16Z(tq1, tsrc1, sws, "sc1");
      wait_vm<0>();
    }
    asm volatile("" : "+v"(tq0), "+v"(tq1) :: "memory");
    {
      const int kap = tid >> 5, ch = tid & 31;
      *(LAS v4u*)(I0 + kap * 512 + ((ch ^ ((kap & 3) << 2)) << 4)) = tq0;
      *(LAS v4u*)(I0 + (kap + 16) * 512 + ((ch ^ ((kap & 3) << 2)) << 4)) = tq1;
    }
    WVal Wd;
    if (special) {
      const int m2nd = 128 * w + 1024;
      asm volatile("" : "+v"(xqr), "+v"(xqi), "+v"(xtr), "+v"(xti));
      w_put(wscr, xqr, xqi, lane);
      asm volatile("s_waitcnt lgkmcnt(0)" ::: "memory");
      WRawL raw2; const int off2 = w_off(m2nd, lane);
      w_get(raw2, wscr, off2, lane, 0); w_get(raw2, wscr, off2, lane, 1); w_get(raw2, wscr, off2, lane, 2); w_get(raw2, wscr, off2, lane, 3);
      Wd = w_unpack(raw2, w_tail(xtr, xti), off2, m2nd, lane);
    }
    BAR_LGKM();
    FT(21);
    LAS char* ta = I0 + (8 * hh + q4) * 512 + (((4 * w + 2 * blk + (p4 >> 1)) ^ (q4 << 2)) << 4) + 8 * (p4 & 1);
    const int n = lane & 31;
    LAS char* so = I1 + n * 512 + 8 * hh;
    {
      f32x16 acc = {};
      acc = mfma32(tr_read2(ta, ta + 2048), tb[6], acc);
      acc = mfma32(tr_read2(ta + 8192, ta + 10240), tb[7], acc);
#pragma unroll
      for (int Gq = 0; Gq < 4; ++Gq) {
        v2u o; o.x = pk2(acc[4 * Gq], acc[4 * Gq + 1]); o.y = pk2(acc[4 * Gq + 2], acc[4 * Gq + 3]);
        *(LAS v2u*)(so + (((4 * w + Gq) ^ (n & 15)) << 4)) = o;
      }
    }
    bar_vm<0>();
    if (!published && tid == 0) flag_store(p.ws, flh_own, tag);
    w2_issue1(G, 4, 0); w2_issue1(G, 4, 1); w2_issue1(G, 5, 0); w2_issue1(G, 5, 1);
    FT(22);
    const f32x4 z4 = {0.f, 0.f, 0.f, 0.f};
    const v4u F16fr = tb[0], F16fi = tb[1];
    v4u F16ir, F16ii;
    F16ir.x = F16fr.x; F16ir.y = F16fr.y; F16ir.z = F16fr.z ^ 0x80008000u; F16ir.w = F16fr.w ^ 0x80008000u;
    F16ii.x = F16fi.x ^ 0x80008000u; F16ii.y = F16fi.y ^ 0x80008000u; F16ii.z = F16fi.z; F16ii.w = F16fi.w;
    f32x4 zr[2], zi[2];
#pragma unroll
    for (int pp = 0; pp < 2; ++pp) {
      const int n0 = 4 * w + 2 * pp, n1 = n0 + 1;
      const int ch = 2 * (4 * g + q4) + (p4 >> 1);
      const v4u a1 = tr_read2(I1 + n0 * 512 + ((ch ^ (n0 & 15)) << 4) + 8 * (p4 & 1), I1 + n1 * 512 + ((ch ^ (n1 & 15)) << 4) + 8 * (p4 & 1));
      const f32x4 dr = mfma16(a1, pp ? tb[2] : F16fr, z4), di = mfma16(a1, pp ? tb[3] : F16fi, z4);
      const v4u b3 = cmul_pack<false>(dr, di, asf4(tb[10 + 2 * pp]), asf4(tb[11 + 2 * pp]));
      zr[pp] = mfma16(F16fr, b3, z4); zi[pp] = mfma16(F16fi, b3, z4);
    }
    v4u a4[2];
    if (!special) {
      a4[0] = cmul_pack<false>(zr[0], zi[0], Wc.r0, Wc.i0);
      a4[1] = cmul_pack<false>(zr[1], zi[1], Wc.r1, Wc.i1);
    } else {
      BAR_LGKM();
      LAS char* Z = I0;
#pragma unroll
      for (int pp = 0; pp < 2; ++pp)
#pragma unroll
        for (int r = 0; r < 4; ++r) { f32x2 v = {zr[pp][r], zi[pp][r]}; *(LAS f32x2*)(Z + ((w * 2 + pp) * 256 + l15 + 16 * (4 * g + r)) * 8) = v; }
      BAR_LGKM();
      const cf om = {cosrev((float)w * 0.0625f), -sinrev((float)w * 0.0625f)};
#pragma unroll
      for (int pp = 0; pp < 2; ++pp) {
        float yr[4], yi[4];
#pragma unroll
        for (int r = 0; r < 4; ++r) {
          const int K = l15 + 16 * (4 * g + r), Kn = pp ? 255 - K : (256 - K) & 255;
          const f32x2 y = *(LAS f32x2*)(Z + ((((8 - w) & 7) * 2 + pp) * 256 + Kn) * 8);
          const float zre = zr[pp][r], zim = zi[pp][r], cr = y[0], ci = -y[1];
          const cf E = {0.5f * (zre + cr), 0.5f * (zim + ci)};
          const cf O = {0.5f * (zim - ci), -0.5f * (zre - cr)};
          const float war = pp ? Wc.r1[r] : Wc.r0[r], wai = pp ? Wc.i1[r] : Wc.i0[r];
          const float wbr = pp ? Wd.r1[r] : Wd.r0[r], wbi = pp ? Wd.i1[r] : Wd.i0[r];
          const cf sp = {war + wbr, wai + wbi}, sm = {war - wbr, wai - wbi};
          const cf t1 = cmulc(sm, om);
          const cf cE = {sp.r - t1.i, sp.i + t1.r};
          const cf t2 = cmul(sm, om);
          const cf cO = {t2.r - sp.i, t2.i + sp.r};
          const cf a = cmul(E, cE), bq = cmul(O, cO);
          yr[r] = 0.5f * (a.r + bq.r); yi[r] = 0.5f * (a.i + bq.i);
        }
        a4[pp].x = pk2(yr[0], yr[1]); a4[pp].y = pk2(yr[2], yr[3]); a4[pp].z = pk2(yi[0], yi[1]); a4[pp].w = pk2(yi[2], yi[3]);
      }
      BAR_LGKM();
    }
    w2_issue1(G, 6, 0); w2_issue1(G, 6, 1); w2_issue1(G, 7, 0); w2_issue1(G, 7, 1);
#pragma unroll
    for (int pp = 0; pp < 2; ++pp) {
      const f32x4 dr = mfma16(a4[pp], F16ir, z4), di = mfma16(a4[pp], F16ii, z4);
      const v4u a5 = cmul_pack<true>(dr, di, asf4(tb[pp ? 14 : 10]), asf4(tb[pp ? 15 : 11]));
      const f32x4 er = mfma16(a5, pp ? tb[4] : F16ir, z4), ei = mfma16(a5, pp ? tb[5] : F16ii, z4);
      const int n0 = 4 * w + 2 * pp, n1 = n0 + 1;
      const int ch = 2 * l15 + (g >> 1);
      v2u o; o.x = pk2(er[0], er[1]); o.y = pk2(er[2], er[3]);
      *(LAS v2u*)(I0 + n0 * 512 + ((ch ^ ((n0 & 3) << 2)) << 4) + 8 * (g & 1)) = o;
      o.x = pk2(ei[0], ei[1]); o.y = pk2(ei[2], ei[3]);
      *(LAS v2u*)(I0 + n1 * 512 + ((ch ^ ((n1 & 3) << 2)) << 4) + 8 * (g & 1)) = o;
    }
    BAR_LGKM();
    FT(23);
    m5a = tb[8]; m5b = tb[9];
  }
  return tag;
}

DI void make_geo(Geo& G, const Params& p, int tz) {
  G.tid = tz; G.lane = G.tid & 63; G.w = __builtin_amdgcn_readfirstlane(G.tid >> 6);
  G.b = blockIdx.x;
  const int Lx = G.b >> 3, x8 = G.b & 7;
  {
    const int a = x8 & 3, h = x8 >> 2, rbi = Lx >> 3, c = Lx & 7;
    G.i = 2 * a + h; G.i1 = 2 * a + (rbi >> 1); G.rb = rbi & 1;
    G.fc = c < 4 ? 4 * h + c : 4 + 4 * h + c;
    G.at = rbi * 4 + c; G.m1 = ((rbi * 4 + c - 4) << 3) | x8;
  }
  G.hh = G.lane >> 5; G.blk = (G.lane >> 4) & 1; G.q4 = (G.lane & 15) >> 2; G.p4 = G.lane & 3;
  G.sx = make_srd(p.x, NS * ND * 2); G.sws = make_srd(p.ws, 16u << 20); G.sw2 = make_srd(p.w2, NF * ND * 2);
  G.jb = Lx >> 3; G.dt = Lx & 7; G.rh = G.w & 1; G.q = G.w >> 1;
  G.j = 64 * G.jb + 32 * G.rh + (G.lane & 31);
  const int rho = 16 * G.rh + (G.lane >> 3), chs = (G.lane & 7) ^ (4 * ((G.lane >> 4) & 1));
  G.vw20 = (unsigned)(rho * 1024 + G.dt * 128 + chs * 16); G.vw21 = G.vw20 + 8 * 1024;
  G.ldsw = uni((unsigned)(G.q * 4096 + G.rh * 2048));
  G.fq0b = uni((unsigned)(768 + 256 * G.q) * 1024u); G.fq1b = uni((unsigned)(192 * G.q) * 1024u);
}

__global__ __launch_bounds__(512, 2) void gm_kernel(Params p) {
  extern __shared__ __attribute__((aligned(16))) char smem[];
  LAS char* L = (LAS char*)smem;
  long long pad_t = 0;
  STAMP(0);
  FT(0);
  Geo G;
  make_geo(G, p, threadIdx.x);
  const bool isA = G.fc < 8;
  v4u gf[4], m5a, m5b;
  unsigned tag;
  if (isA) tag = phase1<true>(pad_t, p, L, G, m5a, m5b); else tag = phase1<false>(pad_t, p, L, G, m5a, m5b);
  { int tz = threadIdx.x; asm volatile("" : "+v"(tz)); make_geo(G, p, tz); }
  const int tid = G.tid, lane = G.lane, w = G.w, b = G.b, i = G.i, jb = G.jb, dt = G.dt, rh = G.rh, q = G.q, j = G.j;
  const int hh = G.hh, blk = G.blk, q4 = G.q4, p4 = G.p4;
  const v4u sws = G.sws;
  const unsigned ldsw = G.ldsw;
  const int m1 = G.m1;
  STAMP(7);
  FT(25);
  if (!isA) {
    LAS char* I0 = L + 128 * 1024;
    LAS char* I1 = L + 128 * 1024 + 16384;
    LAS char* ta = I0 + (8 * hh + q4) * 512 + (((4 * w + 2 * blk + (p4 >> 1)) ^ (q4 << 2)) << 4) + 8 * (p4 & 1);
    const int n = lane & 31;
    LAS char* so = I1 + n * 512 + 8 * hh;
    f32x16 acc = {};
    acc = mfma32(tr_read2(ta, ta + 2048), m5a, acc);
    acc = mfma32(tr_read2(ta + 8192, ta + 10240), m5b, acc);
#pragma unroll
    for (int Gq = 0; Gq < 4; ++Gq) {
      v2u o; o.x = pk2(acc[4 * Gq], acc[4 * Gq + 1]); o.y = pk2(acc[4 * Gq + 2], acc[4 * Gq + 3]);
      *(LAS v2u*)(so + (((4 * w + Gq) ^ (n & 15)) << 4)) = o;
    }
    BAR_LGKM();
    const int nn = tid >> 5, ch = tid & 31, ii = (nn >> 1) & 7, d0 = nn & 1;
    const bool special = (m1 == 0);
    const int g0 = special ? 2 * ii : ii, g1 = special ? 2 * ii + 1 : 8 + ii;
    const v4u v0 = lds_ld16(I1 + nn * 512 + ((ch ^ (nn & 15)) << 4));
    const v4u v1 = lds_ld16(I1 + (nn + 16) * 512 + ((ch ^ (nn & 15)) << 4));
    const unsigned ub = WS_U + (unsigned)m1 * 16384u + (unsigned)(d0 * 512 + ch * 16);
    st16_sc1(sws, ub + (unsigned)g0 * 1024u, v0);
    st16_sc1(sws, ub + (unsigned)g1 * 1024u, v1);
    SCHED();
  }
  const int sz = (q4 >> 1) & 1;
  LAS char* ra0 = L + q * 4096 + (4 * hh + q4) * 128 + (((0 ^ (4 * sz)) + 2 * blk + (p4 >> 1)) << 4) + 8 * (p4 & 1);
  LAS char* ra1 = L + q * 4096 + (4 * hh + q4) * 128 + (((4 ^ (4 * sz)) + 2 * blk + (p4 >> 1)) << 4) + 8 * (p4 & 1);
  v4u wp[4][4];
  BAR_LGKM();
#pragma unroll
  for (int t = 0; t < 4; ++t) {
    LAS char* s0 = ra0 + t * 16384; LAS char* s1 = ra1 + t * 16384;
    wp[t][0] = tr_read2(s0, s0 + 1024); wp[t][1] = tr_read2(s1, s1 + 1024);
    wp[t][2] = tr_read2(s0 + 2048, s0 + 3072); wp[t][3] = tr_read2(s1 + 2048, s1 + 3072);
  }
#define W2_8_11() do { w2_issue1(G, 8, 0); w2_issue1(G, 8, 1); w2_issue1(G, 9, 0); w2_issue1(G, 9, 1); w2_issue1(G, 10, 0); w2_issue1(G, 10, 1); w2_issue1(G, 11, 0); w2_issue1(G, 11, 1); } while (0)
  if (isA) { BAR_LGKM(); W2_8_11(); }
  const unsigned flh = WS_FLH + (unsigned)((i * 2 + (jb >> 1)) * 16) * FS;
  if (w == 0) poll_words(sws, flh, 16, tag, lane);
  BAR_LGKM();
  STAMP(8);
  FT(26);
  const unsigned vh = (unsigned)lane * 16u;
  const unsigned hbase = WS_H + (unsigned)((i * 8 + 2 * jb + rh) * 112) * 1024u;
  const unsigned hb0 = uni(hbase + (G.fq0b >> 4)), hb1 = uni(hbase + (G.fq1b >> 4));
  v2u gg;
  v4u hf[14][2];
#define H_ISSUE(t) do { LD16I(hf[t][0], vh, sws, ((t) < 8 ? hb0 : hb1), (2 * ((t) & 7)) * 1024, "sc1"); LD16I(hf[t][1], vh, sws, ((t) < 8 ? hb0 : hb1), (2 * ((t) & 7) + 1) * 1024, "sc1"); } while (0)
  H_ISSUE(0); H_ISSUE(1);
  asm volatile("s_barrier" ::: "memory");
  H_ISSUE(2); H_ISSUE(3); H_ISSUE(4); H_ISSUE(5); H_ISSUE(6);
  LD8Z(gg, WS_GATE + (unsigned)(i * 256 + j) * 8u, sws, "sc1");
  SCHED();
  f32x16 accb[2] = {}, acca[2] = {};
  v4u wf[2][4];
  unsigned fl0, fl1;
  v4u ub0, ub1, ub2, ub3;
  const unsigned vu = WS_U + (unsigned)(((64 * q + 16 * rh + (lane >> 3)) * 8 + i) * 1024 + dt * 128 + ((lane & 7) ^ (4 * ((lane >> 4) & 1))) * 16);
#define G2_BAR(n, t) asm volatile("s_waitcnt vmcnt(" STR(n) ") lgkmcnt(0)\n\ts_barrier" : "+v"(hf[t][0]), "+v"(hf[t][1]) :: "memory")
#define G2_CORE(t, c, n, RD, acc, b0, b1, X0, X1, X2)                                             \
  {                                                                                                 \
    FT(30 + (t));                                                                                   \
    LAS char* s0 = ra0 + (((t) + 1) & 7) * 16384; LAS char* s1 = ra1 + (((t) + 1) & 7) * 16384;    \
    asm volatile("" : "+v"(c[0]), "+v"(c[1]), "+v"(c[2]), "+v"(c[3]));                             \
    asm volatile("" :: "v"(acc[0]), "v"(acc[1]));                                                   \
    SCHED();                                                                                        \
    acc[0] = mfma32(c[0], b0, acc[0]); if (RD) n[0] = tr_read2(s0, s0 + 1024); SCHED();            \
    acc[1] = mfma32(c[1], b0, acc[1]); if (RD) n[1] = tr_read2(s1, s1 + 1024); X0; SCHED();        \
    acc[0] = mfma32(c[2], b1, acc[0]); if (RD) n[2] = tr_read2(s0 + 2048, s0 + 3072); X1; SCHED(); \
    acc[1] = mfma32(c[3], b1, acc[1]); if (RD) n[3] = tr_read2(s1 + 2048, s1 + 3072); X2; SCHED(); \
  }
#define G2_PRE(t, X2) G2_CORE(t, wp[t], wf[0], (t) == 3, accb, hf[t][0], hf[t][1], if (!isA) w2_issue1(G, (t) + 8, 0), if (!isA) w2_issue1(G, (t) + 8, 1), X2)
#define G2_STEP(t, X0, X1, X2) G2_CORE(t, wf[(t) & 1], wf[((t) + 1) & 1], true, accb, hf[t][0], hf[t][1], X0, X1, X2)
  G2_BAR(13, 0);
  if (!isA) { if (tid == 0) flag_store(p.ws, WS_FLB + (unsigned)m1 * FS, tag); STAMP(14); }
  G2_PRE(0, H_ISSUE(7))
  G2_BAR(13, 1); G2_PRE(1, H_ISSUE(8))
  G2_BAR(13, 2); G2_PRE(2, H_ISSUE(9))
  G2_BAR(13, 3); G2_PRE(3, H_ISSUE(10))
  G2_BAR(13, 4); G2_STEP(4, w2_issue1(G, 12, 0), w2_issue1(G, 12, 1), H_ISSUE(11))
  G2_BAR(15, 5); G2_STEP(5, w2_issue1(G, 13, 0), w2_issue1(G, 13, 1), H_ISSUE(12))
  if (w == 0) { LD4Z(fl0, WS_FLB + (unsigned)lane * FS, sws, "sc1"); LD4Z(fl1, WS_FLB + (64u + (unsigned)lane) * FS, sws, "sc1"); }
  G2_BAR(17, 6); G2_STEP(6, NOP, NOP, H_ISSUE(13))
  STAMP(9);
  G2_BAR(16, 7); G2_STEP(7, NOP, NOP, NOP)
  if (w == 0) {
    asm volatile("s_waitcnt vmcnt(2)" : "+v"(fl0), "+v"(fl1) :: "memory");
    if (!__all(fl0 == tag && fl1 == tag)) { poll_words(sws, WS_FLB, 64, tag, lane); poll_words(sws, WS_FLB + 64u * FS, 64, tag, lane); }
  }
  G2_BAR(14, 8);
  STAMP(15);
  LD16Z(ub0, vu, sws, "sc1"); LD16Z(ub1, vu + 8u * 8192u, sws, "sc1"); LD16Z(ub2, vu + 32u * 8192u, sws, "sc1"); LD16Z(ub3, vu + 40u * 8192u, sws, "sc1");
  GRot grot;
  G2_STEP(8, gen_g_init(G, grot), NOP, gen_g_frag<0>(G, grot, gf[0]))
  unsigned e0, e1, e2, e3;
  if (b == 0 && w == 1) {
    LD4Z(e0, WS_FLH + (unsigned)lane * FS, sws, "sc1"); LD4Z(e1, WS_FLH + (64u + (unsigned)lane) * FS, sws, "sc1");
    LD4Z(e2, WS_FLH + (128u + (unsigned)lane) * FS, sws, "sc1"); LD4Z(e3, WS_FLH + (192u + (unsigned)lane) * FS, sws, "sc1");
  }
  G2_BAR(16, 9); G2_STEP(9, NOP, NOP, gen_g_frag<1>(G, grot, gf[1]))
  G2_BAR(14, 10); G2_STEP(10, NOP, NOP, gen_g_frag<2>(G, grot, gf[2]))
  G2_BAR(10, 11); G2_STEP(11, NOP, NOP, gen_g_frag<3>(G, grot, gf[3]))
  G2_BAR(6, 12);
  G2_STEP(12, NOP, NOP, NOP)
  asm volatile("s_waitcnt vmcnt(0)" : "+v"(ub0), "+v"(ub1), "+v"(ub2), "+v"(ub3), "+v"(gg) :: "memory");
  *(LAS v4u*)(L + 6 * 16384 + ldsw + lane * 16) = ub0;
  *(LAS v4u*)(L + 6 * 16384 + ldsw + 1024 + lane * 16) = ub1;
  *(LAS v4u*)(L + 7 * 16384 + ldsw + lane * 16) = ub2;
  *(LAS v4u*)(L + 7 * 16384 + ldsw + 1024 + lane * 16) = ub3;
  G2_BAR(0, 13);
  if (b == 0 && w == 1) {
    asm volatile("" : "+v"(e0), "+v"(e1), "+v"(e2), "+v"(e3));
    if (!__all(e0 == tag && e1 == tag && e2 == tag && e3 == tag)) {
#pragma unroll 1
      for (int u = 0; u < 4; ++u) poll_words(sws, WS_FLH + 64u * FS * u, 64, tag, lane);
    }
    if (lane == 0) flag_store(p.ws, WS_EPOCH, tag);
  }
  G2_STEP(13, NOP, NOP, NOP)
  STAMP(10);
  BAR_LGKM(); G2_CORE(14, wf[0], wf[1], true, acca, gf[0], gf[1], NOP, NOP, NOP)
  BAR_LGKM(); G2_CORE(15, wf[1], wf[0], false, acca, gf[2], gf[3], NOP, NOP, NOP)
  FT(50);
  {
    const float og = asf(gg.y), gs = asf(gg.x) * (1.f / 524288.f);
    const int row = 32 * rh + (lane & 31);
#pragma unroll
    for (int dh = 0; dh < 2; ++dh)
#pragma unroll
      for (int G = 0; G < 4; ++G) {
        f32x4 v;
#pragma unroll
        for (int e = 0; e < 4; ++e) v[e] = __builtin_fmaf(og, accb[dh][4 * G + e], gs * acca[dh][4 * G + e]);
        *(LAS f32x4*)(L + q * 16384 + row * 256 + (((8 * dh + 2 * G + hh) ^ (row & 7)) << 4)) = v;
      }
  }
  BAR_LGKM();
  FT(51);
  {
    const int row = tid >> 3, c8 = tid & 7;
    f32x4 s0 = {0.f, 0.f, 0.f, 0.f}, s1 = s0;
#pragma unroll
    for (int qq = 0; qq < 4; ++qq) {
      s0 += *(LAS f32x4*)(L + qq * 16384 + row * 256 + (((2 * c8) ^ (row & 7)) << 4));
      s1 += *(LAS f32x4*)(L + qq * 16384 + row * 256 + (((2 * c8 + 1) ^ (row & 7)) << 4));
    }
    v4u o; o.x = pk2(s0[0], s0[1]); o.y = pk2(s0[2], s0[3]); o.z = pk2(s1[0], s1[1]); o.w = pk2(s1[2], s1[3]);
    *(GAS v4u*)((GAS char*)p.out + (size_t)(i + 8 * (64 * jb + row)) * 1024 + dt * 128 + c8 * 16) = o;
  }
  STAMP(11);
  while (__builtin_amdgcn_s_memrealtime() - pad_t < 3000) {}
}

}

extern "C" void kernel_launch(void* const* d_in, const int* in_sizes, int n_in, void* d_out, int out_size, void* d_ws, size_t ws_size,
                              hipStream_t stream) {
  Params p;
  p.x = d_in[0]; p.wr = d_in[1]; p.wi = d_in[2]; p.w1 = d_in[4]; p.w2 = d_in[5]; p.gw = d_in[6];
  p.out = d_out; p.ws = d_ws;
  (void)hipFuncSetAttribute((const void*)gm_kernel, hipFuncAttributeMaxDynamicSharedMemorySize, LDS_BYTES);
  gm_kernel<<<256, 512, LDS_BYTES, stream>>>(p);
}
```

```cpp
#include <hip/hip_runtime.h>
#include <hip/hip_bf16.h>
#include <stdint.h>

namespace {

typedef unsigned v4u __attribute__((ext_vector_type(4)));
typedef unsigned v2u __attribute__((ext_vector_type(2)));
typedef float f32x16 __attribute__((ext_vector_type(16)));
typedef float f32x4 __attribute__((ext_vector_type(4)));
typedef float f32x2 __attribute__((ext_vector_type(2)));
typedef __bf16 bf16x8 __attribute__((ext_vector_type(8)));
typedef __bf16 bf16x2 __attribute__((ext_vector_type(2)));
typedef short s16x4 __attribute__((ext_vector_type(4)));
#define LAS __attribute__((address_space(3)))
#define GAS __attribute__((address_space(1)))
#define DI __device__ __forceinline__

constexpr int NS = 2048, ND = 512, NF = 1792;
constexpr unsigned WS_EPOCH = 0;
constexpr unsigned FS = 64;
constexpr unsigned WS_FLA = 4096;
constexpr unsigned WS_FLH = 16384;
constexpr unsigned WS_FLB = 32768;
constexpr unsigned WS_GATE = 65536;
constexpr unsigned WS_T = 1u << 20;
constexpr unsigned WS_U = 3u << 20;
constexpr unsigned WS_H = 5u << 20;
constexpr unsigned H_BYTES = 8u * 8u * 112u * 1024u;
constexpr int LDS_BYTES = 160 * 1024;
constexpr unsigned kSpinCap = 1u << 20;

struct Params {
  const void* x; const void* wr; const void* wi; const void* w1; const void* w2; const void* gw;
  void* out; void* ws;
};

constexpr double kPi = 3.14159265358979323846;
constexpr double ccos(double x) { double t = 1, s = 1; for (int n = 1; n < 16; ++n) { t *= -x * x / ((2 * n - 1) * (2 * n)); s += t; } return s; }
constexpr double cosn(int e, int n) { e = ((e % n) + n) % n; double x = 2 * kPi * e / n; if (x > kPi) x -= 2 * kPi; return ccos(x); }
constexpr double wre(int e, int n) { return cosn(e, n); }
constexpr double wim(int e, int n) { return -cosn(4 * e - n, 4 * n); }
constexpr unsigned bfb(double v) { float f = (float)v; unsigned u = __builtin_bit_cast(unsigned, f); u += 0x7fffu + ((u >> 16) & 1u); return u >> 16; }
constexpr unsigned bf2(double a, double b) { return bfb(a) | (bfb(b) << 16); }
constexpr unsigned fbits(double v) { return __builtin_bit_cast(unsigned, (float)v); }
struct Tab { unsigned v[16][64][4]; };
constexpr Tab make_tab() {
  Tab t{};
  for (int l = 0; l < 64; ++l) {
    const int g = l >> 4, l15 = l & 15, hh = l >> 5, n = l & 31;
    for (int mtx = 0; mtx < 3; ++mtx) {
      double r[4] = {}, im[4] = {};
      for (int q = 0; q < 4; ++q) {
        const int a = 4 * g + q;
        if (mtx == 0) { r[q] = wre(l15 * a, 16); im[q] = wim(l15 * a, 16); }
        else if (mtx == 1) { r[q] = wre(a * (2 * l15 + 1), 32); im[q] = wim(a * (2 * l15 + 1), 32); }
        else { r[q] = wre(l15 * (2 * a + 1), 32); im[q] = -wim(l15 * (2 * a + 1), 32); }
      }
      t.v[2 * mtx][l][0] = bf2(r[0], r[1]); t.v[2 * mtx][l][1] = bf2(r[2], r[3]); t.v[2 * mtx][l][2] = bf2(-im[0], -im[1]); t.v[2 * mtx][l][3] = bf2(-im[2], -im[3]);
      t.v[2 * mtx + 1][l][0] = bf2(im[0], im[1]); t.v[2 * mtx + 1][l][1] = bf2(im[2], im[3]); t.v[2 * mtx + 1][l][2] = bf2(r[0], r[1]); t.v[2 * mtx + 1][l][3] = bf2(r[2], r[3]);
    }
    {
      const int m2 = n >> 2, pp = (n >> 1) & 1, cp = n & 1;
      double v0[8] = {}, v1[8] = {};
      for (int jj = 0; jj < 8; ++jj) {
        const int ii = (8 * hh + jj) >> 1, d0 = jj & 1;
        const double sg = (d0 & pp) ? -1.0 : 1.0, orr = sg * wre(ii * m2, 8), oi = sg * wim(ii * m2, 8);
        v0[jj] = cp ? oi : orr; v1[jj] = cp ? orr : -oi;
      }
      for (int q = 0; q < 4; ++q) { t.v[6][l][q] = bf2(v0[2 * q], v0[2 * q + 1]); t.v[7][l][q] = bf2(v1[2 * q], v1[2 * q + 1]); }
    }
    {
      const int cq = n >> 4, ii = (n >> 1) & 7, d0 = n & 1;
      for (int s = 0; s < 2; ++s) {
        double v[8] = {};
        for (int jj = 0; jj < 8; ++jj) {
          const int m2k = 4 * s + 2 * hh + (jj >> 2), pk = (jj >> 1) & 1, ck = jj & 1;
          const double sg = (d0 & pk) ? -1.0 : 1.0, orr = sg * wre(ii * m2k, 8), oi = -sg * wim(ii * m2k, 8);
          v[jj] = cq ? (ck ? orr : oi) : (ck ? -oi : orr);
        }
        for (int q = 0; q < 4; ++q) t.v[8 + s][l][q] = bf2(v[2 * q], v[2 * q + 1]);
      }
    }
    for (int q = 0; q < 4; ++q) {
      const int a = 4 * g + q;
      t.v[10][l][q] = fbits(wre(a * (2 * l15), 512));      t.v[11][l][q] = fbits(wim(a * (2 * l15), 512));
      t.v[12][l][q] = fbits(wre(a * (2 * l15 + 1), 512));  t.v[13][l][q] = fbits(wim(a * (2 * l15 + 1), 512));
      t.v[14][l][q] = fbits(wre(l15 * (2 * a + 1), 512));  t.v[15][l][q] = fbits(wim(l15 * (2 * a + 1), 512));
    }
  }
  return t;
}
__device__ const Tab kTab = make_tab();

constexpr unsigned STAMP_OFF = 512 * 1024;
#define STAMP(k) do {} while (0)
#define FT(k) do {} while (0)

DI v4u make_srd(const void* p, unsigned bytes) {
  const unsigned long long a = (unsigned long long)p;
  v4u r; r.x = (unsigned)a; r.y = (unsigned)(a >> 32) & 0xffffu; r.z = bytes; r.w = 0x00020000u;
  return r;
}
template <unsigned LDSI, unsigned SOI> DI void dma16(v4u srd, unsigned voff, unsigned ldsb, unsigned sob) {
  unsigned tmp;
  asm volatile("s_add_u32 m0, %3, %5\n\ts_add_u32 %0, %4, %6\n\tbuffer_load_dwordx4 %1, %2, %0 offen lds"
               : "=&s"(tmp) : "v"(voff), "s"(srd), "s"(ldsb), "s"(sob), "i"(LDSI), "i"(SOI) : "memory", "scc");
}
template <unsigned LDSI, unsigned SOI> DI void dma16z(v4u srd, unsigned voff, unsigned ldsb) {
  unsigned tmp;
  asm volatile("s_add_u32 m0, %3, %4\n\ts_mov_b32 %0, %5\n\tbuffer_load_dwordx4 %1, %2, %0 offen lds"
               : "=&s"(tmp) : "v"(voff), "s"(srd), "s"(ldsb), "i"(LDSI), "i"(SOI) : "memory", "scc");
}
DI unsigned uni(unsigned v) { return (unsigned)__builtin_amdgcn_readfirstlane((int)v); }
#define LD16(dst, voffv, srd, soffv, flags) asm volatile("buffer_load_dwordx4 %0, %1, %2, %3 offen " flags : "=v"(dst) : "v"(voffv), "s"(srd), "s"(soffv) : "memory")
#define LD16I(dst, voffv, srd, sob, soi, flags) do { unsigned tmp_; asm volatile("s_add_u32 %1, %4, %5\n\tbuffer_load_dwordx4 %0, %2, %3, %1 offen " flags : "=v"(dst), "=&s"(tmp_) : "v"(voffv), "s"(srd), "s"(sob), "i"(soi) : "memory", "scc"); } while (0)
#define LD16Z(dst, voffv, srd, flags) asm volatile("buffer_load_dwordx4 %0, %1, %2, 0 offen " flags : "=v"(dst) : "v"(voffv), "s"(srd) : "memory")
#define LD4Z(dst, voffv, srd, flags) asm volatile("buffer_load_dword %0, %1, %2, 0 offen " flags : "=v"(dst) : "v"(voffv), "s"(srd) : "memory")
#define LD2Z(dst, voffv, srd, flags) asm volatile("buffer_load_ushort %0, %1, %2, 0 offen " flags : "=v"(dst) : "v"(voffv), "s"(srd) : "memory")
#define LD8Z(dst, voffv, srd, flags) asm volatile("buffer_load_dwordx2 %0, %1, %2, 0 offen " flags : "=v"(dst) : "v"(voffv), "s"(srd) : "memory")
#define LD8(dst, voffv, srd, soffv, flags) asm volatile("buffer_load_dwordx2 %0, %1, %2, %3 offen " flags : "=v"(dst) : "v"(voffv), "s"(srd), "s"(soffv) : "memory")
#define LD4(dst, voffv, srd, soffv, flags) asm volatile("buffer_load_dword %0, %1, %2, %3 offen " flags : "=v"(dst) : "v"(voffv), "s"(srd), "s"(soffv) : "memory")
#define LD2(dst, voffv, srd, soffv, flags) asm volatile("buffer_load_ushort %0, %1, %2, %3 offen " flags : "=v"(dst) : "v"(voffv), "s"(srd), "s"(soffv) : "memory")
#define XSTR(x) #x
#define STR(x) XSTR(x)
#define BAR_VM(n) asm volatile("s_waitcnt vmcnt(" STR(n) ") lgkmcnt(0)\n\ts_barrier" ::: "memory")
#define BAR_LGKM() asm volatile("s_waitcnt lgkmcnt(0)\n\ts_barrier" ::: "memory")
#define SCHED() __builtin_amdgcn_sched_barrier(0)
template <int N> DI void bar_vm() { asm volatile("s_waitcnt vmcnt(%0) lgkmcnt(0)\n\ts_barrier" :: "i"(N) : "memory"); }
template <int N> DI void wait_vm() { asm volatile("s_waitcnt vmcnt(%0)" :: "i"(N) : "memory"); }

DI unsigned pk2(float a, float b) { f32x2 p = {a, b}; return __builtin_bit_cast(unsigned, __builtin_convertvector(p, bf16x2)); }
DI float bflo(unsigned u) { return __builtin_bit_cast(float, u << 16); }
DI float bfhi(unsigned u) { return __builtin_bit_cast(float, u & 0xffff0000u); }
DI float asf(unsigned u) { return __builtin_bit_cast(float, u); }
DI f32x16 mfma32(v4u a, v4u b, f32x16 c) {
  return __builtin_amdgcn_mfma_f32_32x32x16_bf16(__builtin_bit_cast(bf16x8, a), __builtin_bit_cast(bf16x8, b), c, 0, 0, 0);
}
DI f32x4 mfma16(v4u a, v4u b, f32x4 c) {
  return __builtin_amdgcn_mfma_f32_16x16x32_bf16(__builtin_bit_cast(bf16x8, a), __builtin_bit_cast(bf16x8, b), c, 0, 0, 0);
}
DI v2u tr_read(LAS char* p) { return __builtin_bit_cast(v2u, __builtin_amdgcn_ds_read_tr16_b64_v4i16((LAS s16x4*)p)); }
DI v4u tr_read2(LAS char* p0, LAS char* p1) { v2u a = tr_read(p0), b = tr_read(p1); v4u r; r.x = a.x; r.y = a.y; r.z = b.x; r.w = b.y; return r; }
DI v4u lds_ld16(LAS char* p) { return *(LAS v4u*)p; }
DI float cosrev(float r) { return __builtin_amdgcn_cosf(r); }
DI float sinrev(float r) { return __builtin_amdgcn_sinf(r); }
DI __amdgpu_buffer_rsrc_t as_rsrc(v4u srd) { return __builtin_amdgcn_make_buffer_rsrc((void*)(((unsigned long long)srd.y << 32) | srd.x), 0, srd.z, 0x00020000); }
DI void st16_sc1(v4u srd, unsigned voff, v4u v) { __builtin_amdgcn_raw_buffer_store_b128(v, as_rsrc(srd), voff, 0, 16); }
DI void flag_store(void* ws, unsigned off, unsigned tag) { __hip_atomic_store((GAS unsigned*)((GAS char*)ws + off), tag, __ATOMIC_RELAXED, __HIP_MEMORY_SCOPE_AGENT); }
template <int CTRL, int RMASK> DI float dpp_add(float v) { return v + asf((unsigned)__builtin_amdgcn_update_dpp(0, (int)__builtin_bit_cast(unsigned, v), CTRL, RMASK, 0xf, true)); }
DI void poll_words(v4u sws, unsigned off, int n, unsigned tag, int lane) {
  const unsigned vo = off + FS * (unsigned)(lane < n ? lane : 0);
#pragma unroll 1
  for (unsigned spins = 0; spins < kSpinCap; ++spins) {
    unsigned f;
    asm volatile("buffer_load_dword %0, %1, %2, 0 offen sc1\n\ts_waitcnt vmcnt(0)" : "=&v"(f) : "v"(vo), "s"(sws) : "memory");
    if (__all(f == tag)) return;
    __builtin_amdgcn_s_sleep(4);
  }
}

constexpr unsigned A_XIMG = 64 * 1024, A_FIMG = 128 * 1024;
DI void phaseA_genF(LAS char* L, int i, int mt, int w, int lane) {
  const int mh = w >> 2, sg = w & 3, n = lane & 31, hh = lane >> 5;
  const int m1c = 64 * mt + 32 * mh + n, m1 = m1c >> 1, c = m1c & 1;
  const int j0 = 64 * sg + 32 * hh;
  const float ph = c ? 0.25f : 0.f;
  const float k2 = 2.f * cosrev((float)(8 * m1) * (1.f / 2048.f));
  float vm = cosrev((float)(((i + 8 * (j0 - 1)) * m1) & 2047) * (1.f / 2048.f) + ph);
  float v0 = cosrev((float)(((i + 8 * j0) * m1) & 2047) * (1.f / 2048.f) + ph);
  const unsigned cst = c ? 0x3F800000u : 0x00003F80u;
  const bool sp = m1c < 2;
#pragma unroll
  for (int s = 0; s < 4; ++s) {
    float e[8];
#pragma unroll
    for (int q = 0; q < 8; ++q) { e[q] = v0; const float vn = __builtin_fmaf(k2, v0, -vm); vm = v0; v0 = vn; }
    v4u f; f.x = pk2(e[0], e[1]); f.y = pk2(e[2], e[3]); f.z = pk2(e[4], e[5]); f.w = pk2(e[6], e[7]);
    if (mt == 0 && w < 4) { f.x = sp ? cst : f.x; f.y = sp ? cst : f.y; f.z = sp ? cst : f.z; f.w = sp ? cst : f.w; }
    *(LAS v4u*)(L + A_FIMG + (mh * 16 + sg * 4 + s) * 1024 + lane * 16) = f;
  }
}

struct Frag1 { v4u a[4], b0[4], b1[4]; };
DI void g1_read(Frag1& f, LAS char* wa, LAS char* xb0, LAS char* xb1, LAS char* xb2, LAS char* xb3, int slot) {
  const int so = slot * 32768;
  f.a[0] = tr_read2(wa + so, wa + so + 1024);              f.b0[0] = lds_ld16(xb0 + so); f.b1[0] = lds_ld16(xb0 + so + 4096);
  f.a[1] = tr_read2(wa + so + 4096, wa + so + 5120);       f.b0[1] = lds_ld16(xb1 + so); f.b1[1] = lds_ld16(xb1 + so + 4096);
  f.a[2] = tr_read2(wa + so + 8192, wa + so + 9216);       f.b0[2] = lds_ld16(xb2 + so); f.b1[2] = lds_ld16(xb2 + so + 4096);
  f.a[3] = tr_read2(wa + so + 12288, wa + so + 13312);     f.b0[3] = lds_ld16(xb3 + so); f.b1[3] = lds_ld16(xb3 + so + 4096);
}
DI unsigned silu2(float a, float b) {
  const f32x2 h = {a, b}, t = h * -1.44269504088896341f;
  f32x2 e = {__builtin_amdgcn_exp2f(t[0]), __builtin_amdgcn_exp2f(t[1])};
  e = e + 1.f;
  const f32x2 r = {__builtin_amdgcn_rcpf(e[0]), __builtin_amdgcn_rcpf(e[1])};
  return __builtin_bit_cast(unsigned, __builtin_convertvector(h * r, bf16x2));
}

DI f32x2 lo2(const f32x4& v) { return __builtin_shufflevector(v, v, 0, 1); }
DI f32x2 hi2(const f32x4& v) { return __builtin_shufflevector(v, v, 2, 3); }
DI unsigned pk2v(f32x2 p) { return __builtin_bit_cast(unsigned, __builtin_convertvector(p, bf16x2)); }
DI f32x4 asf4(v4u v) { return __builtin_bit_cast(f32x4, v); }
template <bool CONJ> DI v4u cmul_pack(const f32x4& re, const f32x4& im, const f32x4& tr, const f32x4& ti) {
  const f32x2 r0 = lo2(re), r1 = hi2(re), i0 = lo2(im), i1 = hi2(im), c0 = lo2(tr), c1 = hi2(tr), s0 = lo2(ti), s1 = hi2(ti);
  f32x2 a0, a1, b0, b1;
  if (CONJ) {
    a0 = __builtin_elementwise_fma(r0, c0, i0 * s0); a1 = __builtin_elementwise_fma(r1, c1, i1 * s1);
    b0 = __builtin_elementwise_fma(i0, c0, -(r0 * s0)); b1 = __builtin_elementwise_fma(i1, c1, -(r1 * s1));
  } else {
    a0 = __builtin_elementwise_fma(r0, c0, -(i0 * s0)); a1 = __builtin_elementwise_fma(r1, c1, -(i1 * s1));
    b0 = __builtin_elementwise_fma(r0, s0, i0 * c0); b1 = __builtin_elementwise_fma(r1, s1, i1 * c1);
  }
  v4u r; r.x = pk2v(a0); r.y = pk2v(a1); r.z = pk2v(b0); r.w = pk2v(b1); return r;
}
struct cf { float r, i; };
DI cf cmul(cf a, cf b) { return {a.r * b.r - a.i * b.i, a.r * b.i + a.i * b.r}; }
DI cf cmulc(cf a, cf b) { return {a.r * b.r + a.i * b.i, a.i * b.r - a.r * b.i}; }
DI unsigned w_src(int m, int lane) {
  const int mm = (NS - m) & (NS - 1);
  const int e = lane < 32 ? m * 257 - (m & 1) : mm * 257 + (m & 1);
  return (unsigned)(e * 2 + (lane & 31) * 16);
}
DI unsigned w_tail_src(int m, int lane) {
  const int mm = (NS - m) & (NS - 1), l = lane & 3;
  return (unsigned)((l < 2 ? m * 257 + 255 + l : mm * 257 + (l - 2) * 256) * 2);
}
struct WTail { unsigned r255, r256, rm0, rm256, i255, i256, im0, im256; };
DI WTail w_tail(unsigned tr, unsigned ti) {
  return {(unsigned)__builtin_amdgcn_readlane((int)tr, 0), (unsigned)__builtin_amdgcn_readlane((int)tr, 1), (unsigned)__builtin_amdgcn_readlane((int)tr, 2), (unsigned)__builtin_amdgcn_readlane((int)tr, 3),
          (unsigned)__builtin_amdgcn_readlane((int)ti, 0), (unsigned)__builtin_amdgcn_readlane((int)ti, 1), (unsigned)__builtin_amdgcn_readlane((int)ti, 2), (unsigned)__builtin_amdgcn_readlane((int)ti, 3)};
}
struct WVal { f32x4 r0, i0, r1, i1; };
struct WRawL { unsigned r[4][2], i[4][2]; };
DI void w_put(LAS char* scr, v4u qr, v4u qi, int lane) { *(LAS v4u*)(scr + lane * 16) = qr; *(LAS v4u*)(scr + 1024 + lane * 16) = qi; }
DI int w_off(int m, int lane) { const int k1 = lane & 15, g = lane >> 4, odd = m & 1; return lane < 32 ? (2 * k1 + 128 * g + odd) * 2 : 512 + (511 - 2 * k1 - 128 * g - odd) * 2; }
DI void w_get(WRawL& o, LAS char* scr, int off, int lane, int r) {
  LAS unsigned* a = (LAS unsigned*)(scr + ((off + (lane < 32 ? 64 : -64) * r) & ~3));
  o.r[r][0] = a[0]; o.r[r][1] = a[1]; o.i[r][0] = a[256]; o.i[r][1] = a[257];
}
DI WVal w_unpack(const WRawL& x, const WTail& t, int off, int m, int lane) {
  const bool lo = lane < 32;
  const unsigned sh = (off & 2) * 8;
  unsigned rr[4], ii[4];
#pragma unroll
  for (int r = 0; r < 4; ++r) { rr[r] = __builtin_amdgcn_alignbit(x.r[r][1], x.r[r][0], sh); ii[r] = __builtin_amdgcn_alignbit(x.i[r][1], x.i[r][0], sh); }
  if (m & 1) { rr[3] = lane == 31 ? (rr[3] & 0xffffu) | (t.r255 << 16) : rr[3]; ii[3] = lane == 31 ? (ii[3] & 0xffffu) | (t.i255 << 16) : ii[3]; }
  else { rr[0] = lane == 32 ? (rr[0] & 0xffffu) | (t.rm256 << 16) : rr[0]; ii[0] = lane == 32 ? (ii[0] & 0xffffu) | (t.im256 << 16) : ii[0]; }
  const unsigned s0 = lo ? 16u : 0u, s1 = lo ? 0u : 16u, sg = lo ? 0u : 0x80000000u;
  WVal o;
#pragma unroll
  for (int r = 0; r < 4; ++r) {
    o.r0[r] = asf((rr[r] << s0) & 0xffff0000u); o.r1[r] = asf((rr[r] << s1) & 0xffff0000u);
    o.i0[r] = asf(((ii[r] << s0) & 0xffff0000u) ^ sg); o.i1[r] = asf(((ii[r] << s1) & 0xffff0000u) ^ sg);
  }
  const bool av = (lane & 31) == 0;
  const float xr = bflo(lo ? t.rm0 : t.r256), xi = asf(((lo ? t.im0 : t.i256) << 16) ^ (sg ^ 0x80000000u));
  o.r0[0] = av ? 0.5f * (o.r0[0] + xr) : o.r0[0];
  o.i0[0] = av ? 0.5f * (o.i0[0] + xi) : o.i0[0];
  return o;
}

DI void packed_coef(WVal& a, WVal& b, int m2) {
  const cf om = {cosrev((float)m2 * 0.0625f), -sinrev((float)m2 * 0.0625f)};
#pragma unroll
  for (int pp = 0; pp < 2; ++pp)
#pragma unroll
    for (int r = 0; r < 4; ++r) {
      const float war = pp ? a.r1[r] : a.r0[r], wai = pp ? a.i1[r] : a.i0[r], wbr = pp ? b.r1[r] : b.r0[r], wbi = pp ? b.i1[r] : b.i0[r];
      const cf sp = {war + wbr, wai + wbi}, sm = {war - wbr, wai - wbi};
      const cf t1 = cmulc(sm, om), t2 = cmul(sm, om);
      const cf cE = {sp.r - t1.i, sp.i + t1.r}, cO = {t2.r - sp.i, t2.i + sp.r};
      const float alr = 0.25f * (cE.r + cO.i), ali = 0.25f * (cE.i - cO.r), ber = 0.25f * (cE.r - cO.i), bei = 0.25f * (cE.i + cO.r);
      if (pp) { a.r1[r] = alr; a.i1[r] = ali; b.r1[r] = ber; b.i1[r] = bei; } else { a.r0[r] = alr; a.i0[r] = ali; b.r0[r] = ber; b.i0[r] = bei; }
    }
}

struct Geo {
  int tid, lane, w, b, i, i1, at, m1, rb, fc, hh, blk, q4, p4;
  int jb, dt, rh, q, j;
  v4u sx, sws, sw2;
  unsigned vw20, vw21, ldsw, fq0b, fq1b;
};
template <int T, int U> DI void w2_dma(const v4u& sw2, unsigned vw20, unsigned vw21, unsigned ldsw, unsigned fq0, unsigned fq1) {
  dma16<(T & 7) * 16384u + U * 1024u, (T < 8 ? T : T - 8) * 32768u>(sw2, U ? vw21 : vw20, ldsw, T < 8 ? fq0 : fq1);
}
#define w2_issue1(g, t, u) (w2_dma<t, u>((g).sw2, (g).vw20, (g).vw21, (g).ldsw, (g).fq0b, (g).fq1b))
struct GRot { float c1, s1, c4, s4, c, s; };
DI void gen_g_init(const Geo& g, GRot& r) {
  const int srow = g.i + 8 * g.j;
  const int mb = 32 * g.q + 2 * g.hh;
  const float r1 = (float)(srow & 2047) * (1.f / 2048.f), r4 = (float)((4 * srow) & 2047) * (1.f / 2048.f), r0 = (float)((mb * srow) & 2047) * (1.f / 2048.f);
  r.c1 = cosrev(r1); r.s1 = sinrev(r1); r.c4 = cosrev(r4); r.s4 = sinrev(r4); r.c = cosrev(r0); r.s = sinrev(r0);
}
template <int S> DI void gen_g_frag(const Geo& g, GRot& r, v4u& f) {
#pragma unroll
  for (int h = 0; h < 2; ++h) {
    const float cb = __builtin_fmaf(r.c, r.c1, -(r.s * r.s1)), sb = __builtin_fmaf(r.s, r.c1, r.c * r.s1);
    unsigned w0 = pk2(r.c, -r.s), w1 = pk2(cb, -sb);
    if (S == 0 && h == 0) w0 = (g.q == 0 && g.hh == 0) ? ((g.j & 1) ? 0x3F800000u : 0x00003F80u) : w0;
    if (h) { f.z = w0; f.w = w1; } else { f.x = w0; f.y = w1; }
    const float cn = __builtin_fmaf(r.c, r.c4, -(r.s * r.s4)), sn = __builtin_fmaf(r.s, r.c4, r.c * r.s4);
    r.c = cn; r.s = sn;
  }
  asm volatile("" : "+v"(f));
}
template <int N0, int N1, int NR> DI void bar_w(int w) { if (w == 0) bar_vm<N0>(); else if (w == 1) bar_vm<N1>(); else bar_vm<NR>(); }
#define NOP do {} while (0)

#define TOUCH_POS 1
#define TLBW 0
#define TOUCH() do {                                                                                                      \
    const int gl = G.b * 256 + (w & 3) * 64 + lane;                                                                         \
    const char* base_ = gl < 14336 ? (const char*)p.w2 : gl < 28672 ? (const char*)p.w1 - 14336 * 128 : gl < 36896 ? (const char*)p.wr - 28672 * 128           \
                      : gl < 45120 ? (const char*)p.wi - 36896 * 128 : (const char*)p.x - 45120 * 128;                     \
    const char* ad_ = base_ + (size_t)(gl < 61504 ? gl : 45120) * 128;                                                      \
    if (w >= 4) ad_ = TLBW ? (w == 7 ? (const char*)p.out + lane * 32768 : (const char*)p.ws + (size_t)((w - 4) * 64 + lane + 8) * 65536 + 32768 - 128) : (const char*)p.x; \
    asm volatile("global_load_dword %0, %1, off" : "=v"(touch) : "v"(ad_) : "memory");                                     \
  } while (0)
template <bool ISA>
DI unsigned phase1(const Params& p, LAS char* L, const Geo& G, v4u& m5a, v4u& m5b
) {
  const int tid = G.tid, lane = G.lane, w = G.w, i = G.i, i1 = G.i1, rb = G.rb, fc = G.fc, hh = G.hh, blk = G.blk, q4 = G.q4, p4 = G.p4;
  const v4u sx = G.sx, sws = G.sws, sw1 = make_srd(p.w1, ND * NF * 2);
  unsigned touch;
  unsigned ep; v4u gx, gv;
  const int jg = 128 * rb + 8 * fc + w;
  LD16Z(gx, (unsigned)(lane * 16 + (i1 + 8 * jg) * 1024), sx, "");
  LD16Z(gv, (unsigned)lane * 16u, make_srd(p.gw, ND * 2), "");
  LD4Z(ep, WS_EPOCH, sws, "sc1");
  const int f0 = ISA ? 96 * fc : 768 + 128 * (fc - 8), nf = ISA ? 96 : 128;
  const int mt = G.at >> 2, dt4 = G.at & 3;
  const int m1 = G.m1;
  if (ISA) {
    const unsigned voff = (unsigned)(lane >> 4) * 8192u + (unsigned)(((lane & 15) ^ ((lane >> 4) << 2)) << 4);
    const unsigned xl = uni(A_XIMG + (unsigned)w * 8192u), xs = uni((unsigned)i * 1024u + (unsigned)dt4 * 256u + (unsigned)w * 262144u);
    dma16<0, 0>(sx, voff, xl, xs); dma16<1024, 32768>(sx, voff, xl, xs); dma16<2048, 65536>(sx, voff, xl, xs); dma16<3072, 98304>(sx, voff, xl, xs);
    dma16<4096, 131072>(sx, voff, xl, xs); dma16<5120, 163840>(sx, voff, xl, xs); dma16<6144, 196608>(sx, voff, xl, xs); dma16<7168, 229376>(sx, voff, xl, xs);
  }
  unsigned vx0, vx1, vw0, vw1;
  {
    const int r0 = 16 * w + (lane >> 3), pos = lane & 7, r1 = r0 + 8;
    vx0 = (unsigned)(i1 * 1024 + (128 * rb + r0) * 8192 + ((pos ^ ((r0 >> 1) & 7)) << 4));
    vx1 = (unsigned)(i1 * 1024 + (128 * rb + r1) * 8192 + ((pos ^ ((r1 >> 1) & 7)) << 4));
    const int k0 = 8 * w + (lane >> 4), chunk = (lane & 15) ^ ((lane >> 4) << 2);
    const bool ok = chunk * 8 < nf;
    vw0 = ok ? (unsigned)(k0 * 3584 + f0 * 2 + chunk * 16) : 0x80000000u;
    vw1 = ok ? vw0 + 4u * 3584u : 0x80000000u;
  }
  const unsigned g1l = uni((unsigned)w * 2048u);
#define g1_dma(kt, u) do {                                                                                         \
    if ((u) == 0) dma16z<((kt) & 3) * 32768u, (kt) * 128u>(sx, vx0, g1l);                                           \
    if ((u) == 1) dma16z<((kt) & 3) * 32768u + 1024u, (kt) * 128u>(sx, vx1, g1l);                                   \
    if ((u) == 2) dma16z<((kt) & 3) * 32768u + 16384u, (kt) * (64u * 3584u)>(sw1, vw0, g1l);                        \
    if ((u) == 3) dma16z<((kt) & 3) * 32768u + 17408u, (kt) * (64u * 3584u)>(sw1, vw1, g1l); } while (0)
#define g1_issue(kt) do { g1_dma(kt, 0); g1_dma(kt, 1); g1_dma(kt, 2); g1_dma(kt, 3); } while (0)
  if (ISA) {
    phaseA_genF(L, i, mt, w, lane);
    g1_issue(0); g1_issue(1);
  } else {
    g1_issue(0); asm volatile("s_barrier" ::: "memory");
    g1_issue(1); asm volatile("s_barrier" ::: "memory");
    g1_issue(2); asm volatile("s_barrier" ::: "memory");
    g1_issue(3);
  }
  TOUCH();
  asm volatile("s_waitcnt vmcnt(17)" : "+v"(ep), "+v"(gx), "+v"(gv) :: "memory");
  const unsigned tag = (unsigned)__builtin_amdgcn_readfirstlane((int)ep) + 1u;
  float gate_g, gate_og;
  {
    float z = 0.f;
#pragma unroll
    for (int q = 0; q < 4; ++q) {
      const unsigned pr = pk2(bflo(gx[q]) * bflo(gv[q]), bfhi(gx[q]) * bfhi(gv[q]));
      z += bflo(pr) + bfhi(pr);
    }
    z = dpp_add<0x111, 0xf>(z); z = dpp_add<0x112, 0xf>(z); z = dpp_add<0x114, 0xf>(z); z = dpp_add<0x118, 0xf>(z);
    z = dpp_add<0x142, 0xa>(z); z = dpp_add<0x143, 0xc>(z);
    const float zb = bflo(pk2(z, 0.f));
    const float g = bflo(pk2(__builtin_amdgcn_rcpf(1.f + __builtin_amdgcn_exp2f(zb * -1.44269504088896341f)), 0.f));
    gate_g = asf((unsigned)__builtin_amdgcn_readlane((int)__builtin_bit_cast(unsigned, g), 63));
    gate_og = bflo(pk2(1.f - gate_g, 0.f));
  }
  if (ISA) {
    const int dq = w & 3, mh = w >> 2;
    LAS char* xa = L + A_XIMG + (32 * hh + q4) * 256 + (((4 * dq + 2 * blk + (p4 >> 1)) ^ (q4 << 2)) << 4) + 8 * (p4 & 1);
    LAS char* fb = L + A_FIMG + mh * 16384 + lane * 16;
    const int srow = 32 * mh + (lane & 31);
    LAS char* stw = L + A_FIMG + srow * 256 + 8 * hh;
    const int rrow = tid >> 3;
    LAS char* str = L + A_FIMG + rrow * 256;
    const unsigned tdst = WS_T + (unsigned)(((64 * mt + rrow) * 8 + i) * 1024 + dt4 * 256 + (tid & 7) * 16);
    if (w < 4) bar_vm<9>(); else BAR_LGKM();
    STAMP(1);
    f32x16 acc = {};
    v4u fa[2][4], fbv[2][4];
#pragma unroll
    for (int s = 0; s < 4; ++s) { fa[0][s] = tr_read2(xa + 8 * s * 256, xa + 8 * s * 256 + 1024); fbv[0][s] = lds_ld16(fb + s * 1024); }
#pragma unroll
    for (int g = 0; g < 4; ++g) {
      acc = mfma32(fa[g & 1][0], fbv[g & 1][0], acc);
      SCHED();
      if (g == 1) { if (w >= 4) bar_vm<9>(); else BAR_LGKM(); }
      if (g < 3) {
#pragma unroll
        for (int s = 0; s < 4; ++s) {
          const int ro = (64 * (g + 1) + 8 * s) * 256;
          fa[(g + 1) & 1][s] = tr_read2(xa + ro, xa + ro + 1024); fbv[(g + 1) & 1][s] = lds_ld16(fb + (4 * (g + 1) + s) * 1024);
        }
      }
      SCHED();
#pragma unroll
      for (int s = 1; s < 4; ++s) { acc = mfma32(fa[g & 1][s], fbv[g & 1][s], acc); if (g == 1 && s == 1) { g1_dma(2, 0); g1_dma(2, 1); } if (g == 1 && s == 2) { g1_dma(2, 2); g1_dma(2, 3); } SCHED(); }
    }
    BAR_LGKM();
    STAMP(2);
    g1_issue(3);
#pragma unroll
    for (int Gq = 0; Gq < 4; ++Gq) {
      v2u o; o.x = pk2(acc[4 * Gq], acc[4 * Gq + 1]); o.y = pk2(acc[4 * Gq + 2], acc[4 * Gq + 3]);
      *(LAS v2u*)(stw + (((4 * dq + Gq) ^ (srow & 15)) << 4)) = o;
    }
    BAR_LGKM();
    {
      const v4u v0 = lds_ld16(str + (((tid & 7) ^ (rrow & 15)) << 4)), v1 = lds_ld16(str + ((((tid & 7) + 8) ^ (rrow & 15)) << 4));
      st16_sc1(sws, tdst, v0);
      st16_sc1(sws, tdst + 128u, v1);
    }
    SCHED();
  }
  const int ft = w & 3, rh1 = w >> 2;
  const bool active = ft * 32 < nf;
  f32x16 acc1[2] = {};
  v4u wqr, wqi, tq0, tq1;
  unsigned wtr, wti, xtr, xti;
  v4u xqr, xqi;
  WVal Wc, Wd;
  v4u tb[16];
  unsigned fla;
  int t_ok = 0, t_first = 0; (void)t_first;
  const v4u swr = make_srd(p.wr, NS * 257 * 2), swi = make_srd(p.wi, NS * 257 * 2);
  const int mW = m1 == 0 ? 128 * w : m1 + 256 * w;
  unsigned tsrc0 = 0, tsrc1 = 0;
  const unsigned flaoff = WS_FLA + (unsigned)(((m1 >> 5) & 3) * 32 + (lane & 31)) * FS;
  LAS char* CTL = L + 128 * 1024;
  {
    LAS char* wa = L + 16384 + (8 * hh + q4) * 256 + (((4 * ft + 2 * blk + (p4 >> 1)) ^ (q4 << 2)) << 4) + 8 * (p4 & 1);
    const int r = 64 * rh1 + (lane & 31), fz = (r >> 1) & 7;
    LAS char* xb0 = L + r * 128 + (((0 + hh) ^ fz) << 4);
    LAS char* xb1 = L + r * 128 + (((2 + hh) ^ fz) << 4);
    LAS char* xb2 = L + r * 128 + (((4 + hh) ^ fz) << 4);
    LAS char* xb3 = L + r * 128 + (((6 + hh) ^ fz) << 4);
    Frag1 fr[2];
    bar_vm<ISA ? 15 : 13>();
    STAMP(3);
    if (lane == 0) { v2u gv2; gv2.x = __builtin_bit_cast(unsigned, gate_g); gv2.y = __builtin_bit_cast(unsigned, gate_og); *(LAS v2u*)(CTL + 64 + w * 8) = gv2; }
    g1_read(fr[0], wa, xb0, xb1, xb2, xb3, 0);
#define PIN1(c) asm volatile("" : "+v"(c.a[0]), "+v"(c.a[1]), "+v"(c.a[2]), "+v"(c.a[3]), "+v"(c.b0[0]), "+v"(c.b0[1]), "+v"(c.b0[2]), "+v"(c.b0[3]), \
                             "+v"(c.b1[0]), "+v"(c.b1[1]), "+v"(c.b1[2]), "+v"(c.b1[3]))
#define G1_STEP(kt, X0, X1, X2, X3, X4, X5, X6, X7)                                                                   \
    {                                                                                                                   \
      FT(10 + (kt));                                                                                                    \
      Frag1& c = fr[(kt) & 1];                                                                                          \
      Frag1& n = fr[((kt) + 1) & 1];                                                                                    \
      const int so = (((kt) + 1) & 3) * 32768;                                                                          \
      PIN1(c);                                                                                                          \
      asm volatile("" :: "v"(acc1[0]), "v"(acc1[1]));                                                                   \
      SCHED();                                                                                                          \
      acc1[0] = mfma32(c.a[0], c.b0[0], acc1[0]); if ((kt) < 7) n.a[0] = tr_read2(wa + so, wa + so + 1024); X0; SCHED(); \
      acc1[1] = mfma32(c.a[0], c.b1[0], acc1[1]); if ((kt) < 7) { n.b0[0] = lds_ld16(xb0 + so); n.b1[0] = lds_ld16(xb0 + so + 4096); } X1; SCHED(); \
      acc1[0] = mfma32(c.a[1], c.b0[1], acc1[0]); if ((kt) < 7) n.a[1] = tr_read2(wa + so + 4096, wa + so + 5120); X2; SCHED(); \
      acc1[1] = mfma32(c.a[1], c.b1[1], acc1[1]); if ((kt) < 7) { n.b0[1] = lds_ld16(xb1 + so); n.b1[1] = lds_ld16(xb1 + so + 4096); } X3; SCHED(); \
      acc1[0] = mfma32(c.a[2], c.b0[2], acc1[0]); if ((kt) < 7) n.a[2] = tr_read2(wa + so + 8192, wa + so + 9216); X4; SCHED(); \
      acc1[1] = mfma32(c.a[2], c.b1[2], acc1[1]); if ((kt) < 7) { n.b0[2] = lds_ld16(xb2 + so); n.b1[2] = lds_ld16(xb2 + so + 4096); } X5; SCHED(); \
      acc1[0] = mfma32(c.a[3], c.b0[3], acc1[0]); if ((kt) < 7) n.a[3] = tr_read2(wa + so + 12288, wa + so + 13312); X6; SCHED(); \
      acc1[1] = mfma32(c.a[3], c.b1[3], acc1[1]); if ((kt) < 7) { n.b0[3] = lds_ld16(xb3 + so); n.b1[3] = lds_ld16(xb3 + so + 4096); } X7; SCHED(); \
    }
#define GATE_STORE() do { if (w == 2 && lane < 8) { const v2u gv2 = *(LAS v2u*)(CTL + 64 + lane * 8);                       \
      __builtin_amdgcn_raw_buffer_store_b64(gv2, as_rsrc(sws), WS_GATE + (unsigned)(i1 * 256 + 128 * rb + 8 * fc + lane) * 8u, 0, 16); } } while (0)
#define STEP8 G1_STEP
    if constexpr (ISA) {
      bar_vm<11>();
      STAMP(4);
      STEP8(0, NOP, NOP, NOP, NOP, NOP, NOP, NOP, NOP)
      bar_vm<0>();
      if (tid == 0) flag_store(p.ws, WS_FLA + (unsigned)(mt * 32 + i * 4 + dt4) * FS, tag);
      STAMP(13);
      STEP8(1, g1_dma(4, 0), g1_dma(4, 1), g1_dma(4, 2), g1_dma(4, 3), g1_dma(5, 0), g1_dma(5, 1), g1_dma(5, 2), g1_dma(5, 3))
      BAR_LGKM();
      GATE_STORE();
      STEP8(2, NOP, NOP, NOP, NOP, g1_dma(6, 0), g1_dma(6, 1), g1_dma(6, 2), g1_dma(6, 3))
      bar_vm<8>();
      STEP8(3, NOP, NOP, NOP, NOP, g1_dma(7, 0), g1_dma(7, 1), g1_dma(7, 2), g1_dma(7, 3))
      bar_vm<8>();
      STAMP(12);
      STEP8(4, NOP, NOP, NOP, NOP, w2_issue1(G, 0, 0), w2_issue1(G, 0, 1), w2_issue1(G, 1, 0), w2_issue1(G, 1, 1))
      bar_vm<8>();
      STEP8(5, NOP, NOP, NOP, NOP, w2_issue1(G, 2, 0), w2_issue1(G, 2, 1), w2_issue1(G, 3, 0), w2_issue1(G, 3, 1))
      bar_vm<8>();
      STEP8(6, NOP, NOP, NOP, NOP, w2_issue1(G, 4, 0), w2_issue1(G, 4, 1), w2_issue1(G, 5, 0), w2_issue1(G, 5, 1))
      BAR_LGKM();
    } else {
      const v4u stab = make_srd(&kTab, sizeof(Tab));
      const unsigned wsrc = w_src(mW, lane);
      {
        const int kap = tid >> 5, ii = (kap >> 1) & 7, d0 = kap & 1;
        const int g0 = m1 == 0 ? 2 * ii : ii, g1 = m1 == 0 ? 2 * ii + 1 : 8 + ii;
        const unsigned tb = WS_T + (unsigned)m1 * 16384u + (unsigned)(d0 * 512 + (tid & 31) * 16);
        tsrc0 = tb + (unsigned)g0 * 1024u; tsrc1 = tb + (unsigned)g1 * 1024u;
      }
      const unsigned vw20z = G.vw20 - 16u * 1024u, vw21z = G.vw21 - 16u * 1024u, ldswz = uni(G.ldsw - 2048u);
#define W2B(t, u) do { if (w != 0) w2_issue1(G, t, u); } while (0)
#define W2Z(t, u) do { if (w == 1) w2_dma<t, u>(G.sw2, vw20z, vw21z, ldswz, G.fq0b, G.fq1b); } while (0)
      bar_vm<9>();
      STAMP(4);
      const unsigned wts = w_tail_src(mW, lane);
      if (m1 == 0) {
        const unsigned ws2 = w_src(128 * w + 1024, lane), wt2 = w_tail_src(128 * w + 1024, lane);
        LD16Z(xqr, ws2, swr, ""); LD16Z(xqi, ws2, swi, ""); LD2Z(xtr, wt2, swr, ""); LD2Z(xti, wt2, swi, "");
      }
      STEP8(0, LD16Z(wqr, wsrc, swr, ""), LD16Z(wqi, wsrc, swi, ""), LD2Z(wtr, wts, swr, ""), LD2Z(wti, wts, swi, ""), g1_dma(4, 0), g1_dma(4, 1), g1_dma(4, 2), g1_dma(4, 3))
      bar_vm<13>();
      STEP8(1, NOP, NOP, NOP, NOP, g1_dma(5, 0), g1_dma(5, 1), g1_dma(5, 2), g1_dma(5, 3))
      bar_vm<13>();
      GATE_STORE();
      STEP8(2, NOP, NOP, NOP, NOP, g1_dma(6, 0), g1_dma(6, 1), g1_dma(6, 2), g1_dma(6, 3))
      bar_vm<8>();
      LAS char* wscr0 = L + 128 * 1024 + w * 2048;
      const bool special = m1 == 0;
      const int m2nd = 128 * w + 1024;
      if (special) { asm volatile("" : "+v"(xqr), "+v"(xqi), "+v"(xtr), "+v"(xti)); w_put(wscr0, xqr, xqi, lane); }
      STEP8(3, NOP, NOP, (dma16<128u * 1024u + 16384u, 0>(stab, (unsigned)lane * 16u, g1l, g1l)), (dma16<128u * 1024u + 17408u, 1024>(stab, (unsigned)lane * 16u, g1l, g1l)),
            g1_dma(7, 0), g1_dma(7, 1), g1_dma(7, 2), g1_dma(7, 3))
      bar_vm<10>();
      STAMP(12);
      WRawL raw2; const int off2 = w_off(m2nd, lane);
      if (special) { w_get(raw2, wscr0, off2, lane, 0); w_get(raw2, wscr0, off2, lane, 1); w_get(raw2, wscr0, off2, lane, 2); w_get(raw2, wscr0, off2, lane, 3); }
      const int woff = w_off(mW, lane);
      asm volatile("" : "+v"(wqr), "+v"(wqi), "+v"(wtr), "+v"(wti) :: "memory");
      w_put(wscr0, wqr, wqi, lane);
      STEP8(4, W2Z(0, 0), W2Z(0, 1), W2Z(1, 0), W2Z(1, 1), W2B(0, 0), W2B(0, 1), W2B(1, 0), W2B(1, 1))
      bar_w<6, 14, 10>(w);
      WRawL wraw;
      w_get(wraw, wscr0, woff, lane, 0); w_get(wraw, wscr0, woff, lane, 1); w_get(wraw, wscr0, woff, lane, 2); w_get(wraw, wscr0, woff, lane, 3);
      if (special) Wd = w_unpack(raw2, w_tail(xtr, xti), off2, m2nd, lane);
      STEP8(5, W2Z(2, 0), W2Z(2, 1), W2Z(3, 0), W2Z(3, 1), W2B(2, 0), W2B(2, 1), W2B(3, 0), W2B(3, 1))
      bar_w<0, 16, 8>(w);
      Wc = w_unpack(wraw, w_tail(wtr, wti), woff, mW, lane);
      if (special) packed_coef(Wc, Wd, w);
      STEP8(6, if (w == 0) LD4Z(fla, flaoff, sws, "sc1"), NOP, NOP, NOP, NOP, NOP, NOP, NOP)
      if (w == 0) {
        asm volatile("s_waitcnt vmcnt(0)" : "+v"(fla) :: "memory");
        const unsigned ok = __all(fla == tag) ? 1u : 0u;
        if (lane == 0) *(LAS unsigned*)CTL = ok;
      }
      BAR_LGKM();
      t_ok = __builtin_amdgcn_readfirstlane(*(volatile LAS unsigned*)CTL);
      t_first = t_ok;
    }
    if constexpr (ISA) {
      STEP8(7, NOP, NOP, NOP, NOP, w2_issue1(G, 6, 0), w2_issue1(G, 6, 1), w2_issue1(G, 7, 0), w2_issue1(G, 7, 1))
    } else {
      LAS char* TB = L + 128 * 1024 + 16384 + lane * 16;
#define TBR(f) (tb[f] = lds_ld16(TB + (f) * 1024), tb[(f) + 1] = lds_ld16(TB + ((f) + 1) * 1024))
      STEP8(7, { if (t_ok) LD16Z(tq0, tsrc0, sws, "sc1"); TBR(6); }, { if (t_ok) LD16Z(tq1, tsrc1, sws, "sc1"); TBR(0); },
            { if (!t_ok && w == 0) LD4Z(fla, flaoff, sws, "sc1"); TBR(10); }, TBR(2), TBR(12), TBR(14), TBR(4), TBR(8))
      if (!t_ok) {
        if (w == 0) {
          asm volatile("s_waitcnt vmcnt(0)" : "+v"(fla) :: "memory");
          const unsigned ok = __all(fla == tag) ? 1u : 0u;
          if (lane == 0) *(LAS unsigned*)CTL = ok;
        }
        BAR_LGKM();
        t_ok = __builtin_amdgcn_readfirstlane(*(volatile LAS unsigned*)CTL);
        if (t_ok) { LD16Z(tq0, tsrc0, sws, "sc1"); LD16Z(tq1, tsrc1, sws, "sc1"); }
      }
    }
  }
  asm volatile("" :: "v"(touch));
  STAMP(5);
  FT(18);
  if (active) {
#pragma unroll
    for (int rt = 0; rt < 2; ++rt)
#pragma unroll
      for (int u = 0; u < 2; ++u) {
        v4u o;
        o.x = silu2(acc1[rt][8 * u + 0], acc1[rt][8 * u + 1]);
        o.y = silu2(acc1[rt][8 * u + 2], acc1[rt][8 * u + 3]);
        o.z = silu2(acc1[rt][8 * u + 4], acc1[rt][8 * u + 5]);
        o.w = silu2(acc1[rt][8 * u + 6], acc1[rt][8 * u + 7]);
        const int jb32 = 4 * rb + 2 * rh1 + rt, fs = (f0 >> 4) + 2 * ft + u;
        st16_sc1(sws, WS_H + (unsigned)(((i1 * 8 + jb32) * 112 + fs) * 1024 + lane * 16), o);
      }
  }
  SCHED();
  STAMP(6);
  FT(20);
  const unsigned flh_own = WS_FLH + (unsigned)((i1 * 2 + rb) * 16 + fc) * FS;
  if constexpr (ISA) {
    bar_vm<0>();
    if (tid == 0) flag_store(p.ws, flh_own, tag);
  } else {
    LAS char* I0 = L + 128 * 1024;
    LAS char* I1 = L + 128 * 1024 + 16384;
    const int g = lane >> 4, l15 = lane & 15;
    const bool special = (m1 == 0);
    bool published = false;
    if (t_ok) {
      wait_vm<4>();
      asm volatile("s_barrier" ::: "memory");
    } else {
      bar_vm<0>();
      if (tid == 0) flag_store(p.ws, flh_own, tag);
      published = true;
      if (w == 0) poll_words(sws, WS_FLA + (unsigned)(m1 >> 5) * 32u * FS, 32, tag, lane);
      asm volatile("s_barrier" ::: "memory");
      LD16Z(tq0, tsrc0, sws, "sc1"); LD16Z(tq1, tsrc1, sws, "sc1");
      wait_vm<0>();
    }
    asm volatile("" : "+v"(tq0), "+v"(tq1) :: "memory");
    {
      const int kap = tid >> 5, ch = tid & 31;
      *(LAS v4u*)(I0 + kap * 512 + ((ch ^ ((kap & 3) << 2)) << 4)) = tq0;
      *(LAS v4u*)(I0 + (kap + 16) * 512 + ((ch ^ ((kap & 3) << 2)) << 4)) = tq1;
    }
    BAR_LGKM();
    FT(21);
    LAS char* ta = I0 + (8 * hh + q4) * 512 + (((4 * w + 2 * blk + (p4 >> 1)) ^ (q4 << 2)) << 4) + 8 * (p4 & 1);
    const int n = lane & 31;
    LAS char* so = I1 + n * 512 + 8 * hh;
    {
      f32x16 acc = {};
      acc = mfma32(tr_read2(ta, ta + 2048), tb[6], acc);
      acc = mfma32(tr_read2(ta + 8192, ta + 10240), tb[7], acc);
#pragma unroll
      for (int Gq = 0; Gq < 4; ++Gq) {
        v2u o; o.x = pk2(acc[4 * Gq], acc[4 * Gq + 1]); o.y = pk2(acc[4 * Gq + 2], acc[4 * Gq + 3]);
        *(LAS v2u*)(so + (((4 * w + Gq) ^ (n & 15)) << 4)) = o;
      }
    }
    bar_vm<0>();
    if (!published && tid == 0) flag_store(p.ws, flh_own, tag);
    w2_issue1(G, 4, 0); w2_issue1(G, 4, 1); w2_issue1(G, 5, 0); w2_issue1(G, 5, 1);
    FT(22);
    const f32x4 z4 = {0.f, 0.f, 0.f, 0.f};
    const v4u F16fr = tb[0], F16fi = tb[1];
    v4u F16ir, F16ii;
    F16ir.x = F16fr.x; F16ir.y = F16fr.y; F16ir.z = F16fr.z ^ 0x80008000u; F16ir.w = F16fr.w ^ 0x80008000u;
    F16ii.x = F16fi.x ^ 0x80008000u; F16ii.y = F16fi.y ^ 0x80008000u; F16ii.z = F16fi.z; F16ii.w = F16fi.w;
    f32x4 zr[2], zi[2];
#pragma unroll
    for (int pp = 0; pp < 2; ++pp) {
      const int n0 = 4 * w + 2 * pp, n1 = n0 + 1;
      const int ch = 2 * (4 * g + q4) + (p4 >> 1);
      const v4u a1 = tr_read2(I1 + n0 * 512 + ((ch ^ (n0 & 15)) << 4) + 8 * (p4 & 1), I1 + n1 * 512 + ((ch ^ (n1 & 15)) << 4) + 8 * (p4 & 1));
      const f32x4 dr = mfma16(a1, pp ? tb[2] : F16fr, z4), di = mfma16(a1, pp ? tb[3] : F16fi, z4);
      const v4u b3 = cmul_pack<false>(dr, di, asf4(tb[10 + 2 * pp]), asf4(tb[11 + 2 * pp]));
      zr[pp] = mfma16(F16fr, b3, z4); zi[pp] = mfma16(F16fi, b3, z4);
    }
    v4u a4[2];
    if (!special) {
      a4[0] = cmul_pack<false>(zr[0], zi[0], Wc.r0, Wc.i0);
      a4[1] = cmul_pack<false>(zr[1], zi[1], Wc.r1, Wc.i1);
    } else {
      BAR_LGKM();
      LAS char* Z = I0;
#pragma unroll
      for (int pp = 0; pp < 2; ++pp)
#pragma unroll
        for (int r = 0; r < 4; ++r) { f32x2 v = {zr[pp][r], zi[pp][r]}; *(LAS f32x2*)(Z + ((w * 2 + pp) * 256 + l15 + 16 * (4 * g + r)) * 8) = v; }
      BAR_LGKM();
#pragma unroll
      for (int pp = 0; pp < 2; ++pp) {
        f32x4 cr, ci;
#pragma unroll
        for (int r = 0; r < 4; ++r) {
          const int K = l15 + 16 * (4 * g + r), Kn = pp ? 255 - K : (256 - K) & 255;
          const f32x2 y = *(LAS f32x2*)(Z + ((((8 - w) & 7) * 2 + pp) * 256 + Kn) * 8);
          cr[r] = y[0]; ci[r] = -y[1];
        }
        const f32x4 ar = pp ? Wc.r1 : Wc.r0, ai = pp ? Wc.i1 : Wc.i0, br = pp ? Wd.r1 : Wd.r0, bi = pp ? Wd.i1 : Wd.i0;
        const f32x4 yr = zr[pp] * ar - zi[pp] * ai + cr * br - ci * bi, yi = zr[pp] * ai + zi[pp] * ar + cr * bi + ci * br;
        a4[pp].x = pk2(yr[0], yr[1]); a4[pp].y = pk2(yr[2], yr[3]); a4[pp].z = pk2(yi[0], yi[1]); a4[pp].w = pk2(yi[2], yi[3]);
      }
      BAR_LGKM();
    }
    w2_issue1(G, 6, 0); w2_issue1(G, 6, 1); w2_issue1(G, 7, 0); w2_issue1(G, 7, 1);
#pragma unroll
    for (int pp = 0; pp < 2; ++pp) {
      const f32x4 dr = mfma16(a4[pp], F16ir, z4), di = mfma16(a4[pp], F16ii, z4);
      const v4u a5 = cmul_pack<true>(dr, di, asf4(tb[pp ? 14 : 10]), asf4(tb[pp ? 15 : 11]));
      const f32x4 er = mfma16(a5, pp ? tb[4] : F16ir, z4), ei = mfma16(a5, pp ? tb[5] : F16ii, z4);
      const int n0 = 4 * w + 2 * pp, n1 = n0 + 1;
      const int ch = 2 * l15 + (g >> 1);
      v2u o; o.x = pk2(er[0], er[1]); o.y = pk2(er[2], er[3]);
      *(LAS v2u*)(I0 + n0 * 512 + ((ch ^ ((n0 & 3) << 2)) << 4) + 8 * (g & 1)) = o;
      o.x = pk2(ei[0], ei[1]); o.y = pk2(ei[2], ei[3]);
      *(LAS v2u*)(I0 + n1 * 512 + ((ch ^ ((n1 & 3) << 2)) << 4) + 8 * (g & 1)) = o;
    }
    BAR_LGKM();
    FT(23);
    m5a = tb[8]; m5b = tb[9];
  }
  return tag;
}

DI void make_geo(Geo& G, const Params& p, int tz) {
  G.tid = tz; G.lane = G.tid & 63; G.w = __builtin_amdgcn_readfirstlane(G.tid >> 6);
  G.b = blockIdx.x;
  const int Lx = G.b >> 3, x8 = G.b & 7;
  {
    const int a = x8 & 3, h = x8 >> 2, rbi = Lx >> 3, c = Lx & 7;
    G.i = 2 * a + h; G.i1 = 2 * a + (rbi >> 1); G.rb = rbi & 1;
    G.fc = c < 4 ? 4 * h + c : 4 + 4 * h + c;
    G.at = rbi * 4 + c; G.m1 = ((rbi * 4 + c - 4) << 3) | x8;
  }
  G.hh = G.lane >> 5; G.blk = (G.lane >> 4) & 1; G.q4 = (G.lane & 15) >> 2; G.p4 = G.lane & 3;
  G.sx = make_srd(p.x, NS * ND * 2); G.sws = make_srd(p.ws, 16u << 20); G.sw2 = make_srd(p.w2, NF * ND * 2);
  G.jb = Lx >> 3; G.dt = Lx & 7; G.rh = G.w & 1; G.q = G.w >> 1;
  G.j = 64 * G.jb + 32 * G.rh + (G.lane & 31);
  const int rho = 16 * G.rh + (G.lane >> 3), chs = (G.lane & 7) ^ (4 * ((G.lane >> 4) & 1));
  G.vw20 = (unsigned)(rho * 1024 + G.dt * 128 + chs * 16); G.vw21 = G.vw20 + 8 * 1024;
  G.ldsw = uni((unsigned)(G.q * 4096 + G.rh * 2048));
  G.fq0b = uni((unsigned)(768 + 256 * G.q) * 1024u); G.fq1b = uni((unsigned)(192 * G.q) * 1024u);
}

__global__ __launch_bounds__(512, 2) void gm_kernel(Params p) {
  extern __shared__ __attribute__((aligned(16))) char smem[];
  LAS char* L = (LAS char*)smem;
  STAMP(0);
  FT(0);
  Geo G;
  make_geo(G, p, threadIdx.x);
  const bool isA = G.fc < 8;
  v4u gf[4], m5a, m5b;
  unsigned tag;
  if (isA) tag = phase1<true>(p, L, G, m5a, m5b); else tag = phase1<false>(p, L, G, m5a, m5b);
  { int tz = threadIdx.x; asm volatile("" : "+v"(tz)); make_geo(G, p, tz); }
  const int tid = G.tid, lane = G.lane, w = G.w, b = G.b, i = G.i, jb = G.jb, dt = G.dt, rh = G.rh, q = G.q, j = G.j;
  const int hh = G.hh, blk = G.blk, q4 = G.q4, p4 = G.p4;
  const v4u sws = G.sws;
  const unsigned ldsw = G.ldsw;
  const int m1 = G.m1;
  STAMP(7);
  FT(25);
  if (!isA) {
    LAS char* I0 = L + 128 * 1024;
    LAS char* I1 = L + 128 * 1024 + 16384;
    LAS char* ta = I0 + (8 * hh + q4) * 512 + (((4 * w + 2 * blk + (p4 >> 1)) ^ (q4 << 2)) << 4) + 8 * (p4 & 1);
    const int n = lane & 31;
    LAS char* so = I1 + n * 512 + 8 * hh;
    f32x16 acc = {};
    acc = mfma32(tr_read2(ta, ta + 2048), m5a, acc);
    acc = mfma32(tr_read2(ta + 8192, ta + 10240), m5b, acc);
#pragma unroll
    for (int Gq = 0; Gq < 4; ++Gq) {
      v2u o; o.x = pk2(acc[4 * Gq], acc[4 * Gq + 1]); o.y = pk2(acc[4 * Gq + 2], acc[4 * Gq + 3]);
      *(LAS v2u*)(so + (((4 * w + Gq) ^ (n & 15)) << 4)) = o;
    }
    BAR_LGKM();
    const int nn = tid >> 5, ch = tid & 31, ii = (nn >> 1) & 7, d0 = nn & 1;
    const bool special = (m1 == 0);
    const int g0 = special ? 2 * ii : ii, g1 = special ? 2 * ii + 1 : 8 + ii;
    const v4u v0 = lds_ld16(I1 + nn * 512 + ((ch ^ (nn & 15)) << 4));
    const v4u v1 = lds_ld16(I1 + (nn + 16) * 512 + ((ch ^ (nn & 15)) << 4));
    const unsigned ub = WS_U + (unsigned)m1 * 16384u + (unsigned)(d0 * 512 + ch * 16);
    st16_sc1(sws, ub + (unsigned)g0 * 1024u, v0);
    st16_sc1(sws, ub + (unsigned)g1 * 1024u, v1);
    SCHED();
  }
  const int sz = (q4 >> 1) & 1;
  LAS char* ra0 = L + q * 4096 + (4 * hh + q4) * 128 + (((0 ^ (4 * sz)) + 2 * blk + (p4 >> 1)) << 4) + 8 * (p4 & 1);
  LAS char* ra1 = L + q * 4096 + (4 * hh + q4) * 128 + (((4 ^ (4 * sz)) + 2 * blk + (p4 >> 1)) << 4) + 8 * (p4 & 1);
  v4u wp[4][4];
  BAR_LGKM();
#pragma unroll
  for (int t = 0; t < 4; ++t) {
    LAS char* s0 = ra0 + t * 16384; LAS char* s1 = ra1 + t * 16384;
    wp[t][0] = tr_read2(s0, s0 + 1024); wp[t][1] = tr_read2(s1, s1 + 1024);
    wp[t][2] = tr_read2(s0 + 2048, s0 + 3072); wp[t][3] = tr_read2(s1 + 2048, s1 + 3072);
  }
#define W2_8_11() do { w2_issue1(G, 8, 0); w2_issue1(G, 8, 1); w2_issue1(G, 9, 0); w2_issue1(G, 9, 1); w2_issue1(G, 10, 0); w2_issue1(G, 10, 1); w2_issue1(G, 11, 0); w2_issue1(G, 11, 1); } while (0)
  if (isA) { BAR_LGKM(); W2_8_11(); }
  const unsigned flh = WS_FLH + (unsigned)((i * 2 + (jb >> 1)) * 16) * FS;
  if (w == 0) poll_words(sws, flh, 16, tag, lane);
  if (isA) BAR_LGKM(); else bar_vm<0>();
  if (!isA && tid == 0) flag_store(p.ws, WS_FLB + (unsigned)m1 * FS, tag);
  STAMP(14);
  STAMP(8);
  FT(26);
  const unsigned vh = (unsigned)lane * 16u;
  const unsigned hbase = WS_H + (unsigned)((i * 8 + 2 * jb + rh) * 112) * 1024u;
  const unsigned hb0 = uni(hbase + (G.fq0b >> 4)), hb1 = uni(hbase + (G.fq1b >> 4));
  v2u gg;
  v4u hf[14][2];
#define H_ISSUE(t) do { LD16I(hf[t][0], vh, sws, ((t) < 8 ? hb0 : hb1), (2 * ((t) & 7)) * 1024, "sc1"); LD16I(hf[t][1], vh, sws, ((t) < 8 ? hb0 : hb1), (2 * ((t) & 7) + 1) * 1024, "sc1"); } while (0)
  H_ISSUE(0); H_ISSUE(1);
  asm volatile("s_barrier" ::: "memory");
  H_ISSUE(2); H_ISSUE(3); H_ISSUE(4); H_ISSUE(5); H_ISSUE(6);
  LD8Z(gg, WS_GATE + (unsigned)(i * 256 + j) * 8u, sws, "sc1");
  SCHED();
  f32x16 accb[2] = {}, acca[2] = {};
  v4u wf[2][4];
  unsigned fl0, fl1;
  v4u ub0, ub1, ub2, ub3;
  const unsigned vu = WS_U + (unsigned)(((64 * q + 16 * rh + (lane >> 3)) * 8 + i) * 1024 + dt * 128 + ((lane & 7) ^ (4 * ((lane >> 4) & 1))) * 16);
#define G2_BAR(n, t) asm volatile("s_waitcnt vmcnt(" STR(n) ") lgkmcnt(0)\n\ts_barrier" : "+v"(hf[t][0]), "+v"(hf[t][1]) :: "memory")
#define G2_CORE(t, c, n, RD, acc, b0, b1, X0, X1, X2)                                             \
  {                                                                                                 \
    FT(30 + (t));                                                                                   \
    LAS char* s0 = ra0 + (((t) + 1) & 7) * 16384; LAS char* s1 = ra1 + (((t) + 1) & 7) * 16384;    \
    asm volatile("" : "+v"(c[0]), "+v"(c[1]), "+v"(c[2]), "+v"(c[3]));                             \
    asm volatile("" :: "v"(acc[0]), "v"(acc[1]));                                                   \
    SCHED();                                                                                        \
    acc[0] = mfma32(c[0], b0, acc[0]); if (RD) n[0] = tr_read2(s0, s0 + 1024); SCHED();            \
    acc[1] = mfma32(c[1], b0, acc[1]); if (RD) n[1] = tr_read2(s1, s1 + 1024); X0; SCHED();        \
    acc[0] = mfma32(c[2], b1, acc[0]); if (RD) n[2] = tr_read2(s0 + 2048, s0 + 3072); X1; SCHED(); \
    acc[1] = mfma32(c[3], b1, acc[1]); if (RD) n[3] = tr_read2(s1 + 2048, s1 + 3072); X2; SCHED(); \
  }
#define G2_PRE(t, X2) G2_CORE(t, wp[t], wf[0], (t) == 3, accb, hf[t][0], hf[t][1], if (!isA) w2_issue1(G, (t) + 8, 0), if (!isA) w2_issue1(G, (t) + 8, 1), X2)
#define G2_STEP(t, X0, X1, X2) G2_CORE(t, wf[(t) & 1], wf[((t) + 1) & 1], true, accb, hf[t][0], hf[t][1], X0, X1, X2)
  G2_BAR(13, 0); G2_PRE(0, H_ISSUE(7))
  G2_BAR(13, 1); G2_PRE(1, H_ISSUE(8))
  G2_BAR(13, 2); G2_PRE(2, H_ISSUE(9))
  G2_BAR(13, 3); G2_PRE(3, H_ISSUE(10))
  G2_BAR(13, 4); G2_STEP(4, w2_issue1(G, 12, 0), w2_issue1(G, 12, 1), H_ISSUE(11))
  G2_BAR(15, 5); G2_STEP(5, w2_issue1(G, 13, 0), w2_issue1(G, 13, 1), H_ISSUE(12))
  if (w == 0) { LD4Z(fl0, WS_FLB + (unsigned)lane * FS, sws, "sc1"); LD4Z(fl1, WS_FLB + (64u + (unsigned)lane) * FS, sws, "sc1"); }
  G2_BAR(17, 6); G2_STEP(6, NOP, NOP, H_ISSUE(13))
  STAMP(9);
  G2_BAR(16, 7); G2_STEP(7, NOP, NOP, NOP)
  LAS unsigned* verdict = (LAS unsigned*)(L + 128 * 1024);
  if (w == 0) {
    asm volatile("s_waitcnt vmcnt(2)" : "+v"(fl0), "+v"(fl1) :: "memory");
    const unsigned ok = __all(fl0 == tag && fl1 == tag) ? 1u : 0u;
    if (lane == 0) *verdict = ok;
  }
  G2_BAR(14, 8);
  const int u_early = __builtin_amdgcn_readfirstlane(*(volatile LAS unsigned*)verdict);
  STAMP(15);
#define U_ISSUE() do { LD16Z(ub0, vu, sws, "sc1"); LD16Z(ub1, vu + 8u * 8192u, sws, "sc1"); LD16Z(ub2, vu + 32u * 8192u, sws, "sc1"); LD16Z(ub3, vu + 40u * 8192u, sws, "sc1"); } while (0)
  if (u_early) U_ISSUE();
  GRot grot;
  G2_STEP(8, gen_g_init(G, grot), NOP, gen_g_frag<0>(G, grot, gf[0]))
  unsigned e0, e1, e2, e3;
  if (b == 0 && w == 1) {
    LD4Z(e0, WS_FLH + (unsigned)lane * FS, sws, "sc1"); LD4Z(e1, WS_FLH + (64u + (unsigned)lane) * FS, sws, "sc1");
    LD4Z(e2, WS_FLH + (128u + (unsigned)lane) * FS, sws, "sc1"); LD4Z(e3, WS_FLH + (192u + (unsigned)lane) * FS, sws, "sc1");
  }
  G2_BAR(12, 9); G2_STEP(9, NOP, NOP, gen_g_frag<1>(G, grot, gf[1]))
  G2_BAR(10, 10); G2_STEP(10, NOP, NOP, gen_g_frag<2>(G, grot, gf[2]))
  G2_BAR(6, 11); G2_STEP(11, NOP, NOP, gen_g_frag<3>(G, grot, gf[3]))
  G2_BAR(2, 12);
  G2_STEP(12, NOP, NOP, NOP)
#define U_PUT() do {                                                                                                        \
    asm volatile("s_waitcnt vmcnt(0)" : "+v"(ub0), "+v"(ub1), "+v"(ub2), "+v"(ub3) :: "memory");                             \
    *(LAS v4u*)(L + 6 * 16384 + ldsw + lane * 16) = ub0; *(LAS v4u*)(L + 6 * 16384 + ldsw + 1024 + lane * 16) = ub1;         \
    *(LAS v4u*)(L + 7 * 16384 + ldsw + lane * 16) = ub2; *(LAS v4u*)(L + 7 * 16384 + ldsw + 1024 + lane * 16) = ub3; } while (0)
  if (u_early) U_PUT();
  G2_BAR(0, 13);
  asm volatile("" : "+v"(gg));
  if (b == 0 && w == 1) {
    asm volatile("" : "+v"(e0), "+v"(e1), "+v"(e2), "+v"(e3));
    if (!__all(e0 == tag && e1 == tag && e2 == tag && e3 == tag)) {
#pragma unroll 1
      for (int u = 0; u < 4; ++u) poll_words(sws, WS_FLH + 64u * FS * u, 64, tag, lane);
    }
    if (lane == 0) flag_store(p.ws, WS_EPOCH, tag);
  }
  G2_CORE(13, wf[1], wf[0], u_early, accb, hf[13][0], hf[13][1], NOP, NOP, NOP)
  if (!u_early) {
    if (w == 0) { poll_words(sws, WS_FLB, 64, tag, lane); poll_words(sws, WS_FLB + 64u * FS, 64, tag, lane); }
    BAR_LGKM();
    U_ISSUE();
    U_PUT();
    BAR_LGKM();
    LAS char* s0 = ra0 + 6 * 16384; LAS char* s1 = ra1 + 6 * 16384;
    wf[0][0] = tr_read2(s0, s0 + 1024); wf[0][1] = tr_read2(s1, s1 + 1024); wf[0][2] = tr_read2(s0 + 2048, s0 + 3072); wf[0][3] = tr_read2(s1 + 2048, s1 + 3072);
  }
  STAMP(10);
  BAR_LGKM(); G2_CORE(14, wf[0], wf[1], true, acca, gf[0], gf[1], NOP, NOP, NOP)
  BAR_LGKM(); G2_CORE(15, wf[1], wf[0], false, acca, gf[2], gf[3], NOP, NOP, NOP)
  FT(50);
  {
    const float og = asf(gg.y), gs = asf(gg.x) * (1.f / 524288.f);
    const int row = 32 * rh + (lane & 31);
#pragma unroll
    for (int dh = 0; dh < 2; ++dh)
#pragma unroll
      for (int G = 0; G < 4; ++G) {
        f32x4 v;
#pragma unroll
        for (int e = 0; e < 4; ++e) v[e] = __builtin_fmaf(og, accb[dh][4 * G + e], gs * acca[dh][4 * G + e]);
        *(LAS f32x4*)(L + q * 16384 + row * 256 + (((8 * dh + 2 * G + hh) ^ (row & 7)) << 4)) = v;
      }
  }
  BAR_LGKM();
  FT(51);
  {
    const int row = tid >> 3, c8 = tid & 7;
    f32x4 s0 = {0.f, 0.f, 0.f, 0.f}, s1 = s0;
#pragma unroll
    for (int qq = 0; qq < 4; ++qq) {
      s0 += *(LAS f32x4*)(L + qq * 16384 + row * 256 + (((2 * c8) ^ (row & 7)) << 4));
      s1 += *(LAS f32x4*)(L + qq * 16384 + row * 256 + (((2 * c8 + 1) ^ (row & 7)) << 4));
    }
    v4u o; o.x = pk2(s0[0], s0[1]); o.y = pk2(s0[2], s0[3]); o.z = pk2(s1[0], s1[1]); o.w = pk2(s1[2], s1[3]);
    *(GAS v4u*)((GAS char*)p.out + (size_t)(i + 8 * (64 * jb + row)) * 1024 + dt * 128 + c8 * 16) = o;
  }
  STAMP(11);
}

}

extern "C" void kernel_launch(void* const* d_in, const int* in_sizes, int n_in, void* d_out, int out_size, void* d_ws, size_t ws_size,
                              hipStream_t stream) {
  Params p;
  p.x = d_in[0]; p.wr = d_in[1]; p.wi = d_in[2]; p.w1 = d_in[4]; p.w2 = d_in[5]; p.gw = d_in[6];
  p.out = d_out; p.ws = d_ws;
  (void)hipFuncSetAttribute((const void*)gm_kernel, hipFuncAttributeMaxDynamicSharedMemorySize, LDS_BYTES);
  gm_kernel<<<256, 512, LDS_BYTES, stream>>>(p);
}
```

```cpp
#include <hip/hip_runtime.h>
#include <hip/hip_bf16.h>
#include <stdint.h>

namespace {

typedef unsigned v4u __attribute__((ext_vector_type(4)));
typedef unsigned v2u __attribute__((ext_vector_type(2)));
typedef float f32x16 __attribute__((ext_vector_type(16)));
typedef float f32x4 __attribute__((ext_vector_type(4)));
typedef float f32x2 __attribute__((ext_vector_type(2)));
typedef __bf16 bf16x8 __attribute__((ext_vector_type(8)));
typedef __bf16 bf16x2 __attribute__((ext_vector_type(2)));
typedef short s16x4 __attribute__((ext_vector_type(4)));
#define LAS __attribute__((address_space(3)))
#define GAS __attribute__((address_space(1)))
#define DI __device__ __forceinline__

constexpr int NS = 2048, ND = 512, NF = 1792;
constexpr unsigned WS_EPOCH = 0;
constexpr unsigned FS = 64;
constexpr unsigned WS_FLA = 4096;
constexpr unsigned WS_FLH = 16384;
constexpr unsigned WS_FLB = 32768;
constexpr unsigned WS_GATE = 65536;
constexpr unsigned WS_T = 1u << 20;
constexpr unsigned WS_U = 3u << 20;
constexpr unsigned WS_H = 5u << 20;
constexpr unsigned H_BYTES = 8u * 8u * 112u * 1024u;
constexpr int LDS_BYTES = 160 * 1024;
constexpr unsigned kSpinCap = 1u << 20;

struct Params {
  const void* x; const void* wr; const void* wi; const void* w1; const void* w2; const void* gw;
  void* out; void* ws;
};

constexpr double kPi = 3.14159265358979323846;
constexpr double ccos(double x) { double t = 1, s = 1; for (int n = 1; n < 16; ++n) { t *= -x * x / ((2 * n - 1) * (2 * n)); s += t; } return s; }
constexpr double cosn(int e, int n) { e = ((e % n) + n) % n; double x = 2 * kPi * e / n; if (x > kPi) x -= 2 * kPi; return ccos(x); }
constexpr double wre(int e, int n) { return cosn(e, n); }
constexpr double wim(int e, int n) { return -cosn(4 * e - n, 4 * n); }
constexpr unsigned bfb(double v) { float f = (float)v; unsigned u = __builtin_bit_cast(unsigned, f); u += 0x7fffu + ((u >> 16) & 1u); return u >> 16; }
constexpr unsigned bf2(double a, double b) { return bfb(a) | (bfb(b) << 16); }
constexpr unsigned fbits(double v) { return __builtin_bit_cast(unsigned, (float)v); }
struct Tab { unsigned v[16][64][4]; };
constexpr Tab make_tab() {
  Tab t{};
  for (int l = 0; l < 64; ++l) {
    const int g = l >> 4, l15 = l & 15, hh = l >> 5, n = l & 31;
    for (int mtx = 0; mtx < 3; ++mtx) {
      double r[4] = {}, im[4] = {};
      for (int q = 0; q < 4; ++q) {
        const int a = 4 * g + q;
        if (mtx == 0) { r[q] = wre(l15 * a, 16); im[q] = wim(l15 * a, 16); }
        else if (mtx == 1) { r[q] = wre(a * (2 * l15 + 1), 32); im[q] = wim(a * (2 * l15 + 1), 32); }
        else { r[q] = wre(l15 * (2 * a + 1), 32); im[q] = -wim(l15 * (2 * a + 1), 32); }
      }
      t.v[2 * mtx][l][0] = bf2(r[0], r[1]); t.v[2 * mtx][l][1] = bf2(r[2], r[3]); t.v[2 * mtx][l][2] = bf2(-im[0], -im[1]); t.v[2 * mtx][l][3] = bf2(-im[2], -im[3]);
      t.v[2 * mtx + 1][l][0] = bf2(im[0], im[1]); t.v[2 * mtx + 1][l][1] = bf2(im[2], im[3]); t.v[2 * mtx + 1][l][2] = bf2(r[0], r[1]); t.v[2 * mtx + 1][l][3] = bf2(r[2], r[3]);
    }
    {
      const int m2 = n >> 2, pp = (n >> 1) & 1, cp = n & 1;
      double v0[8] = {}, v1[8] = {};
      for (int jj = 0; jj < 8; ++jj) {
        const int ii = (8 * hh + jj) >> 1, d0 = jj & 1;
        const double sg = (d0 & pp) ? -1.0 : 1.0, orr = sg * wre(ii * m2, 8), oi = sg * wim(ii * m2, 8);
        v0[jj] = cp ? oi : orr; v1[jj] = cp ? orr : -oi;
      }
      for (int q = 0; q < 4; ++q) { t.v[6][l][q] = bf2(v0[2 * q], v0[2 * q + 1]); t.v[7][l][q] = bf2(v1[2 * q], v1[2 * q + 1]); }
    }
    {
      const int cq = n >> 4, ii = (n >> 1) & 7, d0 = n & 1;
      for (int s = 0; s < 2; ++s) {
        double v[8] = {};
        for (int jj = 0; jj < 8; ++jj) {
          const int m2k = 4 * s + 2 * hh + (jj >> 2), pk = (jj >> 1) & 1, ck = jj & 1;
          const double sg = (d0 & pk) ? -1.0 : 1.0, orr = sg * wre(ii * m2k, 8), oi = -sg * wim(ii * m2k, 8);
          v[jj] = cq ? (ck ? orr : oi) : (ck ? -oi : orr);
        }
        for (int q = 0; q < 4; ++q) t.v[8 + s][l][q] = bf2(v[2 * q], v[2 * q + 1]);
      }
    }
    for (int q = 0; q < 4; ++q) {
      const int a = 4 * g + q;
      t.v[10][l][q] = fbits(wre(a * (2 * l15), 512));      t.v[11][l][q] = fbits(wim(a * (2 * l15), 512));
      t.v[12][l][q] = fbits(wre(a * (2 * l15 + 1), 512));  t.v[13][l][q] = fbits(wim(a * (2 * l15 + 1), 512));
      t.v[14][l][q] = fbits(wre(l15 * (2 * a + 1), 512));  t.v[15][l][q] = fbits(wim(l15 * (2 * a + 1), 512));
    }
  }
  return t;
}
__device__ const Tab kTab = make_tab();

constexpr unsigned STAMP_OFF = 512 * 1024;
#define STAMP(k) do { if ((k) == 0) { if (true) asm volatile("s_memrealtime %0\n\ts_waitcnt lgkmcnt(0)" : "=s"(pad_t) :: "memory"); } } while (0)
#define FT(k) do {} while (0)

DI v4u make_srd(const void* p, unsigned bytes) {
  const unsigned long long a = (unsigned long long)p;
  v4u r; r.x = (unsigned)a; r.y = (unsigned)(a >> 32) & 0xffffu; r.z = bytes; r.w = 0x00020000u;
  return r;
}
template <unsigned LDSI, unsigned SOI> DI void dma16(v4u srd, unsigned voff, unsigned ldsb, unsigned sob) {
  unsigned tmp;
  asm volatile("s_add_u32 m0, %3, %5\n\ts_add_u32 %0, %4, %6\n\tbuffer_load_dwordx4 %1, %2, %0 offen lds"
               : "=&s"(tmp) : "v"(voff), "s"(srd), "s"(ldsb), "s"(sob), "i"(LDSI), "i"(SOI) : "memory", "scc");
}
template <unsigned LDSI, unsigned SOI> DI void dma16z(v4u srd, unsigned voff, unsigned ldsb) {
  unsigned tmp;
  asm volatile("s_add_u32 m0, %3, %4\n\ts_mov_b32 %0, %5\n\tbuffer_load_dwordx4 %1, %2, %0 offen lds"
               : "=&s"(tmp) : "v"(voff), "s"(srd), "s"(ldsb), "i"(LDSI), "i"(SOI) : "memory", "scc");
}
DI unsigned uni(unsigned v) { return (unsigned)__builtin_amdgcn_readfirstlane((int)v); }
#define LD16(dst, voffv, srd, soffv, flags) asm volatile("buffer_load_dwordx4 %0, %1, %2, %3 offen " flags : "=v"(dst) : "v"(voffv), "s"(srd), "s"(soffv) : "memory")
#define LD16I(dst, voffv, srd, sob, soi, flags) do { unsigned tmp_; asm volatile("s_add_u32 %1, %4, %5\n\tbuffer_load_dwordx4 %0, %2, %3, %1 offen " flags : "=v"(dst), "=&s"(tmp_) : "v"(voffv), "s"(srd), "s"(sob), "i"(soi) : "memory", "scc"); } while (0)
#define LD16Z(dst, voffv, srd, flags) asm volatile("buffer_load_dwordx4 %0, %1, %2, 0 offen " flags : "=v"(dst) : "v"(voffv), "s"(srd) : "memory")
#define LD4Z(dst, voffv, srd, flags) asm volatile("buffer_load_dword %0, %1, %2, 0 offen " flags : "=v"(dst) : "v"(voffv), "s"(srd) : "memory")
#define LD2Z(dst, voffv, srd, flags) asm volatile("buffer_load_ushort %0, %1, %2, 0 offen " flags : "=v"(dst) : "v"(voffv), "s"(srd) : "memory")
#define LD8Z(dst, voffv, srd, flags) asm volatile("buffer_load_dwordx2 %0, %1, %2, 0 offen " flags : "=v"(dst) : "v"(voffv), "s"(srd) : "memory")
#define LD8(dst, voffv, srd, soffv, flags) asm volatile("buffer_load_dwordx2 %0, %1, %2, %3 offen " flags : "=v"(dst) : "v"(voffv), "s"(srd), "s"(soffv) : "memory")
#define LD4(dst, voffv, srd, soffv, flags) asm volatile("buffer_load_dword %0, %1, %2, %3 offen " flags : "=v"(dst) : "v"(voffv), "s"(srd), "s"(soffv) : "memory")
#define LD2(dst, voffv, srd, soffv, flags) asm volatile("buffer_load_ushort %0, %1, %2, %3 offen " flags : "=v"(dst) : "v"(voffv), "s"(srd), "s"(soffv) : "memory")
#define XSTR(x) #x
#define STR(x) XSTR(x)
#define BAR_VM(n) asm volatile("s_waitcnt vmcnt(" STR(n) ") lgkmcnt(0)\n\ts_barrier" ::: "memory")
#define BAR_LGKM() asm volatile("s_waitcnt lgkmcnt(0)\n\ts_barrier" ::: "memory")
#define SCHED() __builtin_amdgcn_sched_barrier(0)
template <int N> DI void bar_vm() { asm volatile("s_waitcnt vmcnt(%0) lgkmcnt(0)\n\ts_barrier" :: "i"(N) : "memory"); }
template <int N> DI void wait_vm() { asm volatile("s_waitcnt vmcnt(%0)" :: "i"(N) : "memory"); }

DI unsigned pk2(float a, float b) { f32x2 p = {a, b}; return __builtin_bit_cast(unsigned, __builtin_convertvector(p, bf16x2)); }
DI float bflo(unsigned u) { return __builtin_bit_cast(float, u << 16); }
DI float bfhi(unsigned u) { return __builtin_bit_cast(float, u & 0xffff0000u); }
DI float asf(unsigned u) { return __builtin_bit_cast(float, u); }
DI f32x16 mfma32(v4u a, v4u b, f32x16 c) {
  return __builtin_amdgcn_mfma_f32_32x32x16_bf16(__builtin_bit_cast(bf16x8, a), __builtin_bit_cast(bf16x8, b), c, 0, 0, 0);
}
DI f32x4 mfma16(v4u a, v4u b, f32x4 c) {
  return __builtin_amdgcn_mfma_f32_16x16x32_bf16(__builtin_bit_cast(bf16x8, a), __builtin_bit_cast(bf16x8, b), c, 0, 0, 0);
}
DI v2u tr_read(LAS char* p) { return __builtin_bit_cast(v2u, __builtin_amdgcn_ds_read_tr16_b64_v4i16((LAS s16x4*)p)); }
DI v4u tr_read2(LAS char* p0, LAS char* p1) { v2u a = tr_read(p0), b = tr_read(p1); v4u r; r.x = a.x; r.y = a.y; r.z = b.x; r.w = b.y; return r; }
DI v4u lds_ld16(LAS char* p) { return *(LAS v4u*)p; }
DI float cosrev(float r) { return __builtin_amdgcn_cosf(r); }
DI float sinrev(float r) { return __builtin_amdgcn_sinf(r); }
DI __amdgpu_buffer_rsrc_t as_rsrc(v4u srd) { return __builtin_amdgcn_make_buffer_rsrc((void*)(((unsigned long long)srd.y << 32) | srd.x), 0, srd.z, 0x00020000); }
DI void st16_sc1(v4u srd, unsigned voff, v4u v) { __builtin_amdgcn_raw_buffer_store_b128(v, as_rsrc(srd), voff, 0, 16); }
DI void flag_store(void* ws, unsigned off, unsigned tag) { __hip_atomic_store((GAS unsigned*)((GAS char*)ws + off), tag, __ATOMIC_RELAXED, __HIP_MEMORY_SCOPE_AGENT); }
template <int CTRL, int RMASK> DI float dpp_add(float v) { return v + asf((unsigned)__builtin_amdgcn_update_dpp(0, (int)__builtin_bit_cast(unsigned, v), CTRL, RMASK, 0xf, true)); }
DI void poll_words(v4u sws, unsigned off, int n, unsigned tag, int lane) {
  const unsigned vo = off + FS * (unsigned)(lane < n ? lane : 0);
#pragma unroll 1
  for (unsigned spins = 0; spins < kSpinCap; ++spins) {
    unsigned f;
    asm volatile("buffer_load_dword %0, %1, %2, 0 offen sc1\n\ts_waitcnt vmcnt(0)" : "=&v"(f) : "v"(vo), "s"(sws) : "memory");
    if (__all(f == tag)) return;
    __builtin_amdgcn_s_sleep(4);
  }
}

constexpr unsigned A_XIMG = 64 * 1024, A_FIMG = 128 * 1024;
DI void phaseA_genF(LAS char* L, int i, int mt, int w, int lane) {
  const int mh = w >> 2, sg = w & 3, n = lane & 31, hh = lane >> 5;
  const int m1c = 64 * mt + 32 * mh + n, m1 = m1c >> 1, c = m1c & 1;
  const int j0 = 64 * sg + 32 * hh;
  const float ph = c ? 0.25f : 0.f;
  const float k2 = 2.f * cosrev((float)(8 * m1) * (1.f / 2048.f));
  float vm = cosrev((float)(((i + 8 * (j0 - 1)) * m1) & 2047) * (1.f / 2048.f) + ph);
  float v0 = cosrev((float)(((i + 8 * j0) * m1) & 2047) * (1.f / 2048.f) + ph);
  const unsigned cst = c ? 0x3F800000u : 0x00003F80u;
  const bool sp = m1c < 2;
#pragma unroll
  for (int s = 0; s < 4; ++s) {
    float e[8];
#pragma unroll
    for (int q = 0; q < 8; ++q) { e[q] = v0; const float vn = __builtin_fmaf(k2, v0, -vm); vm = v0; v0 = vn; }
    v4u f; f.x = pk2(e[0], e[1]); f.y = pk2(e[2], e[3]); f.z = pk2(e[4], e[5]); f.w = pk2(e[6], e[7]);
    if (mt == 0 && w < 4) { f.x = sp ? cst : f.x; f.y = sp ? cst : f.y; f.z = sp ? cst : f.z; f.w = sp ? cst : f.w; }
    *(LAS v4u*)(L + A_FIMG + (mh * 16 + sg * 4 + s) * 1024 + lane * 16) = f;
  }
}

struct Frag1 { v4u a[4], b0[4], b1[4]; };
DI void g1_read(Frag1& f, LAS char* wa, LAS char* xb0, LAS char* xb1, LAS char* xb2, LAS char* xb3, int slot) {
  const int so = slot * 32768;
  f.a[0] = tr_read2(wa + so, wa + so + 1024);              f.b0[0] = lds_ld16(xb0 + so); f.b1[0] = lds_ld16(xb0 + so + 4096);
  f.a[1] = tr_read2(wa + so + 4096, wa + so + 5120);       f.b0[1] = lds_ld16(xb1 + so); f.b1[1] = lds_ld16(xb1 + so + 4096);
  f.a[2] = tr_read2(wa + so + 8192, wa + so + 9216);       f.b0[2] = lds_ld16(xb2 + so); f.b1[2] = lds_ld16(xb2 + so + 4096);
  f.a[3] = tr_read2(wa + so + 12288, wa + so + 13312);     f.b0[3] = lds_ld16(xb3 + so); f.b1[3] = lds_ld16(xb3 + so + 4096);
}
DI unsigned silu2(float a, float b) {
  const f32x2 h = {a, b}, t = h * -1.44269504088896341f;
  f32x2 e = {__builtin_amdgcn_exp2f(t[0]), __builtin_amdgcn_exp2f(t[1])};
  e = e + 1.f;
  const f32x2 r = {__builtin_amdgcn_rcpf(e[0]), __builtin_amdgcn_rcpf(e[1])};
  return __builtin_bit_cast(unsigned, __builtin_convertvector(h * r, bf16x2));
}

DI f32x2 lo2(const f32x4& v) { return __builtin_shufflevector(v, v, 0, 1); }
DI f32x2 hi2(const f32x4& v) { return __builtin_shufflevector(v, v, 2, 3); }
DI unsigned pk2v(f32x2 p) { return __builtin_bit_cast(unsigned, __builtin_convertvector(p, bf16x2)); }
DI f32x4 asf4(v4u v) { return __builtin_bit_cast(f32x4, v); }
template <bool CONJ> DI v4u cmul_pack(const f32x4& re, const f32x4& im, const f32x4& tr, const f32x4& ti) {
  const f32x2 r0 = lo2(re), r1 = hi2(re), i0 = lo2(im), i1 = hi2(im), c0 = lo2(tr), c1 = hi2(tr), s0 = lo2(ti), s1 = hi2(ti);
  f32x2 a0, a1, b0, b1;
  if (CONJ) {
    a0 = __builtin_elementwise_fma(r0, c0, i0 * s0); a1 = __builtin_elementwise_fma(r1, c1, i1 * s1);
    b0 = __builtin_elementwise_fma(i0, c0, -(r0 * s0)); b1 = __builtin_elementwise_fma(i1, c1, -(r1 * s1));
  } else {
    a0 = __builtin_elementwise_fma(r0, c0, -(i0 * s0)); a1 = __builtin_elementwise_fma(r1, c1, -(i1 * s1));
    b0 = __builtin_elementwise_fma(r0, s0, i0 * c0); b1 = __builtin_elementwise_fma(r1, s1, i1 * c1);
  }
  v4u r; r.x = pk2v(a0); r.y = pk2v(a1); r.z = pk2v(b0); r.w = pk2v(b1); return r;
}
struct cf { float r, i; };
DI cf cmul(cf a, cf b) { return {a.r * b.r - a.i * b.i, a.r * b.i + a.i * b.r}; }
DI cf cmulc(cf a, cf b) { return {a.r * b.r + a.i * b.i, a.i * b.r - a.r * b.i}; }
DI unsigned w_src(int m, int lane) {
  const int mm = (NS - m) & (NS - 1);
  const int e = lane < 32 ? m * 257 - (m & 1) : mm * 257 + (m & 1);
  return (unsigned)(e * 2 + (lane & 31) * 16);
}
DI unsigned w_tail_src(int m, int lane) {
  const int mm = (NS - m) & (NS - 1), l = lane & 3;
  return (unsigned)((l < 2 ? m * 257 + 255 + l : mm * 257 + (l - 2) * 256) * 2);
}
struct WTail { unsigned r255, r256, rm0, rm256, i255, i256, im0, im256; };
DI WTail w_tail(unsigned tr, unsigned ti) {
  return {(unsigned)__builtin_amdgcn_readlane((int)tr, 0), (unsigned)__builtin_amdgcn_readlane((int)tr, 1), (unsigned)__builtin_amdgcn_readlane((int)tr, 2), (unsigned)__builtin_amdgcn_readlane((int)tr, 3),
          (unsigned)__builtin_amdgcn_readlane((int)ti, 0), (unsigned)__builtin_amdgcn_readlane((int)ti, 1), (unsigned)__builtin_amdgcn_readlane((int)ti, 2), (unsigned)__builtin_amdgcn_readlane((int)ti, 3)};
}
struct WVal { f32x4 r0, i0, r1, i1; };
struct WRawL { unsigned r[4][2], i[4][2]; };
DI void w_put(LAS char* scr, v4u qr, v4u qi, int lane) { *(LAS v4u*)(scr + lane * 16) = qr; *(LAS v4u*)(scr + 1024 + lane * 16) = qi; }
DI int w_off(int m, int lane) { const int k1 = lane & 15, g = lane >> 4, odd = m & 1; return lane < 32 ? (2 * k1 + 128 * g + odd) * 2 : 512 + (511 - 2 * k1 - 128 * g - odd) * 2; }
DI void w_get(WRawL& o, LAS char* scr, int off, int lane, int r) {
  LAS unsigned* a = (LAS unsigned*)(scr + ((off + (lane < 32 ? 64 : -64) * r) & ~3));
  o.r[r][0] = a[0]; o.r[r][1] = a[1]; o.i[r][0] = a[256]; o.i[r][1] = a[257];
}
DI WVal w_unpack(const WRawL& x, const WTail& t, int off, int m, int lane) {
  const bool lo = lane < 32;
  const unsigned sh = (off & 2) * 8;
  unsigned rr[4], ii[4];
#pragma unroll
  for (int r = 0; r < 4; ++r) { rr[r] = __builtin_amdgcn_alignbit(x.r[r][1], x.r[r][0], sh); ii[r] = __builtin_amdgcn_alignbit(x.i[r][1], x.i[r][0], sh); }
  if (m & 1) { rr[3] = lane == 31 ? (rr[3] & 0xffffu) | (t.r255 << 16) : rr[3]; ii[3] = lane == 31 ? (ii[3] & 0xffffu) | (t.i255 << 16) : ii[3]; }
  else { rr[0] = lane == 32 ? (rr[0] & 0xffffu) | (t.rm256 << 16) : rr[0]; ii[0] = lane == 32 ? (ii[0] & 0xffffu) | (t.im256 << 16) : ii[0]; }
  const unsigned s0 = lo ? 16u : 0u, s1 = lo ? 0u : 16u, sg = lo ? 0u : 0x80000000u;
  WVal o;
#pragma unroll
  for (int r = 0; r < 4; ++r) {
    o.r0[r] = asf((rr[r] << s0) & 0xffff0000u); o.r1[r] = asf((rr[r] << s1) & 0xffff0000u);
    o.i0[r] = asf(((ii[r] << s0) & 0xffff0000u) ^ sg); o.i1[r] = asf(((ii[r] << s1) & 0xffff0000u) ^ sg);
  }
  const bool av = (lane & 31) == 0;
  const float xr = bflo(lo ? t.rm0 : t.r256), xi = asf(((lo ? t.im0 : t.i256) << 16) ^ (sg ^ 0x80000000u));
  o.r0[0] = av ? 0.5f * (o.r0[0] + xr) : o.r0[0];
  o.i0[0] = av ? 0.5f * (o.i0[0] + xi) : o.i0[0];
  return o;
}

DI void packed_coef(WVal& a, WVal& b, int m2) {
  const cf om = {cosrev((float)m2 * 0.0625f), -sinrev((float)m2 * 0.0625f)};
#pragma unroll
  for (int pp = 0; pp < 2; ++pp)
#pragma unroll
    for (int r = 0; r < 4; ++r) {
      const float war = pp ? a.r1[r] : a.r0[r], wai = pp ? a.i1[r] : a.i0[r], wbr = pp ? b.r1[r] : b.r0[r], wbi = pp ? b.i1[r] : b.i0[r];
      const cf sp = {war + wbr, wai + wbi}, sm = {war - wbr, wai - wbi};
      const cf t1 = cmulc(sm, om), t2 = cmul(sm, om);
      const cf cE = {sp.r - t1.i, sp.i + t1.r}, cO = {t2.r - sp.i, t2.i + sp.r};
      const float alr = 0.25f * (cE.r + cO.i), ali = 0.25f * (cE.i - cO.r), ber = 0.25f * (cE.r - cO.i), bei = 0.25f * (cE.i + cO.r);
      if (pp) { a.r1[r] = alr; a.i1[r] = ali; b.r1[r] = ber; b.i1[r] = bei; } else { a.r0[r] = alr; a.i0[r] = ali; b.r0[r] = ber; b.i0[r] = bei; }
    }
}

struct Geo {
  int tid, lane, w, b, i, i1, at, m1, rb, fc, hh, blk, q4, p4;
  int jb, dt, rh, q, j;
  v4u sx, sws, sw2;
  unsigned vw20, vw21, ldsw, fq0b, fq1b;
};
template <int T, int U> DI void w2_dma(const v4u& sw2, unsigned vw20, unsigned vw21, unsigned ldsw, unsigned fq0, unsigned fq1) {
  dma16<(T & 7) * 16384u + U * 1024u, (T < 8 ? T : T - 8) * 32768u>(sw2, U ? vw21 : vw20, ldsw, T < 8 ? fq0 : fq1);
}
#define w2_issue1(g, t, u) (w2_dma<t, u>((g).sw2, (g).vw20, (g).vw21, (g).ldsw, (g).fq0b, (g).fq1b))
struct GRot { float c1, s1, c4, s4, c, s; };
DI void gen_g_init(const Geo& g, GRot& r) {
  const int srow = g.i + 8 * g.j;
  const int mb = 32 * g.q + 2 * g.hh;
  const float r1 = (float)(srow & 2047) * (1.f / 2048.f), r4 = (float)((4 * srow) & 2047) * (1.f / 2048.f), r0 = (float)((mb * srow) & 2047) * (1.f / 2048.f);
  r.c1 = cosrev(r1); r.s1 = sinrev(r1); r.c4 = cosrev(r4); r.s4 = sinrev(r4); r.c = cosrev(r0); r.s = sinrev(r0);
}
template <int S> DI void gen_g_frag(const Geo& g, GRot& r, v4u& f) {
#pragma unroll
  for (int h = 0; h < 2; ++h) {
    const float cb = __builtin_fmaf(r.c, r.c1, -(r.s * r.s1)), sb = __builtin_fmaf(r.s, r.c1, r.c * r.s1);
    unsigned w0 = pk2(r.c, -r.s), w1 = pk2(cb, -sb);
    if (S == 0 && h == 0) w0 = (g.q == 0 && g.hh == 0) ? ((g.j & 1) ? 0x3F800000u : 0x00003F80u) : w0;
    if (h) { f.z = w0; f.w = w1; } else { f.x = w0; f.y = w1; }
    const float cn = __builtin_fmaf(r.c, r.c4, -(r.s * r.s4)), sn = __builtin_fmaf(r.s, r.c4, r.c * r.s4);
    r.c = cn; r.s = sn;
  }
  asm volatile("" : "+v"(f));
}
template <int N0, int N1, int NR> DI void bar_w(int w) { if (w == 0) bar_vm<N0>(); else if (w == 1) bar_vm<N1>(); else bar_vm<NR>(); }
#define NOP do {} while (0)

#define TOUCH_POS 1
#define TLBW 0
#define TOUCH() do {                                                                                                      \
    const int gl = G.b * 256 + (w & 3) * 64 + lane;                                                                         \
    const char* base_ = gl < 14336 ? (const char*)p.w2 : gl < 28672 ? (const char*)p.w1 - 14336 * 128 : gl < 36896 ? (const char*)p.wr - 28672 * 128           \
                      : gl < 45120 ? (const char*)p.wi - 36896 * 128 : (const char*)p.x - 45120 * 128;                     \
    const char* ad_ = base_ + (size_t)(gl < 61504 ? gl : 45120) * 128;                                                      \
    if (w >= 4) ad_ = TLBW ? (w == 7 ? (const char*)p.out + lane * 32768 : (const char*)p.ws + (size_t)((w - 4) * 64 + lane + 8) * 65536 + 32768 - 128) : (const char*)p.x; \
    asm volatile("global_load_dword %0, %1, off" : "=v"(touch) : "v"(ad_) : "memory");                                     \
  } while (0)
template <bool ISA>
DI unsigned phase1(long long& pad_t, const Params& p, LAS char* L, const Geo& G, v4u& m5a, v4u& m5b
) {
  const int tid = G.tid, lane = G.lane, w = G.w, i = G.i, i1 = G.i1, rb = G.rb, fc = G.fc, hh = G.hh, blk = G.blk, q4 = G.q4, p4 = G.p4;
  const v4u sx = G.sx, sws = G.sws, sw1 = make_srd(p.w1, ND * NF * 2);
  unsigned touch;
  unsigned ep; v4u gx, gv;
  const int jg = 128 * rb + 8 * fc + w;
  LD16Z(gx, (unsigned)(lane * 16 + (i1 + 8 * jg) * 1024), sx, "");
  LD16Z(gv, (unsigned)lane * 16u, make_srd(p.gw, ND * 2), "");
  LD4Z(ep, WS_EPOCH, sws, "sc1");
  const int f0 = ISA ? 96 * fc : 768 + 128 * (fc - 8), nf = ISA ? 96 : 128;
  const int mt = G.at >> 2, dt4 = G.at & 3;
  const int m1 = G.m1;
  if (ISA) {
    const unsigned voff = (unsigned)(lane >> 4) * 8192u + (unsigned)(((lane & 15) ^ ((lane >> 4) << 2)) << 4);
    const unsigned xl = uni(A_XIMG + (unsigned)w * 8192u), xs = uni((unsigned)i * 1024u + (unsigned)dt4 * 256u + (unsigned)w * 262144u);
    dma16<0, 0>(sx, voff, xl, xs); dma16<1024, 32768>(sx, voff, xl, xs); dma16<2048, 65536>(sx, voff, xl, xs); dma16<3072, 98304>(sx, voff, xl, xs);
    dma16<4096, 131072>(sx, voff, xl, xs); dma16<5120, 163840>(sx, voff, xl, xs); dma16<6144, 196608>(sx, voff, xl, xs); dma16<7168, 229376>(sx, voff, xl, xs);
  }
  unsigned vx0, vx1, vw0, vw1;
  {
    const int r0 = 16 * w + (lane >> 3), pos = lane & 7, r1 = r0 + 8;
    vx0 = (unsigned)(i1 * 1024 + (128 * rb + r0) * 8192 + ((pos ^ ((r0 >> 1) & 7)) << 4));
    vx1 = (unsigned)(i1 * 1024 + (128 * rb + r1) * 8192 + ((pos ^ ((r1 >> 1) & 7)) << 4));
    const int k0 = 8 * w + (lane >> 4), chunk = (lane & 15) ^ ((lane >> 4) << 2);
    const bool ok = chunk * 8 < nf;
    vw0 = ok ? (unsigned)(k0 * 3584 + f0 * 2 + chunk * 16) : 0x80000000u;
    vw1 = ok ? vw0 + 4u * 3584u : 0x80000000u;
  }
  const unsigned g1l = uni((unsigned)w * 2048u);
#define g1_dma(kt, u) do {                                                                                         \
    if ((u) == 0) dma16z<((kt) & 3) * 32768u, (kt) * 128u>(sx, vx0, g1l);                                           \
    if ((u) == 1) dma16z<((kt) & 3) * 32768u + 1024u, (kt) * 128u>(sx, vx1, g1l);                                   \
    if ((u) == 2) dma16z<((kt) & 3) * 32768u + 16384u, (kt) * (64u * 3584u)>(sw1, vw0, g1l);                        \
    if ((u) == 3) dma16z<((kt) & 3) * 32768u + 17408u, (kt) * (64u * 3584u)>(sw1, vw1, g1l); } while (0)
#define g1_issue(kt) do { g1_dma(kt, 0); g1_dma(kt, 1); g1_dma(kt, 2); g1_dma(kt, 3); } while (0)
  if (ISA) {
    phaseA_genF(L, i, mt, w, lane);
    g1_issue(0); g1_issue(1);
  } else {
    g1_issue(0); asm volatile("s_barrier" ::: "memory");
    g1_issue(1); asm volatile("s_barrier" ::: "memory");
    g1_issue(2); asm volatile("s_barrier" ::: "memory");
    g1_issue(3);
  }
  TOUCH();
  asm volatile("s_waitcnt vmcnt(17)" : "+v"(ep), "+v"(gx), "+v"(gv) :: "memory");
  const unsigned tag = (unsigned)__builtin_amdgcn_readfirstlane((int)ep) + 1u;
  float gate_g, gate_og;
  {
    float z = 0.f;
#pragma unroll
    for (int q = 0; q < 4; ++q) {
      const unsigned pr = pk2(bflo(gx[q]) * bflo(gv[q]), bfhi(gx[q]) * bfhi(gv[q]));
      z += bflo(pr) + bfhi(pr);
    }
    z = dpp_add<0x111, 0xf>(z); z = dpp_add<0x112, 0xf>(z); z = dpp_add<0x114, 0xf>(z); z = dpp_add<0x118, 0xf>(z);
    z = dpp_add<0x142, 0xa>(z); z = dpp_add<0x143, 0xc>(z);
    const float zb = bflo(pk2(z, 0.f));
    const float g = bflo(pk2(__builtin_amdgcn_rcpf(1.f + __builtin_amdgcn_exp2f(zb * -1.44269504088896341f)), 0.f));
    gate_g = asf((unsigned)__builtin_amdgcn_readlane((int)__builtin_bit_cast(unsigned, g), 63));
    gate_og = bflo(pk2(1.f - gate_g, 0.f));
  }
  if (ISA) {
    const int dq = w & 3, mh = w >> 2;
    LAS char* xa = L + A_XIMG + (32 * hh + q4) * 256 + (((4 * dq + 2 * blk + (p4 >> 1)) ^ (q4 << 2)) << 4) + 8 * (p4 & 1);
    LAS char* fb = L + A_FIMG + mh * 16384 + lane * 16;
    const int srow = 32 * mh + (lane & 31);
    LAS char* stw = L + A_FIMG + srow * 256 + 8 * hh;
    const int rrow = tid >> 3;
    LAS char* str = L + A_FIMG + rrow * 256;
    const unsigned tdst = WS_T + (unsigned)(((64 * mt + rrow) * 8 + i) * 1024 + dt4 * 256 + (tid & 7) * 16);
    if (w < 4) bar_vm<9>(); else BAR_LGKM();
    STAMP(1);
    f32x16 acc = {};
    v4u fa[2][4], fbv[2][4];
#pragma unroll
    for (int s = 0; s < 4; ++s) { fa[0][s] = tr_read2(xa + 8 * s * 256, xa + 8 * s * 256 + 1024); fbv[0][s] = lds_ld16(fb + s * 1024); }
#pragma unroll
    for (int g = 0; g < 4; ++g) {
      acc = mfma32(fa[g & 1][0], fbv[g & 1][0], acc);
      SCHED();
      if (g == 1) { if (w >= 4) bar_vm<9>(); else BAR_LGKM(); }
      if (g < 3) {
#pragma unroll
        for (int s = 0; s < 4; ++s) {
          const int ro = (64 * (g + 1) + 8 * s) * 256;
          fa[(g + 1) & 1][s] = tr_read2(xa + ro, xa + ro + 1024); fbv[(g + 1) & 1][s] = lds_ld16(fb + (4 * (g + 1) + s) * 1024);
        }
      }
      SCHED();
#pragma unroll
      for (int s = 1; s < 4; ++s) { acc = mfma32(fa[g & 1][s], fbv[g & 1][s], acc); if (g == 1 && s == 1) { g1_dma(2, 0); g1_dma(2, 1); } if (g == 1 && s == 2) { g1_dma(2, 2); g1_dma(2, 3); } SCHED(); }
    }
    BAR_LGKM();
    STAMP(2);
    g1_issue(3);
#pragma unroll
    for (int Gq = 0; Gq < 4; ++Gq) {
      v2u o; o.x = pk2(acc[4 * Gq], acc[4 * Gq + 1]); o.y = pk2(acc[4 * Gq + 2], acc[4 * Gq + 3]);
      *(LAS v2u*)(stw + (((4 * dq + Gq) ^ (srow & 15)) << 4)) = o;
    }
    BAR_LGKM();
    {
      const v4u v0 = lds_ld16(str + (((tid & 7) ^ (rrow & 15)) << 4)), v1 = lds_ld16(str + ((((tid & 7) + 8) ^ (rrow & 15)) << 4));
      st16_sc1(sws, tdst, v0);
      st16_sc1(sws, tdst + 128u, v1);
    }
    SCHED();
  }
  const int ft = w & 3, rh1 = w >> 2;
  const bool active = ft * 32 < nf;
  f32x16 acc1[2] = {};
  v4u wqr, wqi, tq0, tq1;
  unsigned wtr, wti, xtr, xti;
  v4u xqr, xqi;
  WVal Wc, Wd;
  v4u tb[16];
  unsigned fla;
  int t_ok = 0, t_first = 0; (void)t_first;
  const v4u swr = make_srd(p.wr, NS * 257 * 2), swi = make_srd(p.wi, NS * 257 * 2);
  const int mW = m1 == 0 ? 128 * w : m1 + 256 * w;
  unsigned tsrc0 = 0, tsrc1 = 0;
  const unsigned flaoff = WS_FLA + (unsigned)(((m1 >> 5) & 3) * 32 + (lane & 31)) * FS;
  LAS char* CTL = L + 128 * 1024;
  {
    LAS char* wa = L + 16384 + (8 * hh + q4) * 256 + (((4 * ft + 2 * blk + (p4 >> 1)) ^ (q4 << 2)) << 4) + 8 * (p4 & 1);
    const int r = 64 * rh1 + (lane & 31), fz = (r >> 1) & 7;
    LAS char* xb0 = L + r * 128 + (((0 + hh) ^ fz) << 4);
    LAS char* xb1 = L + r * 128 + (((2 + hh) ^ fz) << 4);
    LAS char* xb2 = L + r * 128 + (((4 + hh) ^ fz) << 4);
    LAS char* xb3 = L + r * 128 + (((6 + hh) ^ fz) << 4);
    Frag1 fr[2];
    bar_vm<ISA ? 15 : 13>();
    STAMP(3);
    if (lane == 0) { v2u gv2; gv2.x = __builtin_bit_cast(unsigned, gate_g); gv2.y = __builtin_bit_cast(unsigned, gate_og); *(LAS v2u*)(CTL + 64 + w * 8) = gv2; }
    g1_read(fr[0], wa, xb0, xb1, xb2, xb3, 0);
#define PIN1(c) asm volatile("" : "+v"(c.a[0]), "+v"(c.a[1]), "+v"(c.a[2]), "+v"(c.a[3]), "+v"(c.b0[0]), "+v"(c.b0[1]), "+v"(c.b0[2]), "+v"(c.b0[3]), \
                             "+v"(c.b1[0]), "+v"(c.b1[1]), "+v"(c.b1[2]), "+v"(c.b1[3]))
#define G1_STEP(kt, X0, X1, X2, X3, X4, X5, X6, X7)                                                                   \
    {                                                                                                                   \
      FT(10 + (kt));                                                                                                    \
      Frag1& c = fr[(kt) & 1];                                                                                          \
      Frag1& n = fr[((kt) + 1) & 1];                                                                                    \
      const int so = (((kt) + 1) & 3) * 32768;                                                                          \
      PIN1(c);                                                                                                          \
      asm volatile("" :: "v"(acc1[0]), "v"(acc1[1]));                                                                   \
      SCHED();                                                                                                          \
      acc1[0] = mfma32(c.a[0], c.b0[0], acc1[0]); if ((kt) < 7) n.a[0] = tr_read2(wa + so, wa + so + 1024); X0; SCHED(); \
      acc1[1] = mfma32(c.a[0], c.b1[0], acc1[1]); if ((kt) < 7) { n.b0[0] = lds_ld16(xb0 + so); n.b1[0] = lds_ld16(xb0 + so + 4096); } X1; SCHED(); \
      acc1[0] = mfma32(c.a[1], c.b0[1], acc1[0]); if ((kt) < 7) n.a[1] = tr_read2(wa + so + 4096, wa + so + 5120); X2; SCHED(); \
      acc1[1] = mfma32(c.a[1], c.b1[1], acc1[1]); if ((kt) < 7) { n.b0[1] = lds_ld16(xb1 + so); n.b1[1] = lds_ld16(xb1 + so + 4096); } X3; SCHED(); \
      acc1[0] = mfma32(c.a[2], c.b0[2], acc1[0]); if ((kt) < 7) n.a[2] = tr_read2(wa + so + 8192, wa + so + 9216); X4; SCHED(); \
      acc1[1] = mfma32(c.a[2], c.b1[2], acc1[1]); if ((kt) < 7) { n.b0[2] = lds_ld16(xb2 + so); n.b1[2] = lds_ld16(xb2 + so + 4096); } X5; SCHED(); \
      acc1[0] = mfma32(c.a[3], c.b0[3], acc1[0]); if ((kt) < 7) n.a[3] = tr_read2(wa + so + 12288, wa + so + 13312); X6; SCHED(); \
      acc1[1] = mfma32(c.a[3], c.b1[3], acc1[1]); if ((kt) < 7) { n.b0[3] = lds_ld16(xb3 + so); n.b1[3] = lds_ld16(xb3 + so + 4096); } X7; SCHED(); \
    }
#define GATE_STORE() do { if (w == 2 && lane < 8) { const v2u gv2 = *(LAS v2u*)(CTL + 64 + lane * 8);                       \
      __builtin_amdgcn_raw_buffer_store_b64(gv2, as_rsrc(sws), WS_GATE + (unsigned)(i1 * 256 + 128 * rb + 8 * fc + lane) * 8u, 0, 16); } } while (0)
#define STEP8 G1_STEP
    if constexpr (ISA) {
      bar_vm<11>();
      STAMP(4);
      STEP8(0, NOP, NOP, NOP, NOP, NOP, NOP, NOP, NOP)
      bar_vm<0>();
      if (tid == 0) flag_store(p.ws, WS_FLA + (unsigned)(mt * 32 + i * 4 + dt4) * FS, tag);
      STAMP(13);
      STEP8(1, g1_dma(4, 0), g1_dma(4, 1), g1_dma(4, 2), g1_dma(4, 3), g1_dma(5, 0), g1_dma(5, 1), g1_dma(5, 2), g1_dma(5, 3))
      BAR_LGKM();
      GATE_STORE();
      STEP8(2, NOP, NOP, NOP, NOP, g1_dma(6, 0), g1_dma(6, 1), g1_dma(6, 2), g1_dma(6, 3))
      bar_vm<8>();
      STEP8(3, NOP, NOP, NOP, NOP, g1_dma(7, 0), g1_dma(7, 1), g1_dma(7, 2), g1_dma(7, 3))
      bar_vm<8>();
      STAMP(12);
      STEP8(4, NOP, NOP, NOP, NOP, w2_issue1(G, 0, 0), w2_issue1(G, 0, 1), w2_issue1(G, 1, 0), w2_issue1(G, 1, 1))
      bar_vm<8>();
      STEP8(5, NOP, NOP, NOP, NOP, w2_issue1(G, 2, 0), w2_issue1(G, 2, 1), w2_issue1(G, 3, 0), w2_issue1(G, 3, 1))
      bar_vm<8>();
      STEP8(6, NOP, NOP, NOP, NOP, w2_issue1(G, 4, 0), w2_issue1(G, 4, 1), w2_issue1(G, 5, 0), w2_issue1(G, 5, 1))
      BAR_LGKM();
    } else {
      const v4u stab = make_srd(&kTab, sizeof(Tab));
      const unsigned wsrc = w_src(mW, lane);
      {
        const int kap = tid >> 5, ii = (kap >> 1) & 7, d0 = kap & 1;
        const int g0 = m1 == 0 ? 2 * ii : ii, g1 = m1 == 0 ? 2 * ii + 1 : 8 + ii;
        const unsigned tb = WS_T + (unsigned)m1 * 16384u + (unsigned)(d0 * 512 + (tid & 31) * 16);
        tsrc0 = tb + (unsigned)g0 * 1024u; tsrc1 = tb + (unsigned)g1 * 1024u;
      }
      const unsigned vw20z = G.vw20 - 16u * 1024u, vw21z = G.vw21 - 16u * 1024u, ldswz = uni(G.ldsw - 2048u);
#define W2B(t, u) do { if (w != 0) w2_issue1(G, t, u); } while (0)
#define W2Z(t, u) do { if (w == 1) w2_dma<t, u>(G.sw2, vw20z, vw21z, ldswz, G.fq0b, G.fq1b); } while (0)
      bar_vm<9>();
      STAMP(4);
      const unsigned wts = w_tail_src(mW, lane);
      if (m1 == 0) {
        const unsigned ws2 = w_src(128 * w + 1024, lane), wt2 = w_tail_src(128 * w + 1024, lane);
        LD16Z(xqr, ws2, swr, ""); LD16Z(xqi, ws2, swi, ""); LD2Z(xtr, wt2, swr, ""); LD2Z(xti, wt2, swi, "");
      }
      STEP8(0, LD16Z(wqr, wsrc, swr, ""), LD16Z(wqi, wsrc, swi, ""), LD2Z(wtr, wts, swr, ""), LD2Z(wti, wts, swi, ""), g1_dma(4, 0), g1_dma(4, 1), g1_dma(4, 2), g1_dma(4, 3))
      bar_vm<13>();
      STEP8(1, NOP, NOP, NOP, NOP, g1_dma(5, 0), g1_dma(5, 1), g1_dma(5, 2), g1_dma(5, 3))
      bar_vm<13>();
      GATE_STORE();
      STEP8(2, NOP, NOP, NOP, NOP, g1_dma(6, 0), g1_dma(6, 1), g1_dma(6, 2), g1_dma(6, 3))
      bar_vm<8>();
      LAS char* wscr0 = L + 128 * 1024 + w * 2048;
      const bool special = m1 == 0;
      const int m2nd = 128 * w + 1024;
      if (special) { asm volatile("" : "+v"(xqr), "+v"(xqi), "+v"(xtr), "+v"(xti)); w_put(wscr0, xqr, xqi, lane); }
      STEP8(3, NOP, NOP, (dma16<128u * 1024u + 16384u, 0>(stab, (unsigned)lane * 16u, g1l, g1l)), (dma16<128u * 1024u + 17408u, 1024>(stab, (unsigned)lane * 16u, g1l, g1l)),
            g1_dma(7, 0), g1_dma(7, 1), g1_dma(7, 2), g1_dma(7, 3))
      bar_vm<10>();
      STAMP(12);
      WRawL raw2; const int off2 = w_off(m2nd, lane);
      if (special) { w_get(raw2, wscr0, off2, lane, 0); w_get(raw2, wscr0, off2, lane, 1); w_get(raw2, wscr0, off2, lane, 2); w_get(raw2, wscr0, off2, lane, 3); }
      const int woff = w_off(mW, lane);
      asm volatile("" : "+v"(wqr), "+v"(wqi), "+v"(wtr), "+v"(wti) :: "memory");
      w_put(wscr0, wqr, wqi, lane);
      STEP8(4, W2Z(0, 0), W2Z(0, 1), W2Z(1, 0), W2Z(1, 1), W2B(0, 0), W2B(0, 1), W2B(1, 0), W2B(1, 1))
      bar_w<6, 14, 10>(w);
      WRawL wraw;
      w_get(wraw, wscr0, woff, lane, 0); w_get(wraw, wscr0, woff, lane, 1); w_get(wraw, wscr0, woff, lane, 2); w_get(wraw, wscr0, woff, lane, 3);
      if (special) Wd = w_unpack(raw2, w_tail(xtr, xti), off2, m2nd, lane);
      STEP8(5, W2Z(2, 0), W2Z(2, 1), W2Z(3, 0), W2Z(3, 1), W2B(2, 0), W2B(2, 1), W2B(3, 0), W2B(3, 1))
      bar_w<0, 16, 8>(w);
      Wc = w_unpack(wraw, w_tail(wtr, wti), woff, mW, lane);
      if (special) packed_coef(Wc, Wd, w);
      STEP8(6, if (w == 0) LD4Z(fla, flaoff, sws, "sc1"), NOP, NOP, NOP, NOP, NOP, NOP, NOP)
      if (w == 0) {
        asm volatile("s_waitcnt vmcnt(0)" : "+v"(fla) :: "memory");
        const unsigned ok = __all(fla == tag) ? 1u : 0u;
        if (lane == 0) *(LAS unsigned*)CTL = ok;
      }
      BAR_LGKM();
      t_ok = __builtin_amdgcn_readfirstlane(*(volatile LAS unsigned*)CTL);
      t_first = t_ok;
    }
    if constexpr (ISA) {
      STEP8(7, NOP, NOP, NOP, NOP, w2_issue1(G, 6, 0), w2_issue1(G, 6, 1), w2_issue1(G, 7, 0), w2_issue1(G, 7, 1))
    } else {
      LAS char* TB = L + 128 * 1024 + 16384 + lane * 16;
#define TBR(f) (tb[f] = lds_ld16(TB + (f) * 1024), tb[(f) + 1] = lds_ld16(TB + ((f) + 1) * 1024))
      STEP8(7, { if (t_ok) LD16Z(tq0, tsrc0, sws, "sc1"); TBR(6); }, { if (t_ok) LD16Z(tq1, tsrc1, sws, "sc1"); TBR(0); },
            { if (!t_ok && w == 0) LD4Z(fla, flaoff, sws, "sc1"); TBR(10); }, TBR(2), TBR(12), TBR(14), TBR(4), TBR(8))
      if (!t_ok) {
        if (w == 0) {
          asm volatile("s_waitcnt vmcnt(0)" : "+v"(fla) :: "memory");
          const unsigned ok = __all(fla == tag) ? 1u : 0u;
          if (lane == 0) *(LAS unsigned*)CTL = ok;
        }
        BAR_LGKM();
        t_ok = __builtin_amdgcn_readfirstlane(*(volatile LAS unsigned*)CTL);
        if (t_ok) { LD16Z(tq0, tsrc0, sws, "sc1"); LD16Z(tq1, tsrc1, sws, "sc1"); }
      }
    }
  }
  asm volatile("" :: "v"(touch));
  STAMP(5);
  FT(18);
  if (active) {
#pragma unroll
    for (int rt = 0; rt < 2; ++rt)
#pragma unroll
      for (int u = 0; u < 2; ++u) {
        v4u o;
        o.x = silu2(acc1[rt][8 * u + 0], acc1[rt][8 * u + 1]);
        o.y = silu2(acc1[rt][8 * u + 2], acc1[rt][8 * u + 3]);
        o.z = silu2(acc1[rt][8 * u + 4], acc1[rt][8 * u + 5]);
        o.w = silu2(acc1[rt][8 * u + 6], acc1[rt][8 * u + 7]);
        const int jb32 = 4 * rb + 2 * rh1 + rt, fs = (f0 >> 4) + 2 * ft + u;
        st16_sc1(sws, WS_H + (unsigned)(((i1 * 8 + jb32) * 112 + fs) * 1024 + lane * 16), o);
      }
  }
  SCHED();
  STAMP(6);
  FT(20);
  const unsigned flh_own = WS_FLH + (unsigned)((i1 * 2 + rb) * 16 + fc) * FS;
  if constexpr (ISA) {
    bar_vm<0>();
    if (tid == 0) flag_store(p.ws, flh_own, tag);
  } else {
    LAS char* I0 = L + 128 * 1024;
    LAS char* I1 = L + 128 * 1024 + 16384;
    const int g = lane >> 4, l15 = lane & 15;
    const bool special = (m1 == 0);
    bool published = false;
    if (t_ok) {
      wait_vm<4>();
      asm volatile("s_barrier" ::: "memory");
    } else {
      bar_vm<0>();
      if (tid == 0) flag_store(p.ws, flh_own, tag);
      published = true;
      if (w == 0) poll_words(sws, WS_FLA + (unsigned)(m1 >> 5) * 32u * FS, 32, tag, lane);
      asm volatile("s_barrier" ::: "memory");
      LD16Z(tq0, tsrc0, sws, "sc1"); LD16Z(tq1, tsrc1, sws, "sc1");
      wait_vm<0>();
    }
    asm volatile("" : "+v"(tq0), "+v"(tq1) :: "memory");
    {
      const int kap = tid >> 5, ch = tid & 31;
      *(LAS v4u*)(I0 + kap * 512 + ((ch ^ ((kap & 3) << 2)) << 4)) = tq0;
      *(LAS v4u*)(I0 + (kap + 16) * 512 + ((ch ^ ((kap & 3) << 2)) << 4)) = tq1;
    }
    BAR_LGKM();
    FT(21);
    LAS char* ta = I0 + (8 * hh + q4) * 512 + (((4 * w + 2 * blk + (p4 >> 1)) ^ (q4 << 2)) << 4) + 8 * (p4 & 1);
    const int n = lane & 31;
    LAS char* so = I1 + n * 512 + 8 * hh;
    {
      f32x16 acc = {};
      acc = mfma32(tr_read2(ta, ta + 2048), tb[6], acc);
      acc = mfma32(tr_read2(ta + 8192, ta + 10240), tb[7], acc);
#pragma unroll
      for (int Gq = 0; Gq < 4; ++Gq) {
        v2u o; o.x = pk2(acc[4 * Gq], acc[4 * Gq + 1]); o.y = pk2(acc[4 * Gq + 2], acc[4 * Gq + 3]);
        *(LAS v2u*)(so + (((4 * w + Gq) ^ (n & 15)) << 4)) = o;
      }
    }
    bar_vm<0>();
    if (!published && tid == 0) flag_store(p.ws, flh_own, tag);
    w2_issue1(G, 4, 0); w2_issue1(G, 4, 1); w2_issue1(G, 5, 0); w2_issue1(G, 5, 1);
    FT(22);
    const f32x4 z4 = {0.f, 0.f, 0.f, 0.f};
    const v4u F16fr = tb[0], F16fi = tb[1];
    v4u F16ir, F16ii;
    F16ir.x = F16fr.x; F16ir.y = F16fr.y; F16ir.z = F16fr.z ^ 0x80008000u; F16ir.w = F16fr.w ^ 0x80008000u;
    F16ii.x = F16fi.x ^ 0x80008000u; F16ii.y = F16fi.y ^ 0x80008000u; F16ii.z = F16fi.z; F16ii.w = F16fi.w;
    f32x4 zr[2], zi[2];
#pragma unroll
    for (int pp = 0; pp < 2; ++pp) {
      const int n0 = 4 * w + 2 * pp, n1 = n0 + 1;
      const int ch = 2 * (4 * g + q4) + (p4 >> 1);
      const v4u a1 = tr_read2(I1 + n0 * 512 + ((ch ^ (n0 & 15)) << 4) + 8 * (p4 & 1), I1 + n1 * 512 + ((ch ^ (n1 & 15)) << 4) + 8 * (p4 & 1));
      const f32x4 dr = mfma16(a1, pp ? tb[2] : F16fr, z4), di = mfma16(a1, pp ? tb[3] : F16fi, z4);
      const v4u b3 = cmul_pack<false>(dr, di, asf4(tb[10 + 2 * pp]), asf4(tb[11 + 2 * pp]));
      zr[pp] = mfma16(F16fr, b3, z4); zi[pp] = mfma16(F16fi, b3, z4);
    }
    v4u a4[2];
    if (!special) {
      a4[0] = cmul_pack<false>(zr[0], zi[0], Wc.r0, Wc.i0);
      a4[1] = cmul_pack<false>(zr[1], zi[1], Wc.r1, Wc.i1);
    } else {
      BAR_LGKM();
      LAS char* Z = I0;
#pragma unroll
      for (int pp = 0; pp < 2; ++pp)
#pragma unroll
        for (int r = 0; r < 4; ++r) { f32x2 v = {zr[pp][r], zi[pp][r]}; *(LAS f32x2*)(Z + ((w * 2 + pp) * 256 + l15 + 16 * (4 * g + r)) * 8) = v; }
      BAR_LGKM();
#pragma unroll
      for (int pp = 0; pp < 2; ++pp) {
        f32x4 cr, ci;
#pragma unroll
        for (int r = 0; r < 4; ++r) {
          const int K = l15 + 16 * (4 * g + r), Kn = pp ? 255 - K : (256 - K) & 255;
          const f32x2 y = *(LAS f32x2*)(Z + ((((8 - w) & 7) * 2 + pp) * 256 + Kn) * 8);
          cr[r] = y[0]; ci[r] = -y[1];
        }
        const f32x4 ar = pp ? Wc.r1 : Wc.r0, ai = pp ? Wc.i1 : Wc.i0, br = pp ? Wd.r1 : Wd.r0, bi = pp ? Wd.i1 : Wd.i0;
        const f32x4 yr = zr[pp] * ar - zi[pp] * ai + cr * br - ci * bi, yi = zr[pp] * ai + zi[pp] * ar + cr * bi + ci * br;
        a4[pp].x = pk2(yr[0], yr[1]); a4[pp].y = pk2(yr[2], yr[3]); a4[pp].z = pk2(yi[0], yi[1]); a4[pp].w = pk2(yi[2], yi[3]);
      }
      BAR_LGKM();
    }
    w2_issue1(G, 6, 0); w2_issue1(G, 6, 1); w2_issue1(G, 7, 0); w2_issue1(G, 7, 1);
#pragma unroll
    for (int pp = 0; pp < 2; ++pp) {
      const f32x4 dr = mfma16(a4[pp], F16ir, z4), di = mfma16(a4[pp], F16ii, z4);
      const v4u a5 = cmul_pack<true>(dr, di, asf4(tb[pp ? 14 : 10]), asf4(tb[pp ? 15 : 11]));
      const f32x4 er = mfma16(a5, pp ? tb[4] : F16ir, z4), ei = mfma16(a5, pp ? tb[5] : F16ii, z4);
      const int n0 = 4 * w + 2 * pp, n1 = n0 + 1;
      const int ch = 2 * l15 + (g >> 1);
      v2u o; o.x = pk2(er[0], er[1]); o.y = pk2(er[2], er[3]);
      *(LAS v2u*)(I0 + n0 * 512 + ((ch ^ ((n0 & 3) << 2)) << 4) + 8 * (g & 1)) = o;
      o.x = pk2(ei[0], ei[1]); o.y = pk2(ei[2], ei[3]);
      *(LAS v2u*)(I0 + n1 * 512 + ((ch ^ ((n1 & 3) << 2)) << 4) + 8 * (g & 1)) = o;
    }
    BAR_LGKM();
    FT(23);
    m5a = tb[8]; m5b = tb[9];
  }
  return tag;
}

DI void make_geo(Geo& G, const Params& p, int tz) {
  G.tid = tz; G.lane = G.tid & 63; G.w = __builtin_amdgcn_readfirstlane(G.tid >> 6);
  G.b = blockIdx.x;
  const int Lx = G.b >> 3, x8 = G.b & 7;
  {
    const int a = x8 & 3, h = x8 >> 2, rbi = Lx >> 3, c = Lx & 7;
    G.i = 2 * a + h; G.i1 = 2 * a + (rbi >> 1); G.rb = rbi & 1;
    G.fc = c < 4 ? 4 * h + c : 4 + 4 * h + c;
    G.at = rbi * 4 + c; G.m1 = ((rbi * 4 + c - 4) << 3) | x8;
  }
  G.hh = G.lane >> 5; G.blk = (G.lane >> 4) & 1; G.q4 = (G.lane & 15) >> 2; G.p4 = G.lane & 3;
  G.sx = make_srd(p.x, NS * ND * 2); G.sws = make_srd(p.ws, 16u << 20); G.sw2 = make_srd(p.w2, NF * ND * 2);
  G.jb = Lx >> 3; G.dt = Lx & 7; G.rh = G.w & 1; G.q = G.w >> 1;
  G.j = 64 * G.jb + 32 * G.rh + (G.lane & 31);
  const int rho = 16 * G.rh + (G.lane >> 3), chs = (G.lane & 7) ^ (4 * ((G.lane >> 4) & 1));
  G.vw20 = (unsigned)(rho * 1024 + G.dt * 128 + chs * 16); G.vw21 = G.vw20 + 8 * 1024;
  G.ldsw = uni((unsigned)(G.q * 4096 + G.rh * 2048));
  G.fq0b = uni((unsigned)(768 + 256 * G.q) * 1024u); G.fq1b = uni((unsigned)(192 * G.q) * 1024u);
}

__global__ __launch_bounds__(512, 2) void gm_kernel(Params p) {
  extern __shared__ __attribute__((aligned(16))) char smem[];
  LAS char* L = (LAS char*)smem;
  long long pad_t = 0;
  STAMP(0);
  FT(0);
  Geo G;
  make_geo(G, p, threadIdx.x);
  const bool isA = G.fc < 8;
  v4u gf[4], m5a, m5b;
  unsigned tag;
  if (isA) tag = phase1<true>(pad_t, p, L, G, m5a, m5b); else tag = phase1<false>(pad_t, p, L, G, m5a, m5b);
  { int tz = threadIdx.x; asm volatile("" : "+v"(tz)); make_geo(G, p, tz); }
  const int tid = G.tid, lane = G.lane, w = G.w, b = G.b, i = G.i, jb = G.jb, dt = G.dt, rh = G.rh, q = G.q, j = G.j;
  const int hh = G.hh, blk = G.blk, q4 = G.q4, p4 = G.p4;
  const v4u sws = G.sws;
  const unsigned ldsw = G.ldsw;
  const int m1 = G.m1;
  STAMP(7);
  FT(25);
  if (!isA) {
    LAS char* I0 = L + 128 * 1024;
    LAS char* I1 = L + 128 * 1024 + 16384;
    LAS char* ta = I0 + (8 * hh + q4) * 512 + (((4 * w + 2 * blk + (p4 >> 1)) ^ (q4 << 2)) << 4) + 8 * (p4 & 1);
    const int n = lane & 31;
    LAS char* so = I1 + n * 512 + 8 * hh;
    f32x16 acc = {};
    acc = mfma32(tr_read2(ta, ta + 2048), m5a, acc);
    acc = mfma32(tr_read2(ta + 8192, ta + 10240), m5b, acc);
#pragma unroll
    for (int Gq = 0; Gq < 4; ++Gq) {
      v2u o; o.x = pk2(acc[4 * Gq], acc[4 * Gq + 1]); o.y = pk2(acc[4 * Gq + 2], acc[4 * Gq + 3]);
      *(LAS v2u*)(so + (((4 * w + Gq) ^ (n & 15)) << 4)) = o;
    }
    BAR_LGKM();
    const int nn = tid >> 5, ch = tid & 31, ii = (nn >> 1) & 7, d0 = nn & 1;
    const bool special = (m1 == 0);
    const int g0 = special ? 2 * ii : ii, g1 = special ? 2 * ii + 1 : 8 + ii;
    const v4u v0 = lds_ld16(I1 + nn * 512 + ((ch ^ (nn & 15)) << 4));
    const v4u v1 = lds_ld16(I1 + (nn + 16) * 512 + ((ch ^ (nn & 15)) << 4));
    const unsigned ub = WS_U + (unsigned)m1 * 16384u + (unsigned)(d0 * 512 + ch * 16);
    st16_sc1(sws, ub + (unsigned)g0 * 1024u, v0);
    st16_sc1(sws, ub + (unsigned)g1 * 1024u, v1);
    SCHED();
  }
  const int sz = (q4 >> 1) & 1;
  LAS char* ra0 = L + q * 4096 + (4 * hh + q4) * 128 + (((0 ^ (4 * sz)) + 2 * blk + (p4 >> 1)) << 4) + 8 * (p4 & 1);
  LAS char* ra1 = L + q * 4096 + (4 * hh + q4) * 128 + (((4 ^ (4 * sz)) + 2 * blk + (p4 >> 1)) << 4) + 8 * (p4 & 1);
  v4u wp[4][4];
  BAR_LGKM();
#pragma unroll
  for (int t = 0; t < 4; ++t) {
    LAS char* s0 = ra0 + t * 16384; LAS char* s1 = ra1 + t * 16384;
    wp[t][0] = tr_read2(s0, s0 + 1024); wp[t][1] = tr_read2(s1, s1 + 1024);
    wp[t][2] = tr_read2(s0 + 2048, s0 + 3072); wp[t][3] = tr_read2(s1 + 2048, s1 + 3072);
  }
#define W2_8_11() do { w2_issue1(G, 8, 0); w2_issue1(G, 8, 1); w2_issue1(G, 9, 0); w2_issue1(G, 9, 1); w2_issue1(G, 10, 0); w2_issue1(G, 10, 1); w2_issue1(G, 11, 0); w2_issue1(G, 11, 1); } while (0)
  if (isA) { BAR_LGKM(); W2_8_11(); }
  const unsigned flh = WS_FLH + (unsigned)((i * 2 + (jb >> 1)) * 16) * FS;
  if (w == 0) poll_words(sws, flh, 16, tag, lane);
  if (isA) BAR_LGKM(); else bar_vm<0>();
  if (!isA && tid == 0) flag_store(p.ws, WS_FLB + (unsigned)m1 * FS, tag);
  STAMP(14);
  STAMP(8);
  FT(26);
  const unsigned vh = (unsigned)lane * 16u;
  const unsigned hbase = WS_H + (unsigned)((i * 8 + 2 * jb + rh) * 112) * 1024u;
  const unsigned hb0 = uni(hbase + (G.fq0b >> 4)), hb1 = uni(hbase + (G.fq1b >> 4));
  v2u gg;
  v4u hf[14][2];
#define H_ISSUE(t) do { LD16I(hf[t][0], vh, sws, ((t) < 8 ? hb0 : hb1), (2 * ((t) & 7)) * 1024, "sc1"); LD16I(hf[t][1], vh, sws, ((t) < 8 ? hb0 : hb1), (2 * ((t) & 7) + 1) * 1024, "sc1"); } while (0)
  H_ISSUE(0); H_ISSUE(1);
  asm volatile("s_barrier" ::: "memory");
  H_ISSUE(2); H_ISSUE(3); H_ISSUE(4); H_ISSUE(5); H_ISSUE(6);
  LD8Z(gg, WS_GATE + (unsigned)(i * 256 + j) * 8u, sws, "sc1");
  SCHED();
  f32x16 accb[2] = {}, acca[2] = {};
  v4u wf[2][4];
  unsigned fl0, fl1;
  v4u ub0, ub1, ub2, ub3;
  const unsigned vu = WS_U + (unsigned)(((64 * q + 16 * rh + (lane >> 3)) * 8 + i) * 1024 + dt * 128 + ((lane & 7) ^ (4 * ((lane >> 4) & 1))) * 16);
#define G2_BAR(n, t) asm volatile("s_waitcnt vmcnt(" STR(n) ") lgkmcnt(0)\n\ts_barrier" : "+v"(hf[t][0]), "+v"(hf[t][1]) :: "memory")
#define G2_CORE(t, c, n, RD, acc, b0, b1, X0, X1, X2)                                             \
  {                                                                                                 \
    FT(30 + (t));                                                                                   \
    LAS char* s0 = ra0 + (((t) + 1) & 7) * 16384; LAS char* s1 = ra1 + (((t) + 1) & 7) * 16384;    \
    asm volatile("" : "+v"(c[0]), "+v"(c[1]), "+v"(c[2]), "+v"(c[3]));                             \
    asm volatile("" :: "v"(acc[0]), "v"(acc[1]));                                                   \
    SCHED();                                                                                        \
    acc[0] = mfma32(c[0], b0, acc[0]); if (RD) n[0] = tr_read2(s0, s0 + 1024); SCHED();            \
    acc[1] = mfma32(c[1], b0, acc[1]); if (RD) n[1] = tr_read2(s1, s1 + 1024); X0; SCHED();        \
    acc[0] = mfma32(c[2], b1, acc[0]); if (RD) n[2] = tr_read2(s0 + 2048, s0 + 3072); X1; SCHED(); \
    acc[1] = mfma32(c[3], b1, acc[1]); if (RD) n[3] = tr_read2(s1 + 2048, s1 + 3072); X2; SCHED(); \
  }
#define G2_PRE(t, X2) G2_CORE(t, wp[t], wf[0], (t) == 3, accb, hf[t][0], hf[t][1], if (!isA) w2_issue1(G, (t) + 8, 0), if (!isA) w2_issue1(G, (t) + 8, 1), X2)
#define G2_STEP(t, X0, X1, X2) G2_CORE(t, wf[(t) & 1], wf[((t) + 1) & 1], true, accb, hf[t][0], hf[t][1], X0, X1, X2)
  G2_BAR(13, 0); G2_PRE(0, H_ISSUE(7))
  G2_BAR(13, 1); G2_PRE(1, H_ISSUE(8))
  G2_BAR(13, 2); G2_PRE(2, H_ISSUE(9))
  G2_BAR(13, 3); G2_PRE(3, H_ISSUE(10))
  G2_BAR(13, 4); G2_STEP(4, w2_issue1(G, 12, 0), w2_issue1(G, 12, 1), H_ISSUE(11))
  G2_BAR(15, 5); G2_STEP(5, w2_issue1(G, 13, 0), w2_issue1(G, 13, 1), H_ISSUE(12))
  if (w == 0) { LD4Z(fl0, WS_FLB + (unsigned)lane * FS, sws, "sc1"); LD4Z(fl1, WS_FLB + (64u + (unsigned)lane) * FS, sws, "sc1"); }
  G2_BAR(17, 6); G2_STEP(6, NOP, NOP, H_ISSUE(13))
  STAMP(9);
  G2_BAR(16, 7); G2_STEP(7, NOP, NOP, NOP)
  LAS unsigned* verdict = (LAS unsigned*)(L + 128 * 1024);
  if (w == 0) {
    asm volatile("s_waitcnt vmcnt(2)" : "+v"(fl0), "+v"(fl1) :: "memory");
    const unsigned ok = __all(fl0 == tag && fl1 == tag) ? 1u : 0u;
    if (lane == 0) *verdict = ok;
  }
  G2_BAR(14, 8);
  const int u_early = __builtin_amdgcn_readfirstlane(*(volatile LAS unsigned*)verdict);
  STAMP(15);
#define U_ISSUE() do { LD16Z(ub0, vu, sws, "sc1"); LD16Z(ub1, vu + 8u * 8192u, sws, "sc1"); LD16Z(ub2, vu + 32u * 8192u, sws, "sc1"); LD16Z(ub3, vu + 40u * 8192u, sws, "sc1"); } while (0)
  if (u_early) U_ISSUE();
  GRot grot;
  G2_STEP(8, gen_g_init(G, grot), NOP, gen_g_frag<0>(G, grot, gf[0]))
  unsigned e0, e1, e2, e3;
  if (b == 0 && w == 1) {
    LD4Z(e0, WS_FLH + (unsigned)lane * FS, sws, "sc1"); LD4Z(e1, WS_FLH + (64u + (unsigned)lane) * FS, sws, "sc1");
    LD4Z(e2, WS_FLH + (128u + (unsigned)lane) * FS, sws, "sc1"); LD4Z(e3, WS_FLH + (192u + (unsigned)lane) * FS, sws, "sc1");
  }
  G2_BAR(12, 9); G2_STEP(9, NOP, NOP, gen_g_frag<1>(G, grot, gf[1]))
  G2_BAR(10, 10); G2_STEP(10, NOP, NOP, gen_g_frag<2>(G, grot, gf[2]))
  G2_BAR(6, 11); G2_STEP(11, NOP, NOP, gen_g_frag<3>(G, grot, gf[3]))
  G2_BAR(2, 12);
  G2_STEP(12, NOP, NOP, NOP)
#define U_PUT() do {                                                                                                        \
    asm volatile("s_waitcnt vmcnt(0)" : "+v"(ub0), "+v"(ub1), "+v"(ub2), "+v"(ub3) :: "memory");                             \
    *(LAS v4u*)(L + 6 * 16384 + ldsw + lane * 16) = ub0; *(LAS v4u*)(L + 6 * 16384 + ldsw + 1024 + lane * 16) = ub1;         \
    *(LAS v4u*)(L + 7 * 16384 + ldsw + lane * 16) = ub2; *(LAS v4u*)(L + 7 * 16384 + ldsw + 1024 + lane * 16) = ub3; } while (0)
  if (u_early) U_PUT();
  G2_BAR(0, 13);
  asm volatile("" : "+v"(gg));
  if (b == 0 && w == 1) {
    asm volatile("" : "+v"(e0), "+v"(e1), "+v"(e2), "+v"(e3));
    if (!__all(e0 == tag && e1 == tag && e2 == tag && e3 == tag)) {
#pragma unroll 1
      for (int u = 0; u < 4; ++u) poll_words(sws, WS_FLH + 64u * FS * u, 64, tag, lane);
    }
    if (lane == 0) flag_store(p.ws, WS_EPOCH, tag);
  }
  G2_CORE(13, wf[1], wf[0], u_early, accb, hf[13][0], hf[13][1], NOP, NOP, NOP)
  if (!u_early) {
    if (w == 0) { poll_words(sws, WS_FLB, 64, tag, lane); poll_words(sws, WS_FLB + 64u * FS, 64, tag, lane); }
    BAR_LGKM();
    U_ISSUE();
    U_PUT();
    BAR_LGKM();
    LAS char* s0 = ra0 + 6 * 16384; LAS char* s1 = ra1 + 6 * 16384;
    wf[0][0] = tr_read2(s0, s0 + 1024); wf[0][1] = tr_read2(s1, s1 + 1024); wf[0][2] = tr_read2(s0 + 2048, s0 + 3072); wf[0][3] = tr_read2(s1 + 2048, s1 + 3072);
  }
  STAMP(10);
  BAR_LGKM(); G2_CORE(14, wf[0], wf[1], true, acca, gf[0], gf[1], NOP, NOP, NOP)
  BAR_LGKM(); G2_CORE(15, wf[1], wf[0], false, acca, gf[2], gf[3], NOP, NOP, NOP)
  FT(50);
  {
    const float og = asf(gg.y), gs = asf(gg.x) * (1.f / 524288.f);
    const int row = 32 * rh + (lane & 31);
#pragma unroll
    for (int dh = 0; dh < 2; ++dh)
#pragma unroll
      for (int G = 0; G < 4; ++G) {
        f32x4 v;
#pragma unroll
        for (int e = 0; e < 4; ++e) v[e] = __builtin_fmaf(og, accb[dh][4 * G + e], gs * acca[dh][4 * G + e]);
        *(LAS f32x4*)(L + q * 16384 + row * 256 + (((8 * dh + 2 * G + hh) ^ (row & 7)) << 4)) = v;
      }
  }
  BAR_LGKM();
  FT(51);
  {
    const int row = tid >> 3, c8 = tid & 7;
    f32x4 s0 = {0.f, 0.f, 0.f, 0.f}, s1 = s0;
#pragma unroll
    for (int qq = 0; qq < 4; ++qq) {
      s0 += *(LAS f32x4*)(L + qq * 16384 + row * 256 + (((2 * c8) ^ (row & 7)) << 4));
      s1 += *(LAS f32x4*)(L + qq * 16384 + row * 256 + (((2 * c8 + 1) ^ (row & 7)) << 4));
    }
    v4u o; o.x = pk2(s0[0], s0[1]); o.y = pk2(s0[2], s0[3]); o.z = pk2(s1[0], s1[1]); o.w = pk2(s1[2], s1[3]);
    *(GAS v4u*)((GAS char*)p.out + (size_t)(i + 8 * (64 * jb + row)) * 1024 + dt * 128 + c8 * 16) = o;
  }
  STAMP(11);
  if (threadIdx.x == 0) { asm volatile("s_waitcnt vmcnt(0)\n\tbuffer_wbl2 sc0 sc1\n\ts_waitcnt vmcnt(0)" ::: "memory"); }
  while (__builtin_amdgcn_s_memrealtime() - pad_t < 3000) {}
}

}

extern "C" void kernel_launch(void* const* d_in, const int* in_sizes, int n_in, void* d_out, int out_size, void* d_ws, size_t ws_size,
                              hipStream_t stream) {
  Params p;
  p.x = d_in[0]; p.wr = d_in[1]; p.wi = d_in[2]; p.w1 = d_in[4]; p.w2 = d_in[5]; p.gw = d_in[6];
  p.out = d_out; p.ws = d_ws;
  (void)hipFuncSetAttribute((const void*)gm_kernel, hipFuncAttributeMaxDynamicSharedMemorySize, LDS_BYTES);
  gm_kernel<<<256, 512, LDS_BYTES, stream>>>(p);
}
```

```cpp
#include <hip/hip_runtime.h>
#include <hip/hip_bf16.h>
#include <stdint.h>

namespace {

typedef unsigned v4u __attribute__((ext_vector_type(4)));
typedef unsigned v2u __attribute__((ext_vector_type(2)));
typedef float f32x16 __attribute__((ext_vector_type(16)));
typedef float f32x4 __attribute__((ext_vector_type(4)));
typedef float f32x2 __attribute__((ext_vector_type(2)));
typedef __bf16 bf16x8 __attribute__((ext_vector_type(8)));
typedef __bf16 bf16x2 __attribute__((ext_vector_type(2)));
typedef short s16x4 __attribute__((ext_vector_type(4)));
#define LAS __attribute__((address_space(3)))
#define GAS __attribute__((address_space(1)))
#define DI __device__ __forceinline__

constexpr int NS = 2048, ND = 512, NF = 1792;
constexpr unsigned WS_EPOCH = 0;
constexpr unsigned FS = 64;
constexpr unsigned WS_FLA = 4096;
constexpr unsigned WS_FLH = 16384;
constexpr unsigned WS_FLB = 32768;
constexpr unsigned WS_GATE = 65536;
constexpr unsigned WS_T = 1u << 20;
constexpr unsigned WS_U = 3u << 20;
constexpr unsigned WS_H = 5u << 20;
constexpr unsigned H_BYTES = 8u * 8u * 112u * 1024u;
constexpr int LDS_BYTES = 160 * 1024;
constexpr unsigned kSpinCap = 1u << 20;

struct Params {
  const void* x; const void* wr; const void* wi; const void* w1; const void* w2; const void* gw;
  void* out; void* ws;
};

constexpr double kPi = 3.14159265358979323846;
constexpr double ccos(double x) { double t = 1, s = 1; for (int n = 1; n < 16; ++n) { t *= -x * x / ((2 * n - 1) * (2 * n)); s += t; } return s; }
constexpr double cosn(int e, int n) { e = ((e % n) + n) % n; double x = 2 * kPi * e / n; if (x > kPi) x -= 2 * kPi; return ccos(x); }
constexpr double wre(int e, int n) { return cosn(e, n); }
constexpr double wim(int e, int n) { return -cosn(4 * e - n, 4 * n); }
constexpr unsigned bfb(double v) { float f = (float)v; unsigned u = __builtin_bit_cast(unsigned, f); u += 0x7fffu + ((u >> 16) & 1u); return u >> 16; }
constexpr unsigned bf2(double a, double b) { return bfb(a) | (bfb(b) << 16); }
constexpr unsigned fbits(double v) { return __builtin_bit_cast(unsigned, (float)v); }
struct Tab { unsigned v[16][64][4]; };
constexpr Tab make_tab() {
  Tab t{};
  for (int l = 0; l < 64; ++l) {
    const int g = l >> 4, l15 = l & 15, hh = l >> 5, n = l & 31;
    for (int mtx = 0; mtx < 3; ++mtx) {
      double r[4] = {}, im[4] = {};
      for (int q = 0; q < 4; ++q) {
        const int a = 4 * g + q;
        if (mtx == 0) { r[q] = wre(l15 * a, 16); im[q] = wim(l15 * a, 16); }
        else if (mtx == 1) { r[q] = wre(a * (2 * l15 + 1), 32); im[q] = wim(a * (2 * l15 + 1), 32); }
        else { r[q] = wre(l15 * (2 * a + 1), 32); im[q] = -wim(l15 * (2 * a + 1), 32); }
      }
      t.v[2 * mtx][l][0] = bf2(r[0], r[1]); t.v[2 * mtx][l][1] = bf2(r[2], r[3]); t.v[2 * mtx][l][2] = bf2(-im[0], -im[1]); t.v[2 * mtx][l][3] = bf2(-im[2], -im[3]);
      t.v[2 * mtx + 1][l][0] = bf2(im[0], im[1]); t.v[2 * mtx + 1][l][1] = bf2(im[2], im[3]); t.v[2 * mtx + 1][l][2] = bf2(r[0], r[1]); t.v[2 * mtx + 1][l][3] = bf2(r[2], r[3]);
    }
    {
      const int m2 = n >> 2, pp = (n >> 1) & 1, cp = n & 1;
      double v0[8] = {}, v1[8] = {};
      for (int jj = 0; jj < 8; ++jj) {
        const int ii = (8 * hh + jj) >> 1, d0 = jj & 1;
        const double sg = (d0 & pp) ? -1.0 : 1.0, orr = sg * wre(ii * m2, 8), oi = sg * wim(ii * m2, 8);
        v0[jj] = cp ? oi : orr; v1[jj] = cp ? orr : -oi;
      }
      for (int q = 0; q < 4; ++q) { t.v[6][l][q] = bf2(v0[2 * q], v0[2 * q + 1]); t.v[7][l][q] = bf2(v1[2 * q], v1[2 * q + 1]); }
    }
    {
      const int cq = n >> 4, ii = (n >> 1) & 7, d0 = n & 1;
      for (int s = 0; s < 2; ++s) {
        double v[8] = {};
        for (int jj = 0; jj < 8; ++jj) {
          const int m2k = 4 * s + 2 * hh + (jj >> 2), pk = (jj >> 1) & 1, ck = jj & 1;
          const double sg = (d0 & pk) ? -1.0 : 1.0, orr = sg * wre(ii * m2k, 8), oi = -sg * wim(ii * m2k, 8);
          v[jj] = cq ? (ck ? orr : oi) : (ck ? -oi : orr);
        }
        for (int q = 0; q < 4; ++q) t.v[8 + s][l][q] = bf2(v[2 * q], v[2 * q + 1]);
      }
    }
    for (int q = 0; q < 4; ++q) {
      const int a = 4 * g + q;
      t.v[10][l][q] = fbits(wre(a * (2 * l15), 512));      t.v[11][l][q] = fbits(wim(a * (2 * l15), 512));
      t.v[12][l][q] = fbits(wre(a * (2 * l15 + 1), 512));  t.v[13][l][q] = fbits(wim(a * (2 * l15 + 1), 512));
      t.v[14][l][q] = fbits(wre(l15 * (2 * a + 1), 512));  t.v[15][l][q] = fbits(wim(l15 * (2 * a + 1), 512));
    }
  }
  return t;
}
__device__ const Tab kTab = make_tab();

constexpr unsigned STAMP_OFF = 512 * 1024;
#define STAMP(k) do {} while (0)
#define FT(k) do {} while (0)

DI v4u make_srd(const void* p, unsigned bytes) {
  const unsigned long long a = (unsigned long long)p;
  v4u r; r.x = (unsigned)a; r.y = (unsigned)(a >> 32) & 0xffffu; r.z = bytes; r.w = 0x00020000u;
  return r;
}
template <unsigned LDSI, unsigned SOI> DI void dma16(v4u srd, unsigned voff, unsigned ldsb, unsigned sob) {
  unsigned tmp;
  asm volatile("s_add_u32 m0, %3, %5\n\ts_add_u32 %0, %4, %6\n\tbuffer_load_dwordx4 %1, %2, %0 offen lds"
               : "=&s"(tmp) : "v"(voff), "s"(srd), "s"(ldsb), "s"(sob), "i"(LDSI), "i"(SOI) : "memory", "scc");
}
template <unsigned LDSI, unsigned SOI> DI void dma16z(v4u srd, unsigned voff, unsigned ldsb) {
  unsigned tmp;
  asm volatile("s_add_u32 m0, %3, %4\n\ts_mov_b32 %0, %5\n\tbuffer_load_dwordx4 %1, %2, %0 offen lds"
               : "=&s"(tmp) : "v"(voff), "s"(srd), "s"(ldsb), "i"(LDSI), "i"(SOI) : "memory", "scc");
}
DI unsigned uni(unsigned v) { return (unsigned)__builtin_amdgcn_readfirstlane((int)v); }
#define LD16(dst, voffv, srd, soffv, flags) asm volatile("buffer_load_dwordx4 %0, %1, %2, %3 offen " flags : "=v"(dst) : "v"(voffv), "s"(srd), "s"(soffv) : "memory")
#define LD16I(dst, voffv, srd, sob, soi, flags) do { unsigned tmp_; asm volatile("s_add_u32 %1, %4, %5\n\tbuffer_load_dwordx4 %0, %2, %3, %1 offen " flags : "=v"(dst), "=&s"(tmp_) : "v"(voffv), "s"(srd), "s"(sob), "i"(soi) : "memory", "scc"); } while (0)
#define LD16Z(dst, voffv, srd, flags) asm volatile("buffer_load_dwordx4 %0, %1, %2, 0 offen " flags : "=v"(dst) : "v"(voffv), "s"(srd) : "memory")
#define LD4Z(dst, voffv, srd, flags) asm volatile("buffer_load_dword %0, %1, %2, 0 offen " flags : "=v"(dst) : "v"(voffv), "s"(srd) : "memory")
#define LD2Z(dst, voffv, srd, flags) asm volatile("buffer_load_ushort %0, %1, %2, 0 offen " flags : "=v"(dst) : "v"(voffv), "s"(srd) : "memory")
#define LD8Z(dst, voffv, srd, flags) asm volatile("buffer_load_dwordx2 %0, %1, %2, 0 offen " flags : "=v"(dst) : "v"(voffv), "s"(srd) : "memory")
#define LD8(dst, voffv, srd, soffv, flags) asm volatile("buffer_load_dwordx2 %0, %1, %2, %3 offen " flags : "=v"(dst) : "v"(voffv), "s"(srd), "s"(soffv) : "memory")
#define LD4(dst, voffv, srd, soffv, flags) asm volatile("buffer_load_dword %0, %1, %2, %3 offen " flags : "=v"(dst) : "v"(voffv), "s"(srd), "s"(soffv) : "memory")
#define LD2(dst, voffv, srd, soffv, flags) asm volatile("buffer_load_ushort %0, %1, %2, %3 offen " flags : "=v"(dst) : "v"(voffv), "s"(srd), "s"(soffv) : "memory")
#define XSTR(x) #x
#define STR(x) XSTR(x)
#define BAR_VM(n) asm volatile("s_waitcnt vmcnt(" STR(n) ") lgkmcnt(0)\n\ts_barrier" ::: "memory")
#define BAR_LGKM() asm volatile("s_waitcnt lgkmcnt(0)\n\ts_barrier" ::: "memory")
#define SCHED() __builtin_amdgcn_sched_barrier(0)
template <int N> DI void bar_vm() { asm volatile("s_waitcnt vmcnt(%0) lgkmcnt(0)\n\ts_barrier" :: "i"(N) : "memory"); }
template <int N> DI void wait_vm() { asm volatile("s_waitcnt vmcnt(%0)" :: "i"(N) : "memory"); }

DI unsigned pk2(float a, float b) { f32x2 p = {a, b}; return __builtin_bit_cast(unsigned, __builtin_convertvector(p, bf16x2)); }
DI float bflo(unsigned u) { return __builtin_bit_cast(float, u << 16); }
DI float bfhi(unsigned u) { return __builtin_bit_cast(float, u & 0xffff0000u); }
DI float asf(unsigned u) { return __builtin_bit_cast(float, u); }
DI f32x16 mfma32(v4u a, v4u b, f32x16 c) {
  return __builtin_amdgcn_mfma_f32_32x32x16_bf16(__builtin_bit_cast(bf16x8, a), __builtin_bit_cast(bf16x8, b), c, 0, 0, 0);
}
DI f32x4 mfma16(v4u a, v4u b, f32x4 c) {
  return __builtin_amdgcn_mfma_f32_16x16x32_bf16(__builtin_bit_cast(bf16x8, a), __builtin_bit_cast(bf16x8, b), c, 0, 0, 0);
}
DI v2u tr_read(LAS char* p) { return __builtin_bit_cast(v2u, __builtin_amdgcn_ds_read_tr16_b64_v4i16((LAS s16x4*)p)); }
DI v4u tr_read2(LAS char* p0, LAS char* p1) { v2u a = tr_read(p0), b = tr_read(p1); v4u r; r.x = a.x; r.y = a.y; r.z = b.x; r.w = b.y; return r; }
DI v4u lds_ld16(LAS char* p) { return *(LAS v4u*)p; }
DI float cosrev(float r) { return __builtin_amdgcn_cosf(r); }
DI float sinrev(float r) { return __builtin_amdgcn_sinf(r); }
DI __amdgpu_buffer_rsrc_t as_rsrc(v4u srd) { return __builtin_amdgcn_make_buffer_rsrc((void*)(((unsigned long long)srd.y << 32) | srd.x), 0, srd.z, 0x00020000); }
DI void st16_sc1(v4u srd, unsigned voff, v4u v) { __builtin_amdgcn_raw_buffer_store_b128(v, as_rsrc(srd), voff, 0, 16); }
DI void flag_store(void* ws, unsigned off, unsigned tag) { __hip_atomic_store((GAS unsigned*)((GAS char*)ws + off), tag, __ATOMIC_RELAXED, __HIP_MEMORY_SCOPE_AGENT); }
template <int CTRL, int RMASK> DI float dpp_add(float v) { return v + asf((unsigned)__builtin_amdgcn_update_dpp(0, (int)__builtin_bit_cast(unsigned, v), CTRL, RMASK, 0xf, true)); }
DI void poll_words(v4u sws, unsigned off, int n, unsigned tag, int lane) {
  const unsigned vo = off + FS * (unsigned)(lane < n ? lane : 0);
#pragma unroll 1
  for (unsigned spins = 0; spins < kSpinCap; ++spins) {
    unsigned f;
    asm volatile("buffer_load_dword %0, %1, %2, 0 offen sc1\n\ts_waitcnt vmcnt(0)" : "=&v"(f) : "v"(vo), "s"(sws) : "memory");
    if (__all(f == tag)) return;
    __builtin_amdgcn_s_sleep(4);
  }
}

constexpr unsigned A_XIMG = 64 * 1024, A_FIMG = 128 * 1024;
DI void phaseA_genF(LAS char* L, int i, int mt, int w, int lane) {
  const int mh = w >> 2, sg = w & 3, n = lane & 31, hh = lane >> 5;
  const int m1c = 64 * mt + 32 * mh + n, m1 = m1c >> 1, c = m1c & 1;
  const int j0 = 64 * sg + 32 * hh;
  const float ph = c ? 0.25f : 0.f;
  const float k2 = 2.f * cosrev((float)(8 * m1) * (1.f / 2048.f));
  float vm = cosrev((float)(((i + 8 * (j0 - 1)) * m1) & 2047) * (1.f / 2048.f) + ph);
  float v0 = cosrev((float)(((i + 8 * j0) * m1) & 2047) * (1.f / 2048.f) + ph);
  const unsigned cst = c ? 0x3F800000u : 0x00003F80u;
  const bool sp = m1c < 2;
#pragma unroll
  for (int s = 0; s < 4; ++s) {
    float e[8];
#pragma unroll
    for (int q = 0; q < 8; ++q) { e[q] = v0; const float vn = __builtin_fmaf(k2, v0, -vm); vm = v0; v0 = vn; }
    v4u f; f.x = pk2(e[0], e[1]); f.y = pk2(e[2], e[3]); f.z = pk2(e[4], e[5]); f.w = pk2(e[6], e[7]);
    if (mt == 0 && w < 4) { f.x = sp ? cst : f.x; f.y = sp ? cst : f.y; f.z = sp ? cst : f.z; f.w = sp ? cst : f.w; }
    *(LAS v4u*)(L + A_FIMG + (mh * 16 + sg * 4 + s) * 1024 + lane * 16) = f;
  }
}

struct Frag1 { v4u a[4], b0[4], b1[4]; };
DI void g1_read(Frag1& f, LAS char* wa, LAS char* xb0, LAS char* xb1, LAS char* xb2, LAS char* xb3, int slot) {
  const int so = slot * 32768;
  f.a[0] = tr_read2(wa + so, wa + so + 1024);              f.b0[0] = lds_ld16(xb0 + so); f.b1[0] = lds_ld16(xb0 + so + 4096);
  f.a[1] = tr_read2(wa + so + 4096, wa + so + 5120);       f.b0[1] = lds_ld16(xb1 + so); f.b1[1] = lds_ld16(xb1 + so + 4096);
  f.a[2] = tr_read2(wa + so + 8192, wa + so + 9216);       f.b0[2] = lds_ld16(xb2 + so); f.b1[2] = lds_ld16(xb2 + so + 4096);
  f.a[3] = tr_read2(wa + so + 12288, wa + so + 13312);     f.b0[3] = lds_ld16(xb3 + so); f.b1[3] = lds_ld16(xb3 + so + 4096);
}
DI unsigned silu2(float a, float b) {
  const f32x2 h = {a, b}, t = h * -1.44269504088896341f;
  f32x2 e = {__builtin_amdgcn_exp2f(t[0]), __builtin_amdgcn_exp2f(t[1])};
  e = e + 1.f;
  const f32x2 r = {__builtin_amdgcn_rcpf(e[0]), __builtin_amdgcn_rcpf(e[1])};
  return __builtin_bit_cast(unsigned, __builtin_convertvector(h * r, bf16x2));
}

DI f32x2 lo2(const f32x4& v) { return __builtin_shufflevector(v, v, 0, 1); }
DI f32x2 hi2(const f32x4& v) { return __builtin_shufflevector(v, v, 2, 3); }
DI unsigned pk2v(f32x2 p) { return __builtin_bit_cast(unsigned, __builtin_convertvector(p, bf16x2)); }
DI f32x4 asf4(v4u v) { return __builtin_bit_cast(f32x4, v); }
template <bool CONJ> DI v4u cmul_pack(const f32x4& re, const f32x4& im, const f32x4& tr, const f32x4& ti) {
  const f32x2 r0 = lo2(re), r1 = hi2(re), i0 = lo2(im), i1 = hi2(im), c0 = lo2(tr), c1 = hi2(tr), s0 = lo2(ti), s1 = hi2(ti);
  f32x2 a0, a1, b0, b1;
  if (CONJ) {
    a0 = __builtin_elementwise_fma(r0, c0, i0 * s0); a1 = __builtin_elementwise_fma(r1, c1, i1 * s1);
    b0 = __builtin_elementwise_fma(i0, c0, -(r0 * s0)); b1 = __builtin_elementwise_fma(i1, c1, -(r1 * s1));
  } else {
    a0 = __builtin_elementwise_fma(r0, c0, -(i0 * s0)); a1 = __builtin_elementwise_fma(r1, c1, -(i1 * s1));
    b0 = __builtin_elementwise_fma(r0, s0, i0 * c0); b1 = __builtin_elementwise_fma(r1, s1, i1 * c1);
  }
  v4u r; r.x = pk2v(a0); r.y = pk2v(a1); r.z = pk2v(b0); r.w = pk2v(b1); return r;
}
struct cf { float r, i; };
DI cf cmul(cf a, cf b) { return {a.r * b.r - a.i * b.i, a.r * b.i + a.i * b.r}; }
DI cf cmulc(cf a, cf b) { return {a.r * b.r + a.i * b.i, a.i * b.r - a.r * b.i}; }
DI unsigned w_src(int m, int lane) {
  const int mm = (NS - m) & (NS - 1);
  const int e = lane < 32 ? m * 257 - (m & 1) : mm * 257 + (m & 1);
  return (unsigned)(e * 2 + (lane & 31) * 16);
}
DI unsigned w_tail_src(int m, int lane) {
  const int mm = (NS - m) & (NS - 1), l = lane & 3;
  return (unsigned)((l < 2 ? m * 257 + 255 + l : mm * 257 + (l - 2) * 256) * 2);
}
struct WTail { unsigned r255, r256, rm0, rm256, i255, i256, im0, im256; };
DI WTail w_tail(unsigned tr, unsigned ti) {
  return {(unsigned)__builtin_amdgcn_readlane((int)tr, 0), (unsigned)__builtin_amdgcn_readlane((int)tr, 1), (unsigned)__builtin_amdgcn_readlane((int)tr, 2), (unsigned)__builtin_amdgcn_readlane((int)tr, 3),
          (unsigned)__builtin_amdgcn_readlane((int)ti, 0), (unsigned)__builtin_amdgcn_readlane((int)ti, 1), (unsigned)__builtin_amdgcn_readlane((int)ti, 2), (unsigned)__builtin_amdgcn_readlane((int)ti, 3)};
}
struct WVal { f32x4 r0, i0, r1, i1; };
struct WRawL { unsigned r[4][2], i[4][2]; };
DI void w_put(LAS char* scr, v4u qr, v4u qi, int lane) { *(LAS v4u*)(scr + lane * 16) = qr; *(LAS v4u*)(scr + 1024 + lane * 16) = qi; }
DI int w_off(int m, int lane) { const int k1 = lane & 15, g = lane >> 4, odd = m & 1; return lane < 32 ? (2 * k1 + 128 * g + odd) * 2 : 512 + (511 - 2 * k1 - 128 * g - odd) * 2; }
DI void w_get(WRawL& o, LAS char* scr, int off, int lane, int r) {
  LAS unsigned* a = (LAS unsigned*)(scr + ((off + (lane < 32 ? 64 : -64) * r) & ~3));
  o.r[r][0] = a[0]; o.r[r][1] = a[1]; o.i[r][0] = a[256]; o.i[r][1] = a[257];
}
DI WVal w_unpack(const WRawL& x, const WTail& t, int off, int m, int lane) {
  const bool lo = lane < 32;
  const unsigned sh = (off & 2) * 8;
  unsigned rr[4], ii[4];
#pragma unroll
  for (int r = 0; r < 4; ++r) { rr[r] = __builtin_amdgcn_alignbit(x.r[r][1], x.r[r][0], sh); ii[r] = __builtin_amdgcn_alignbit(x.i[r][1], x.i[r][0], sh); }
  if (m & 1) { rr[3] = lane == 31 ? (rr[3] & 0xffffu) | (t.r255 << 16) : rr[3]; ii[3] = lane == 31 ? (ii[3] & 0xffffu) | (t.i255 << 16) : ii[3]; }
  else { rr[0] = lane == 32 ? (rr[0] & 0xffffu) | (t.rm256 << 16) : rr[0]; ii[0] = lane == 32 ? (ii[0] & 0xffffu) | (t.im256 << 16) : ii[0]; }
  const unsigned s0 = lo ? 16u : 0u, s1 = lo ? 0u : 16u, sg = lo ? 0u : 0x80000000u;
  WVal o;
#pragma unroll
  for (int r = 0; r < 4; ++r) {
    o.r0[r] = asf((rr[r] << s0) & 0xffff0000u); o.r1[r] = asf((rr[r] << s1) & 0xffff0000u);
    o.i0[r] = asf(((ii[r] << s0) & 0xffff0000u) ^ sg); o.i1[r] = asf(((ii[r] << s1) & 0xffff0000u) ^ sg);
  }
  const bool av = (lane & 31) == 0;
  const float xr = bflo(lo ? t.rm0 : t.r256), xi = asf(((lo ? t.im0 : t.i256) << 16) ^ (sg ^ 0x80000000u));
  o.r0[0] = av ? 0.5f * (o.r0[0] + xr) : o.r0[0];
  o.i0[0] = av ? 0.5f * (o.i0[0] + xi) : o.i0[0];
  return o;
}

DI void packed_coef(WVal& a, WVal& b, int m2) {
  const cf om = {cosrev((float)m2 * 0.0625f), -sinrev((float)m2 * 0.0625f)};
#pragma unroll
  for (int pp = 0; pp < 2; ++pp)
#pragma unroll
    for (int r = 0; r < 4; ++r) {
      const float war = pp ? a.r1[r] : a.r0[r], wai = pp ? a.i1[r] : a.i0[r], wbr = pp ? b.r1[r] : b.r0[r], wbi = pp ? b.i1[r] : b.i0[r];
      const cf sp = {war + wbr, wai + wbi}, sm = {war - wbr, wai - wbi};
      const cf t1 = cmulc(sm, om), t2 = cmul(sm, om);
      const cf cE = {sp.r - t1.i, sp.i + t1.r}, cO = {t2.r - sp.i, t2.i + sp.r};
      const float alr = 0.25f * (cE.r + cO.i), ali = 0.25f * (cE.i - cO.r), ber = 0.25f * (cE.r - cO.i), bei = 0.25f * (cE.i + cO.r);
      if (pp) { a.r1[r] = alr; a.i1[r] = ali; b.r1[r] = ber; b.i1[r] = bei; } else { a.r0[r] = alr; a.i0[r] = ali; b.r0[r] = ber; b.i0[r] = bei; }
    }
}

struct Geo {
  int tid, lane, w, b, i, i1, at, m1, rb, fc, hh, blk, q4, p4;
  int jb, dt, rh, q, j;
  v4u sx, sws, sw2;
  unsigned vw20, vw21, ldsw, fq0b, fq1b;
};
template <int T, int U> DI void w2_dma(const v4u& sw2, unsigned vw20, unsigned vw21, unsigned ldsw, unsigned fq0, unsigned fq1) {
  dma16<(T & 7) * 16384u + U * 1024u, (T < 8 ? T : T - 8) * 32768u>(sw2, U ? vw21 : vw20, ldsw, T < 8 ? fq0 : fq1);
}
#define w2_issue1(g, t, u) (w2_dma<t, u>((g).sw2, (g).vw20, (g).vw21, (g).ldsw, (g).fq0b, (g).fq1b))
struct GRot { float c1, s1, c4, s4, c, s; };
DI void gen_g_init(const Geo& g, GRot& r) {
  const int srow = g.i + 8 * g.j;
  const int mb = 32 * g.q + 2 * g.hh;
  const float r1 = (float)(srow & 2047) * (1.f / 2048.f), r4 = (float)((4 * srow) & 2047) * (1.f / 2048.f), r0 = (float)((mb * srow) & 2047) * (1.f / 2048.f);
  r.c1 = cosrev(r1); r.s1 = sinrev(r1); r.c4 = cosrev(r4); r.s4 = sinrev(r4); r.c = cosrev(r0); r.s = sinrev(r0);
}
template <int S> DI void gen_g_frag(const Geo& g, GRot& r, v4u& f) {
#pragma unroll
  for (int h = 0; h < 2; ++h) {
    const float cb = __builtin_fmaf(r.c, r.c1, -(r.s * r.s1)), sb = __builtin_fmaf(r.s, r.c1, r.c * r.s1);
    unsigned w0 = pk2(r.c, -r.s), w1 = pk2(cb, -sb);
    if (S == 0 && h == 0) w0 = (g.q == 0 && g.hh == 0) ? ((g.j & 1) ? 0x3F800000u : 0x00003F80u) : w0;
    if (h) { f.z = w0; f.w = w1; } else { f.x = w0; f.y = w1; }
    const float cn = __builtin_fmaf(r.c, r.c4, -(r.s * r.s4)), sn = __builtin_fmaf(r.s, r.c4, r.c * r.s4);
    r.c = cn; r.s = sn;
  }
  asm volatile("" : "+v"(f));
}
template <int N0, int N1, int NR> DI void bar_w(int w) { if (w == 0) bar_vm<N0>(); else if (w == 1) bar_vm<N1>(); else bar_vm<NR>(); }
#define NOP do {} while (0)

#define TOUCH_POS 1
#define TLBW 0
#define TOUCH() do {                                                                                                      \
    const int gl = G.b * 256 + (w & 3) * 64 + lane;                                                                         \
    const char* base_ = gl < 14336 ? (const char*)p.w2 : gl < 28672 ? (const char*)p.w1 - 14336 * 128 : gl < 36896 ? (const char*)p.wr - 28672 * 128           \
                      : gl < 45120 ? (const char*)p.wi - 36896 * 128 : (const char*)p.x - 45120 * 128;                     \
    const char* ad_ = base_ + (size_t)(gl < 61504 ? gl : 45120) * 128;                                                      \
    if (w >= 4) ad_ = TLBW ? (w == 7 ? (const char*)p.out + lane * 32768 : (const char*)p.ws + (size_t)((w - 4) * 64 + lane + 8) * 65536 + 32768 - 128) : (const char*)p.x; \
    asm volatile("global_load_dword %0, %1, off" : "=v"(touch) : "v"(ad_) : "memory");                                     \
  } while (0)
template <bool ISA>
DI unsigned phase1(const Params& p, LAS char* L, const Geo& G, v4u& m5a, v4u& m5b
) {
  const int tid = G.tid, lane = G.lane, w = G.w, i = G.i, i1 = G.i1, rb = G.rb, fc = G.fc, hh = G.hh, blk = G.blk, q4 = G.q4, p4 = G.p4;
  const v4u sx = G.sx, sws = G.sws, sw1 = make_srd(p.w1, ND * NF * 2);
  unsigned touch;
  unsigned ep; v4u gx, gv;
  const int jg = 128 * rb + 8 * fc + w;
  LD16Z(gx, (unsigned)(lane * 16 + (i1 + 8 * jg) * 1024), sx, "");
  LD16Z(gv, (unsigned)lane * 16u, make_srd(p.gw, ND * 2), "");
  LD4Z(ep, WS_EPOCH, sws, "sc1");
  const int f0 = ISA ? 96 * fc : 768 + 128 * (fc - 8), nf = ISA ? 96 : 128;
  const int mt = G.at >> 2, dt4 = G.at & 3;
  const int m1 = G.m1;
  if (ISA) {
    const unsigned voff = (unsigned)(lane >> 4) * 8192u + (unsigned)(((lane & 15) ^ ((lane >> 4) << 2)) << 4);
    const unsigned xl = uni(A_XIMG + (unsigned)w * 8192u), xs = uni((unsigned)i * 1024u + (unsigned)dt4 * 256u + (unsigned)w * 262144u);
    dma16<0, 0>(sx, voff, xl, xs); dma16<1024, 32768>(sx, voff, xl, xs); dma16<2048, 65536>(sx, voff, xl, xs); dma16<3072, 98304>(sx, voff, xl, xs);
    dma16<4096, 131072>(sx, voff, xl, xs); dma16<5120, 163840>(sx, voff, xl, xs); dma16<6144, 196608>(sx, voff, xl, xs); dma16<7168, 229376>(sx, voff, xl, xs);
  }
  unsigned vx0, vx1, vw0, vw1;
  {
    const int r0 = 16 * w + (lane >> 3), pos = lane & 7, r1 = r0 + 8;
    vx0 = (unsigned)(i1 * 1024 + (128 * rb + r0) * 8192 + ((pos ^ ((r0 >> 1) & 7)) << 4));
    vx1 = (unsigned)(i1 * 1024 + (128 * rb + r1) * 8192 + ((pos ^ ((r1 >> 1) & 7)) << 4));
    const int k0 = 8 * w + (lane >> 4), chunk = (lane & 15) ^ ((lane >> 4) << 2);
    const bool ok = chunk * 8 < nf;
    vw0 = ok ? (unsigned)(k0 * 3584 + f0 * 2 + chunk * 16) : 0x80000000u;
    vw1 = ok ? vw0 + 4u * 3584u : 0x80000000u;
  }
  const unsigned g1l = uni((unsigned)w * 2048u);
#define g1_dma(kt, u) do {                                                                                         \
    if ((u) == 0) dma16z<((kt) & 3) * 32768u, (kt) * 128u>(sx, vx0, g1l);                                           \
    if ((u) == 1) dma16z<((kt) & 3) * 32768u + 1024u, (kt) * 128u>(sx, vx1, g1l);                                   \
    if ((u) == 2) dma16z<((kt) & 3) * 32768u + 16384u, (kt) * (64u * 3584u)>(sw1, vw0, g1l);                        \
    if ((u) == 3) dma16z<((kt) & 3) * 32768u + 17408u, (kt) * (64u * 3584u)>(sw1, vw1, g1l); } while (0)
#define g1_issue(kt) do { g1_dma(kt, 0); g1_dma(kt, 1); g1_dma(kt, 2); g1_dma(kt, 3); } while (0)
  if (ISA) {
    phaseA_genF(L, i, mt, w, lane);
    g1_issue(0); g1_issue(1);
  } else {
    g1_issue(0); asm volatile("s_barrier" ::: "memory");
    g1_issue(1); asm volatile("s_barrier" ::: "memory");
    g1_issue(2); asm volatile("s_barrier" ::: "memory");
    g1_issue(3);
  }
  TOUCH();
  asm volatile("s_waitcnt vmcnt(17)" : "+v"(ep), "+v"(gx), "+v"(gv) :: "memory");
  const unsigned tag = (unsigned)__builtin_amdgcn_readfirstlane((int)ep) + 1u;
  float gate_g, gate_og;
  {
    float z = 0.f;
#pragma unroll
    for (int q = 0; q < 4; ++q) {
      const unsigned pr = pk2(bflo(gx[q]) * bflo(gv[q]), bfhi(gx[q]) * bfhi(gv[q]));
      z += bflo(pr) + bfhi(pr);
    }
    z = dpp_add<0x111, 0xf>(z); z = dpp_add<0x112, 0xf>(z); z = dpp_add<0x114, 0xf>(z); z = dpp_add<0x118, 0xf>(z);
    z = dpp_add<0x142, 0xa>(z); z = dpp_add<0x143, 0xc>(z);
    const float zb = bflo(pk2(z, 0.f));
    const float g = bflo(pk2(__builtin_amdgcn_rcpf(1.f + __builtin_amdgcn_exp2f(zb * -1.44269504088896341f)), 0.f));
    gate_g = asf((unsigned)__builtin_amdgcn_readlane((int)__builtin_bit_cast(unsigned, g), 63));
    gate_og = bflo(pk2(1.f - gate_g, 0.f));
  }
  if (ISA) {
    const int dq = w & 3, mh = w >> 2;
    LAS char* xa = L + A_XIMG + (32 * hh + q4) * 256 + (((4 * dq + 2 * blk + (p4 >> 1)) ^ (q4 << 2)) << 4) + 8 * (p4 & 1);
    LAS char* fb = L + A_FIMG + mh * 16384 + lane * 16;
    const int srow = 32 * mh + (lane & 31);
    LAS char* stw = L + A_FIMG + srow * 256 + 8 * hh;
    const int rrow = tid >> 3;
    LAS char* str = L + A_FIMG + rrow * 256;
    const unsigned tdst = WS_T + (unsigned)(((64 * mt + rrow) * 8 + i) * 1024 + dt4 * 256 + (tid & 7) * 16);
    if (w < 4) bar_vm<9>(); else BAR_LGKM();
    STAMP(1);
    f32x16 acc = {};
    v4u fa[2][4], fbv[2][4];
#pragma unroll
    for (int s = 0; s < 4; ++s) { fa[0][s] = tr_read2(xa + 8 * s * 256, xa + 8 * s * 256 + 1024); fbv[0][s] = lds_ld16(fb + s * 1024); }
#pragma unroll
    for (int g = 0; g < 4; ++g) {
      acc = mfma32(fa[g & 1][0], fbv[g & 1][0], acc);
      SCHED();
      if (g == 1) { if (w >= 4) bar_vm<9>(); else BAR_LGKM(); }
      if (g < 3) {
#pragma unroll
        for (int s = 0; s < 4; ++s) {
          const int ro = (64 * (g + 1) + 8 * s) * 256;
          fa[(g + 1) & 1][s] = tr_read2(xa + ro, xa + ro + 1024); fbv[(g + 1) & 1][s] = lds_ld16(fb + (4 * (g + 1) + s) * 1024);
        }
      }
      SCHED();
#pragma unroll
      for (int s = 1; s < 4; ++s) { acc = mfma32(fa[g & 1][s], fbv[g & 1][s], acc); if (g == 1 && s == 1) { g1_dma(2, 0); g1_dma(2, 1); } if (g == 1 && s == 2) { g1_dma(2, 2); g1_dma(2, 3); } SCHED(); }
    }
    BAR_LGKM();
    STAMP(2);
    g1_issue(3);
#pragma unroll
    for (int Gq = 0; Gq < 4; ++Gq) {
      v2u o; o.x = pk2(acc[4 * Gq], acc[4 * Gq + 1]); o.y = pk2(acc[4 * Gq + 2], acc[4 * Gq + 3]);
      *(LAS v2u*)(stw + (((4 * dq + Gq) ^ (srow & 15)) << 4)) = o;
    }
    BAR_LGKM();
    {
      const v4u v0 = lds_ld16(str + (((tid & 7) ^ (rrow & 15)) << 4)), v1 = lds_ld16(str + ((((tid & 7) + 8) ^ (rrow & 15)) << 4));
      st16_sc1(sws, tdst, v0);
      st16_sc1(sws, tdst + 128u, v1);
    }
    SCHED();
  }
  const int ft = w & 3, rh1 = w >> 2;
  const bool active = ft * 32 < nf;
  f32x16 acc1[2] = {};
  v4u wqr, wqi, tq0, tq1;
  unsigned wtr, wti, xtr, xti;
  v4u xqr, xqi;
  WVal Wc, Wd;
  v4u tb[16];
  unsigned fla;
  int t_ok = 0, t_first = 0; (void)t_first;
  const v4u swr = make_srd(p.wr, NS * 257 * 2), swi = make_srd(p.wi, NS * 257 * 2);
  const int mW = m1 == 0 ? 128 * w : m1 + 256 * w;
  unsigned tsrc0 = 0, tsrc1 = 0;
  const unsigned flaoff = WS_FLA + (unsigned)(((m1 >> 5) & 3) * 32 + (lane & 31)) * FS;
  LAS char* CTL = L + 128 * 1024;
  {
    LAS char* wa = L + 16384 + (8 * hh + q4) * 256 + (((4 * ft + 2 * blk + (p4 >> 1)) ^ (q4 << 2)) << 4) + 8 * (p4 & 1);
    const int r = 64 * rh1 + (lane & 31), fz = (r >> 1) & 7;
    LAS char* xb0 = L + r * 128 + (((0 + hh) ^ fz) << 4);
    LAS char* xb1 = L + r * 128 + (((2 + hh) ^ fz) << 4);
    LAS char* xb2 = L + r * 128 + (((4 + hh) ^ fz) << 4);
    LAS char* xb3 = L + r * 128 + (((6 + hh) ^ fz) << 4);
    Frag1 fr[2];
    bar_vm<ISA ? 15 : 13>();
    STAMP(3);
    if (lane == 0) { v2u gv2; gv2.x = __builtin_bit_cast(unsigned, gate_g); gv2.y = __builtin_bit_cast(unsigned, gate_og); *(LAS v2u*)(CTL + 64 + w * 8) = gv2; }
    g1_read(fr[0], wa, xb0, xb1, xb2, xb3, 0);
#define PIN1(c) asm volatile("" : "+v"(c.a[0]), "+v"(c.a[1]), "+v"(c.a[2]), "+v"(c.a[3]), "+v"(c.b0[0]), "+v"(c.b0[1]), "+v"(c.b0[2]), "+v"(c.b0[3]), \
                             "+v"(c.b1[0]), "+v"(c.b1[1]), "+v"(c.b1[2]), "+v"(c.b1[3]))
#define G1_STEP(kt, X0, X1, X2, X3, X4, X5, X6, X7)                                                                   \
    {                                                                                                                   \
      FT(10 + (kt));                                                                                                    \
      Frag1& c = fr[(kt) & 1];                                                                                          \
      Frag1& n = fr[((kt) + 1) & 1];                                                                                    \
      const int so = (((kt) + 1) & 3) * 32768;                                                                          \
      PIN1(c);                                                                                                          \
      asm volatile("" :: "v"(acc1[0]), "v"(acc1[1]));                                                                   \
      SCHED();                                                                                                          \
      acc1[0] = mfma32(c.a[0], c.b0[0], acc1[0]); if ((kt) < 7) n.a[0] = tr_read2(wa + so, wa + so + 1024); X0; SCHED(); \
      acc1[1] = mfma32(c.a[0], c.b1[0], acc1[1]); if ((kt) < 7) { n.b0[0] = lds_ld16(xb0 + so); n.b1[0] = lds_ld16(xb0 + so + 4096); } X1; SCHED(); \
      acc1[0] = mfma32(c.a[1], c.b0[1], acc1[0]); if ((kt) < 7) n.a[1] = tr_read2(wa + so + 4096, wa + so + 5120); X2; SCHED(); \
      acc1[1] = mfma32(c.a[1], c.b1[1], acc1[1]); if ((kt) < 7) { n.b0[1] = lds_ld16(xb1 + so); n.b1[1] = lds_ld16(xb1 + so + 4096); } X3; SCHED(); \
      acc1[0] = mfma32(c.a[2], c.b0[2], acc1[0]); if ((kt) < 7) n.a[2] = tr_read2(wa + so + 8192, wa + so + 9216); X4; SCHED(); \
      acc1[1] = mfma32(c.a[2], c.b1[2], acc1[1]); if ((kt) < 7) { n.b0[2] = lds_ld16(xb2 + so); n.b1[2] = lds_ld16(xb2 + so + 4096); } X5; SCHED(); \
      acc1[0] = mfma32(c.a[3], c.b0[3], acc1[0]); if ((kt) < 7) n.a[3] = tr_read2(wa + so + 12288, wa + so + 13312); X6; SCHED(); \
      acc1[1] = mfma32(c.a[3], c.b1[3], acc1[1]); if ((kt) < 7) { n.b0[3] = lds_ld16(xb3 + so); n.b1[3] = lds_ld16(xb3 + so + 4096); } X7; SCHED(); \
    }
#define GATE_STORE() do { if (w == 2 && lane < 8) { const v2u gv2 = *(LAS v2u*)(CTL + 64 + lane * 8);                       \
      __builtin_amdgcn_raw_buffer_store_b64(gv2, as_rsrc(sws), WS_GATE + (unsigned)(i1 * 256 + 128 * rb + 8 * fc + lane) * 8u, 0, 16); } } while (0)
#define STEP8 G1_STEP
    if constexpr (ISA) {
      bar_vm<11>();
      STAMP(4);
      STEP8(0, NOP, NOP, NOP, NOP, NOP, NOP, NOP, NOP)
      bar_vm<0>();
      if (tid == 0) flag_store(p.ws, WS_FLA + (unsigned)(mt * 32 + i * 4 + dt4) * FS, tag);
      STAMP(13);
      STEP8(1, g1_dma(4, 0), g1_dma(4, 1), g1_dma(4, 2), g1_dma(4, 3), g1_dma(5, 0), g1_dma(5, 1), g1_dma(5, 2), g1_dma(5, 3))
      BAR_LGKM();
      GATE_STORE();
      STEP8(2, NOP, NOP, NOP, NOP, g1_dma(6, 0), g1_dma(6, 1), g1_dma(6, 2), g1_dma(6, 3))
      bar_vm<8>();
      STEP8(3, NOP, NOP, NOP, NOP, g1_dma(7, 0), g1_dma(7, 1), g1_dma(7, 2), g1_dma(7, 3))
      bar_vm<8>();
      STAMP(12);
      STEP8(4, NOP, NOP, NOP, NOP, w2_issue1(G, 0, 0), w2_issue1(G, 0, 1), w2_issue1(G, 1, 0), w2_issue1(G, 1, 1))
      bar_vm<8>();
      STEP8(5, NOP, NOP, NOP, NOP, w2_issue1(G, 2, 0), w2_issue1(G, 2, 1), w2_issue1(G, 3, 0), w2_issue1(G, 3, 1))
      bar_vm<8>();
      STEP8(6, NOP, NOP, NOP, NOP, w2_issue1(G, 4, 0), w2_issue1(G, 4, 1), w2_issue1(G, 5, 0), w2_issue1(G, 5, 1))
      BAR_LGKM();
    } else {
      const v4u stab = make_srd(&kTab, sizeof(Tab));
      const unsigned wsrc = w_src(mW, lane);
      {
        const int kap = tid >> 5, ii = (kap >> 1) & 7, d0 = kap & 1;
        const int g0 = m1 == 0 ? 2 * ii : ii, g1 = m1 == 0 ? 2 * ii + 1 : 8 + ii;
        const unsigned tb = WS_T + (unsigned)m1 * 16384u + (unsigned)(d0 * 512 + (tid & 31) * 16);
        tsrc0 = tb + (unsigned)g0 * 1024u; tsrc1 = tb + (unsigned)g1 * 1024u;
      }
      const unsigned vw20z = G.vw20 - 16u * 1024u, vw21z = G.vw21 - 16u * 1024u, ldswz = uni(G.ldsw - 2048u);
#define W2B(t, u) do { if (w != 0) w2_issue1(G, t, u); } while (0)
#define W2Z(t, u) do { if (w == 1) w2_dma<t, u>(G.sw2, vw20z, vw21z, ldswz, G.fq0b, G.fq1b); } while (0)
      bar_vm<9>();
      STAMP(4);
      const unsigned wts = w_tail_src(mW, lane);
      if (m1 == 0) {
        const unsigned ws2 = w_src(128 * w + 1024, lane), wt2 = w_tail_src(128 * w + 1024, lane);
        LD16Z(xqr, ws2, swr, ""); LD16Z(xqi, ws2, swi, ""); LD2Z(xtr, wt2, swr, ""); LD2Z(xti, wt2, swi, "");
      }
      STEP8(0, LD16Z(wqr, wsrc, swr, ""), LD16Z(wqi, wsrc, swi, ""), LD2Z(wtr, wts, swr, ""), LD2Z(wti, wts, swi, ""), g1_dma(4, 0), g1_dma(4, 1), g1_dma(4, 2), g1_dma(4, 3))
      bar_vm<13>();
      STEP8(1, NOP, NOP, NOP, NOP, g1_dma(5, 0), g1_dma(5, 1), g1_dma(5, 2), g1_dma(5, 3))
      bar_vm<13>();
      GATE_STORE();
      STEP8(2, NOP, NOP, NOP, NOP, g1_dma(6, 0), g1_dma(6, 1), g1_dma(6, 2), g1_dma(6, 3))
      bar_vm<8>();
      LAS char* wscr0 = L + 128 * 1024 + w * 2048;
      const bool special = m1 == 0;
      const int m2nd = 128 * w + 1024;
      if (special) { asm volatile("" : "+v"(xqr), "+v"(xqi), "+v"(xtr), "+v"(xti)); w_put(wscr0, xqr, xqi, lane); }
      STEP8(3, NOP, NOP, (dma16<128u * 1024u + 16384u, 0>(stab, (unsigned)lane * 16u, g1l, g1l)), (dma16<128u * 1024u + 17408u, 1024>(stab, (unsigned)lane * 16u, g1l, g1l)),
            g1_dma(7, 0), g1_dma(7, 1), g1_dma(7, 2), g1_dma(7, 3))
      bar_vm<10>();
      STAMP(12);
      WRawL raw2; const int off2 = w_off(m2nd, lane);
      if (special) { w_get(raw2, wscr0, off2, lane, 0); w_get(raw2, wscr0, off2, lane, 1); w_get(raw2, wscr0, off2, lane, 2); w_get(raw2, wscr0, off2, lane, 3); }
      const int woff = w_off(mW, lane);
      asm volatile("" : "+v"(wqr), "+v"(wqi), "+v"(wtr), "+v"(wti) :: "memory");
      w_put(wscr0, wqr, wqi, lane);
      STEP8(4, W2Z(0, 0), W2Z(0, 1), W2Z(1, 0), W2Z(1, 1), W2B(0, 0), W2B(0, 1), W2B(1, 0), W2B(1, 1))
      bar_w<6, 14, 10>(w);
      WRawL wraw;
      w_get(wraw, wscr0, woff, lane, 0); w_get(wraw, wscr0, woff, lane, 1); w_get(wraw, wscr0, woff, lane, 2); w_get(wraw, wscr0, woff, lane, 3);
      if (special) Wd = w_unpack(raw2, w_tail(xtr, xti), off2, m2nd, lane);
      STEP8(5, W2Z(2, 0), W2Z(2, 1), W2Z(3, 0), W2Z(3, 1), W2B(2, 0), W2B(2, 1), W2B(3, 0), W2B(3, 1))
      bar_w<0, 16, 8>(w);
      Wc = w_unpack(wraw, w_tail(wtr, wti), woff, mW, lane);
      if (special) packed_coef(Wc, Wd, w);
      STEP8(6, if (w == 0) LD4Z(fla, flaoff, sws, "sc1"), W2Z(4, 0), W2Z(4, 1), W2Z(5, 0), W2B(4, 0), W2B(4, 1), W2B(5, 0), W2B(5, 1))
      W2Z(5, 1);
      if (w == 0) {
        asm volatile("s_waitcnt vmcnt(0)" : "+v"(fla) :: "memory");
        const unsigned ok = __all(fla == tag) ? 1u : 0u;
        if (lane == 0) *(LAS unsigned*)CTL = ok;
      }
      BAR_LGKM();
      t_ok = __builtin_amdgcn_readfirstlane(*(volatile LAS unsigned*)CTL);
      t_first = t_ok;
    }
    if constexpr (ISA) {
      STEP8(7, NOP, NOP, NOP, NOP, w2_issue1(G, 6, 0), w2_issue1(G, 6, 1), w2_issue1(G, 7, 0), w2_issue1(G, 7, 1))
    } else {
      LAS char* TB = L + 128 * 1024 + 16384 + lane * 16;
#define TBR(f) (tb[f] = lds_ld16(TB + (f) * 1024), tb[(f) + 1] = lds_ld16(TB + ((f) + 1) * 1024))
      const unsigned vw20z = G.vw20 - 16u * 1024u, vw21z = G.vw21 - 16u * 1024u, ldswz = uni(G.ldsw - 2048u);
      STEP8(7, { if (t_ok) LD16Z(tq0, tsrc0, sws, "sc1"); TBR(6); }, { if (t_ok) LD16Z(tq1, tsrc1, sws, "sc1"); TBR(0); },
            { if (!t_ok && w == 0) LD4Z(fla, flaoff, sws, "sc1"); TBR(10); }, { TBR(2); W2Z(6, 0); W2Z(6, 1); }, { TBR(12); W2B(6, 0); }, { TBR(14); W2B(6, 1); },
            { TBR(4); W2B(7, 0); W2Z(7, 0); }, { TBR(8); W2B(7, 1); W2Z(7, 1); })
      if (!t_ok) {
        if (w == 0) {
          asm volatile("s_waitcnt vmcnt(0)" : "+v"(fla) :: "memory");
          const unsigned ok = __all(fla == tag) ? 1u : 0u;
          if (lane == 0) *(LAS unsigned*)CTL = ok;
        }
        BAR_LGKM();
        t_ok = __builtin_amdgcn_readfirstlane(*(volatile LAS unsigned*)CTL);
        if (t_ok) { LD16Z(tq0, tsrc0, sws, "sc1"); LD16Z(tq1, tsrc1, sws, "sc1"); }
      }
    }
  }
  asm volatile("" :: "v"(touch));
  STAMP(5);
  FT(18);
  if (active) {
#pragma unroll
    for (int rt = 0; rt < 2; ++rt)
#pragma unroll
      for (int u = 0; u < 2; ++u) {
        v4u o;
        o.x = silu2(acc1[rt][8 * u + 0], acc1[rt][8 * u + 1]);
        o.y = silu2(acc1[rt][8 * u + 2], acc1[rt][8 * u + 3]);
        o.z = silu2(acc1[rt][8 * u + 4], acc1[rt][8 * u + 5]);
        o.w = silu2(acc1[rt][8 * u + 6], acc1[rt][8 * u + 7]);
        const int jb32 = 4 * rb + 2 * rh1 + rt, fs = (f0 >> 4) + 2 * ft + u;
        st16_sc1(sws, WS_H + (unsigned)(((i1 * 8 + jb32) * 112 + fs) * 1024 + lane * 16), o);
      }
  }
  SCHED();
  STAMP(6);
  FT(20);
  const unsigned flh_own = WS_FLH + (unsigned)((i1 * 2 + rb) * 16 + fc) * FS;
  if constexpr (ISA) {
    bar_vm<0>();
    if (tid == 0) flag_store(p.ws, flh_own, tag);
  } else {
    LAS char* I0 = L + 128 * 1024;
    LAS char* I1 = L + 128 * 1024 + 16384;
    const int g = lane >> 4, l15 = lane & 15;
    const bool special = (m1 == 0);
    bool published = false;
    if (t_ok) {
      if (!t_first || w == 0) wait_vm<4>(); else if (w == 1) wait_vm<12>(); else wait_vm<8>();
      asm volatile("s_barrier" ::: "memory");
    } else {
      bar_vm<0>();
      if (tid == 0) flag_store(p.ws, flh_own, tag);
      published = true;
      if (w == 0) poll_words(sws, WS_FLA + (unsigned)(m1 >> 5) * 32u * FS, 32, tag, lane);
      asm volatile("s_barrier" ::: "memory");
      LD16Z(tq0, tsrc0, sws, "sc1"); LD16Z(tq1, tsrc1, sws, "sc1");
      wait_vm<0>();
    }
    asm volatile("" : "+v"(tq0), "+v"(tq1) :: "memory");
    {
      const int kap = tid >> 5, ch = tid & 31;
      *(LAS v4u*)(I0 + kap * 512 + ((ch ^ ((kap & 3) << 2)) << 4)) = tq0;
      *(LAS v4u*)(I0 + (kap + 16) * 512 + ((ch ^ ((kap & 3) << 2)) << 4)) = tq1;
    }
    BAR_LGKM();
    FT(21);
    LAS char* ta = I0 + (8 * hh + q4) * 512 + (((4 * w + 2 * blk + (p4 >> 1)) ^ (q4 << 2)) << 4) + 8 * (p4 & 1);
    const int n = lane & 31;
    LAS char* so = I1 + n * 512 + 8 * hh;
    {
      f32x16 acc = {};
      acc = mfma32(tr_read2(ta, ta + 2048), tb[6], acc);
      acc = mfma32(tr_read2(ta + 8192, ta + 10240), tb[7], acc);
#pragma unroll
      for (int Gq = 0; Gq < 4; ++Gq) {
        v2u o; o.x = pk2(acc[4 * Gq], acc[4 * Gq + 1]); o.y = pk2(acc[4 * Gq + 2], acc[4 * Gq + 3]);
        *(LAS v2u*)(so + (((4 * w + Gq) ^ (n & 15)) << 4)) = o;
      }
    }
    BAR_LGKM();
    FT(22);
    const f32x4 z4 = {0.f, 0.f, 0.f, 0.f};
    const v4u F16fr = tb[0], F16fi = tb[1];
    v4u F16ir, F16ii;
    F16ir.x = F16fr.x; F16ir.y = F16fr.y; F16ir.z = F16fr.z ^ 0x80008000u; F16ir.w = F16fr.w ^ 0x80008000u;
    F16ii.x = F16fi.x ^ 0x80008000u; F16ii.y = F16fi.y ^ 0x80008000u; F16ii.z = F16fi.z; F16ii.w = F16fi.w;
    f32x4 zr[2], zi[2];
#pragma unroll
    for (int pp = 0; pp < 2; ++pp) {
      const int n0 = 4 * w + 2 * pp, n1 = n0 + 1;
      const int ch = 2 * (4 * g + q4) + (p4 >> 1);
      const v4u a1 = tr_read2(I1 + n0 * 512 + ((ch ^ (n0 & 15)) << 4) + 8 * (p4 & 1), I1 + n1 * 512 + ((ch ^ (n1 & 15)) << 4) + 8 * (p4 & 1));
      const f32x4 dr = mfma16(a1, pp ? tb[2] : F16fr, z4), di = mfma16(a1, pp ? tb[3] : F16fi, z4);
      const v4u b3 = cmul_pack<false>(dr, di, asf4(tb[10 + 2 * pp]), asf4(tb[11 + 2 * pp]));
      zr[pp] = mfma16(F16fr, b3, z4); zi[pp] = mfma16(F16fi, b3, z4);
    }
    v4u a4[2];
    if (!special) {
      a4[0] = cmul_pack<false>(zr[0], zi[0], Wc.r0, Wc.i0);
      a4[1] = cmul_pack<false>(zr[1], zi[1], Wc.r1, Wc.i1);
    } else {
      BAR_LGKM();
      LAS char* Z = I0;
#pragma unroll
      for (int pp = 0; pp < 2; ++pp)
#pragma unroll
        for (int r = 0; r < 4; ++r) { f32x2 v = {zr[pp][r], zi[pp][r]}; *(LAS f32x2*)(Z + ((w * 2 + pp) * 256 + l15 + 16 * (4 * g + r)) * 8) = v; }
      BAR_LGKM();
#pragma unroll
      for (int pp = 0; pp < 2; ++pp) {
        f32x4 cr, ci;
#pragma unroll
        for (int r = 0; r < 4; ++r) {
          const int K = l15 + 16 * (4 * g + r), Kn = pp ? 255 - K : (256 - K) & 255;
          const f32x2 y = *(LAS f32x2*)(Z + ((((8 - w) & 7) * 2 + pp) * 256 + Kn) * 8);
          cr[r] = y[0]; ci[r] = -y[1];
        }
        const f32x4 ar = pp ? Wc.r1 : Wc.r0, ai = pp ? Wc.i1 : Wc.i0, br = pp ? Wd.r1 : Wd.r0, bi = pp ? Wd.i1 : Wd.i0;
        const f32x4 yr = zr[pp] * ar - zi[pp] * ai + cr * br - ci * bi, yi = zr[pp] * ai + zi[pp] * ar + cr * bi + ci * br;
        a4[pp].x = pk2(yr[0], yr[1]); a4[pp].y = pk2(yr[2], yr[3]); a4[pp].z = pk2(yi[0], yi[1]); a4[pp].w = pk2(yi[2], yi[3]);
      }
      BAR_LGKM();
    }
#pragma unroll
    for (int pp = 0; pp < 2; ++pp) {
      const f32x4 dr = mfma16(a4[pp], F16ir, z4), di = mfma16(a4[pp], F16ii, z4);
      const v4u a5 = cmul_pack<true>(dr, di, asf4(tb[pp ? 14 : 10]), asf4(tb[pp ? 15 : 11]));
      const f32x4 er = mfma16(a5, pp ? tb[4] : F16ir, z4), ei = mfma16(a5, pp ? tb[5] : F16ii, z4);
      const int n0 = 4 * w + 2 * pp, n1 = n0 + 1;
      const int ch = 2 * l15 + (g >> 1);
      v2u o; o.x = pk2(er[0], er[1]); o.y = pk2(er[2], er[3]);
      *(LAS v2u*)(I0 + n0 * 512 + ((ch ^ ((n0 & 3) << 2)) << 4) + 8 * (g & 1)) = o;
      o.x = pk2(ei[0], ei[1]); o.y = pk2(ei[2], ei[3]);
      *(LAS v2u*)(I0 + n1 * 512 + ((ch ^ ((n1 & 3) << 2)) << 4) + 8 * (g & 1)) = o;
    }
    bar_vm<0>();
    if (!published && tid == 0) flag_store(p.ws, flh_own, tag);
    FT(23);
    m5a = tb[8]; m5b = tb[9];
  }
  return tag;
}

DI void make_geo(Geo& G, const Params& p, int tz) {
  G.tid = tz; G.lane = G.tid & 63; G.w = __builtin_amdgcn_readfirstlane(G.tid >> 6);
  G.b = blockIdx.x;
  const int Lx = G.b >> 3, x8 = G.b & 7;
  {
    const int a = x8 & 3, h = x8 >> 2, rbi = Lx >> 3, c = Lx & 7;
    G.i = 2 * a + h; G.i1 = 2 * a + (rbi >> 1); G.rb = rbi & 1;
    G.fc = c < 4 ? 4 * h + c : 4 + 4 * h + c;
    G.at = rbi * 4 + c; G.m1 = ((rbi * 4 + c - 4) << 3) | x8;
  }
  G.hh = G.lane >> 5; G.blk = (G.lane >> 4) & 1; G.q4 = (G.lane & 15) >> 2; G.p4 = G.lane & 3;
  G.sx = make_srd(p.x, NS * ND * 2); G.sws = make_srd(p.ws, 16u << 20); G.sw2 = make_srd(p.w2, NF * ND * 2);
  G.jb = Lx >> 3; G.dt = Lx & 7; G.rh = G.w & 1; G.q = G.w >> 1;
  G.j = 64 * G.jb + 32 * G.rh + (G.lane & 31);
  const int rho = 16 * G.rh + (G.lane >> 3), chs = (G.lane & 7) ^ (4 * ((G.lane >> 4) & 1));
  G.vw20 = (unsigned)(rho * 1024 + G.dt * 128 + chs * 16); G.vw21 = G.vw20 + 8 * 1024;
  G.ldsw = uni((unsigned)(G.q * 4096 + G.rh * 2048));
  G.fq0b = uni((unsigned)(768 + 256 * G.q) * 1024u); G.fq1b = uni((unsigned)(192 * G.q) * 1024u);
}

__global__ __launch_bounds__(512, 2) void gm_kernel(Params p) {
  extern __shared__ __attribute__((aligned(16))) char smem[];
  LAS char* L = (LAS char*)smem;
  STAMP(0);
  FT(0);
  Geo G;
  make_geo(G, p, threadIdx.x);
  const bool isA = G.fc < 8;
  v4u gf[4], m5a, m5b;
  unsigned tag;
  if (isA) tag = phase1<true>(p, L, G, m5a, m5b); else tag = phase1<false>(p, L, G, m5a, m5b);
  { int tz = threadIdx.x; asm volatile("" : "+v"(tz)); make_geo(G, p, tz); }
  const int tid = G.tid, lane = G.lane, w = G.w, b = G.b, i = G.i, jb = G.jb, dt = G.dt, rh = G.rh, q = G.q, j = G.j;
  const int hh = G.hh, blk = G.blk, q4 = G.q4, p4 = G.p4;
  const v4u sws = G.sws;
  const unsigned ldsw = G.ldsw;
  const int m1 = G.m1;
  STAMP(7);
  FT(25);
  if (!isA) {
    LAS char* I0 = L + 128 * 1024;
    LAS char* I1 = L + 128 * 1024 + 16384;
    LAS char* ta = I0 + (8 * hh + q4) * 512 + (((4 * w + 2 * blk + (p4 >> 1)) ^ (q4 << 2)) << 4) + 8 * (p4 & 1);
    const int n = lane & 31;
    LAS char* so = I1 + n * 512 + 8 * hh;
    f32x16 acc = {};
    acc = mfma32(tr_read2(ta, ta + 2048), m5a, acc);
    acc = mfma32(tr_read2(ta + 8192, ta + 10240), m5b, acc);
#pragma unroll
    for (int Gq = 0; Gq < 4; ++Gq) {
      v2u o; o.x = pk2(acc[4 * Gq], acc[4 * Gq + 1]); o.y = pk2(acc[4 * Gq + 2], acc[4 * Gq + 3]);
      *(LAS v2u*)(so + (((4 * w + Gq) ^ (n & 15)) << 4)) = o;
    }
    BAR_LGKM();
    const bool special = (m1 == 0);
    auto u_store = [&](int t2) {
      const int nn = t2 >> 5, ch = t2 & 31, ii = (nn >> 1) & 7, d0 = nn & 1;
      const int g0 = special ? 2 * ii : ii, g1 = special ? 2 * ii + 1 : 8 + ii;
      const v4u v0 = lds_ld16(I1 + nn * 512 + ((ch ^ (nn & 15)) << 4));
      const v4u v1 = lds_ld16(I1 + (nn + 16) * 512 + ((ch ^ (nn & 15)) << 4));
      const unsigned ub = WS_U + (unsigned)m1 * 16384u + (unsigned)(d0 * 512 + ch * 16);
      st16_sc1(sws, ub + (unsigned)g0 * 1024u, v0);
      st16_sc1(sws, ub + (unsigned)g1 * 1024u, v1);
    };
    if (w != 0) u_store(tid);
    if (w == 1) u_store(tid - 64);
    SCHED();
  }
  const int sz = (q4 >> 1) & 1;
  LAS char* ra0 = L + q * 4096 + (4 * hh + q4) * 128 + (((0 ^ (4 * sz)) + 2 * blk + (p4 >> 1)) << 4) + 8 * (p4 & 1);
  LAS char* ra1 = L + q * 4096 + (4 * hh + q4) * 128 + (((4 ^ (4 * sz)) + 2 * blk + (p4 >> 1)) << 4) + 8 * (p4 & 1);
  v4u wp[4][4];
  BAR_LGKM();
#pragma unroll
  for (int t = 0; t < 4; ++t) {
    LAS char* s0 = ra0 + t * 16384; LAS char* s1 = ra1 + t * 16384;
    wp[t][0] = tr_read2(s0, s0 + 1024); wp[t][1] = tr_read2(s1, s1 + 1024);
    wp[t][2] = tr_read2(s0 + 2048, s0 + 3072); wp[t][3] = tr_read2(s1 + 2048, s1 + 3072);
  }
#define W2_8_11() do { w2_issue1(G, 8, 0); w2_issue1(G, 8, 1); w2_issue1(G, 9, 0); w2_issue1(G, 9, 1); w2_issue1(G, 10, 0); w2_issue1(G, 10, 1); w2_issue1(G, 11, 0); w2_issue1(G, 11, 1); } while (0)
  if (isA) { BAR_LGKM(); W2_8_11(); }
  const unsigned flh = WS_FLH + (unsigned)((i * 2 + (jb >> 1)) * 16) * FS;
  if (w == 0) poll_words(sws, flh, 16, tag, lane);
  BAR_LGKM();
  STAMP(8);
  FT(26);
  const unsigned vh = (unsigned)lane * 16u;
  const unsigned hbase = WS_H + (unsigned)((i * 8 + 2 * jb + rh) * 112) * 1024u;
  const unsigned hb0 = uni(hbase + (G.fq0b >> 4)), hb1 = uni(hbase + (G.fq1b >> 4));
  v2u gg;
  v4u hf[14][2];
#define H_ISSUE(t) do { LD16I(hf[t][0], vh, sws, ((t) < 8 ? hb0 : hb1), (2 * ((t) & 7)) * 1024, "sc1"); LD16I(hf[t][1], vh, sws, ((t) < 8 ? hb0 : hb1), (2 * ((t) & 7) + 1) * 1024, "sc1"); } while (0)
  H_ISSUE(0); H_ISSUE(1);
  asm volatile("s_barrier" ::: "memory");
  H_ISSUE(2); H_ISSUE(3); H_ISSUE(4); H_ISSUE(5); H_ISSUE(6);
  LD8Z(gg, WS_GATE + (unsigned)(i * 256 + j) * 8u, sws, "sc1");
  SCHED();
  f32x16 accb[2] = {}, acca[2] = {};
  v4u wf[2][4];
  unsigned fl0, fl1;
  v4u ub0, ub1, ub2, ub3;
  const unsigned vu = WS_U + (unsigned)(((64 * q + 16 * rh + (lane >> 3)) * 8 + i) * 1024 + dt * 128 + ((lane & 7) ^ (4 * ((lane >> 4) & 1))) * 16);
#define G2_BAR(n, t) asm volatile("s_waitcnt vmcnt(" STR(n) ") lgkmcnt(0)\n\ts_barrier" : "+v"(hf[t][0]), "+v"(hf[t][1]) :: "memory")
#define G2_CORE(t, c, n, RD, acc, b0, b1, X0, X1, X2)                                             \
  {                                                                                                 \
    FT(30 + (t));                                                                                   \
    LAS char* s0 = ra0 + (((t) + 1) & 7) * 16384; LAS char* s1 = ra1 + (((t) + 1) & 7) * 16384;    \
    asm volatile("" : "+v"(c[0]), "+v"(c[1]), "+v"(c[2]), "+v"(c[3]));                             \
    asm volatile("" :: "v"(acc[0]), "v"(acc[1]));                                                   \
    SCHED();                                                                                        \
    acc[0] = mfma32(c[0], b0, acc[0]); if (RD) n[0] = tr_read2(s0, s0 + 1024); SCHED();            \
    acc[1] = mfma32(c[1], b0, acc[1]); if (RD) n[1] = tr_read2(s1, s1 + 1024); X0; SCHED();        \
    acc[0] = mfma32(c[2], b1, acc[0]); if (RD) n[2] = tr_read2(s0 + 2048, s0 + 3072); X1; SCHED(); \
    acc[1] = mfma32(c[3], b1, acc[1]); if (RD) n[3] = tr_read2(s1 + 2048, s1 + 3072); X2; SCHED(); \
  }
#define G2_PRE(t, X2) G2_CORE(t, wp[t], wf[0], (t) == 3, accb, hf[t][0], hf[t][1], if (!isA) w2_issue1(G, (t) + 8, 0), if (!isA) w2_issue1(G, (t) + 8, 1), X2)
#define G2_STEP(t, X0, X1, X2) G2_CORE(t, wf[(t) & 1], wf[((t) + 1) & 1], true, accb, hf[t][0], hf[t][1], X0, X1, X2)
  G2_BAR(13, 0);
  if (!isA && tid == 0) flag_store(p.ws, WS_FLB + (unsigned)m1 * FS, tag);
  STAMP(14);
  G2_PRE(0, H_ISSUE(7))
  G2_BAR(13, 1); G2_PRE(1, H_ISSUE(8))
  G2_BAR(13, 2); G2_PRE(2, H_ISSUE(9))
  G2_BAR(13, 3); G2_PRE(3, H_ISSUE(10))
  G2_BAR(13, 4); G2_STEP(4, w2_issue1(G, 12, 0), w2_issue1(G, 12, 1), H_ISSUE(11))
  G2_BAR(15, 5); G2_STEP(5, w2_issue1(G, 13, 0), w2_issue1(G, 13, 1), H_ISSUE(12))
  if (w == 0) { LD4Z(fl0, WS_FLB + (unsigned)lane * FS, sws, "sc1"); LD4Z(fl1, WS_FLB + (64u + (unsigned)lane) * FS, sws, "sc1"); }
  G2_BAR(17, 6); G2_STEP(6, NOP, NOP, H_ISSUE(13))
  STAMP(9);
  G2_BAR(16, 7); G2_STEP(7, NOP, NOP, NOP)
  LAS unsigned* verdict = (LAS unsigned*)(L + 128 * 1024);
  if (w == 0) {
    asm volatile("s_waitcnt vmcnt(2)" : "+v"(fl0), "+v"(fl1) :: "memory");
    const unsigned ok = __all(fl0 == tag && fl1 == tag) ? 1u : 0u;
    if (lane == 0) *verdict = ok;
  }
  G2_BAR(14, 8);
  const int u_early = __builtin_amdgcn_readfirstlane(*(volatile LAS unsigned*)verdict);
  STAMP(15);
#define U_ISSUE() do { LD16Z(ub0, vu, sws, "sc1"); LD16Z(ub1, vu + 8u * 8192u, sws, "sc1"); LD16Z(ub2, vu + 32u * 8192u, sws, "sc1"); LD16Z(ub3, vu + 40u * 8192u, sws, "sc1"); } while (0)
  if (u_early) U_ISSUE();
  GRot grot;
  G2_STEP(8, gen_g_init(G, grot), NOP, gen_g_frag<0>(G, grot, gf[0]))
  unsigned e0, e1, e2, e3;
  if (b == 0 && w == 1) {
    LD4Z(e0, WS_FLH + (unsigned)lane * FS, sws, "sc1"); LD4Z(e1, WS_FLH + (64u + (unsigned)lane) * FS, sws, "sc1");
    LD4Z(e2, WS_FLH + (128u + (unsigned)lane) * FS, sws, "sc1"); LD4Z(e3, WS_FLH + (192u + (unsigned)lane) * FS, sws, "sc1");
  }
  G2_BAR(12, 9); G2_STEP(9, NOP, NOP, gen_g_frag<1>(G, grot, gf[1]))
  G2_BAR(10, 10); G2_STEP(10, NOP, NOP, gen_g_frag<2>(G, grot, gf[2]))
  G2_BAR(6, 11); G2_STEP(11, NOP, NOP, gen_g_frag<3>(G, grot, gf[3]))
  G2_BAR(2, 12);
  G2_STEP(12, NOP, NOP, NOP)
#define U_PUT() do {                                                                                                        \
    asm volatile("s_waitcnt vmcnt(0)" : "+v"(ub0), "+v"(ub1), "+v"(ub2), "+v"(ub3) :: "memory");                             \
    *(LAS v4u*)(L + 6 * 16384 + ldsw + lane * 16) = ub0; *(LAS v4u*)(L + 6 * 16384 + ldsw + 1024 + lane * 16) = ub1;         \
    *(LAS v4u*)(L + 7 * 16384 + ldsw + lane * 16) = ub2; *(LAS v4u*)(L + 7 * 16384 + ldsw + 1024 + lane * 16) = ub3; } while (0)
  if (u_early) U_PUT();
  G2_BAR(0, 13);
  asm volatile("" : "+v"(gg));
  if (b == 0 && w == 1) {
    asm volatile("" : "+v"(e0), "+v"(e1), "+v"(e2), "+v"(e3));
    if (!__all(e0 == tag && e1 == tag && e2 == tag && e3 == tag)) {
#pragma unroll 1
      for (int u = 0; u < 4; ++u) poll_words(sws, WS_FLH + 64u * FS * u, 64, tag, lane);
    }
    if (lane == 0) flag_store(p.ws, WS_EPOCH, tag);
  }
  G2_CORE(13, wf[1], wf[0], u_early, accb, hf[13][0], hf[13][1], NOP, NOP, NOP)
  if (!u_early) {
    if (w == 0) { poll_words(sws, WS_FLB, 64, tag, lane); poll_words(sws, WS_FLB + 64u * FS, 64, tag, lane); }
    BAR_LGKM();
    U_ISSUE();
    U_PUT();
    BAR_LGKM();
    LAS char* s0 = ra0 + 6 * 16384; LAS char* s1 = ra1 + 6 * 16384;
    wf[0][0] = tr_read2(s0, s0 + 1024); wf[0][1] = tr_read2(s1, s1 + 1024); wf[0][2] = tr_read2(s0 + 2048, s0 + 3072); wf[0][3] = tr_read2(s1 + 2048, s1 + 3072);
  }
  STAMP(10);
  BAR_LGKM(); G2_CORE(14, wf[0], wf[1], true, acca, gf[0], gf[1], NOP, NOP, NOP)
  BAR_LGKM(); G2_CORE(15, wf[1], wf[0], false, acca, gf[2], gf[3], NOP, NOP, NOP)
  FT(50);
  {
    const float og = asf(gg.y), gs = asf(gg.x) * (1.f / 524288.f);
    const int row = 32 * rh + (lane & 31);
#pragma unroll
    for (int dh = 0; dh < 2; ++dh)
#pragma unroll
      for (int G = 0; G < 4; ++G) {
        f32x4 v;
#pragma unroll
        for (int e = 0; e < 4; ++e) v[e] = __builtin_fmaf(og, accb[dh][4 * G + e], gs * acca[dh][4 * G + e]);
        *(LAS f32x4*)(L + q * 16384 + row * 256 + (((8 * dh + 2 * G + hh) ^ (row & 7)) << 4)) = v;
      }
  }
  BAR_LGKM();
  FT(51);
  {
    const int row = tid >> 3, c8 = tid & 7;
    f32x4 s0 = {0.f, 0.f, 0.f, 0.f}, s1 = s0;
#pragma unroll
    for (int qq = 0; qq < 4; ++qq) {
      s0 += *(LAS f32x4*)(L + qq * 16384 + row * 256 + (((2 * c8) ^ (row & 7)) << 4));
      s1 += *(LAS f32x4*)(L + qq * 16384 + row * 256 + (((2 * c8 + 1) ^ (row & 7)) << 4));
    }
    v4u o; o.x = pk2(s0[0], s0[1]); o.y = pk2(s0[2], s0[3]); o.z = pk2(s1[0], s1[1]); o.w = pk2(s1[2], s1[3]);
    *(GAS v4u*)((GAS char*)p.out + (size_t)(i + 8 * (64 * jb + row)) * 1024 + dt * 128 + c8 * 16) = o;
  }
  STAMP(11);
}

}

extern "C" void kernel_launch(void* const* d_in, const int* in_sizes, int n_in, void* d_out, int out_size, void* d_ws, size_t ws_size,
                              hipStream_t stream) {
  Params p;
  p.x = d_in[0]; p.wr = d_in[1]; p.wi = d_in[2]; p.w1 = d_in[4]; p.w2 = d_in[5]; p.gw = d_in[6];
  p.out = d_out; p.ws = d_ws;
  (void)hipFuncSetAttribute((const void*)gm_kernel, hipFuncAttributeMaxDynamicSharedMemorySize, LDS_BYTES);
  gm_kernel<<<256, 512, LDS_BYTES, stream>>>(p);
}
```

```cpp
#include <hip/hip_runtime.h>
#include <hip/hip_bf16.h>
#include <stdint.h>

namespace {

typedef unsigned v4u __attribute__((ext_vector_type(4)));
typedef unsigned v2u __attribute__((ext_vector_type(2)));
typedef float f32x16 __attribute__((ext_vector_type(16)));
typedef float f32x4 __attribute__((ext_vector_type(4)));
typedef float f32x2 __attribute__((ext_vector_type(2)));
typedef __bf16 bf16x8 __attribute__((ext_vector_type(8)));
typedef __bf16 bf16x2 __attribute__((ext_vector_type(2)));
typedef short s16x4 __attribute__((ext_vector_type(4)));
#define LAS __attribute__((address_space(3)))
#define GAS __attribute__((address_space(1)))
#define DI __device__ __forceinline__

constexpr int NS = 2048, ND = 512, NF = 1792;
constexpr unsigned WS_EPOCH = 0;
constexpr unsigned FS = 64;
constexpr unsigned WS_FLA = 4096;
constexpr unsigned WS_FLH = 16384;
constexpr unsigned WS_FLB = 32768;
constexpr unsigned WS_GATE = 65536;
constexpr unsigned WS_T = 1u << 20;
constexpr unsigned WS_U = 3u << 20;
constexpr unsigned WS_H = 5u << 20;
constexpr unsigned H_BYTES = 8u * 8u * 112u * 1024u;
constexpr int LDS_BYTES = 160 * 1024;
constexpr unsigned kSpinCap = 1u << 20;

struct Params {
  const void* x; const void* wr; const void* wi; const void* w1; const void* w2; const void* gw;
  void* out; void* ws;
};

constexpr double kPi = 3.14159265358979323846;
constexpr double ccos(double x) { double t = 1, s = 1; for (int n = 1; n < 16; ++n) { t *= -x * x / ((2 * n - 1) * (2 * n)); s += t; } return s; }
constexpr double cosn(int e, int n) { e = ((e % n) + n) % n; double x = 2 * kPi * e / n; if (x > kPi) x -= 2 * kPi; return ccos(x); }
constexpr double wre(int e, int n) { return cosn(e, n); }
constexpr double wim(int e, int n) { return -cosn(4 * e - n, 4 * n); }
constexpr unsigned bfb(double v) { float f = (float)v; unsigned u = __builtin_bit_cast(unsigned, f); u += 0x7fffu + ((u >> 16) & 1u); return u >> 16; }
constexpr unsigned bf2(double a, double b) { return bfb(a) | (bfb(b) << 16); }
constexpr unsigned fbits(double v) { return __builtin_bit_cast(unsigned, (float)v); }
struct Tab { unsigned v[16][64][4]; };
constexpr Tab make_tab() {
  Tab t{};
  for (int l = 0; l < 64; ++l) {
    const int g = l >> 4, l15 = l & 15, hh = l >> 5, n = l & 31;
    for (int mtx = 0; mtx < 3; ++mtx) {
      double r[4] = {}, im[4] = {};
      for (int q = 0; q < 4; ++q) {
        const int a = 4 * g + q;
        if (mtx == 0) { r[q] = wre(l15 * a, 16); im[q] = wim(l15 * a, 16); }
        else if (mtx == 1) { r[q] = wre(a * (2 * l15 + 1), 32); im[q] = wim(a * (2 * l15 + 1), 32); }
        else { r[q] = wre(l15 * (2 * a + 1), 32); im[q] = -wim(l15 * (2 * a + 1), 32); }
      }
      t.v[2 * mtx][l][0] = bf2(r[0], r[1]); t.v[2 * mtx][l][1] = bf2(r[2], r[3]); t.v[2 * mtx][l][2] = bf2(-im[0], -im[1]); t.v[2 * mtx][l][3] = bf2(-im[2], -im[3]);
      t.v[2 * mtx + 1][l][0] = bf2(im[0], im[1]); t.v[2 * mtx + 1][l][1] = bf2(im[2], im[3]); t.v[2 * mtx + 1][l][2] = bf2(r[0], r[1]); t.v[2 * mtx + 1][l][3] = bf2(r[2], r[3]);
    }
    {
      const int m2 = n >> 2, pp = (n >> 1) & 1, cp = n & 1;
      double v0[8] = {}, v1[8] = {};
      for (int jj = 0; jj < 8; ++jj) {
        const int ii = (8 * hh + jj) >> 1, d0 = jj & 1;
        const double sg = (d0 & pp) ? -1.0 : 1.0, orr = sg * wre(ii * m2, 8), oi = sg * wim(ii * m2, 8);
        v0[jj] = cp ? oi : orr; v1[jj] = cp ? orr : -oi;
      }
      for (int q = 0; q < 4; ++q) { t.v[6][l][q] = bf2(v0[2 * q], v0[2 * q + 1]); t.v[7][l][q] = bf2(v1[2 * q], v1[2 * q + 1]); }
    }
    {
      const int cq = n >> 4, ii = (n >> 1) & 7, d0 = n & 1;
      for (int s = 0; s < 2; ++s) {
        double v[8] = {};
        for (int jj = 0; jj < 8; ++jj) {
          const int m2k = 4 * s + 2 * hh + (jj >> 2), pk = (jj >> 1) & 1, ck = jj & 1;
          const double sg = (d0 & pk) ? -1.0 : 1.0, orr = sg * wre(ii * m2k, 8), oi = -sg * wim(ii * m2k, 8);
          v[jj] = cq ? (ck ? orr : oi) : (ck ? -oi : orr);
        }
        for (int q = 0; q < 4; ++q) t.v[8 + s][l][q] = bf2(v[2 * q], v[2 * q + 1]);
      }
    }
    for (int q = 0; q < 4; ++q) {
      const int a = 4 * g + q;
      t.v[10][l][q] = fbits(wre(a * (2 * l15), 512));      t.v[11][l][q] = fbits(wim(a * (2 * l15), 512));
      t.v[12][l][q] = fbits(wre(a * (2 * l15 + 1), 512));  t.v[13][l][q] = fbits(wim(a * (2 * l15 + 1), 512));
      t.v[14][l][q] = fbits(wre(l15 * (2 * a + 1), 512));  t.v[15][l][q] = fbits(wim(l15 * (2 * a + 1), 512));
    }
  }
  return t;
}
__device__ const Tab kTab = make_tab();

constexpr unsigned STAMP_OFF = 512 * 1024;
#define STAMP(k) do { if ((k) == 4) { if ((((blockIdx.x >> 3) & 7) >= 4)) asm volatile("s_memrealtime %0\n\ts_waitcnt lgkmcnt(0)" : "=s"(pad_t) :: "memory"); } } while (0)
#define FT(k) do {} while (0)

DI v4u make_srd(const void* p, unsigned bytes) {
  const unsigned long long a = (unsigned long long)p;
  v4u r; r.x = (unsigned)a; r.y = (unsigned)(a >> 32) & 0xffffu; r.z = bytes; r.w = 0x00020000u;
  return r;
}
template <unsigned LDSI, unsigned SOI> DI void dma16(v4u srd, unsigned voff, unsigned ldsb, unsigned sob) {
  unsigned tmp;
  asm volatile("s_add_u32 m0, %3, %5\n\ts_add_u32 %0, %4, %6\n\tbuffer_load_dwordx4 %1, %2, %0 offen lds"
               : "=&s"(tmp) : "v"(voff), "s"(srd), "s"(ldsb), "s"(sob), "i"(LDSI), "i"(SOI) : "memory", "scc");
}
template <unsigned LDSI, unsigned SOI> DI void dma16z(v4u srd, unsigned voff, unsigned ldsb) {
  unsigned tmp;
  asm volatile("s_add_u32 m0, %3, %4\n\ts_mov_b32 %0, %5\n\tbuffer_load_dwordx4 %1, %2, %0 offen lds"
               : "=&s"(tmp) : "v"(voff), "s"(srd), "s"(ldsb), "i"(LDSI), "i"(SOI) : "memory", "scc");
}
DI unsigned uni(unsigned v) { return (unsigned)__builtin_amdgcn_readfirstlane((int)v); }
#define LD16(dst, voffv, srd, soffv, flags) asm volatile("buffer_load_dwordx4 %0, %1, %2, %3 offen " flags : "=v"(dst) : "v"(voffv), "s"(srd), "s"(soffv) : "memory")
#define LD16I(dst, voffv, srd, sob, soi, flags) do { unsigned tmp_; asm volatile("s_add_u32 %1, %4, %5\n\tbuffer_load_dwordx4 %0, %2, %3, %1 offen " flags : "=v"(dst), "=&s"(tmp_) : "v"(voffv), "s"(srd), "s"(sob), "i"(soi) : "memory", "scc"); } while (0)
#define LD16Z(dst, voffv, srd, flags) asm volatile("buffer_load_dwordx4 %0, %1, %2, 0 offen " flags : "=v"(dst) : "v"(voffv), "s"(srd) : "memory")
#define LD4Z(dst, voffv, srd, flags) asm volatile("buffer_load_dword %0, %1, %2, 0 offen " flags : "=v"(dst) : "v"(voffv), "s"(srd) : "memory")
#define LD2Z(dst, voffv, srd, flags) asm volatile("buffer_load_ushort %0, %1, %2, 0 offen " flags : "=v"(dst) : "v"(voffv), "s"(srd) : "memory")
#define LD8Z(dst, voffv, srd, flags) asm volatile("buffer_load_dwordx2 %0, %1, %2, 0 offen " flags : "=v"(dst) : "v"(voffv), "s"(srd) : "memory")
#define LD8(dst, voffv, srd, soffv, flags) asm volatile("buffer_load_dwordx2 %0, %1, %2, %3 offen " flags : "=v"(dst) : "v"(voffv), "s"(srd), "s"(soffv) : "memory")
#define LD4(dst, voffv, srd, soffv, flags) asm volatile("buffer_load_dword %0, %1, %2, %3 offen " flags : "=v"(dst) : "v"(voffv), "s"(srd), "s"(soffv) : "memory")
#define LD2(dst, voffv, srd, soffv, flags) asm volatile("buffer_load_ushort %0, %1, %2, %3 offen " flags : "=v"(dst) : "v"(voffv), "s"(srd), "s"(soffv) : "memory")
#define XSTR(x) #x
#define STR(x) XSTR(x)
#define BAR_VM(n) asm volatile("s_waitcnt vmcnt(" STR(n) ") lgkmcnt(0)\n\ts_barrier" ::: "memory")
#define BAR_LGKM() asm volatile("s_waitcnt lgkmcnt(0)\n\ts_barrier" ::: "memory")
#define SCHED() __builtin_amdgcn_sched_barrier(0)
template <int N> DI void bar_vm() { asm volatile("s_waitcnt vmcnt(%0) lgkmcnt(0)\n\ts_barrier" :: "i"(N) : "memory"); }
template <int N> DI void wait_vm() { asm volatile("s_waitcnt vmcnt(%0)" :: "i"(N) : "memory"); }

DI unsigned pk2(float a, float b) { f32x2 p = {a, b}; return __builtin_bit_cast(unsigned, __builtin_convertvector(p, bf16x2)); }
DI float bflo(unsigned u) { return __builtin_bit_cast(float, u << 16); }
DI float bfhi(unsigned u) { return __builtin_bit_cast(float, u & 0xffff0000u); }
DI float asf(unsigned u) { return __builtin_bit_cast(float, u); }
DI f32x16 mfma32(v4u a, v4u b, f32x16 c) {
  return __builtin_amdgcn_mfma_f32_32x32x16_bf16(__builtin_bit_cast(bf16x8, a), __builtin_bit_cast(bf16x8, b), c, 0, 0, 0);
}
DI f32x4 mfma16(v4u a, v4u b, f32x4 c) {
  return __builtin_amdgcn_mfma_f32_16x16x32_bf16(__builtin_bit_cast(bf16x8, a), __builtin_bit_cast(bf16x8, b), c, 0, 0, 0);
}
DI v2u tr_read(LAS char* p) { return __builtin_bit_cast(v2u, __builtin_amdgcn_ds_read_tr16_b64_v4i16((LAS s16x4*)p)); }
DI v4u tr_read2(LAS char* p0, LAS char* p1) { v2u a = tr_read(p0), b = tr_read(p1); v4u r; r.x = a.x; r.y = a.y; r.z = b.x; r.w = b.y; return r; }
DI v4u lds_ld16(LAS char* p) { return *(LAS v4u*)p; }
DI float cosrev(float r) { return __builtin_amdgcn_cosf(r); }
DI float sinrev(float r) { return __builtin_amdgcn_sinf(r); }
DI __amdgpu_buffer_rsrc_t as_rsrc(v4u srd) { return __builtin_amdgcn_make_buffer_rsrc((void*)(((unsigned long long)srd.y << 32) | srd.x), 0, srd.z, 0x00020000); }
DI void st16_sc1(v4u srd, unsigned voff, v4u v) { __builtin_amdgcn_raw_buffer_store_b128(v, as_rsrc(srd), voff, 0, 16); }
DI void flag_store(void* ws, unsigned off, unsigned tag) { __hip_atomic_store((GAS unsigned*)((GAS char*)ws + off), tag, __ATOMIC_RELAXED, __HIP_MEMORY_SCOPE_AGENT); }
template <int CTRL, int RMASK> DI float dpp_add(float v) { return v + asf((unsigned)__builtin_amdgcn_update_dpp(0, (int)__builtin_bit_cast(unsigned, v), CTRL, RMASK, 0xf, true)); }
DI void poll_words(v4u sws, unsigned off, int n, unsigned tag, int lane) {
  const unsigned vo = off + FS * (unsigned)(lane < n ? lane : 0);
#pragma unroll 1
  for (unsigned spins = 0; spins < kSpinCap; ++spins) {
    unsigned f;
    asm volatile("buffer_load_dword %0, %1, %2, 0 offen sc1\n\ts_waitcnt vmcnt(0)" : "=&v"(f) : "v"(vo), "s"(sws) : "memory");
    if (__all(f == tag)) return;
    __builtin_amdgcn_s_sleep(4);
  }
}

constexpr unsigned A_XIMG = 64 * 1024, A_FIMG = 128 * 1024;
DI void phaseA_genF(LAS char* L, int i, int mt, int w, int lane) {
  const int mh = w >> 2, sg = w & 3, n = lane & 31, hh = lane >> 5;
  const int m1c = 64 * mt + 32 * mh + n, m1 = m1c >> 1, c = m1c & 1;
  const int j0 = 64 * sg + 32 * hh;
  const float ph = c ? 0.25f : 0.f;
  const float k2 = 2.f * cosrev((float)(8 * m1) * (1.f / 2048.f));
  float vm = cosrev((float)(((i + 8 * (j0 - 1)) * m1) & 2047) * (1.f / 2048.f) + ph);
  float v0 = cosrev((float)(((i + 8 * j0) * m1) & 2047) * (1.f / 2048.f) + ph);
  const unsigned cst = c ? 0x3F800000u : 0x00003F80u;
  const bool sp = m1c < 2;
#pragma unroll
  for (int s = 0; s < 4; ++s) {
    float e[8];
#pragma unroll
    for (int q = 0; q < 8; ++q) { e[q] = v0; const float vn = __builtin_fmaf(k2, v0, -vm); vm = v0; v0 = vn; }
    v4u f; f.x = pk2(e[0], e[1]); f.y = pk2(e[2], e[3]); f.z = pk2(e[4], e[5]); f.w = pk2(e[6], e[7]);
    if (mt == 0 && w < 4) { f.x = sp ? cst : f.x; f.y = sp ? cst : f.y; f.z = sp ? cst : f.z; f.w = sp ? cst : f.w; }
    *(LAS v4u*)(L + A_FIMG + (mh * 16 + sg * 4 + s) * 1024 + lane * 16) = f;
  }
}

struct Frag1 { v4u a[4], b0[4], b1[4]; };
DI void g1_read(Frag1& f, LAS char* wa, LAS char* xb0, LAS char* xb1, LAS char* xb2, LAS char* xb3, int slot) {
  const int so = slot * 32768;
  f.a[0] = tr_read2(wa + so, wa + so + 1024);              f.b0[0] = lds_ld16(xb0 + so); f.b1[0] = lds_ld16(xb0 + so + 4096);
  f.a[1] = tr_read2(wa + so + 4096, wa + so + 5120);       f.b0[1] = lds_ld16(xb1 + so); f.b1[1] = lds_ld16(xb1 + so + 4096);
  f.a[2] = tr_read2(wa + so + 8192, wa + so + 9216);       f.b0[2] = lds_ld16(xb2 + so); f.b1[2] = lds_ld16(xb2 + so + 4096);
  f.a[3] = tr_read2(wa + so + 12288, wa + so + 13312);     f.b0[3] = lds_ld16(xb3 + so); f.b1[3] = lds_ld16(xb3 + so + 4096);
}
DI unsigned silu2(float a, float b) {
  const f32x2 h = {a, b}, t = h * -1.44269504088896341f;
  f32x2 e = {__builtin_amdgcn_exp2f(t[0]), __builtin_amdgcn_exp2f(t[1])};
  e = e + 1.f;
  const f32x2 r = {__builtin_amdgcn_rcpf(e[0]), __builtin_amdgcn_rcpf(e[1])};
  return __builtin_bit_cast(unsigned, __builtin_convertvector(h * r, bf16x2));
}

DI f32x2 lo2(const f32x4& v) { return __builtin_shufflevector(v, v, 0, 1); }
DI f32x2 hi2(const f32x4& v) { return __builtin_shufflevector(v, v, 2, 3); }
DI unsigned pk2v(f32x2 p) { return __builtin_bit_cast(unsigned, __builtin_convertvector(p, bf16x2)); }
DI f32x4 asf4(v4u v) { return __builtin_bit_cast(f32x4, v); }
template <bool CONJ> DI v4u cmul_pack(const f32x4& re, const f32x4& im, const f32x4& tr, const f32x4& ti) {
  const f32x2 r0 = lo2(re), r1 = hi2(re), i0 = lo2(im), i1 = hi2(im), c0 = lo2(tr), c1 = hi2(tr), s0 = lo2(ti), s1 = hi2(ti);
  f32x2 a0, a1, b0, b1;
  if (CONJ) {
    a0 = __builtin_elementwise_fma(r0, c0, i0 * s0); a1 = __builtin_elementwise_fma(r1, c1, i1 * s1);
    b0 = __builtin_elementwise_fma(i0, c0, -(r0 * s0)); b1 = __builtin_elementwise_fma(i1, c1, -(r1 * s1));
  } else {
    a0 = __builtin_elementwise_fma(r0, c0, -(i0 * s0)); a1 = __builtin_elementwise_fma(r1, c1, -(i1 * s1));
    b0 = __builtin_elementwise_fma(r0, s0, i0 * c0); b1 = __builtin_elementwise_fma(r1, s1, i1 * c1);
  }
  v4u r; r.x = pk2v(a0); r.y = pk2v(a1); r.z = pk2v(b0); r.w = pk2v(b1); return r;
}
struct cf { float r, i; };
DI cf cmul(cf a, cf b) { return {a.r * b.r - a.i * b.i, a.r * b.i + a.i * b.r}; }
DI cf cmulc(cf a, cf b) { return {a.r * b.r + a.i * b.i, a.i * b.r - a.r * b.i}; }
DI unsigned w_src(int m, int lane) {
  const int mm = (NS - m) & (NS - 1);
  const int e = lane < 32 ? m * 257 - (m & 1) : mm * 257 + (m & 1);
  return (unsigned)(e * 2 + (lane & 31) * 16);
}
DI unsigned w_tail_src(int m, int lane) {
  const int mm = (NS - m) & (NS - 1), l = lane & 3;
  return (unsigned)((l < 2 ? m * 257 + 255 + l : mm * 257 + (l - 2) * 256) * 2);
}
struct WTail { unsigned r255, r256, rm0, rm256, i255, i256, im0, im256; };
DI WTail w_tail(unsigned tr, unsigned ti) {
  return {(unsigned)__builtin_amdgcn_readlane((int)tr, 0), (unsigned)__builtin_amdgcn_readlane((int)tr, 1), (unsigned)__builtin_amdgcn_readlane((int)tr, 2), (unsigned)__builtin_amdgcn_readlane((int)tr, 3),
          (unsigned)__builtin_amdgcn_readlane((int)ti, 0), (unsigned)__builtin_amdgcn_readlane((int)ti, 1), (unsigned)__builtin_amdgcn_readlane((int)ti, 2), (unsigned)__builtin_amdgcn_readlane((int)ti, 3)};
}
struct WVal { f32x4 r0, i0, r1, i1; };
struct WRawL { unsigned r[4][2], i[4][2]; };
DI void w_put(LAS char* scr, v4u qr, v4u qi, int lane) { *(LAS v4u*)(scr + lane * 16) = qr; *(LAS v4u*)(scr + 1024 + lane * 16) = qi; }
DI int w_off(int m, int lane) { const int k1 = lane & 15, g = lane >> 4, odd = m & 1; return lane < 32 ? (2 * k1 + 128 * g + odd) * 2 : 512 + (511 - 2 * k1 - 128 * g - odd) * 2; }
DI void w_get(WRawL& o, LAS char* scr, int off, int lane, int r) {
  LAS unsigned* a = (LAS unsigned*)(scr + ((off + (lane < 32 ? 64 : -64) * r) & ~3));
  o.r[r][0] = a[0]; o.r[r][1] = a[1]; o.i[r][0] = a[256]; o.i[r][1] = a[257];
}
DI WVal w_unpack(const WRawL& x, const WTail& t, int off, int m, int lane) {
  const bool lo = lane < 32;
  const unsigned sh = (off & 2) * 8;
  unsigned rr[4], ii[4];
#pragma unroll
  for (int r = 0; r < 4; ++r) { rr[r] = __builtin_amdgcn_alignbit(x.r[r][1], x.r[r][0], sh); ii[r] = __builtin_amdgcn_alignbit(x.i[r][1], x.i[r][0], sh); }
  if (m & 1) { rr[3] = lane == 31 ? (rr[3] & 0xffffu) | (t.r255 << 16) : rr[3]; ii[3] = lane == 31 ? (ii[3] & 0xffffu) | (t.i255 << 16) : ii[3]; }
  else { rr[0] = lane == 32 ? (rr[0] & 0xffffu) | (t.rm256 << 16) : rr[0]; ii[0] = lane == 32 ? (ii[0] & 0xffffu) | (t.im256 << 16) : ii[0]; }
  const unsigned s0 = lo ? 16u : 0u, s1 = lo ? 0u : 16u, sg = lo ? 0u : 0x80000000u;
  WVal o;
#pragma unroll
  for (int r = 0; r < 4; ++r) {
    o.r0[r] = asf((rr[r] << s0) & 0xffff0000u); o.r1[r] = asf((rr[r] << s1) & 0xffff0000u);
    o.i0[r] = asf(((ii[r] << s0) & 0xffff0000u) ^ sg); o.i1[r] = asf(((ii[r] << s1) & 0xffff0000u) ^ sg);
  }
  const bool av = (lane & 31) == 0;
  const float xr = bflo(lo ? t.rm0 : t.r256), xi = asf(((lo ? t.im0 : t.i256) << 16) ^ (sg ^ 0x80000000u));
  o.r0[0] = av ? 0.5f * (o.r0[0] + xr) : o.r0[0];
  o.i0[0] = av ? 0.5f * (o.i0[0] + xi) : o.i0[0];
  return o;
}

DI void packed_coef(WVal& a, WVal& b, int m2) {
  const cf om = {cosrev((float)m2 * 0.0625f), -sinrev((float)m2 * 0.0625f)};
#pragma unroll
  for (int pp = 0; pp < 2; ++pp)
#pragma unroll
    for (int r = 0; r < 4; ++r) {
      const float war = pp ? a.r1[r] : a.r0[r], wai = pp ? a.i1[r] : a.i0[r], wbr = pp ? b.r1[r] : b.r0[r], wbi = pp ? b.i1[r] : b.i0[r];
      const cf sp = {war + wbr, wai + wbi}, sm = {war - wbr, wai - wbi};
      const cf t1 = cmulc(sm, om), t2 = cmul(sm, om);
      const cf cE = {sp.r - t1.i, sp.i + t1.r}, cO = {t2.r - sp.i, t2.i + sp.r};
      const float alr = 0.25f * (cE.r + cO.i), ali = 0.25f * (cE.i - cO.r), ber = 0.25f * (cE.r - cO.i), bei = 0.25f * (cE.i + cO.r);
      if (pp) { a.r1[r] = alr; a.i1[r] = ali; b.r1[r] = ber; b.i1[r] = bei; } else { a.r0[r] = alr; a.i0[r] = ali; b.r0[r] = ber; b.i0[r] = bei; }
    }
}

struct Geo {
  int tid, lane, w, b, i, i1, at, m1, rb, fc, hh, blk, q4, p4;
  int jb, dt, rh, q, j;
  v4u sx, sws, sw2;
  unsigned vw20, vw21, ldsw, fq0b, fq1b;
};
template <int T, int U> DI void w2_dma(const v4u& sw2, unsigned vw20, unsigned vw21, unsigned ldsw, unsigned fq0, unsigned fq1) {
  dma16<(T & 7) * 16384u + U * 1024u, (T < 8 ? T : T - 8) * 32768u>(sw2, U ? vw21 : vw20, ldsw, T < 8 ? fq0 : fq1);
}
#define w2_issue1(g, t, u) (w2_dma<t, u>((g).sw2, (g).vw20, (g).vw21, (g).ldsw, (g).fq0b, (g).fq1b))
struct GRot { float c1, s1, c4, s4, c, s; };
DI void gen_g_init(const Geo& g, GRot& r) {
  const int srow = g.i + 8 * g.j;
  const int mb = 32 * g.q + 2 * g.hh;
  const float r1 = (float)(srow & 2047) * (1.f / 2048.f), r4 = (float)((4 * srow) & 2047) * (1.f / 2048.f), r0 = (float)((mb * srow) & 2047) * (1.f / 2048.f);
  r.c1 = cosrev(r1); r.s1 = sinrev(r1); r.c4 = cosrev(r4); r.s4 = sinrev(r4); r.c = cosrev(r0); r.s = sinrev(r0);
}
template <int S> DI void gen_g_frag(const Geo& g, GRot& r, v4u& f) {
#pragma unroll
  for (int h = 0; h < 2; ++h) {
    const float cb = __builtin_fmaf(r.c, r.c1, -(r.s * r.s1)), sb = __builtin_fmaf(r.s, r.c1, r.c * r.s1);
    unsigned w0 = pk2(r.c, -r.s), w1 = pk2(cb, -sb);
    if (S == 0 && h == 0) w0 = (g.q == 0 && g.hh == 0) ? ((g.j & 1) ? 0x3F800000u : 0x00003F80u) : w0;
    if (h) { f.z = w0; f.w = w1; } else { f.x = w0; f.y = w1; }
    const float cn = __builtin_fmaf(r.c, r.c4, -(r.s * r.s4)), sn = __builtin_fmaf(r.s, r.c4, r.c * r.s4);
    r.c = cn; r.s = sn;
  }
  asm volatile("" : "+v"(f));
}
template <int N0, int N1, int NR> DI void bar_w(int w) { if (w == 0) bar_vm<N0>(); else if (w == 1) bar_vm<N1>(); else bar_vm<NR>(); }
#define NOP do {} while (0)

#define TOUCH_POS 1
#define TLBW 0
#define TOUCH() do {                                                                                                      \
    const int gl = G.b * 256 + (w & 3) * 64 + lane;                                                                         \
    const char* base_ = gl < 14336 ? (const char*)p.w2 : gl < 28672 ? (const char*)p.w1 - 14336 * 128 : gl < 36896 ? (const char*)p.wr - 28672 * 128           \
                      : gl < 45120 ? (const char*)p.wi - 36896 * 128 : (const char*)p.x - 45120 * 128;                     \
    const char* ad_ = base_ + (size_t)(gl < 61504 ? gl : 45120) * 128;                                                      \
    if (w >= 4) ad_ = TLBW ? (w == 7 ? (const char*)p.out + lane * 32768 : (const char*)p.ws + (size_t)((w - 4) * 64 + lane + 8) * 65536 + 32768 - 128) : (const char*)p.x; \
    asm volatile("global_load_dword %0, %1, off" : "=v"(touch) : "v"(ad_) : "memory");                                     \
  } while (0)
template <bool ISA>
DI unsigned phase1(long long& pad_t, const Params& p, LAS char* L, const Geo& G, v4u& m5a, v4u& m5b
) {
  const int tid = G.tid, lane = G.lane, w = G.w, i = G.i, i1 = G.i1, rb = G.rb, fc = G.fc, hh = G.hh, blk = G.blk, q4 = G.q4, p4 = G.p4;
  const v4u sx = G.sx, sws = G.sws, sw1 = make_srd(p.w1, ND * NF * 2);
  unsigned touch;
  unsigned ep; v4u gx, gv;
  const int jg = 128 * rb + 8 * fc + w;
  LD16Z(gx, (unsigned)(lane * 16 + (i1 + 8 * jg) * 1024), sx, "");
  LD16Z(gv, (unsigned)lane * 16u, make_srd(p.gw, ND * 2), "");
  LD4Z(ep, WS_EPOCH, sws, "sc1");
  const int f0 = ISA ? 96 * fc : 768 + 128 * (fc - 8), nf = ISA ? 96 : 128;
  const int mt = G.at >> 2, dt4 = G.at & 3;
  const int m1 = G.m1;
  if (ISA) {
    const unsigned voff = (unsigned)(lane >> 4) * 8192u + (unsigned)(((lane & 15) ^ ((lane >> 4) << 2)) << 4);
    const unsigned xl = uni(A_XIMG + (unsigned)w * 8192u), xs = uni((unsigned)i * 1024u + (unsigned)dt4 * 256u + (unsigned)w * 262144u);
    dma16<0, 0>(sx, voff, xl, xs); dma16<1024, 32768>(sx, voff, xl, xs); dma16<2048, 65536>(sx, voff, xl, xs); dma16<3072, 98304>(sx, voff, xl, xs);
    dma16<4096, 131072>(sx, voff, xl, xs); dma16<5120, 163840>(sx, voff, xl, xs); dma16<6144, 196608>(sx, voff, xl, xs); dma16<7168, 229376>(sx, voff, xl, xs);
  }
  unsigned vx0, vx1, vw0, vw1;
  {
    const int r0 = 16 * w + (lane >> 3), pos = lane & 7, r1 = r0 + 8;
    vx0 = (unsigned)(i1 * 1024 + (128 * rb + r0) * 8192 + ((pos ^ ((r0 >> 1) & 7)) << 4));
    vx1 = (unsigned)(i1 * 1024 + (128 * rb + r1) * 8192 + ((pos ^ ((r1 >> 1) & 7)) << 4));
    const int k0 = 8 * w + (lane >> 4), chunk = (lane & 15) ^ ((lane >> 4) << 2);
    const bool ok = chunk * 8 < nf;
    vw0 = ok ? (unsigned)(k0 * 3584 + f0 * 2 + chunk * 16) : 0x80000000u;
    vw1 = ok ? vw0 + 4u * 3584u : 0x80000000u;
  }
  const unsigned g1l = uni((unsigned)w * 2048u);
#define g1_dma(kt, u) do {                                                                                         \
    if ((u) == 0) dma16z<((kt) & 3) * 32768u, (kt) * 128u>(sx, vx0, g1l);                                           \
    if ((u) == 1) dma16z<((kt) & 3) * 32768u + 1024u, (kt) * 128u>(sx, vx1, g1l);                                   \
    if ((u) == 2) dma16z<((kt) & 3) * 32768u + 16384u, (kt) * (64u * 3584u)>(sw1, vw0, g1l);                        \
    if ((u) == 3) dma16z<((kt) & 3) * 32768u + 17408u, (kt) * (64u * 3584u)>(sw1, vw1, g1l); } while (0)
#define g1_issue(kt) do { g1_dma(kt, 0); g1_dma(kt, 1); g1_dma(kt, 2); g1_dma(kt, 3); } while (0)
  if (ISA) {
    phaseA_genF(L, i, mt, w, lane);
    g1_issue(0); g1_issue(1);
  } else {
    g1_issue(0); asm volatile("s_barrier" ::: "memory");
    g1_issue(1); asm volatile("s_barrier" ::: "memory");
    g1_issue(2); asm volatile("s_barrier" ::: "memory");
    g1_issue(3);
  }
  TOUCH();
  asm volatile("s_waitcnt vmcnt(17)" : "+v"(ep), "+v"(gx), "+v"(gv) :: "memory");
  const unsigned tag = (unsigned)__builtin_amdgcn_readfirstlane((int)ep) + 1u;
  float gate_g, gate_og;
  {
    float z = 0.f;
#pragma unroll
    for (int q = 0; q < 4; ++q) {
      const unsigned pr = pk2(bflo(gx[q]) * bflo(gv[q]), bfhi(gx[q]) * bfhi(gv[q]));
      z += bflo(pr) + bfhi(pr);
    }
    z = dpp_add<0x111, 0xf>(z); z = dpp_add<0x112, 0xf>(z); z = dpp_add<0x114, 0xf>(z); z = dpp_add<0x118, 0xf>(z);
    z = dpp_add<0x142, 0xa>(z); z = dpp_add<0x143, 0xc>(z);
    const float zb = bflo(pk2(z, 0.f));
    const float g = bflo(pk2(__builtin_amdgcn_rcpf(1.f + __builtin_amdgcn_exp2f(zb * -1.44269504088896341f)), 0.f));
    gate_g = asf((unsigned)__builtin_amdgcn_readlane((int)__builtin_bit_cast(unsigned, g), 63));
    gate_og = bflo(pk2(1.f - gate_g, 0.f));
  }
  if (ISA) {
    const int dq = w & 3, mh = w >> 2;
    LAS char* xa = L + A_XIMG + (32 * hh + q4) * 256 + (((4 * dq + 2 * blk + (p4 >> 1)) ^ (q4 << 2)) << 4) + 8 * (p4 & 1);
    LAS char* fb = L + A_FIMG + mh * 16384 + lane * 16;
    const int srow = 32 * mh + (lane & 31);
    LAS char* stw = L + A_FIMG + srow * 256 + 8 * hh;
    const int rrow = tid >> 3;
    LAS char* str = L + A_FIMG + rrow * 256;
    const unsigned tdst = WS_T + (unsigned)(((64 * mt + rrow) * 8 + i) * 1024 + dt4 * 256 + (tid & 7) * 16);
    if (w < 4) bar_vm<9>(); else BAR_LGKM();
    STAMP(1);
    f32x16 acc = {};
    v4u fa[2][4], fbv[2][4];
#pragma unroll
    for (int s = 0; s < 4; ++s) { fa[0][s] = tr_read2(xa + 8 * s * 256, xa + 8 * s * 256 + 1024); fbv[0][s] = lds_ld16(fb + s * 1024); }
#pragma unroll
    for (int g = 0; g < 4; ++g) {
      acc = mfma32(fa[g & 1][0], fbv[g & 1][0], acc);
      SCHED();
      if (g == 1) { if (w >= 4) bar_vm<9>(); else BAR_LGKM(); }
      if (g < 3) {
#pragma unroll
        for (int s = 0; s < 4; ++s) {
          const int ro = (64 * (g + 1) + 8 * s) * 256;
          fa[(g + 1) & 1][s] = tr_read2(xa + ro, xa + ro + 1024); fbv[(g + 1) & 1][s] = lds_ld16(fb + (4 * (g + 1) + s) * 1024);
        }
      }
      SCHED();
#pragma unroll
      for (int s = 1; s < 4; ++s) { acc = mfma32(fa[g & 1][s], fbv[g & 1][s], acc); if (g == 1 && s == 1) { g1_dma(2, 0); g1_dma(2, 1); } if (g == 1 && s == 2) { g1_dma(2, 2); g1_dma(2, 3); } SCHED(); }
    }
    BAR_LGKM();
    STAMP(2);
    g1_issue(3);
#pragma unroll
    for (int Gq = 0; Gq < 4; ++Gq) {
      v2u o; o.x = pk2(acc[4 * Gq], acc[4 * Gq + 1]); o.y = pk2(acc[4 * Gq + 2], acc[4 * Gq + 3]);
      *(LAS v2u*)(stw + (((4 * dq + Gq) ^ (srow & 15)) << 4)) = o;
    }
    BAR_LGKM();
    {
      const v4u v0 = lds_ld16(str + (((tid & 7) ^ (rrow & 15)) << 4)), v1 = lds_ld16(str + ((((tid & 7) + 8) ^ (rrow & 15)) << 4));
      st16_sc1(sws, tdst, v0);
      st16_sc1(sws, tdst + 128u, v1);
    }
    SCHED();
  }
  const int ft = w & 3, rh1 = w >> 2;
  const bool active = ft * 32 < nf;
  f32x16 acc1[2] = {};
  v4u wqr, wqi, tq0, tq1;
  unsigned wtr, wti, xtr, xti;
  v4u xqr, xqi;
  WVal Wc, Wd;
  v4u tb[16];
  unsigned fla;
  int t_ok = 0, t_first = 0; (void)t_first;
  const v4u swr = make_srd(p.wr, NS * 257 * 2), swi = make_srd(p.wi, NS * 257 * 2);
  const int mW = m1 == 0 ? 128 * w : m1 + 256 * w;
  unsigned tsrc0 = 0, tsrc1 = 0;
  const unsigned flaoff = WS_FLA + (unsigned)(((m1 >> 5) & 3) * 32 + (lane & 31)) * FS;
  LAS char* CTL = L + 128 * 1024;
  {
    LAS char* wa = L + 16384 + (8 * hh + q4) * 256 + (((4 * ft + 2 * blk + (p4 >> 1)) ^ (q4 << 2)) << 4) + 8 * (p4 & 1);
    const int r = 64 * rh1 + (lane & 31), fz = (r >> 1) & 7;
    LAS char* xb0 = L + r * 128 + (((0 + hh) ^ fz) << 4);
    LAS char* xb1 = L + r * 128 + (((2 + hh) ^ fz) << 4);
    LAS char* xb2 = L + r * 128 + (((4 + hh) ^ fz) << 4);
    LAS char* xb3 = L + r * 128 + (((6 + hh) ^ fz) << 4);
    Frag1 fr[2];
    bar_vm<ISA ? 15 : 13>();
    STAMP(3);
    if (lane == 0) { v2u gv2; gv2.x = __builtin_bit_cast(unsigned, gate_g); gv2.y = __builtin_bit_cast(unsigned, gate_og); *(LAS v2u*)(CTL + 64 + w * 8) = gv2; }
    g1_read(fr[0], wa, xb0, xb1, xb2, xb3, 0);
#define PIN1(c) asm volatile("" : "+v"(c.a[0]), "+v"(c.a[1]), "+v"(c.a[2]), "+v"(c.a[3]), "+v"(c.b0[0]), "+v"(c.b0[1]), "+v"(c.b0[2]), "+v"(c.b0[3]), \
                             "+v"(c.b1[0]), "+v"(c.b1[1]), "+v"(c.b1[2]), "+v"(c.b1[3]))
#define G1_STEP(kt, X0, X1, X2, X3, X4, X5, X6, X7)                                                                   \
    {                                                                                                                   \
      FT(10 + (kt));                                                                                                    \
      Frag1& c = fr[(kt) & 1];                                                                                          \
      Frag1& n = fr[((kt) + 1) & 1];                                                                                    \
      const int so = (((kt) + 1) & 3) * 32768;                                                                          \
      PIN1(c);                                                                                                          \
      asm volatile("" :: "v"(acc1[0]), "v"(acc1[1]));                                                                   \
      SCHED();                                                                                                          \
      acc1[0] = mfma32(c.a[0], c.b0[0], acc1[0]); if ((kt) < 7) n.a[0] = tr_read2(wa + so, wa + so + 1024); X0; SCHED(); \
      acc1[1] = mfma32(c.a[0], c.b1[0], acc1[1]); if ((kt) < 7) { n.b0[0] = lds_ld16(xb0 + so); n.b1[0] = lds_ld16(xb0 + so + 4096); } X1; SCHED(); \
      acc1[0] = mfma32(c.a[1], c.b0[1], acc1[0]); if ((kt) < 7) n.a[1] = tr_read2(wa + so + 4096, wa + so + 5120); X2; SCHED(); \
      acc1[1] = mfma32(c.a[1], c.b1[1], acc1[1]); if ((kt) < 7) { n.b0[1] = lds_ld16(xb1 + so); n.b1[1] = lds_ld16(xb1 + so + 4096); } X3; SCHED(); \
      acc1[0] = mfma32(c.a[2], c.b0[2], acc1[0]); if ((kt) < 7) n.a[2] = tr_read2(wa + so + 8192, wa + so + 9216); X4; SCHED(); \
      acc1[1] = mfma32(c.a[2], c.b1[2], acc1[1]); if ((kt) < 7) { n.b0[2] = lds_ld16(xb2 + so); n.b1[2] = lds_ld16(xb2 + so + 4096); } X5; SCHED(); \
      acc1[0] = mfma32(c.a[3], c.b0[3], acc1[0]); if ((kt) < 7) n.a[3] = tr_read2(wa + so + 12288, wa + so + 13312); X6; SCHED(); \
      acc1[1] = mfma32(c.a[3], c.b1[3], acc1[1]); if ((kt) < 7) { n.b0[3] = lds_ld16(xb3 + so); n.b1[3] = lds_ld16(xb3 + so + 4096); } X7; SCHED(); \
    }
#define GATE_STORE() do { if (w == 2 && lane < 8) { const v2u gv2 = *(LAS v2u*)(CTL + 64 + lane * 8);                       \
      __builtin_amdgcn_raw_buffer_store_b64(gv2, as_rsrc(sws), WS_GATE + (unsigned)(i1 * 256 + 128 * rb + 8 * fc + lane) * 8u, 0, 16); } } while (0)
#define STEP8 G1_STEP
    if constexpr (ISA) {
      bar_vm<11>();
      STAMP(4);
      STEP8(0, NOP, NOP, NOP, NOP, NOP, NOP, NOP, NOP)
      bar_vm<0>();
      if (tid == 0) flag_store(p.ws, WS_FLA + (unsigned)(mt * 32 + i * 4 + dt4) * FS, tag);
      STAMP(13);
      STEP8(1, g1_dma(4, 0), g1_dma(4, 1), g1_dma(4, 2), g1_dma(4, 3), g1_dma(5, 0), g1_dma(5, 1), g1_dma(5, 2), g1_dma(5, 3))
      BAR_LGKM();
      GATE_STORE();
      STEP8(2, NOP, NOP, NOP, NOP, g1_dma(6, 0), g1_dma(6, 1), g1_dma(6, 2), g1_dma(6, 3))
      bar_vm<8>();
      STEP8(3, NOP, NOP, NOP, NOP, g1_dma(7, 0), g1_dma(7, 1), g1_dma(7, 2), g1_dma(7, 3))
      bar_vm<8>();
      STAMP(12);
      STEP8(4, NOP, NOP, NOP, NOP, w2_issue1(G, 0, 0), w2_issue1(G, 0, 1), w2_issue1(G, 1, 0), w2_issue1(G, 1, 1))
      bar_vm<8>();
      STEP8(5, NOP, NOP, NOP, NOP, w2_issue1(G, 2, 0), w2_issue1(G, 2, 1), w2_issue1(G, 3, 0), w2_issue1(G, 3, 1))
      bar_vm<8>();
      STEP8(6, NOP, NOP, NOP, NOP, w2_issue1(G, 4, 0), w2_issue1(G, 4, 1), w2_issue1(G, 5, 0), w2_issue1(G, 5, 1))
      BAR_LGKM();
    } else {
      const v4u stab = make_srd(&kTab, sizeof(Tab));
      const unsigned wsrc = w_src(mW, lane);
      {
        const int kap = tid >> 5, ii = (kap >> 1) & 7, d0 = kap & 1;
        const int g0 = m1 == 0 ? 2 * ii : ii, g1 = m1 == 0 ? 2 * ii + 1 : 8 + ii;
        const unsigned tb = WS_T + (unsigned)m1 * 16384u + (unsigned)(d0 * 512 + (tid & 31) * 16);
        tsrc0 = tb + (unsigned)g0 * 1024u; tsrc1 = tb + (unsigned)g1 * 1024u;
      }
      const unsigned vw20z = G.vw20 - 16u * 1024u, vw21z = G.vw21 - 16u * 1024u, ldswz = uni(G.ldsw - 2048u);
#define W2B(t, u) do { if (w != 0) w2_issue1(G, t, u); } while (0)
#define W2Z(t, u) do { if (w == 1) w2_dma<t, u>(G.sw2, vw20z, vw21z, ldswz, G.fq0b, G.fq1b); } while (0)
      bar_vm<9>();
      STAMP(4);
      const unsigned wts = w_tail_src(mW, lane);
      if (m1 == 0) {
        const unsigned ws2 = w_src(128 * w + 1024, lane), wt2 = w_tail_src(128 * w + 1024, lane);
        LD16Z(xqr, ws2, swr, ""); LD16Z(xqi, ws2, swi, ""); LD2Z(xtr, wt2, swr, ""); LD2Z(xti, wt2, swi, "");
      }
      STEP8(0, LD16Z(wqr, wsrc, swr, ""), LD16Z(wqi, wsrc, swi, ""), LD2Z(wtr, wts, swr, ""), LD2Z(wti, wts, swi, ""), g1_dma(4, 0), g1_dma(4, 1), g1_dma(4, 2), g1_dma(4, 3))
      bar_vm<13>();
      STEP8(1, NOP, NOP, NOP, NOP, g1_dma(5, 0), g1_dma(5, 1), g1_dma(5, 2), g1_dma(5, 3))
      bar_vm<13>();
      GATE_STORE();
      STEP8(2, NOP, NOP, NOP, NOP, g1_dma(6, 0), g1_dma(6, 1), g1_dma(6, 2), g1_dma(6, 3))
      bar_vm<8>();
      LAS char* wscr0 = L + 128 * 1024 + w * 2048;
      const bool special = m1 == 0;
      const int m2nd = 128 * w + 1024;
      if (special) { asm volatile("" : "+v"(xqr), "+v"(xqi), "+v"(xtr), "+v"(xti)); w_put(wscr0, xqr, xqi, lane); }
      STEP8(3, NOP, NOP, (dma16<128u * 1024u + 16384u, 0>(stab, (unsigned)lane * 16u, g1l, g1l)), (dma16<128u * 1024u + 17408u, 1024>(stab, (unsigned)lane * 16u, g1l, g1l)),
            g1_dma(7, 0), g1_dma(7, 1), g1_dma(7, 2), g1_dma(7, 3))
      bar_vm<10>();
      STAMP(12);
      WRawL raw2; const int off2 = w_off(m2nd, lane);
      if (special) { w_get(raw2, wscr0, off2, lane, 0); w_get(raw2, wscr0, off2, lane, 1); w_get(raw2, wscr0, off2, lane, 2); w_get(raw2, wscr0, off2, lane, 3); }
      const int woff = w_off(mW, lane);
      asm volatile("" : "+v"(wqr), "+v"(wqi), "+v"(wtr), "+v"(wti) :: "memory");
      w_put(wscr0, wqr, wqi, lane);
      STEP8(4, W2Z(0, 0), W2Z(0, 1), W2Z(1, 0), W2Z(1, 1), W2B(0, 0), W2B(0, 1), W2B(1, 0), W2B(1, 1))
      bar_w<6, 14, 10>(w);
      WRawL wraw;
      w_get(wraw, wscr0, woff, lane, 0); w_get(wraw, wscr0, woff, lane, 1); w_get(wraw, wscr0, woff, lane, 2); w_get(wraw, wscr0, woff, lane, 3);
      if (special) Wd = w_unpack(raw2, w_tail(xtr, xti), off2, m2nd, lane);
      STEP8(5, W2Z(2, 0), W2Z(2, 1), W2Z(3, 0), W2Z(3, 1), W2B(2, 0), W2B(2, 1), W2B(3, 0), W2B(3, 1))
      bar_w<0, 16, 8>(w);
      Wc = w_unpack(wraw, w_tail(wtr, wti), woff, mW, lane);
      if (special) packed_coef(Wc, Wd, w);
      STEP8(6, if (w == 0) LD4Z(fla, flaoff, sws, "sc1"), W2Z(4, 0), W2Z(4, 1), W2Z(5, 0), W2B(4, 0), W2B(4, 1), W2B(5, 0), W2B(5, 1))
      W2Z(5, 1);
      if (w == 0) {
        asm volatile("s_waitcnt vmcnt(0)" : "+v"(fla) :: "memory");
        const unsigned ok = __all(fla == tag) ? 1u : 0u;
        if (lane == 0) *(LAS unsigned*)CTL = ok;
      }
      BAR_LGKM();
      t_ok = __builtin_amdgcn_readfirstlane(*(volatile LAS unsigned*)CTL);
      t_first = t_ok;
    }
    if constexpr (ISA) {
      STEP8(7, NOP, NOP, NOP, NOP, w2_issue1(G, 6, 0), w2_issue1(G, 6, 1), w2_issue1(G, 7, 0), w2_issue1(G, 7, 1))
    } else {
      LAS char* TB = L + 128 * 1024 + 16384 + lane * 16;
#define TBR(f) (tb[f] = lds_ld16(TB + (f) * 1024), tb[(f) + 1] = lds_ld16(TB + ((f) + 1) * 1024))
      const unsigned vw20z = G.vw20 - 16u * 1024u, vw21z = G.vw21 - 16u * 1024u, ldswz = uni(G.ldsw - 2048u);
      STEP8(7, { if (t_ok) LD16Z(tq0, tsrc0, sws, "sc1"); TBR(6); }, { if (t_ok) LD16Z(tq1, tsrc1, sws, "sc1"); TBR(0); },
            { if (!t_ok && w == 0) LD4Z(fla, flaoff, sws, "sc1"); TBR(10); }, { TBR(2); W2Z(6, 0); W2Z(6, 1); }, { TBR(12); W2B(6, 0); }, { TBR(14); W2B(6, 1); },
            { TBR(4); W2B(7, 0); W2Z(7, 0); }, { TBR(8); W2B(7, 1); W2Z(7, 1); })
      if (!t_ok) {
        if (w == 0) {
          asm volatile("s_waitcnt vmcnt(0)" : "+v"(fla) :: "memory");
          const unsigned ok = __all(fla == tag) ? 1u : 0u;
          if (lane == 0) *(LAS unsigned*)CTL = ok;
        }
        BAR_LGKM();
        t_ok = __builtin_amdgcn_readfirstlane(*(volatile LAS unsigned*)CTL);
        if (t_ok) { LD16Z(tq0, tsrc0, sws, "sc1"); LD16Z(tq1, tsrc1, sws, "sc1"); }
      }
    }
  }
  asm volatile("" :: "v"(touch));
  STAMP(5);
  FT(18);
  if (active) {
#pragma unroll
    for (int rt = 0; rt < 2; ++rt)
#pragma unroll
      for (int u = 0; u < 2; ++u) {
        v4u o;
        o.x = silu2(acc1[rt][8 * u + 0], acc1[rt][8 * u + 1]);
        o.y = silu2(acc1[rt][8 * u + 2], acc1[rt][8 * u + 3]);
        o.z = silu2(acc1[rt][8 * u + 4], acc1[rt][8 * u + 5]);
        o.w = silu2(acc1[rt][8 * u + 6], acc1[rt][8 * u + 7]);
        const int jb32 = 4 * rb + 2 * rh1 + rt, fs = (f0 >> 4) + 2 * ft + u;
        st16_sc1(sws, WS_H + (unsigned)(((i1 * 8 + jb32) * 112 + fs) * 1024 + lane * 16), o);
      }
  }
  SCHED();
  STAMP(6);
  FT(20);
  const unsigned flh_own = WS_FLH + (unsigned)((i1 * 2 + rb) * 16 + fc) * FS;
  if constexpr (ISA) {
    bar_vm<0>();
    if (tid == 0) flag_store(p.ws, flh_own, tag);
  } else {
    LAS char* I0 = L + 128 * 1024;
    LAS char* I1 = L + 128 * 1024 + 16384;
    const int g = lane >> 4, l15 = lane & 15;
    const bool special = (m1 == 0);
    bool published = false;
    if (t_ok) {
      if (!t_first || w == 0) wait_vm<4>(); else if (w == 1) wait_vm<12>(); else wait_vm<8>();
      asm volatile("s_barrier" ::: "memory");
    } else {
      bar_vm<0>();
      if (tid == 0) flag_store(p.ws, flh_own, tag);
      published = true;
      if (w == 0) poll_words(sws, WS_FLA + (unsigned)(m1 >> 5) * 32u * FS, 32, tag, lane);
      asm volatile("s_barrier" ::: "memory");
      LD16Z(tq0, tsrc0, sws, "sc1"); LD16Z(tq1, tsrc1, sws, "sc1");
      wait_vm<0>();
    }
    asm volatile("" : "+v"(tq0), "+v"(tq1) :: "memory");
    {
      const int kap = tid >> 5, ch = tid & 31;
      *(LAS v4u*)(I0 + kap * 512 + ((ch ^ ((kap & 3) << 2)) << 4)) = tq0;
      *(LAS v4u*)(I0 + (kap + 16) * 512 + ((ch ^ ((kap & 3) << 2)) << 4)) = tq1;
    }
    BAR_LGKM();
    FT(21);
    LAS char* ta = I0 + (8 * hh + q4) * 512 + (((4 * w + 2 * blk + (p4 >> 1)) ^ (q4 << 2)) << 4) + 8 * (p4 & 1);
    const int n = lane & 31;
    LAS char* so = I1 + n * 512 + 8 * hh;
    {
      f32x16 acc = {};
      acc = mfma32(tr_read2(ta, ta + 2048), tb[6], acc);
      acc = mfma32(tr_read2(ta + 8192, ta + 10240), tb[7], acc);
#pragma unroll
      for (int Gq = 0; Gq < 4; ++Gq) {
        v2u o; o.x = pk2(acc[4 * Gq], acc[4 * Gq + 1]); o.y = pk2(acc[4 * Gq + 2], acc[4 * Gq + 3]);
        *(LAS v2u*)(so + (((4 * w + Gq) ^ (n & 15)) << 4)) = o;
      }
    }
    BAR_LGKM();
    FT(22);
    const f32x4 z4 = {0.f, 0.f, 0.f, 0.f};
    const v4u F16fr = tb[0], F16fi = tb[1];
    v4u F16ir, F16ii;
    F16ir.x = F16fr.x; F16ir.y = F16fr.y; F16ir.z = F16fr.z ^ 0x80008000u; F16ir.w = F16fr.w ^ 0x80008000u;
    F16ii.x = F16fi.x ^ 0x80008000u; F16ii.y = F16fi.y ^ 0x80008000u; F16ii.z = F16fi.z; F16ii.w = F16fi.w;
    f32x4 zr[2], zi[2];
#pragma unroll
    for (int pp = 0; pp < 2; ++pp) {
      const int n0 = 4 * w + 2 * pp, n1 = n0 + 1;
      const int ch = 2 * (4 * g + q4) + (p4 >> 1);
      const v4u a1 = tr_read2(I1 + n0 * 512 + ((ch ^ (n0 & 15)) << 4) + 8 * (p4 & 1), I1 + n1 * 512 + ((ch ^ (n1 & 15)) << 4) + 8 * (p4 & 1));
      const f32x4 dr = mfma16(a1, pp ? tb[2] : F16fr, z4), di = mfma16(a1, pp ? tb[3] : F16fi, z4);
      const v4u b3 = cmul_pack<false>(dr, di, asf4(tb[10 + 2 * pp]), asf4(tb[11 + 2 * pp]));
      zr[pp] = mfma16(F16fr, b3, z4); zi[pp] = mfma16(F16fi, b3, z4);
    }
    v4u a4[2];
    if (!special) {
      a4[0] = cmul_pack<false>(zr[0], zi[0], Wc.r0, Wc.i0);
      a4[1] = cmul_pack<false>(zr[1], zi[1], Wc.r1, Wc.i1);
    } else {
      BAR_LGKM();
      LAS char* Z = I0;
#pragma unroll
      for (int pp = 0; pp < 2; ++pp)
#pragma unroll
        for (int r = 0; r < 4; ++r) { f32x2 v = {zr[pp][r], zi[pp][r]}; *(LAS f32x2*)(Z + ((w * 2 + pp) * 256 + l15 + 16 * (4 * g + r)) * 8) = v; }
      BAR_LGKM();
#pragma unroll
      for (int pp = 0; pp < 2; ++pp) {
        f32x4 cr, ci;
#pragma unroll
        for (int r = 0; r < 4; ++r) {
          const int K = l15 + 16 * (4 * g + r), Kn = pp ? 255 - K : (256 - K) & 255;
          const f32x2 y = *(LAS f32x2*)(Z + ((((8 - w) & 7) * 2 + pp) * 256 + Kn) * 8);
          cr[r] = y[0]; ci[r] = -y[1];
        }
        const f32x4 ar = pp ? Wc.r1 : Wc.r0, ai = pp ? Wc.i1 : Wc.i0, br = pp ? Wd.r1 : Wd.r0, bi = pp ? Wd.i1 : Wd.i0;
        const f32x4 yr = zr[pp] * ar - zi[pp] * ai + cr * br - ci * bi, yi = zr[pp] * ai + zi[pp] * ar + cr * bi + ci * br;
        a4[pp].x = pk2(yr[0], yr[1]); a4[pp].y = pk2(yr[2], yr[3]); a4[pp].z = pk2(yi[0], yi[1]); a4[pp].w = pk2(yi[2], yi[3]);
      }
      BAR_LGKM();
    }
#pragma unroll
    for (int pp = 0; pp < 2; ++pp) {
      const f32x4 dr = mfma16(a4[pp], F16ir, z4), di = mfma16(a4[pp], F16ii, z4);
      const v4u a5 = cmul_pack<true>(dr, di, asf4(tb[pp ? 14 : 10]), asf4(tb[pp ? 15 : 11]));
      const f32x4 er = mfma16(a5, pp ? tb[4] : F16ir, z4), ei = mfma16(a5, pp ? tb[5] : F16ii, z4);
      const int n0 = 4 * w + 2 * pp, n1 = n0 + 1;
      const int ch = 2 * l15 + (g >> 1);
      v2u o; o.x = pk2(er[0], er[1]); o.y = pk2(er[2], er[3]);
      *(LAS v2u*)(I0 + n0 * 512 + ((ch ^ ((n0 & 3) << 2)) << 4) + 8 * (g & 1)) = o;
      o.x = pk2(ei[0], ei[1]); o.y = pk2(ei[2], ei[3]);
      *(LAS v2u*)(I0 + n1 * 512 + ((ch ^ ((n1 & 3) << 2)) << 4) + 8 * (g & 1)) = o;
    }
    bar_vm<0>();
    if (!published && tid == 0) flag_store(p.ws, flh_own, tag);
    FT(23);
    m5a = tb[8]; m5b = tb[9];
  }
  return tag;
}

DI void make_geo(Geo& G, const Params& p, int tz) {
  G.tid = tz; G.lane = G.tid & 63; G.w = __builtin_amdgcn_readfirstlane(G.tid >> 6);
  G.b = blockIdx.x;
  const int Lx = G.b >> 3, x8 = G.b & 7;
  {
    const int a = x8 & 3, h = x8 >> 2, rbi = Lx >> 3, c = Lx & 7;
    G.i = 2 * a + h; G.i1 = 2 * a + (rbi >> 1); G.rb = rbi & 1;
    G.fc = c < 4 ? 4 * h + c : 4 + 4 * h + c;
    G.at = rbi * 4 + c; G.m1 = ((rbi * 4 + c - 4) << 3) | x8;
  }
  G.hh = G.lane >> 5; G.blk = (G.lane >> 4) & 1; G.q4 = (G.lane & 15) >> 2; G.p4 = G.lane & 3;
  G.sx = make_srd(p.x, NS * ND * 2); G.sws = make_srd(p.ws, 16u << 20); G.sw2 = make_srd(p.w2, NF * ND * 2);
  G.jb = Lx >> 3; G.dt = Lx & 7; G.rh = G.w & 1; G.q = G.w >> 1;
  G.j = 64 * G.jb + 32 * G.rh + (G.lane & 31);
  const int rho = 16 * G.rh + (G.lane >> 3), chs = (G.lane & 7) ^ (4 * ((G.lane >> 4) & 1));
  G.vw20 = (unsigned)(rho * 1024 + G.dt * 128 + chs * 16); G.vw21 = G.vw20 + 8 * 1024;
  G.ldsw = uni((unsigned)(G.q * 4096 + G.rh * 2048));
  G.fq0b = uni((unsigned)(768 + 256 * G.q) * 1024u); G.fq1b = uni((unsigned)(192 * G.q) * 1024u);
}

__global__ __launch_bounds__(512, 2) void gm_kernel(Params p) {
  extern __shared__ __attribute__((aligned(16))) char smem[];
  LAS char* L = (LAS char*)smem;
  long long pad_t = 0;
  STAMP(0);
  FT(0);
  Geo G;
  make_geo(G, p, threadIdx.x);
  const bool isA = G.fc < 8;
  v4u gf[4], m5a, m5b;
  unsigned tag;
  if (isA) tag = phase1<true>(pad_t, p, L, G, m5a, m5b); else tag = phase1<false>(pad_t, p, L, G, m5a, m5b);
  { int tz = threadIdx.x; asm volatile("" : "+v"(tz)); make_geo(G, p, tz); }
  const int tid = G.tid, lane = G.lane, w = G.w, b = G.b, i = G.i, jb = G.jb, dt = G.dt, rh = G.rh, q = G.q, j = G.j;
  const int hh = G.hh, blk = G.blk, q4 = G.q4, p4 = G.p4;
  const v4u sws = G.sws;
  const unsigned ldsw = G.ldsw;
  const int m1 = G.m1;
  STAMP(7);
  FT(25);
  if (!isA) {
    LAS char* I0 = L + 128 * 1024;
    LAS char* I1 = L + 128 * 1024 + 16384;
    LAS char* ta = I0 + (8 * hh + q4) * 512 + (((4 * w + 2 * blk + (p4 >> 1)) ^ (q4 << 2)) << 4) + 8 * (p4 & 1);
    const int n = lane & 31;
    LAS char* so = I1 + n * 512 + 8 * hh;
    f32x16 acc = {};
    acc = mfma32(tr_read2(ta, ta + 2048), m5a, acc);
    acc = mfma32(tr_read2(ta + 8192, ta + 10240), m5b, acc);
#pragma unroll
    for (int Gq = 0; Gq < 4; ++Gq) {
      v2u o; o.x = pk2(acc[4 * Gq], acc[4 * Gq + 1]); o.y = pk2(acc[4 * Gq + 2], acc[4 * Gq + 3]);
      *(LAS v2u*)(so + (((4 * w + Gq) ^ (n & 15)) << 4)) = o;
    }
    BAR_LGKM();
    const bool special = (m1 == 0);
    auto u_store = [&](int t2) {
      const int nn = t2 >> 5, ch = t2 & 31, ii = (nn >> 1) & 7, d0 = nn & 1;
      const int g0 = special ? 2 * ii : ii, g1 = special ? 2 * ii + 1 : 8 + ii;
      const v4u v0 = lds_ld16(I1 + nn * 512 + ((ch ^ (nn & 15)) << 4));
      const v4u v1 = lds_ld16(I1 + (nn + 16) * 512 + ((ch ^ (nn & 15)) << 4));
      const unsigned ub = WS_U + (unsigned)m1 * 16384u + (unsigned)(d0 * 512 + ch * 16);
      st16_sc1(sws, ub + (unsigned)g0 * 1024u, v0);
      st16_sc1(sws, ub + (unsigned)g1 * 1024u, v1);
    };
    if (w != 0) u_store(tid);
    if (w == 1) u_store(tid - 64);
    SCHED();
  }
  const int sz = (q4 >> 1) & 1;
  LAS char* ra0 = L + q * 4096 + (4 * hh + q4) * 128 + (((0 ^ (4 * sz)) + 2 * blk + (p4 >> 1)) << 4) + 8 * (p4 & 1);
  LAS char* ra1 = L + q * 4096 + (4 * hh + q4) * 128 + (((4 ^ (4 * sz)) + 2 * blk + (p4 >> 1)) << 4) + 8 * (p4 & 1);
  v4u wp[4][4];
  BAR_LGKM();
#pragma unroll
  for (int t = 0; t < 4; ++t) {
    LAS char* s0 = ra0 + t * 16384; LAS char* s1 = ra1 + t * 16384;
    wp[t][0] = tr_read2(s0, s0 + 1024); wp[t][1] = tr_read2(s1, s1 + 1024);
    wp[t][2] = tr_read2(s0 + 2048, s0 + 3072); wp[t][3] = tr_read2(s1 + 2048, s1 + 3072);
  }
#define W2_8_11() do { w2_issue1(G, 8, 0); w2_issue1(G, 8, 1); w2_issue1(G, 9, 0); w2_issue1(G, 9, 1); w2_issue1(G, 10, 0); w2_issue1(G, 10, 1); w2_issue1(G, 11, 0); w2_issue1(G, 11, 1); } while (0)
  if (isA) { BAR_LGKM(); W2_8_11(); }
  const unsigned flh = WS_FLH + (unsigned)((i * 2 + (jb >> 1)) * 16) * FS;
  if (w == 0) poll_words(sws, flh, 16, tag, lane);
  BAR_LGKM();
  STAMP(8);
  FT(26);
  const unsigned vh = (unsigned)lane * 16u;
  const unsigned hbase = WS_H + (unsigned)((i * 8 + 2 * jb + rh) * 112) * 1024u;
  const unsigned hb0 = uni(hbase + (G.fq0b >> 4)), hb1 = uni(hbase + (G.fq1b >> 4));
  v2u gg;
  v4u hf[14][2];
#define H_ISSUE(t) do { LD16I(hf[t][0], vh, sws, ((t) < 8 ? hb0 : hb1), (2 * ((t) & 7)) * 1024, "sc1"); LD16I(hf[t][1], vh, sws, ((t) < 8 ? hb0 : hb1), (2 * ((t) & 7) + 1) * 1024, "sc1"); } while (0)
  H_ISSUE(0); H_ISSUE(1);
  asm volatile("s_barrier" ::: "memory");
  H_ISSUE(2); H_ISSUE(3); H_ISSUE(4); H_ISSUE(5); H_ISSUE(6);
  LD8Z(gg, WS_GATE + (unsigned)(i * 256 + j) * 8u, sws, "sc1");
  SCHED();
  f32x16 accb[2] = {}, acca[2] = {};
  v4u wf[2][4];
  unsigned fl0, fl1;
  v4u ub0, ub1, ub2, ub3;
  const unsigned vu = WS_U + (unsigned)(((64 * q + 16 * rh + (lane >> 3)) * 8 + i) * 1024 + dt * 128 + ((lane & 7) ^ (4 * ((lane >> 4) & 1))) * 16);
#define G2_BAR(n, t) asm volatile("s_waitcnt vmcnt(" STR(n) ") lgkmcnt(0)\n\ts_barrier" : "+v"(hf[t][0]), "+v"(hf[t][1]) :: "memory")
#define G2_CORE(t, c, n, RD, acc, b0, b1, X0, X1, X2)                                             \
  {                                                                                                 \
    FT(30 + (t));                                                                                   \
    LAS char* s0 = ra0 + (((t) + 1) & 7) * 16384; LAS char* s1 = ra1 + (((t) + 1) & 7) * 16384;    \
    asm volatile("" : "+v"(c[0]), "+v"(c[1]), "+v"(c[2]), "+v"(c[3]));                             \
    asm volatile("" :: "v"(acc[0]), "v"(acc[1]));                                                   \
    SCHED();                                                                                        \
    acc[0] = mfma32(c[0], b0, acc[0]); if (RD) n[0] = tr_read2(s0, s0 + 1024); SCHED();            \
    acc[1] = mfma32(c[1], b0, acc[1]); if (RD) n[1] = tr_read2(s1, s1 + 1024); X0; SCHED();        \
    acc[0] = mfma32(c[2], b1, acc[0]); if (RD) n[2] = tr_read2(s0 + 2048, s0 + 3072); X1; SCHED(); \
    acc[1] = mfma32(c[3], b1, acc[1]); if (RD) n[3] = tr_read2(s1 + 2048, s1 + 3072); X2; SCHED(); \
  }
#define G2_PRE(t, X2) G2_CORE(t, wp[t], wf[0], (t) == 3, accb, hf[t][0], hf[t][1], if (!isA) w2_issue1(G, (t) + 8, 0), if (!isA) w2_issue1(G, (t) + 8, 1), X2)
#define G2_STEP(t, X0, X1, X2) G2_CORE(t, wf[(t) & 1], wf[((t) + 1) & 1], true, accb, hf[t][0], hf[t][1], X0, X1, X2)
  G2_BAR(13, 0);
  if (!isA && tid == 0) flag_store(p.ws, WS_FLB + (unsigned)m1 * FS, tag);
  STAMP(14);
  G2_PRE(0, H_ISSUE(7))
  G2_BAR(13, 1); G2_PRE(1, H_ISSUE(8))
  G2_BAR(13, 2); G2_PRE(2, H_ISSUE(9))
  G2_BAR(13, 3); G2_PRE(3, H_ISSUE(10))
  G2_BAR(13, 4); G2_STEP(4, w2_issue1(G, 12, 0), w2_issue1(G, 12, 1), H_ISSUE(11))
  G2_BAR(15, 5); G2_STEP(5, w2_issue1(G, 13, 0), w2_issue1(G, 13, 1), H_ISSUE(12))
  if (w == 0) { LD4Z(fl0, WS_FLB + (unsigned)lane * FS, sws, "sc1"); LD4Z(fl1, WS_FLB + (64u + (unsigned)lane) * FS, sws, "sc1"); }
  G2_BAR(17, 6); G2_STEP(6, NOP, NOP, H_ISSUE(13))
  STAMP(9);
  G2_BAR(16, 7); G2_STEP(7, NOP, NOP, NOP)
  LAS unsigned* verdict = (LAS unsigned*)(L + 128 * 1024);
  if (w == 0) {
    asm volatile("s_waitcnt vmcnt(2)" : "+v"(fl0), "+v"(fl1) :: "memory");
    const unsigned ok = __all(fl0 == tag && fl1 == tag) ? 1u : 0u;
    if (lane == 0) *verdict = ok;
  }
  G2_BAR(14, 8);
  const int u_early = __builtin_amdgcn_readfirstlane(*(volatile LAS unsigned*)verdict);
  STAMP(15);
#define U_ISSUE() do { LD16Z(ub0, vu, sws, "sc1"); LD16Z(ub1, vu + 8u * 8192u, sws, "sc1"); LD16Z(ub2, vu + 32u * 8192u, sws, "sc1"); LD16Z(ub3, vu + 40u * 8192u, sws, "sc1"); } while (0)
  if (u_early) U_ISSUE();
  GRot grot;
  G2_STEP(8, gen_g_init(G, grot), NOP, gen_g_frag<0>(G, grot, gf[0]))
  unsigned e0, e1, e2, e3;
  if (b == 0 && w == 1) {
    LD4Z(e0, WS_FLH + (unsigned)lane * FS, sws, "sc1"); LD4Z(e1, WS_FLH + (64u + (unsigned)lane) * FS, sws, "sc1");
    LD4Z(e2, WS_FLH + (128u + (unsigned)lane) * FS, sws, "sc1"); LD4Z(e3, WS_FLH + (192u + (unsigned)lane) * FS, sws, "sc1");
  }
  G2_BAR(12, 9); G2_STEP(9, NOP, NOP, gen_g_frag<1>(G, grot, gf[1]))
  G2_BAR(10, 10); G2_STEP(10, NOP, NOP, gen_g_frag<2>(G, grot, gf[2]))
  G2_BAR(6, 11); G2_STEP(11, NOP, NOP, gen_g_frag<3>(G, grot, gf[3]))
  G2_BAR(2, 12);
  G2_STEP(12, NOP, NOP, NOP)
#define U_PUT() do {                                                                                                        \
    asm volatile("s_waitcnt vmcnt(0)" : "+v"(ub0), "+v"(ub1), "+v"(ub2), "+v"(ub3) :: "memory");                             \
    *(LAS v4u*)(L + 6 * 16384 + ldsw + lane * 16) = ub0; *(LAS v4u*)(L + 6 * 16384 + ldsw + 1024 + lane * 16) = ub1;         \
    *(LAS v4u*)(L + 7 * 16384 + ldsw + lane * 16) = ub2; *(LAS v4u*)(L + 7 * 16384 + ldsw + 1024 + lane * 16) = ub3; } while (0)
  if (u_early) U_PUT();
  G2_BAR(0, 13);
  asm volatile("" : "+v"(gg));
  if (b == 0 && w == 1) {
    asm volatile("" : "+v"(e0), "+v"(e1), "+v"(e2), "+v"(e3));
    if (!__all(e0 == tag && e1 == tag && e2 == tag && e3 == tag)) {
#pragma unroll 1
      for (int u = 0; u < 4; ++u) poll_words(sws, WS_FLH + 64u * FS * u, 64, tag, lane);
    }
    if (lane == 0) flag_store(p.ws, WS_EPOCH, tag);
  }
  G2_CORE(13, wf[1], wf[0], u_early, accb, hf[13][0], hf[13][1], NOP, NOP, NOP)
  if (!u_early) {
    if (w == 0) { poll_words(sws, WS_FLB, 64, tag, lane); poll_words(sws, WS_FLB + 64u * FS, 64, tag, lane); }
    BAR_LGKM();
    U_ISSUE();
    U_PUT();
    BAR_LGKM();
    LAS char* s0 = ra0 + 6 * 16384; LAS char* s1 = ra1 + 6 * 16384;
    wf[0][0] = tr_read2(s0, s0 + 1024); wf[0][1] = tr_read2(s1, s1 + 1024); wf[0][2] = tr_read2(s0 + 2048, s0 + 3072); wf[0][3] = tr_read2(s1 + 2048, s1 + 3072);
  }
  STAMP(10);
  BAR_LGKM(); G2_CORE(14, wf[0], wf[1], true, acca, gf[0], gf[1], NOP, NOP, NOP)
  BAR_LGKM(); G2_CORE(15, wf[1], wf[0], false, acca, gf[2], gf[3], NOP, NOP, NOP)
  FT(50);
  {
    const float og = asf(gg.y), gs = asf(gg.x) * (1.f / 524288.f);
    const int row = 32 * rh + (lane & 31);
#pragma unroll
    for (int dh = 0; dh < 2; ++dh)
#pragma unroll
      for (int G = 0; G < 4; ++G) {
        f32x4 v;
#pragma unroll
        for (int e = 0; e < 4; ++e) v[e] = __builtin_fmaf(og, accb[dh][4 * G + e], gs * acca[dh][4 * G + e]);
        *(LAS f32x4*)(L + q * 16384 + row * 256 + (((8 * dh + 2 * G + hh) ^ (row & 7)) << 4)) = v;
      }
  }
  BAR_LGKM();
  FT(51);
  {
    const int row = tid >> 3, c8 = tid & 7;
    f32x4 s0 = {0.f, 0.f, 0.f, 0.f}, s1 = s0;
#pragma unroll
    for (int qq = 0; qq < 4; ++qq) {
      s0 += *(LAS f32x4*)(L + qq * 16384 + row * 256 + (((2 * c8) ^ (row & 7)) << 4));
      s1 += *(LAS f32x4*)(L + qq * 16384 + row * 256 + (((2 * c8 + 1) ^ (row & 7)) << 4));
    }
    v4u o; o.x = pk2(s0[0], s0[1]); o.y = pk2(s0[2], s0[3]); o.z = pk2(s1[0], s1[1]); o.w = pk2(s1[2], s1[3]);
    *(GAS v4u*)((GAS char*)p.out + (size_t)(i + 8 * (64 * jb + row)) * 1024 + dt * 128 + c8 * 16) = o;
  }
  STAMP(11);
  while (__builtin_amdgcn_s_memrealtime() - pad_t < 3000) {}
}

}

extern "C" void kernel_launch(void* const* d_in, const int* in_sizes, int n_in, void* d_out, int out_size, void* d_ws, size_t ws_size,
                              hipStream_t stream) {
  Params p;
  p.x = d_in[0]; p.wr = d_in[1]; p.wi = d_in[2]; p.w1 = d_in[4]; p.w2 = d_in[5]; p.gw = d_in[6];
  p.out = d_out; p.ws = d_ws;
  (void)hipFuncSetAttribute((const void*)gm_kernel, hipFuncAttributeMaxDynamicSharedMemorySize, LDS_BYTES);
  gm_kernel<<<256, 512, LDS_BYTES, stream>>>(p);
}
```

```cpp
#include <hip/hip_runtime.h>
#include <hip/hip_bf16.h>
#include <stdint.h>

namespace {

typedef unsigned v4u __attribute__((ext_vector_type(4)));
typedef unsigned v2u __attribute__((ext_vector_type(2)));
typedef float f32x16 __attribute__((ext_vector_type(16)));
typedef float f32x4 __attribute__((ext_vector_type(4)));
typedef float f32x2 __attribute__((ext_vector_type(2)));
typedef __bf16 bf16x8 __attribute__((ext_vector_type(8)));
typedef __bf16 bf16x2 __attribute__((ext_vector_type(2)));
typedef short s16x4 __attribute__((ext_vector_type(4)));
#define LAS __attribute__((address_space(3)))
#define GAS __attribute__((address_space(1)))
#define DI __device__ __forceinline__

constexpr int NS = 2048, ND = 512, NF = 1792;
constexpr unsigned WS_EPOCH = 0;
constexpr unsigned FS = 64;
constexpr unsigned WS_FLA = 4096;
constexpr unsigned WS_FLH = 16384;
constexpr unsigned WS_FLB = 32768;
constexpr unsigned WS_GATE = 65536;
constexpr unsigned WS_T = 1u << 20;
constexpr unsigned WS_U = 3u << 20;
constexpr unsigned WS_H = 5u << 20;
constexpr unsigned H_BYTES = 8u * 8u * 112u * 1024u;
constexpr int LDS_BYTES = 160 * 1024;
constexpr unsigned kSpinCap = 1u << 20;

struct Params {
  const void* x; const void* wr; const void* wi; const void* w1; const void* w2; const void* gw;
  void* out; void* ws;
};

constexpr double kPi = 3.14159265358979323846;
constexpr double ccos(double x) { double t = 1, s = 1; for (int n = 1; n < 16; ++n) { t *= -x * x / ((2 * n - 1) * (2 * n)); s += t; } return s; }
constexpr double cosn(int e, int n) { e = ((e % n) + n) % n; double x = 2 * kPi * e / n; if (x > kPi) x -= 2 * kPi; return ccos(x); }
constexpr double wre(int e, int n) { return cosn(e, n); }
constexpr double wim(int e, int n) { return -cosn(4 * e - n, 4 * n); }
constexpr unsigned bfb(double v) { float f = (float)v; unsigned u = __builtin_bit_cast(unsigned, f); u += 0x7fffu + ((u >> 16) & 1u); return u >> 16; }
constexpr unsigned bf2(double a, double b) { return bfb(a) | (bfb(b) << 16); }
constexpr unsigned fbits(double v) { return __builtin_bit_cast(unsigned, (float)v); }
struct Tab { unsigned v[16][64][4]; };
constexpr Tab make_tab() {
  Tab t{};
  for (int l = 0; l < 64; ++l) {
    const int g = l >> 4, l15 = l & 15, hh = l >> 5, n = l & 31;
    for (int mtx = 0; mtx < 3; ++mtx) {
      double r[4] = {}, im[4] = {};
      for (int q = 0; q < 4; ++q) {
        const int a = 4 * g + q;
        if (mtx == 0) { r[q] = wre(l15 * a, 16); im[q] = wim(l15 * a, 16); }
        else if (mtx == 1) { r[q] = wre(a * (2 * l15 + 1), 32); im[q] = wim(a * (2 * l15 + 1), 32); }
        else { r[q] = wre(l15 * (2 * a + 1), 32); im[q] = -wim(l15 * (2 * a + 1), 32); }
      }
      t.v[2 * mtx][l][0] = bf2(r[0], r[1]); t.v[2 * mtx][l][1] = bf2(r[2], r[3]); t.v[2 * mtx][l][2] = bf2(-im[0], -im[1]); t.v[2 * mtx][l][3] = bf2(-im[2], -im[3]);
      t.v[2 * mtx + 1][l][0] = bf2(im[0], im[1]); t.v[2 * mtx + 1][l][1] = bf2(im[2], im[3]); t.v[2 * mtx + 1][l][2] = bf2(r[0], r[1]); t.v[2 * mtx + 1][l][3] = bf2(r[2], r[3]);
    }
    {
      const int m2 = n >> 2, pp = (n >> 1) & 1, cp = n & 1;
      double v0[8] = {}, v1[8] = {};
      for (int jj = 0; jj < 8; ++jj) {
        const int ii = (8 * hh + jj) >> 1, d0 = jj & 1;
        const double sg = (d0 & pp) ? -1.0 : 1.0, orr = sg * wre(ii * m2, 8), oi = sg * wim(ii * m2, 8);
        v0[jj] = cp ? oi : orr; v1[jj] = cp ? orr : -oi;
      }
      for (int q = 0; q < 4; ++q) { t.v[6][l][q] = bf2(v0[2 * q], v0[2 * q + 1]); t.v[7][l][q] = bf2(v1[2 * q], v1[2 * q + 1]); }
    }
    {
      const int cq = n >> 4, ii = (n >> 1) & 7, d0 = n & 1;
      for (int s = 0; s < 2; ++s) {
        double v[8] = {};
        for (int jj = 0; jj < 8; ++jj) {
          const int m2k = 4 * s + 2 * hh + (jj >> 2), pk = (jj >> 1) & 1, ck = jj & 1;
          const double sg = (d0 & pk) ? -1.0 : 1.0, orr = sg * wre(ii * m2k, 8), oi = -sg * wim(ii * m2k, 8);
          v[jj] = cq ? (ck ? orr : oi) : (ck ? -oi : orr);
        }
        for (int q = 0; q < 4; ++q) t.v[8 + s][l][q] = bf2(v[2 * q], v[2 * q + 1]);
      }
    }
    for (int q = 0; q < 4; ++q) {
      const int a = 4 * g + q;
      t.v[10][l][q] = fbits(wre(a * (2 * l15), 512));      t.v[11][l][q] = fbits(wim(a * (2 * l15), 512));
      t.v[12][l][q] = fbits(wre(a * (2 * l15 + 1), 512));  t.v[13][l][q] = fbits(wim(a * (2 * l15 + 1), 512));
      t.v[14][l][q] = fbits(wre(l15 * (2 * a + 1), 512));  t.v[15][l][q] = fbits(wim(l15 * (2 * a + 1), 512));
    }
  }
  return t;
}
__device__ const Tab kTab = make_tab();

constexpr unsigned STAMP_OFF = 512 * 1024;
#define STAMP(k) do {} while (0)
#define FT(k) do {} while (0)

DI v4u make_srd(const void* p, unsigned bytes) {
  const unsigned long long a = (unsigned long long)p;
  v4u r; r.x = (unsigned)a; r.y = (unsigned)(a >> 32) & 0xffffu; r.z = bytes; r.w = 0x00020000u;
  return r;
}
template <unsigned LDSI, unsigned SOI> DI void dma16(v4u srd, unsigned voff, unsigned ldsb, unsigned sob) {
  unsigned tmp;
  asm volatile("s_add_u32 m0, %3, %5\n\ts_add_u32 %0, %4, %6\n\tbuffer_load_dwordx4 %1, %2, %0 offen lds"
               : "=&s"(tmp) : "v"(voff), "s"(srd), "s"(ldsb), "s"(sob), "i"(LDSI), "i"(SOI) : "memory", "scc");
}
template <unsigned LDSI, unsigned SOI> DI void dma16z(v4u srd, unsigned voff, unsigned ldsb) {
  unsigned tmp;
  asm volatile("s_add_u32 m0, %3, %4\n\ts_mov_b32 %0, %5\n\tbuffer_load_dwordx4 %1, %2, %0 offen lds"
               : "=&s"(tmp) : "v"(voff), "s"(srd), "s"(ldsb), "i"(LDSI), "i"(SOI) : "memory", "scc");
}
DI unsigned uni(unsigned v) { return (unsigned)__builtin_amdgcn_readfirstlane((int)v); }
#define LD16(dst, voffv, srd, soffv, flags) asm volatile("buffer_load_dwordx4 %0, %1, %2, %3 offen " flags : "=v"(dst) : "v"(voffv), "s"(srd), "s"(soffv) : "memory")
#define LD16I(dst, voffv, srd, sob, soi, flags) do { unsigned tmp_; asm volatile("s_add_u32 %1, %4, %5\n\tbuffer_load_dwordx4 %0, %2, %3, %1 offen " flags : "=v"(dst), "=&s"(tmp_) : "v"(voffv), "s"(srd), "s"(sob), "i"(soi) : "memory", "scc"); } while (0)
#define LD16Z(dst, voffv, srd, flags) asm volatile("buffer_load_dwordx4 %0, %1, %2, 0 offen " flags : "=v"(dst) : "v"(voffv), "s"(srd) : "memory")
#define LD4Z(dst, voffv, srd, flags) asm volatile("buffer_load_dword %0, %1, %2, 0 offen " flags : "=v"(dst) : "v"(voffv), "s"(srd) : "memory")
#define LD2Z(dst, voffv, srd, flags) asm volatile("buffer_load_ushort %0, %1, %2, 0 offen " flags : "=v"(dst) : "v"(voffv), "s"(srd) : "memory")
#define LD8Z(dst, voffv, srd, flags) asm volatile("buffer_load_dwordx2 %0, %1, %2, 0 offen " flags : "=v"(dst) : "v"(voffv), "s"(srd) : "memory")
#define LD8(dst, voffv, srd, soffv, flags) asm volatile("buffer_load_dwordx2 %0, %1, %2, %3 offen " flags : "=v"(dst) : "v"(voffv), "s"(srd), "s"(soffv) : "memory")
#define LD4(dst, voffv, srd, soffv, flags) asm volatile("buffer_load_dword %0, %1, %2, %3 offen " flags : "=v"(dst) : "v"(voffv), "s"(srd), "s"(soffv) : "memory")
#define LD2(dst, voffv, srd, soffv, flags) asm volatile("buffer_load_ushort %0, %1, %2, %3 offen " flags : "=v"(dst) : "v"(voffv), "s"(srd), "s"(soffv) : "memory")
#define XSTR(x) #x
#define STR(x) XSTR(x)
#define BAR_VM(n) asm volatile("s_waitcnt vmcnt(" STR(n) ") lgkmcnt(0)\n\ts_barrier" ::: "memory")
#define BAR_LGKM() asm volatile("s_waitcnt lgkmcnt(0)\n\ts_barrier" ::: "memory")
#define SCHED() __builtin_amdgcn_sched_barrier(0)
template <int N> DI void bar_vm() { asm volatile("s_waitcnt vmcnt(%0) lgkmcnt(0)\n\ts_barrier" :: "i"(N) : "memory"); }
template <int N> DI void wait_vm() { asm volatile("s_waitcnt vmcnt(%0)" :: "i"(N) : "memory"); }

DI unsigned pk2(float a, float b) { f32x2 p = {a, b}; return __builtin_bit_cast(unsigned, __builtin_convertvector(p, bf16x2)); }
DI float bflo(unsigned u) { return __builtin_bit_cast(float, u << 16); }
DI float bfhi(unsigned u) { return __builtin_bit_cast(float, u & 0xffff0000u); }
DI float asf(unsigned u) { return __builtin_bit_cast(float, u); }
DI f32x16 mfma32(v4u a, v4u b, f32x16 c) {
  return __builtin_amdgcn_mfma_f32_32x32x16_bf16(__builtin_bit_cast(bf16x8, a), __builtin_bit_cast(bf16x8, b), c, 0, 0, 0);
}
DI f32x4 mfma16(v4u a, v4u b, f32x4 c) {
  return __builtin_amdgcn_mfma_f32_16x16x32_bf16(__builtin_bit_cast(bf16x8, a), __builtin_bit_cast(bf16x8, b), c, 0, 0, 0);
}
DI v2u tr_read(LAS char* p) { return __builtin_bit_cast(v2u, __builtin_amdgcn_ds_read_tr16_b64_v4i16((LAS s16x4*)p)); }
DI v4u tr_read2(LAS char* p0, LAS char* p1) { v2u a = tr_read(p0), b = tr_read(p1); v4u r; r.x = a.x; r.y = a.y; r.z = b.x; r.w = b.y; return r; }
DI v4u lds_ld16(LAS char* p) { return *(LAS v4u*)p; }
DI float cosrev(float r) { return __builtin_amdgcn_cosf(r); }
DI float sinrev(float r) { return __builtin_amdgcn_sinf(r); }
DI __amdgpu_buffer_rsrc_t as_rsrc(v4u srd) { return __builtin_amdgcn_make_buffer_rsrc((void*)(((unsigned long long)srd.y << 32) | srd.x), 0, srd.z, 0x00020000); }
DI void st16_sc1(v4u srd, unsigned voff, v4u v) { __builtin_amdgcn_raw_buffer_store_b128(v, as_rsrc(srd), voff, 0, 16); }
DI void flag_store(void* ws, unsigned off, unsigned tag) { __hip_atomic_store((GAS unsigned*)((GAS char*)ws + off), tag, __ATOMIC_RELAXED, __HIP_MEMORY_SCOPE_AGENT); }
template <int CTRL, int RMASK> DI float dpp_add(float v) { return v + asf((unsigned)__builtin_amdgcn_update_dpp(0, (int)__builtin_bit_cast(unsigned, v), CTRL, RMASK, 0xf, true)); }
DI void poll_words(v4u sws, unsigned off, int n, unsigned tag, int lane) {
  const unsigned vo = off + FS * (unsigned)(lane < n ? lane : 0);
#pragma unroll 1
  for (unsigned spins = 0; spins < kSpinCap; ++spins) {
    unsigned f;
    asm volatile("buffer_load_dword %0, %1, %2, 0 offen sc1\n\ts_waitcnt vmcnt(0)" : "=&v"(f) : "v"(vo), "s"(sws) : "memory");
    if (__all(f == tag)) return;
    __builtin_amdgcn_s_sleep(4);
  }
}

constexpr unsigned A_XIMG = 64 * 1024, A_FIMG = 128 * 1024;
DI void phaseA_genF(LAS char* L, int i, int mt, int w, int lane) {
  const int mh = w >> 2, sg = w & 3, n = lane & 31, hh = lane >> 5;
  const int m1c = 64 * mt + 32 * mh + n, m1 = m1c >> 1, c = m1c & 1;
  const int j0 = 64 * sg + 32 * hh;
  const float ph = c ? 0.25f : 0.f;
  const float k2 = 2.f * cosrev((float)(8 * m1) * (1.f / 2048.f));
  float vm = cosrev((float)(((i + 8 * (j0 - 1)) * m1) & 2047) * (1.f / 2048.f) + ph);
  float v0 = cosrev((float)(((i + 8 * j0) * m1) & 2047) * (1.f / 2048.f) + ph);
  const unsigned cst = c ? 0x3F800000u : 0x00003F80u;
  const bool sp = m1c < 2;
#pragma unroll
  for (int s = 0; s < 4; ++s) {
    float e[8];
#pragma unroll
    for (int q = 0; q < 8; ++q) { e[q] = v0; const float vn = __builtin_fmaf(k2, v0, -vm); vm = v0; v0 = vn; }
    v4u f; f.x = pk2(e[0], e[1]); f.y = pk2(e[2], e[3]); f.z = pk2(e[4], e[5]); f.w = pk2(e[6], e[7]);
    if (mt == 0 && w < 4) { f.x = sp ? cst : f.x; f.y = sp ? cst : f.y; f.z = sp ? cst : f.z; f.w = sp ? cst : f.w; }
    *(LAS v4u*)(L + A_FIMG + (mh * 16 + sg * 4 + s) * 1024 + lane * 16) = f;
  }
}

struct Frag1 { v4u a[4], b0[4], b1[4]; };
DI void g1_read(Frag1& f, LAS char* wa, LAS char* xb0, LAS char* xb1, LAS char* xb2, LAS char* xb3, int slot) {
  const int so = slot * 32768;
  f.a[0] = tr_read2(wa + so, wa + so + 1024);              f.b0[0] = lds_ld16(xb0 + so); f.b1[0] = lds_ld16(xb0 + so + 4096);
  f.a[1] = tr_read2(wa + so + 4096, wa + so + 5120);       f.b0[1] = lds_ld16(xb1 + so); f.b1[1] = lds_ld16(xb1 + so + 4096);
  f.a[2] = tr_read2(wa + so + 8192, wa + so + 9216);       f.b0[2] = lds_ld16(xb2 + so); f.b1[2] = lds_ld16(xb2 + so + 4096);
  f.a[3] = tr_read2(wa + so + 12288, wa + so + 13312);     f.b0[3] = lds_ld16(xb3 + so); f.b1[3] = lds_ld16(xb3 + so + 4096);
}
DI unsigned silu2(float a, float b) {
  const f32x2 h = {a, b}, t = h * -1.44269504088896341f;
  f32x2 e = {__builtin_amdgcn_exp2f(t[0]), __builtin_amdgcn_exp2f(t[1])};
  e = e + 1.f;
  const f32x2 r = {__builtin_amdgcn_rcpf(e[0]), __builtin_amdgcn_rcpf(e[1])};
  return __builtin_bit_cast(unsigned, __builtin_convertvector(h * r, bf16x2));
}

DI f32x2 lo2(const f32x4& v) { return __builtin_shufflevector(v, v, 0, 1); }
DI f32x2 hi2(const f32x4& v) { return __builtin_shufflevector(v, v, 2, 3); }
DI unsigned pk2v(f32x2 p) { return __builtin_bit_cast(unsigned, __builtin_convertvector(p, bf16x2)); }
DI f32x4 asf4(v4u v) { return __builtin_bit_cast(f32x4, v); }
template <bool CONJ> DI v4u cmul_pack(const f32x4& re, const f32x4& im, const f32x4& tr, const f32x4& ti) {
  const f32x2 r0 = lo2(re), r1 = hi2(re), i0 = lo2(im), i1 = hi2(im), c0 = lo2(tr), c1 = hi2(tr), s0 = lo2(ti), s1 = hi2(ti);
  f32x2 a0, a1, b0, b1;
  if (CONJ) {
    a0 = __builtin_elementwise_fma(r0, c0, i0 * s0); a1 = __builtin_elementwise_fma(r1, c1, i1 * s1);
    b0 = __builtin_elementwise_fma(i0, c0, -(r0 * s0)); b1 = __builtin_elementwise_fma(i1, c1, -(r1 * s1));
  } else {
    a0 = __builtin_elementwise_fma(r0, c0, -(i0 * s0)); a1 = __builtin_elementwise_fma(r1, c1, -(i1 * s1));
    b0 = __builtin_elementwise_fma(r0, s0, i0 * c0); b1 = __builtin_elementwise_fma(r1, s1, i1 * c1);
  }
  v4u r; r.x = pk2v(a0); r.y = pk2v(a1); r.z = pk2v(b0); r.w = pk2v(b1); return r;
}
struct cf { float r, i; };
DI cf cmul(cf a, cf b) { return {a.r * b.r - a.i * b.i, a.r * b.i + a.i * b.r}; }
DI cf cmulc(cf a, cf b) { return {a.r * b.r + a.i * b.i, a.i * b.r - a.r * b.i}; }
DI unsigned w_src(int m, int lane) {
  const int mm = (NS - m) & (NS - 1);
  const int e = lane < 32 ? m * 257 - (m & 1) : mm * 257 + (m & 1);
  return (unsigned)(e * 2 + (lane & 31) * 16);
}
DI unsigned w_tail_src(int m, int lane) {
  const int mm = (NS - m) & (NS - 1), l = lane & 3;
  return (unsigned)((l < 2 ? m * 257 + 255 + l : mm * 257 + (l - 2) * 256) * 2);
}
struct WTail { unsigned r255, r256, rm0, rm256, i255, i256, im0, im256; };
DI WTail w_tail(unsigned tr, unsigned ti) {
  return {(unsigned)__builtin_amdgcn_readlane((int)tr, 0), (unsigned)__builtin_amdgcn_readlane((int)tr, 1), (unsigned)__builtin_amdgcn_readlane((int)tr, 2), (unsigned)__builtin_amdgcn_readlane((int)tr, 3),
          (unsigned)__builtin_amdgcn_readlane((int)ti, 0), (unsigned)__builtin_amdgcn_readlane((int)ti, 1), (unsigned)__builtin_amdgcn_readlane((int)ti, 2), (unsigned)__builtin_amdgcn_readlane((int)ti, 3)};
}
struct WVal { f32x4 r0, i0, r1, i1; };
struct WRawL { unsigned r[4][2], i[4][2]; };
DI void w_put(LAS char* scr, v4u qr, v4u qi, int lane) { *(LAS v4u*)(scr + lane * 16) = qr; *(LAS v4u*)(scr + 1024 + lane * 16) = qi; }
DI int w_off(int m, int lane) { const int k1 = lane & 15, g = lane >> 4, odd = m & 1; return lane < 32 ? (2 * k1 + 128 * g + odd) * 2 : 512 + (511 - 2 * k1 - 128 * g - odd) * 2; }
DI void w_get(WRawL& o, LAS char* scr, int off, int lane, int r) {
  LAS unsigned* a = (LAS unsigned*)(scr + ((off + (lane < 32 ? 64 : -64) * r) & ~3));
  o.r[r][0] = a[0]; o.r[r][1] = a[1]; o.i[r][0] = a[256]; o.i[r][1] = a[257];
}
DI WVal w_unpack(const WRawL& x, const WTail& t, int off, int m, int lane) {
  const bool lo = lane < 32;
  const unsigned sh = (off & 2) * 8;
  unsigned rr[4], ii[4];
#pragma unroll
  for (int r = 0; r < 4; ++r) { rr[r] = __builtin_amdgcn_alignbit(x.r[r][1], x.r[r][0], sh); ii[r] = __builtin_amdgcn_alignbit(x.i[r][1], x.i[r][0], sh); }
  if (m & 1) { rr[3] = lane == 31 ? (rr[3] & 0xffffu) | (t.r255 << 16) : rr[3]; ii[3] = lane == 31 ? (ii[3] & 0xffffu) | (t.i255 << 16) : ii[3]; }
  else { rr[0] = lane == 32 ? (rr[0] & 0xffffu) | (t.rm256 << 16) : rr[0]; ii[0] = lane == 32 ? (ii[0] & 0xffffu) | (t.im256 << 16) : ii[0]; }
  const unsigned s0 = lo ? 16u : 0u, s1 = lo ? 0u : 16u, sg = lo ? 0u : 0x80000000u;
  WVal o;
#pragma unroll
  for (int r = 0; r < 4; ++r) {
    o.r0[r] = asf((rr[r] << s0) & 0xffff0000u); o.r1[r] = asf((rr[r] << s1) & 0xffff0000u);
    o.i0[r] = asf(((ii[r] << s0) & 0xffff0000u) ^ sg); o.i1[r] = asf(((ii[r] << s1) & 0xffff0000u) ^ sg);
  }
  const bool av = (lane & 31) == 0;
  const float xr = bflo(lo ? t.rm0 : t.r256), xi = asf(((lo ? t.im0 : t.i256) << 16) ^ (sg ^ 0x80000000u));
  o.r0[0] = av ? 0.5f * (o.r0[0] + xr) : o.r0[0];
  o.i0[0] = av ? 0.5f * (o.i0[0] + xi) : o.i0[0];
  return o;
}

DI void packed_coef(WVal& a, WVal& b, int m2) {
  const cf om = {cosrev((float)m2 * 0.0625f), -sinrev((float)m2 * 0.0625f)};
#pragma unroll
  for (int pp = 0; pp < 2; ++pp)
#pragma unroll
    for (int r = 0; r < 4; ++r) {
      const float war = pp ? a.r1[r] : a.r0[r], wai = pp ? a.i1[r] : a.i0[r], wbr = pp ? b.r1[r] : b.r0[r], wbi = pp ? b.i1[r] : b.i0[r];
      const cf sp = {war + wbr, wai + wbi}, sm = {war - wbr, wai - wbi};
      const cf t1 = cmulc(sm, om), t2 = cmul(sm, om);
      const cf cE = {sp.r - t1.i, sp.i + t1.r}, cO = {t2.r - sp.i, t2.i + sp.r};
      const float alr = 0.25f * (cE.r + cO.i), ali = 0.25f * (cE.i - cO.r), ber = 0.25f * (cE.r - cO.i), bei = 0.25f * (cE.i + cO.r);
      if (pp) { a.r1[r] = alr; a.i1[r] = ali; b.r1[r] = ber; b.i1[r] = bei; } else { a.r0[r] = alr; a.i0[r] = ali; b.r0[r] = ber; b.i0[r] = bei; }
    }
}

struct Geo {
  int tid, lane, w, b, i, i1, at, m1, rb, fc, hh, blk, q4, p4;
  int jb, dt, rh, q, j;
  v4u sx, sws, sw2;
  unsigned vw20, vw21, ldsw, fq0b, fq1b;
};
template <int T, int U> DI void w2_dma(const v4u& sw2, unsigned vw20, unsigned vw21, unsigned ldsw, unsigned fq0, unsigned fq1) {
  dma16<(T & 7) * 16384u + U * 1024u, (T < 8 ? T : T - 8) * 32768u>(sw2, U ? vw21 : vw20, ldsw, T < 8 ? fq0 : fq1);
}
#define w2_issue1(g, t, u) (w2_dma<t, u>((g).sw2, (g).vw20, (g).vw21, (g).ldsw, (g).fq0b, (g).fq1b))
struct GRot { float c1, s1, c4, s4, c, s; };
DI void gen_g_init(const Geo& g, GRot& r) {
  const int srow = g.i + 8 * g.j;
  const int mb = 32 * g.q + 2 * g.hh;
  const float r1 = (float)(srow & 2047) * (1.f / 2048.f), r4 = (float)((4 * srow) & 2047) * (1.f / 2048.f), r0 = (float)((mb * srow) & 2047) * (1.f / 2048.f);
  r.c1 = cosrev(r1); r.s1 = sinrev(r1); r.c4 = cosrev(r4); r.s4 = sinrev(r4); r.c = cosrev(r0); r.s = sinrev(r0);
}
template <int S> DI void gen_g_frag(const Geo& g, GRot& r, v4u& f) {
#pragma unroll
  for (int h = 0; h < 2; ++h) {
    const float cb = __builtin_fmaf(r.c, r.c1, -(r.s * r.s1)), sb = __builtin_fmaf(r.s, r.c1, r.c * r.s1);
    unsigned w0 = pk2(r.c, -r.s), w1 = pk2(cb, -sb);
    if (S == 0 && h == 0) w0 = (g.q == 0 && g.hh == 0) ? ((g.j & 1) ? 0x3F800000u : 0x00003F80u) : w0;
    if (h) { f.z = w0; f.w = w1; } else { f.x = w0; f.y = w1; }
    const float cn = __builtin_fmaf(r.c, r.c4, -(r.s * r.s4)), sn = __builtin_fmaf(r.s, r.c4, r.c * r.s4);
    r.c = cn; r.s = sn;
  }
  asm volatile("" : "+v"(f));
}
template <int N0, int N1, int NR> DI void bar_w(int w) { if (w == 0) bar_vm<N0>(); else if (w == 1) bar_vm<N1>(); else bar_vm<NR>(); }
#define NOP do {} while (0)

#define TLBW 0
#define TOUCH() do {                                                                                                      \
    const int gl = G.b * 256 + (w & 3) * 64 + lane;                                                                         \
    const char* base_ = gl < 14336 ? (const char*)p.w2 : gl < 28672 ? (const char*)p.w1 - 14336 * 128 : gl < 36896 ? (const char*)p.wr - 28672 * 128           \
                      : gl < 45120 ? (const char*)p.wi - 36896 * 128 : (const char*)p.x - 45120 * 128;                     \
    const char* ad_ = base_ + (size_t)(gl < 61504 ? gl : 45120) * 128;                                                      \
    if (w >= 4) ad_ = TLBW ? (w == 7 ? (const char*)p.out + lane * 32768 : (const char*)p.ws + (size_t)((w - 4) * 64 + lane + 8) * 65536 + 32768 - 128) : (const char*)p.x; \
    asm volatile("global_load_dword %0, %1, off" : "=v"(touch) : "v"(ad_) : "memory");                                     \
  } while (0)
template <bool ISA>
DI unsigned phase1(const Params& p, LAS char* L, const Geo& G, v4u& m5a, v4u& m5b
) {
  const int tid = G.tid, lane = G.lane, w = G.w, i = G.i, i1 = G.i1, rb = G.rb, fc = G.fc, hh = G.hh, blk = G.blk, q4 = G.q4, p4 = G.p4;
  const v4u sx = G.sx, sws = G.sws, sw1 = make_srd(p.w1, ND * NF * 2);
  unsigned touch;
  unsigned ep; v4u gx, gv;
  const int jg = 128 * rb + 8 * fc + w;
  LD16Z(gx, (unsigned)(lane * 16 + (i1 + 8 * jg) * 1024), sx, "");
  LD16Z(gv, (unsigned)lane * 16u, make_srd(p.gw, ND * 2), "");
  LD4Z(ep, WS_EPOCH, sws, "sc1");
  const int f0 = ISA ? 96 * fc : 768 + 128 * (fc - 8), nf = ISA ? 96 : 128;
  const int mt = G.at >> 2, dt4 = G.at & 3;
  const int m1 = G.m1;
  const v4u swr = make_srd(p.wr, NS * 257 * 2), swi = make_srd(p.wi, NS * 257 * 2);
  const int mW = m1 == 0 ? 128 * w : m1 + 256 * w;
  v4u wqr, wqi, xqr, xqi;
  unsigned wtr, wti, xtr, xti;
  WVal Wc, Wd;
  if (ISA) {
    const unsigned voff = (unsigned)(lane >> 4) * 8192u + (unsigned)(((lane & 15) ^ ((lane >> 4) << 2)) << 4);
    const unsigned xl = uni(A_XIMG + (unsigned)w * 8192u), xs = uni((unsigned)i * 1024u + (unsigned)dt4 * 256u + (unsigned)w * 262144u);
    dma16<0, 0>(sx, voff, xl, xs); dma16<1024, 32768>(sx, voff, xl, xs); dma16<2048, 65536>(sx, voff, xl, xs); dma16<3072, 98304>(sx, voff, xl, xs);
    dma16<4096, 131072>(sx, voff, xl, xs); dma16<5120, 163840>(sx, voff, xl, xs); dma16<6144, 196608>(sx, voff, xl, xs); dma16<7168, 229376>(sx, voff, xl, xs);
  }
  unsigned vx0, vx1, vw0, vw1;
  {
    const int r0 = 16 * w + (lane >> 3), pos = lane & 7, r1 = r0 + 8;
    vx0 = (unsigned)(i1 * 1024 + (128 * rb + r0) * 8192 + ((pos ^ ((r0 >> 1) & 7)) << 4));
    vx1 = (unsigned)(i1 * 1024 + (128 * rb + r1) * 8192 + ((pos ^ ((r1 >> 1) & 7)) << 4));
    const int k0 = 8 * w + (lane >> 4), chunk = (lane & 15) ^ ((lane >> 4) << 2);
    const bool ok = chunk * 8 < nf;
    vw0 = ok ? (unsigned)(k0 * 3584 + f0 * 2 + chunk * 16) : 0x80000000u;
    vw1 = ok ? vw0 + 4u * 3584u : 0x80000000u;
  }
  const unsigned g1l = uni((unsigned)w * 2048u);
#define g1_dma(kt, u) do {                                                                                         \
    if ((u) == 0) dma16z<((kt) & 3) * 32768u, (kt) * 128u>(sx, vx0, g1l);                                           \
    if ((u) == 1) dma16z<((kt) & 3) * 32768u + 1024u, (kt) * 128u>(sx, vx1, g1l);                                   \
    if ((u) == 2) dma16z<((kt) & 3) * 32768u + 16384u, (kt) * (64u * 3584u)>(sw1, vw0, g1l);                        \
    if ((u) == 3) dma16z<((kt) & 3) * 32768u + 17408u, (kt) * (64u * 3584u)>(sw1, vw1, g1l); } while (0)
#define g1_issue(kt) do { g1_dma(kt, 0); g1_dma(kt, 1); g1_dma(kt, 2); g1_dma(kt, 3); } while (0)
  if (ISA) {
    phaseA_genF(L, i, mt, w, lane);
    g1_issue(0); g1_issue(1);
  } else {
    g1_issue(0);
    if (m1 == 0) {
      const unsigned ws2 = w_src(128 * w + 1024, lane), wt2 = w_tail_src(128 * w + 1024, lane);
      LD16Z(xqr, ws2, swr, ""); LD16Z(xqi, ws2, swi, ""); LD2Z(xtr, wt2, swr, ""); LD2Z(xti, wt2, swi, "");
    }
    { const unsigned wsrc = w_src(mW, lane), wts = w_tail_src(mW, lane);
      LD16Z(wqr, wsrc, swr, ""); LD16Z(wqi, wsrc, swi, ""); LD2Z(wtr, wts, swr, ""); LD2Z(wti, wts, swi, ""); }
    asm volatile("s_barrier" ::: "memory");
    g1_issue(1); asm volatile("s_barrier" ::: "memory");
    g1_issue(2); asm volatile("s_barrier" ::: "memory");
    g1_issue(3);
  }
  TOUCH();
  if (ISA) wait_vm<17>(); else wait_vm<21>();
  asm volatile("" : "+v"(ep), "+v"(gx), "+v"(gv) :: "memory");
  const unsigned tag = (unsigned)__builtin_amdgcn_readfirstlane((int)ep) + 1u;
  float gate_g, gate_og;
  {
    float z = 0.f;
#pragma unroll
    for (int q = 0; q < 4; ++q) {
      const unsigned pr = pk2(bflo(gx[q]) * bflo(gv[q]), bfhi(gx[q]) * bfhi(gv[q]));
      z += bflo(pr) + bfhi(pr);
    }
    z = dpp_add<0x111, 0xf>(z); z = dpp_add<0x112, 0xf>(z); z = dpp_add<0x114, 0xf>(z); z = dpp_add<0x118, 0xf>(z);
    z = dpp_add<0x142, 0xa>(z); z = dpp_add<0x143, 0xc>(z);
    const float zb = bflo(pk2(z, 0.f));
    const float g = bflo(pk2(__builtin_amdgcn_rcpf(1.f + __builtin_amdgcn_exp2f(zb * -1.44269504088896341f)), 0.f));
    gate_g = asf((unsigned)__builtin_amdgcn_readlane((int)__builtin_bit_cast(unsigned, g), 63));
    gate_og = bflo(pk2(1.f - gate_g, 0.f));
  }
  if (!ISA) {
    asm volatile("s_waitcnt vmcnt(13)" : "+v"(wqr), "+v"(wqi), "+v"(wtr), "+v"(wti) :: "memory");
    LAS char* wscr0 = L + 128 * 1024 + w * 2048;
    const int woff = w_off(mW, lane);
    w_put(wscr0, wqr, wqi, lane);
    asm volatile("s_waitcnt lgkmcnt(0)" ::: "memory");
    WRawL wraw;
    w_get(wraw, wscr0, woff, lane, 0); w_get(wraw, wscr0, woff, lane, 1); w_get(wraw, wscr0, woff, lane, 2); w_get(wraw, wscr0, woff, lane, 3);
    Wc = w_unpack(wraw, w_tail(wtr, wti), woff, mW, lane);
    asm volatile("" : "+v"(Wc.r0), "+v"(Wc.i0), "+v"(Wc.r1), "+v"(Wc.i1));
  }
  if (ISA) {
    const int dq = w & 3, mh = w >> 2;
    LAS char* xa = L + A_XIMG + (32 * hh + q4) * 256 + (((4 * dq + 2 * blk + (p4 >> 1)) ^ (q4 << 2)) << 4) + 8 * (p4 & 1);
    LAS char* fb = L + A_FIMG + mh * 16384 + lane * 16;
    const int srow = 32 * mh + (lane & 31);
    LAS char* stw = L + A_FIMG + srow * 256 + 8 * hh;
    const int rrow = tid >> 3;
    LAS char* str = L + A_FIMG + rrow * 256;
    const unsigned tdst = WS_T + (unsigned)(((64 * mt + rrow) * 8 + i) * 1024 + dt4 * 256 + (tid & 7) * 16);
    if (w < 4) bar_vm<9>(); else BAR_LGKM();
    STAMP(1);
    f32x16 acc = {};
    v4u fa[2][4], fbv[2][4];
#pragma unroll
    for (int s = 0; s < 4; ++s) { fa[0][s] = tr_read2(xa + 8 * s * 256, xa + 8 * s * 256 + 1024); fbv[0][s] = lds_ld16(fb + s * 1024); }
#pragma unroll
    for (int g = 0; g < 4; ++g) {
      acc = mfma32(fa[g & 1][0], fbv[g & 1][0], acc);
      SCHED();
      if (g == 1) { if (w >= 4) bar_vm<9>(); else BAR_LGKM(); }
      if (g < 3) {
#pragma unroll
        for (int s = 0; s < 4; ++s) {
          const int ro = (64 * (g + 1) + 8 * s) * 256;
          fa[(g + 1) & 1][s] = tr_read2(xa + ro, xa + ro + 1024); fbv[(g + 1) & 1][s] = lds_ld16(fb + (4 * (g + 1) + s) * 1024);
        }
      }
      SCHED();
#pragma unroll
      for (int s = 1; s < 4; ++s) { acc = mfma32(fa[g & 1][s], fbv[g & 1][s], acc); if (g == 1 && s == 1) { g1_dma(2, 0); g1_dma(2, 1); } if (g == 1 && s == 2) { g1_dma(2, 2); g1_dma(2, 3); } SCHED(); }
    }
    BAR_LGKM();
    STAMP(2);
    g1_issue(3);
#pragma unroll
    for (int Gq = 0; Gq < 4; ++Gq) {
      v2u o; o.x = pk2(acc[4 * Gq], acc[4 * Gq + 1]); o.y = pk2(acc[4 * Gq + 2], acc[4 * Gq + 3]);
      *(LAS v2u*)(stw + (((4 * dq + Gq) ^ (srow & 15)) << 4)) = o;
    }
    BAR_LGKM();
    {
      const v4u v0 = lds_ld16(str + (((tid & 7) ^ (rrow & 15)) << 4)), v1 = lds_ld16(str + ((((tid & 7) + 8) ^ (rrow & 15)) << 4));
      st16_sc1(sws, tdst, v0);
      st16_sc1(sws, tdst + 128u, v1);
    }
    SCHED();
  }
  const int ft = w & 3, rh1 = w >> 2;
  const bool active = ft * 32 < nf;
  f32x16 acc1[2] = {};
  v4u tq0, tq1;
  v4u tb[16];
  unsigned fla;
  int t_ok = 0;
  unsigned tsrc0 = 0, tsrc1 = 0;
  const unsigned flaoff = WS_FLA + (unsigned)(((m1 >> 5) & 3) * 32 + (lane & 31)) * FS;
  LAS char* CTL = L + 128 * 1024;
  {
    LAS char* wa = L + 16384 + (8 * hh + q4) * 256 + (((4 * ft + 2 * blk + (p4 >> 1)) ^ (q4 << 2)) << 4) + 8 * (p4 & 1);
    const int r = 64 * rh1 + (lane & 31), fz = (r >> 1) & 7;
    LAS char* xb0 = L + r * 128 + (((0 + hh) ^ fz) << 4);
    LAS char* xb1 = L + r * 128 + (((2 + hh) ^ fz) << 4);
    LAS char* xb2 = L + r * 128 + (((4 + hh) ^ fz) << 4);
    LAS char* xb3 = L + r * 128 + (((6 + hh) ^ fz) << 4);
    Frag1 fr[2];
    bar_vm<ISA ? 15 : 13>();
    STAMP(3);
    if (lane == 0) { v2u gv2; gv2.x = __builtin_bit_cast(unsigned, gate_g); gv2.y = __builtin_bit_cast(unsigned, gate_og); *(LAS v2u*)(CTL + 64 + w * 8) = gv2; }
    g1_read(fr[0], wa, xb0, xb1, xb2, xb3, 0);
#define PIN1(c) asm volatile("" : "+v"(c.a[0]), "+v"(c.a[1]), "+v"(c.a[2]), "+v"(c.a[3]), "+v"(c.b0[0]), "+v"(c.b0[1]), "+v"(c.b0[2]), "+v"(c.b0[3]), \
                             "+v"(c.b1[0]), "+v"(c.b1[1]), "+v"(c.b1[2]), "+v"(c.b1[3]))
#define G1_STEP(kt, X0, X1, X2, X3, X4, X5, X6, X7)                                                                   \
    {                                                                                                                   \
      FT(10 + (kt));                                                                                                    \
      Frag1& c = fr[(kt) & 1];                                                                                          \
      Frag1& n = fr[((kt) + 1) & 1];                                                                                    \
      const int so = (((kt) + 1) & 3) * 32768;                                                                          \
      PIN1(c);                                                                                                          \
      asm volatile("" :: "v"(acc1[0]), "v"(acc1[1]));                                                                   \
      SCHED();                                                                                                          \
      acc1[0] = mfma32(c.a[0], c.b0[0], acc1[0]); if ((kt) < 7) n.a[0] = tr_read2(wa + so, wa + so + 1024); X0; SCHED(); \
      acc1[1] = mfma32(c.a[0], c.b1[0], acc1[1]); if ((kt) < 7) { n.b0[0] = lds_ld16(xb0 + so); n.b1[0] = lds_ld16(xb0 + so + 4096); } X1; SCHED(); \
      acc1[0] = mfma32(c.a[1], c.b0[1], acc1[0]); if ((kt) < 7) n.a[1] = tr_read2(wa + so + 4096, wa + so + 5120); X2; SCHED(); \
      acc1[1] = mfma32(c.a[1], c.b1[1], acc1[1]); if ((kt) < 7) { n.b0[1] = lds_ld16(xb1 + so); n.b1[1] = lds_ld16(xb1 + so + 4096); } X3; SCHED(); \
      acc1[0] = mfma32(c.a[2], c.b0[2], acc1[0]); if ((kt) < 7) n.a[2] = tr_read2(wa + so + 8192, wa + so + 9216); X4; SCHED(); \
      acc1[1] = mfma32(c.a[2], c.b1[2], acc1[1]); if ((kt) < 7) { n.b0[2] = lds_ld16(xb2 + so); n.b1[2] = lds_ld16(xb2 + so + 4096); } X5; SCHED(); \
      acc1[0] = mfma32(c.a[3], c.b0[3], acc1[0]); if ((kt) < 7) n.a[3] = tr_read2(wa + so + 12288, wa + so + 13312); X6; SCHED(); \
      acc1[1] = mfma32(c.a[3], c.b1[3], acc1[1]); if ((kt) < 7) { n.b0[3] = lds_ld16(xb3 + so); n.b1[3] = lds_ld16(xb3 + so + 4096); } X7; SCHED(); \
    }
#define GATE_STORE() do { if (w == 2 && lane < 8) { const v2u gv2 = *(LAS v2u*)(CTL + 64 + lane * 8);                       \
      __builtin_amdgcn_raw_buffer_store_b64(gv2, as_rsrc(sws), WS_GATE + (unsigned)(i1 * 256 + 128 * rb + 8 * fc + lane) * 8u, 0, 16); } } while (0)
#define STEP8 G1_STEP
    if constexpr (ISA) {
      bar_vm<11>();
      STAMP(4);
      STEP8(0, NOP, NOP, NOP, NOP, NOP, NOP, NOP, NOP)
      bar_vm<0>();
      if (tid == 0) flag_store(p.ws, WS_FLA + (unsigned)(mt * 32 + i * 4 + dt4) * FS, tag);
      STAMP(13);
      STEP8(1, g1_dma(4, 0), g1_dma(4, 1), g1_dma(4, 2), g1_dma(4, 3), g1_dma(5, 0), g1_dma(5, 1), g1_dma(5, 2), g1_dma(5, 3))
      BAR_LGKM();
      GATE_STORE();
      STEP8(2, NOP, NOP, NOP, NOP, g1_dma(6, 0), g1_dma(6, 1), g1_dma(6, 2), g1_dma(6, 3))
      bar_vm<8>();
      STEP8(3, NOP, NOP, NOP, NOP, g1_dma(7, 0), g1_dma(7, 1), g1_dma(7, 2), g1_dma(7, 3))
      bar_vm<8>();
      STAMP(12);
      STEP8(4, NOP, NOP, NOP, NOP, w2_issue1(G, 0, 0), w2_issue1(G, 0, 1), w2_issue1(G, 1, 0), w2_issue1(G, 1, 1))
      bar_vm<8>();
      STEP8(5, NOP, NOP, NOP, NOP, w2_issue1(G, 2, 0), w2_issue1(G, 2, 1), w2_issue1(G, 3, 0), w2_issue1(G, 3, 1))
      bar_vm<8>();
      STEP8(6, NOP, NOP, NOP, NOP, w2_issue1(G, 4, 0), w2_issue1(G, 4, 1), w2_issue1(G, 5, 0), w2_issue1(G, 5, 1))
      BAR_LGKM();
    } else {
      const v4u stab = make_srd(&kTab, sizeof(Tab));
      {
        const int kap = tid >> 5, ii = (kap >> 1) & 7, d0 = kap & 1;
        const int g0 = m1 == 0 ? 2 * ii : ii, g1 = m1 == 0 ? 2 * ii + 1 : 8 + ii;
        const unsigned tb = WS_T + (unsigned)m1 * 16384u + (unsigned)(d0 * 512 + (tid & 31) * 16);
        tsrc0 = tb + (unsigned)g0 * 1024u; tsrc1 = tb + (unsigned)g1 * 1024u;
      }
      bar_vm<9>();
      STAMP(4);
      STEP8(0, NOP, NOP, NOP, NOP, g1_dma(4, 0), g1_dma(4, 1), g1_dma(4, 2), g1_dma(4, 3))
      bar_vm<9>();
      STEP8(1, NOP, NOP, NOP, NOP, g1_dma(5, 0), g1_dma(5, 1), g1_dma(5, 2), g1_dma(5, 3))
      bar_vm<9>();
      GATE_STORE();
      STEP8(2, NOP, NOP, NOP, NOP, g1_dma(6, 0), g1_dma(6, 1), g1_dma(6, 2), g1_dma(6, 3))
      bar_vm<8>();
      LAS char* wscr0 = L + 128 * 1024 + w * 2048;
      const bool special = m1 == 0;
      const int m2nd = 128 * w + 1024, off2 = w_off(m2nd, lane);
      if (special) { asm volatile("" : "+v"(xqr), "+v"(xqi), "+v"(xtr), "+v"(xti)); w_put(wscr0, xqr, xqi, lane); }
      STEP8(3, NOP, NOP, (dma16<128u * 1024u + 16384u, 0>(stab, (unsigned)lane * 16u, g1l, g1l)), (dma16<128u * 1024u + 17408u, 1024>(stab, (unsigned)lane * 16u, g1l, g1l)),
            g1_dma(7, 0), g1_dma(7, 1), g1_dma(7, 2), g1_dma(7, 3))
      bar_vm<10>();
      STAMP(12);
      WRawL raw2;
      if (special) { w_get(raw2, wscr0, off2, lane, 0); w_get(raw2, wscr0, off2, lane, 1); w_get(raw2, wscr0, off2, lane, 2); w_get(raw2, wscr0, off2, lane, 3); }
      STEP8(4, NOP, NOP, NOP, NOP, w2_issue1(G, 0, 0), w2_issue1(G, 0, 1), w2_issue1(G, 1, 0), w2_issue1(G, 1, 1))
      bar_vm<10>();
      if (special) { Wd = w_unpack(raw2, w_tail(xtr, xti), off2, m2nd, lane); packed_coef(Wc, Wd, w); }
      STEP8(5, NOP, NOP, NOP, NOP, w2_issue1(G, 2, 0), w2_issue1(G, 2, 1), w2_issue1(G, 3, 0), w2_issue1(G, 3, 1))
      bar_vm<8>();
      STEP8(6, if (w == 0) LD4Z(fla, flaoff, sws, "sc1"), NOP, NOP, NOP, w2_issue1(G, 4, 0), w2_issue1(G, 4, 1), w2_issue1(G, 5, 0), w2_issue1(G, 5, 1))
      BAR_LGKM();
    }
    if constexpr (ISA) {
      STEP8(7, NOP, NOP, NOP, NOP, w2_issue1(G, 6, 0), w2_issue1(G, 6, 1), w2_issue1(G, 7, 0), w2_issue1(G, 7, 1))
    } else {
      LAS char* TB = L + 128 * 1024 + 16384 + lane * 16;
#define TBR(f) (tb[f] = lds_ld16(TB + (f) * 1024), tb[(f) + 1] = lds_ld16(TB + ((f) + 1) * 1024))
      STEP8(7, TBR(6), TBR(0), TBR(10), TBR(2), { TBR(12); w2_issue1(G, 6, 0); }, { TBR(14); w2_issue1(G, 6, 1); }, { TBR(4); w2_issue1(G, 7, 0); }, { TBR(8); w2_issue1(G, 7, 1); })
      if (w == 0) {
        asm volatile("s_waitcnt vmcnt(8)" : "+v"(fla) :: "memory");
        const unsigned ok = __all(fla == tag) ? 1u : 0u;
        if (lane == 0) *(LAS unsigned*)CTL = ok;
      }
      BAR_LGKM();
      t_ok = __builtin_amdgcn_readfirstlane(*(volatile LAS unsigned*)CTL);
      if (t_ok) { LD16Z(tq0, tsrc0, sws, "sc1"); LD16Z(tq1, tsrc1, sws, "sc1"); }
    }
  }
  asm volatile("" :: "v"(touch));
  STAMP(5);
  FT(18);
  if (active) {
#pragma unroll
    for (int rt = 0; rt < 2; ++rt)
#pragma unroll
      for (int u = 0; u < 2; ++u) {
        v4u o;
        o.x = silu2(acc1[rt][8 * u + 0], acc1[rt][8 * u + 1]);
        o.y = silu2(acc1[rt][8 * u + 2], acc1[rt][8 * u + 3]);
        o.z = silu2(acc1[rt][8 * u + 4], acc1[rt][8 * u + 5]);
        o.w = silu2(acc1[rt][8 * u + 6], acc1[rt][8 * u + 7]);
        const int jb32 = 4 * rb + 2 * rh1 + rt, fs = (f0 >> 4) + 2 * ft + u;
        st16_sc1(sws, WS_H + (unsigned)(((i1 * 8 + jb32) * 112 + fs) * 1024 + lane * 16), o);
      }
  }
  SCHED();
  STAMP(6);
  FT(20);
  const unsigned flh_own = WS_FLH + (unsigned)((i1 * 2 + rb) * 16 + fc) * FS;
  if constexpr (ISA) {
    bar_vm<0>();
    if (tid == 0) flag_store(p.ws, flh_own, tag);
  } else {
    LAS char* I0 = L + 128 * 1024;
    LAS char* I1 = L + 128 * 1024 + 16384;
    const int g = lane >> 4, l15 = lane & 15;
    const bool special = (m1 == 0);
    bool published = false;
    if (t_ok) {
      wait_vm<4>();
      asm volatile("s_barrier" ::: "memory");
    } else {
      bar_vm<0>();
      if (tid == 0) flag_store(p.ws, flh_own, tag);
      published = true;
      if (w == 0) poll_words(sws, WS_FLA + (unsigned)(m1 >> 5) * 32u * FS, 32, tag, lane);
      asm volatile("s_barrier" ::: "memory");
      LD16Z(tq0, tsrc0, sws, "sc1"); LD16Z(tq1, tsrc1, sws, "sc1");
      wait_vm<0>();
    }
    asm volatile("" : "+v"(tq0), "+v"(tq1) :: "memory");
    {
      const int kap = tid >> 5, ch = tid & 31;
      *(LAS v4u*)(I0 + kap * 512 + ((ch ^ ((kap & 3) << 2)) << 4)) = tq0;
      *(LAS v4u*)(I0 + (kap + 16) * 512 + ((ch ^ ((kap & 3) << 2)) << 4)) = tq1;
    }
    BAR_LGKM();
    FT(21);
    LAS char* ta = I0 + (8 * hh + q4) * 512 + (((4 * w + 2 * blk + (p4 >> 1)) ^ (q4 << 2)) << 4) + 8 * (p4 & 1);
    const int n = lane & 31;
    LAS char* so = I1 + n * 512 + 8 * hh;
    {
      f32x16 acc = {};
      acc = mfma32(tr_read2(ta, ta + 2048), tb[6], acc);
      acc = mfma32(tr_read2(ta + 8192, ta + 10240), tb[7], acc);
#pragma unroll
      for (int Gq = 0; Gq < 4; ++Gq) {
        v2u o; o.x = pk2(acc[4 * Gq], acc[4 * Gq + 1]); o.y = pk2(acc[4 * Gq + 2], acc[4 * Gq + 3]);
        *(LAS v2u*)(so + (((4 * w + Gq) ^ (n & 15)) << 4)) = o;
      }
    }
    BAR_LGKM();
    FT(22);
    const f32x4 z4 = {0.f, 0.f, 0.f, 0.f};
    const v4u F16fr = tb[0], F16fi = tb[1];
    v4u F16ir, F16ii;
    F16ir.x = F16fr.x; F16ir.y = F16fr.y; F16ir.z = F16fr.z ^ 0x80008000u; F16ir.w = F16fr.w ^ 0x80008000u;
    F16ii.x = F16fi.x ^ 0x80008000u; F16ii.y = F16fi.y ^ 0x80008000u; F16ii.z = F16fi.z; F16ii.w = F16fi.w;
    f32x4 zr[2], zi[2];
#pragma unroll
    for (int pp = 0; pp < 2; ++pp) {
      const int n0 = 4 * w + 2 * pp, n1 = n0 + 1;
      const int ch = 2 * (4 * g + q4) + (p4 >> 1);
      const v4u a1 = tr_read2(I1 + n0 * 512 + ((ch ^ (n0 & 15)) << 4) + 8 * (p4 & 1), I1 + n1 * 512 + ((ch ^ (n1 & 15)) << 4) + 8 * (p4 & 1));
      const f32x4 dr = mfma16(a1, pp ? tb[2] : F16fr, z4), di = mfma16(a1, pp ? tb[3] : F16fi, z4);
      const v4u b3 = cmul_pack<false>(dr, di, asf4(tb[10 + 2 * pp]), asf4(tb[11 + 2 * pp]));
      zr[pp] = mfma16(F16fr, b3, z4); zi[pp] = mfma16(F16fi, b3, z4);
    }
    v4u a4[2];
    if (!special) {
      a4[0] = cmul_pack<false>(zr[0], zi[0], Wc.r0, Wc.i0);
      a4[1] = cmul_pack<false>(zr[1], zi[1], Wc.r1, Wc.i1);
    } else {
      BAR_LGKM();
      LAS char* Z = I0;
#pragma unroll
      for (int pp = 0; pp < 2; ++pp)
#pragma unroll
        for (int r = 0; r < 4; ++r) { f32x2 v = {zr[pp][r], zi[pp][r]}; *(LAS f32x2*)(Z + ((w * 2 + pp) * 256 + l15 + 16 * (4 * g + r)) * 8) = v; }
      BAR_LGKM();
#pragma unroll
      for (int pp = 0; pp < 2; ++pp) {
        f32x4 cr, ci;
#pragma unroll
        for (int r = 0; r < 4; ++r) {
          const int K = l15 + 16 * (4 * g + r), Kn = pp ? 255 - K : (256 - K) & 255;
          const f32x2 y = *(LAS f32x2*)(Z + ((((8 - w) & 7) * 2 + pp) * 256 + Kn) * 8);
          cr[r] = y[0]; ci[r] = -y[1];
        }
        const f32x4 ar = pp ? Wc.r1 : Wc.r0, ai = pp ? Wc.i1 : Wc.i0, br = pp ? Wd.r1 : Wd.r0, bi = pp ? Wd.i1 : Wd.i0;
        const f32x4 yr = zr[pp] * ar - zi[pp] * ai + cr * br - ci * bi, yi = zr[pp] * ai + zi[pp] * ar + cr * bi + ci * br;
        a4[pp].x = pk2(yr[0], yr[1]); a4[pp].y = pk2(yr[2], yr[3]); a4[pp].z = pk2(yi[0], yi[1]); a4[pp].w = pk2(yi[2], yi[3]);
      }
      BAR_LGKM();
    }
#pragma unroll
    for (int pp = 0; pp < 2; ++pp) {
      const f32x4 dr = mfma16(a4[pp], F16ir, z4), di = mfma16(a4[pp], F16ii, z4);
      const v4u a5 = cmul_pack<true>(dr, di, asf4(tb[pp ? 14 : 10]), asf4(tb[pp ? 15 : 11]));
      const f32x4 er = mfma16(a5, pp ? tb[4] : F16ir, z4), ei = mfma16(a5, pp ? tb[5] : F16ii, z4);
      const int n0 = 4 * w + 2 * pp, n1 = n0 + 1;
      const int ch = 2 * l15 + (g >> 1);
      v2u o; o.x = pk2(er[0], er[1]); o.y = pk2(er[2], er[3]);
      *(LAS v2u*)(I0 + n0 * 512 + ((ch ^ ((n0 & 3) << 2)) << 4) + 8 * (g & 1)) = o;
      o.x = pk2(ei[0], ei[1]); o.y = pk2(ei[2], ei[3]);
      *(LAS v2u*)(I0 + n1 * 512 + ((ch ^ ((n1 & 3) << 2)) << 4) + 8 * (g & 1)) = o;
    }
    bar_vm<0>();
    if (!published && tid == 0) flag_store(p.ws, flh_own, tag);
    FT(23);
    m5a = tb[8]; m5b = tb[9];
  }
  return tag;
}

DI void make_geo(Geo& G, const Params& p, int tz) {
  G.tid = tz; G.lane = G.tid & 63; G.w = __builtin_amdgcn_readfirstlane(G.tid >> 6);
  G.b = blockIdx.x;
  const int Lx = G.b >> 3, x8 = G.b & 7;
  {
    const int a = x8 & 3, h = x8 >> 2, rbi = Lx >> 3, c = Lx & 7;
    G.i = 2 * a + h; G.i1 = 2 * a + (rbi >> 1); G.rb = rbi & 1;
    G.fc = c < 4 ? 4 * h + c : 4 + 4 * h + c;
    G.at = rbi * 4 + c; G.m1 = ((rbi * 4 + c - 4) << 3) | x8;
  }
  G.hh = G.lane >> 5; G.blk = (G.lane >> 4) & 1; G.q4 = (G.lane & 15) >> 2; G.p4 = G.lane & 3;
  G.sx = make_srd(p.x, NS * ND * 2); G.sws = make_srd(p.ws, 16u << 20); G.sw2 = make_srd(p.w2, NF * ND * 2);
  G.jb = Lx >> 3; G.dt = Lx & 7; G.rh = G.w & 1; G.q = G.w >> 1;
  G.j = 64 * G.jb + 32 * G.rh + (G.lane & 31);
  const int rho = 16 * G.rh + (G.lane >> 3), chs = (G.lane & 7) ^ (4 * ((G.lane >> 4) & 1));
  G.vw20 = (unsigned)(rho * 1024 + G.dt * 128 + chs * 16); G.vw21 = G.vw20 + 8 * 1024;
  G.ldsw = uni((unsigned)(G.q * 4096 + G.rh * 2048));
  G.fq0b = uni((unsigned)(768 + 256 * G.q) * 1024u); G.fq1b = uni((unsigned)(192 * G.q) * 1024u);
}

__global__ __launch_bounds__(512, 2) void gm_kernel(Params p) {
  extern __shared__ __attribute__((aligned(16))) char smem[];
  LAS char* L = (LAS char*)smem;
  STAMP(0);
  FT(0);
  Geo G;
  make_geo(G, p, threadIdx.x);
  const bool isA = G.fc < 8;
  v4u gf[4], m5a, m5b;
  unsigned tag;
  if (isA) tag = phase1<true>(p, L, G, m5a, m5b); else tag = phase1<false>(p, L, G, m5a, m5b);
  { int tz = threadIdx.x; asm volatile("" : "+v"(tz)); make_geo(G, p, tz); }
  const int tid = G.tid, lane = G.lane, w = G.w, b = G.b, i = G.i, jb = G.jb, dt = G.dt, rh = G.rh, q = G.q, j = G.j;
  const int hh = G.hh, blk = G.blk, q4 = G.q4, p4 = G.p4;
  const v4u sws = G.sws;
  const unsigned ldsw = G.ldsw;
  const int m1 = G.m1;
  STAMP(7);
  FT(25);
  if (!isA) {
    LAS char* I0 = L + 128 * 1024;
    LAS char* I1 = L + 128 * 1024 + 16384;
    LAS char* ta = I0 + (8 * hh + q4) * 512 + (((4 * w + 2 * blk + (p4 >> 1)) ^ (q4 << 2)) << 4) + 8 * (p4 & 1);
    const int n = lane & 31;
    LAS char* so = I1 + n * 512 + 8 * hh;
    f32x16 acc = {};
    acc = mfma32(tr_read2(ta, ta + 2048), m5a, acc);
    acc = mfma32(tr_read2(ta + 8192, ta + 10240), m5b, acc);
#pragma unroll
    for (int Gq = 0; Gq < 4; ++Gq) {
      v2u o; o.x = pk2(acc[4 * Gq], acc[4 * Gq + 1]); o.y = pk2(acc[4 * Gq + 2], acc[4 * Gq + 3]);
      *(LAS v2u*)(so + (((4 * w + Gq) ^ (n & 15)) << 4)) = o;
    }
    BAR_LGKM();
    const bool special = (m1 == 0);
    auto u_store = [&](int t2) {
      const int nn = t2 >> 5, ch = t2 & 31, ii = (nn >> 1) & 7, d0 = nn & 1;
      const int g0 = special ? 2 * ii : ii, g1 = special ? 2 * ii + 1 : 8 + ii;
      const v4u v0 = lds_ld16(I1 + nn * 512 + ((ch ^ (nn & 15)) << 4));
      const v4u v1 = lds_ld16(I1 + (nn + 16) * 512 + ((ch ^ (nn & 15)) << 4));
      const unsigned ub = WS_U + (unsigned)m1 * 16384u + (unsigned)(d0 * 512 + ch * 16);
      st16_sc1(sws, ub + (unsigned)g0 * 1024u, v0);
      st16_sc1(sws, ub + (unsigned)g1 * 1024u, v1);
    };
    if (w != 0) u_store(tid);
    if (w == 1) u_store(tid - 64);
    SCHED();
  }
  const int sz = (q4 >> 1) & 1;
  LAS char* ra0 = L + q * 4096 + (4 * hh + q4) * 128 + (((0 ^ (4 * sz)) + 2 * blk + (p4 >> 1)) << 4) + 8 * (p4 & 1);
  LAS char* ra1 = L + q * 4096 + (4 * hh + q4) * 128 + (((4 ^ (4 * sz)) + 2 * blk + (p4 >> 1)) << 4) + 8 * (p4 & 1);
  v4u wp[4][4];
  BAR_LGKM();
#pragma unroll
  for (int t = 0; t < 4; ++t) {
    LAS char* s0 = ra0 + t * 16384; LAS char* s1 = ra1 + t * 16384;
    wp[t][0] = tr_read2(s0, s0 + 1024); wp[t][1] = tr_read2(s1, s1 + 1024);
    wp[t][2] = tr_read2(s0 + 2048, s0 + 3072); wp[t][3] = tr_read2(s1 + 2048, s1 + 3072);
  }
#define W2_8_11() do { w2_issue1(G, 8, 0); w2_issue1(G, 8, 1); w2_issue1(G, 9, 0); w2_issue1(G, 9, 1); w2_issue1(G, 10, 0); w2_issue1(G, 10, 1); w2_issue1(G, 11, 0); w2_issue1(G, 11, 1); } while (0)
  if (isA) { BAR_LGKM(); W2_8_11(); }
  const unsigned flh = WS_FLH + (unsigned)((i * 2 + (jb >> 1)) * 16) * FS;
  if (w == 0) poll_words(sws, flh, 16, tag, lane);
  BAR_LGKM();
  STAMP(8);
  FT(26);
  const unsigned vh = (unsigned)lane * 16u;
  const unsigned hbase = WS_H + (unsigned)((i * 8 + 2 * jb + rh) * 112) * 1024u;
  const unsigned hb0 = uni(hbase + (G.fq0b >> 4)), hb1 = uni(hbase + (G.fq1b >> 4));
  v2u gg;
  v4u hf[14][2];
#define H_ISSUE(t) do { LD16I(hf[t][0], vh, sws, ((t) < 8 ? hb0 : hb1), (2 * ((t) & 7)) * 1024, "sc1"); LD16I(hf[t][1], vh, sws, ((t) < 8 ? hb0 : hb1), (2 * ((t) & 7) + 1) * 1024, "sc1"); } while (0)
  H_ISSUE(0); H_ISSUE(1);
  asm volatile("s_barrier" ::: "memory");
  H_ISSUE(2); H_ISSUE(3); H_ISSUE(4); H_ISSUE(5); H_ISSUE(6);
  LD8Z(gg, WS_GATE + (unsigned)(i * 256 + j) * 8u, sws, "sc1");
  SCHED();
  f32x16 accb[2] = {}, acca[2] = {};
  v4u wf[2][4];
  unsigned fl0, fl1;
  v4u ub0, ub1, ub2, ub3;
  const unsigned vu = WS_U + (unsigned)(((64 * q + 16 * rh + (lane >> 3)) * 8 + i) * 1024 + dt * 128 + ((lane & 7) ^ (4 * ((lane >> 4) & 1))) * 16);
#define G2_BAR(n, t) asm volatile("s_waitcnt vmcnt(" STR(n) ") lgkmcnt(0)\n\ts_barrier" : "+v"(hf[t][0]), "+v"(hf[t][1]) :: "memory")
#define G2_CORE(t, c, n, RD, acc, b0, b1, X0, X1, X2)                                             \
  {                                                                                                 \
    FT(30 + (t));                                                                                   \
    LAS char* s0 = ra0 + (((t) + 1) & 7) * 16384; LAS char* s1 = ra1 + (((t) + 1) & 7) * 16384;    \
    asm volatile("" : "+v"(c[0]), "+v"(c[1]), "+v"(c[2]), "+v"(c[3]));                             \
    asm volatile("" :: "v"(acc[0]), "v"(acc[1]));                                                   \
    SCHED();                                                                                        \
    acc[0] = mfma32(c[0], b0, acc[0]); if (RD) n[0] = tr_read2(s0, s0 + 1024); SCHED();            \
    acc[1] = mfma32(c[1], b0, acc[1]); if (RD) n[1] = tr_read2(s1, s1 + 1024); X0; SCHED();        \
    acc[0] = mfma32(c[2], b1, acc[0]); if (RD) n[2] = tr_read2(s0 + 2048, s0 + 3072); X1; SCHED(); \
    acc[1] = mfma32(c[3], b1, acc[1]); if (RD) n[3] = tr_read2(s1 + 2048, s1 + 3072); X2; SCHED(); \
  }
#define G2_PRE(t, X2) G2_CORE(t, wp[t], wf[0], (t) == 3, accb, hf[t][0], hf[t][1], if (!isA) w2_issue1(G, (t) + 8, 0), if (!isA) w2_issue1(G, (t) + 8, 1), X2)
#define G2_STEP(t, X0, X1, X2) G2_CORE(t, wf[(t) & 1], wf[((t) + 1) & 1], true, accb, hf[t][0], hf[t][1], X0, X1, X2)
  G2_BAR(13, 0);
  if (!isA && tid == 0) flag_store(p.ws, WS_FLB + (unsigned)m1 * FS, tag);
  STAMP(14);
  G2_PRE(0, H_ISSUE(7))
  G2_BAR(13, 1); G2_PRE(1, H_ISSUE(8))
  G2_BAR(13, 2); G2_PRE(2, H_ISSUE(9))
  G2_BAR(13, 3); G2_PRE(3, H_ISSUE(10))
  G2_BAR(13, 4); G2_STEP(4, w2_issue1(G, 12, 0), w2_issue1(G, 12, 1), H_ISSUE(11))
  G2_BAR(15, 5); G2_STEP(5, w2_issue1(G, 13, 0), w2_issue1(G, 13, 1), H_ISSUE(12))
  if (w == 0) { LD4Z(fl0, WS_FLB + (unsigned)lane * FS, sws, "sc1"); LD4Z(fl1, WS_FLB + (64u + (unsigned)lane) * FS, sws, "sc1"); }
  G2_BAR(17, 6); G2_STEP(6, NOP, NOP, H_ISSUE(13))
  STAMP(9);
  G2_BAR(16, 7); G2_STEP(7, NOP, NOP, NOP)
  LAS unsigned* verdict = (LAS unsigned*)(L + 128 * 1024);
  if (w == 0) {
    asm volatile("s_waitcnt vmcnt(2)" : "+v"(fl0), "+v"(fl1) :: "memory");
    const unsigned ok = __all(fl0 == tag && fl1 == tag) ? 1u : 0u;
    if (lane == 0) *verdict = ok;
  }
  G2_BAR(14, 8);
  const int u_early = __builtin_amdgcn_readfirstlane(*(volatile LAS unsigned*)verdict);
  STAMP(15);
#define U_ISSUE() do { LD16Z(ub0, vu, sws, "sc1"); LD16Z(ub1, vu + 8u * 8192u, sws, "sc1"); LD16Z(ub2, vu + 32u * 8192u, sws, "sc1"); LD16Z(ub3, vu + 40u * 8192u, sws, "sc1"); } while (0)
  if (u_early) U_ISSUE();
  GRot grot;
  G2_STEP(8, gen_g_init(G, grot), NOP, gen_g_frag<0>(G, grot, gf[0]))
  unsigned e0, e1, e2, e3;
  if (b == 0 && w == 1) {
    LD4Z(e0, WS_FLH + (unsigned)lane * FS, sws, "sc1"); LD4Z(e1, WS_FLH + (64u + (unsigned)lane) * FS, sws, "sc1");
    LD4Z(e2, WS_FLH + (128u + (unsigned)lane) * FS, sws, "sc1"); LD4Z(e3, WS_FLH + (192u + (unsigned)lane) * FS, sws, "sc1");
  }
  G2_BAR(12, 9); G2_STEP(9, NOP, NOP, gen_g_frag<1>(G, grot, gf[1]))
  G2_BAR(10, 10); G2_STEP(10, NOP, NOP, gen_g_frag<2>(G, grot, gf[2]))
  G2_BAR(6, 11); G2_STEP(11, NOP, NOP, gen_g_frag<3>(G, grot, gf[3]))
  G2_BAR(2, 12);
  G2_STEP(12, NOP, NOP, NOP)
#define U_PUT() do {                                                                                                        \
    asm volatile("s_waitcnt vmcnt(0)" : "+v"(ub0), "+v"(ub1), "+v"(ub2), "+v"(ub3) :: "memory");                             \
    *(LAS v4u*)(L + 6 * 16384 + ldsw + lane * 16) = ub0; *(LAS v4u*)(L + 6 * 16384 + ldsw + 1024 + lane * 16) = ub1;         \
    *(LAS v4u*)(L + 7 * 16384 + ldsw + lane * 16) = ub2; *(LAS v4u*)(L + 7 * 16384 + ldsw + 1024 + lane * 16) = ub3; } while (0)
  if (u_early) U_PUT();
  G2_BAR(0, 13);
  asm volatile("" : "+v"(gg));
  if (b == 0 && w == 1) {
    asm volatile("" : "+v"(e0), "+v"(e1), "+v"(e2), "+v"(e3));
    if (!__all(e0 == tag && e1 == tag && e2 == tag && e3 == tag)) {
#pragma unroll 1
      for (int u = 0; u < 4; ++u) poll_words(sws, WS_FLH + 64u * FS * u, 64, tag, lane);
    }
    if (lane == 0) flag_store(p.ws, WS_EPOCH, tag);
  }
  G2_CORE(13, wf[1], wf[0], u_early, accb, hf[13][0], hf[13][1], NOP, NOP, NOP)
  if (!u_early) {
    if (w == 0) { poll_words(sws, WS_FLB, 64, tag, lane); poll_words(sws, WS_FLB + 64u * FS, 64, tag, lane); }
    BAR_LGKM();
    U_ISSUE();
    U_PUT();
    BAR_LGKM();
    LAS char* s0 = ra0 + 6 * 16384; LAS char* s1 = ra1 + 6 * 16384;
    wf[0][0] = tr_read2(s0, s0 + 1024); wf[0][1] = tr_read2(s1, s1 + 1024); wf[0][2] = tr_read2(s0 + 2048, s0 + 3072); wf[0][3] = tr_read2(s1 + 2048, s1 + 3072);
  }
  STAMP(10);
  BAR_LGKM(); G2_CORE(14, wf[0], wf[1], true, acca, gf[0], gf[1], NOP, NOP, NOP)
  BAR_LGKM(); G2_CORE(15, wf[1], wf[0], false, acca, gf[2], gf[3], NOP, NOP, NOP)
  FT(50);
  {
    const float og = asf(gg.y), gs = asf(gg.x) * (1.f / 524288.f);
    const int row = 32 * rh + (lane & 31);
#pragma unroll
    for (int dh = 0; dh < 2; ++dh)
#pragma unroll
      for (int G = 0; G < 4; ++G) {
        f32x4 v;
#pragma unroll
        for (int e = 0; e < 4; ++e) v[e] = __builtin_fmaf(og, accb[dh][4 * G + e], gs * acca[dh][4 * G + e]);
        *(LAS f32x4*)(L + q * 16384 + row * 256 + (((8 * dh + 2 * G + hh) ^ (row & 7)) << 4)) = v;
      }
  }
  BAR_LGKM();
  FT(51);
  {
    const int row = tid >> 3, c8 = tid & 7;
    f32x4 s0 = {0.f, 0.f, 0.f, 0.f}, s1 = s0;
#pragma unroll
    for (int qq = 0; qq < 4; ++qq) {
      s0 += *(LAS f32x4*)(L + qq * 16384 + row * 256 + (((2 * c8) ^ (row & 7)) << 4));
      s1 += *(LAS f32x4*)(L + qq * 16384 + row * 256 + (((2 * c8 + 1) ^ (row & 7)) << 4));
    }
    v4u o; o.x = pk2(s0[0], s0[1]); o.y = pk2(s0[2], s0[3]); o.z = pk2(s1[0], s1[1]); o.w = pk2(s1[2], s1[3]);
    *(GAS v4u*)((GAS char*)p.out + (size_t)(i + 8 * (64 * jb + row)) * 1024 + dt * 128 + c8 * 16) = o;
  }
  STAMP(11);
}

}

extern "C" void kernel_launch(void* const* d_in, const int* in_sizes, int n_in, void* d_out, int out_size, void* d_ws, size_t ws_size,
                              hipStream_t stream) {
  Params p;
  p.x = d_in[0]; p.wr = d_in[1]; p.wi = d_in[2]; p.w1 = d_in[4]; p.w2 = d_in[5]; p.gw = d_in[6];
  p.out = d_out; p.ws = d_ws;
  (void)hipFuncSetAttribute((const void*)gm_kernel, hipFuncAttributeMaxDynamicSharedMemorySize, LDS_BYTES);
  gm_kernel<<<256, 512, LDS_BYTES, stream>>>(p);
}
```

```cpp
#include <hip/hip_runtime.h>
#include <hip/hip_bf16.h>
#include <stdint.h>

namespace {

typedef unsigned v4u __attribute__((ext_vector_type(4)));
typedef unsigned v2u __attribute__((ext_vector_type(2)));
typedef float f32x16 __attribute__((ext_vector_type(16)));
typedef float f32x4 __attribute__((ext_vector_type(4)));
typedef float f32x2 __attribute__((ext_vector_type(2)));
typedef __bf16 bf16x8 __attribute__((ext_vector_type(8)));
typedef __bf16 bf16x2 __attribute__((ext_vector_type(2)));
typedef short s16x4 __attribute__((ext_vector_type(4)));
#define LAS __attribute__((address_space(3)))
#define GAS __attribute__((address_space(1)))
#define DI __device__ __forceinline__

constexpr int NS = 2048, ND = 512, NF = 1792;
constexpr unsigned WS_EPOCH = 0;
constexpr unsigned FS = 64;
constexpr unsigned WS_FLA = 4096;
constexpr unsigned WS_FLH = 16384;
constexpr unsigned WS_FLB = 32768;
constexpr unsigned WS_GATE = 65536;
constexpr unsigned WS_T = 1u << 20;
constexpr unsigned WS_U = 3u << 20;
constexpr unsigned WS_H = 5u << 20;
constexpr unsigned H_BYTES = 8u * 8u * 112u * 1024u;
constexpr int LDS_BYTES = 160 * 1024;
constexpr unsigned kSpinCap = 1u << 20;

struct Params {
  const void* x; const void* wr; const void* wi; const void* w1; const void* w2; const void* gw;
  void* out; void* ws;
};

constexpr double kPi = 3.14159265358979323846;
constexpr double ccos(double x) { double t = 1, s = 1; for (int n = 1; n < 16; ++n) { t *= -x * x / ((2 * n - 1) * (2 * n)); s += t; } return s; }
constexpr double cosn(int e, int n) { e = ((e % n) + n) % n; double x = 2 * kPi * e / n; if (x > kPi) x -= 2 * kPi; return ccos(x); }
constexpr double wre(int e, int n) { return cosn(e, n); }
constexpr double wim(int e, int n) { return -cosn(4 * e - n, 4 * n); }
constexpr unsigned bfb(double v) { float f = (float)v; unsigned u = __builtin_bit_cast(unsigned, f); u += 0x7fffu + ((u >> 16) & 1u); return u >> 16; }
constexpr unsigned bf2(double a, double b) { return bfb(a) | (bfb(b) << 16); }
constexpr unsigned fbits(double v) { return __builtin_bit_cast(unsigned, (float)v); }
struct Tab { unsigned v[16][64][4]; };
constexpr Tab make_tab() {
  Tab t{};
  for (int l = 0; l < 64; ++l) {
    const int g = l >> 4, l15 = l & 15, hh = l >> 5, n = l & 31;
    for (int mtx = 0; mtx < 3; ++mtx) {
      double r[4] = {}, im[4] = {};
      for (int q = 0; q < 4; ++q) {
        const int a = 4 * g + q;
        if (mtx == 0) { r[q] = wre(l15 * a, 16); im[q] = wim(l15 * a, 16); }
        else if (mtx == 1) { r[q] = wre(a * (2 * l15 + 1), 32); im[q] = wim(a * (2 * l15 + 1), 32); }
        else { r[q] = wre(l15 * (2 * a + 1), 32); im[q] = -wim(l15 * (2 * a + 1), 32); }
      }
      t.v[2 * mtx][l][0] = bf2(r[0], r[1]); t.v[2 * mtx][l][1] = bf2(r[2], r[3]); t.v[2 * mtx][l][2] = bf2(-im[0], -im[1]); t.v[2 * mtx][l][3] = bf2(-im[2], -im[3]);
      t.v[2 * mtx + 1][l][0] = bf2(im[0], im[1]); t.v[2 * mtx + 1][l][1] = bf2(im[2], im[3]); t.v[2 * mtx + 1][l][2] = bf2(r[0], r[1]); t.v[2 * mtx + 1][l][3] = bf2(r[2], r[3]);
    }
    {
      const int m2 = n >> 2, pp = (n >> 1) & 1, cp = n & 1;
      double v0[8] = {}, v1[8] = {};
      for (int jj = 0; jj < 8; ++jj) {
        const int ii = (8 * hh + jj) >> 1, d0 = jj & 1;
        const double sg = (d0 & pp) ? -1.0 : 1.0, orr = sg * wre(ii * m2, 8), oi = sg * wim(ii * m2, 8);
        v0[jj] = cp ? oi : orr; v1[jj] = cp ? orr : -oi;
      }
      for (int q = 0; q < 4; ++q) { t.v[6][l][q] = bf2(v0[2 * q], v0[2 * q + 1]); t.v[7][l][q] = bf2(v1[2 * q], v1[2 * q + 1]); }
    }
    {
      const int cq = n >> 4, ii = (n >> 1) & 7, d0 = n & 1;
      for (int s = 0; s < 2; ++s) {
        double v[8] = {};
        for (int jj = 0; jj < 8; ++jj) {
          const int m2k = 4 * s + 2 * hh + (jj >> 2), pk = (jj >> 1) & 1, ck = jj & 1;
          const double sg = (d0 & pk) ? -1.0 : 1.0, orr = sg * wre(ii * m2k, 8), oi = -sg * wim(ii * m2k, 8);
          v[jj] = cq ? (ck ? orr : oi) : (ck ? -oi : orr);
        }
        for (int q = 0; q < 4; ++q) t.v[8 + s][l][q] = bf2(v[2 * q], v[2 * q + 1]);
      }
    }
    for (int q = 0; q < 4; ++q) {
      const int a = 4 * g + q;
      t.v[10][l][q] = fbits(wre(a * (2 * l15), 512));      t.v[11][l][q] = fbits(wim(a * (2 * l15), 512));
      t.v[12][l][q] = fbits(wre(a * (2 * l15 + 1), 512));  t.v[13][l][q] = fbits(wim(a * (2 * l15 + 1), 512));
      t.v[14][l][q] = fbits(wre(l15 * (2 * a + 1), 512));  t.v[15][l][q] = fbits(wim(l15 * (2 * a + 1), 512));
    }
  }
  return t;
}
__device__ const Tab kTab = make_tab();

constexpr unsigned STAMP_OFF = 512 * 1024;
#define STAMP(k) do { if ((k) == 0) { if (true) asm volatile("s_memrealtime %0\n\ts_waitcnt lgkmcnt(0)" : "=s"(pad_t) :: "memory"); } } while (0)
#define FT(k) do {} while (0)

DI v4u make_srd(const void* p, unsigned bytes) {
  const unsigned long long a = (unsigned long long)p;
  v4u r; r.x = (unsigned)a; r.y = (unsigned)(a >> 32) & 0xffffu; r.z = bytes; r.w = 0x00020000u;
  return r;
}
template <unsigned LDSI, unsigned SOI> DI void dma16(v4u srd, unsigned voff, unsigned ldsb, unsigned sob) {
  unsigned tmp;
  asm volatile("s_add_u32 m0, %3, %5\n\ts_add_u32 %0, %4, %6\n\tbuffer_load_dwordx4 %1, %2, %0 offen lds"
               : "=&s"(tmp) : "v"(voff), "s"(srd), "s"(ldsb), "s"(sob), "i"(LDSI), "i"(SOI) : "memory", "scc");
}
template <unsigned LDSI, unsigned SOI> DI void dma16z(v4u srd, unsigned voff, unsigned ldsb) {
  unsigned tmp;
  asm volatile("s_add_u32 m0, %3, %4\n\ts_mov_b32 %0, %5\n\tbuffer_load_dwordx4 %1, %2, %0 offen lds"
               : "=&s"(tmp) : "v"(voff), "s"(srd), "s"(ldsb), "i"(LDSI), "i"(SOI) : "memory", "scc");
}
DI unsigned uni(unsigned v) { return (unsigned)__builtin_amdgcn_readfirstlane((int)v); }
#define LD16(dst, voffv, srd, soffv, flags) asm volatile("buffer_load_dwordx4 %0, %1, %2, %3 offen " flags : "=v"(dst) : "v"(voffv), "s"(srd), "s"(soffv) : "memory")
#define LD16I(dst, voffv, srd, sob, soi, flags) do { unsigned tmp_; asm volatile("s_add_u32 %1, %4, %5\n\tbuffer_load_dwordx4 %0, %2, %3, %1 offen " flags : "=v"(dst), "=&s"(tmp_) : "v"(voffv), "s"(srd), "s"(sob), "i"(soi) : "memory", "scc"); } while (0)
#define LD16Z(dst, voffv, srd, flags) asm volatile("buffer_load_dwordx4 %0, %1, %2, 0 offen " flags : "=v"(dst) : "v"(voffv), "s"(srd) : "memory")
#define LD4Z(dst, voffv, srd, flags) asm volatile("buffer_load_dword %0, %1, %2, 0 offen " flags : "=v"(dst) : "v"(voffv), "s"(srd) : "memory")
#define LD2Z(dst, voffv, srd, flags) asm volatile("buffer_load_ushort %0, %1, %2, 0 offen " flags : "=v"(dst) : "v"(voffv), "s"(srd) : "memory")
#define LD8Z(dst, voffv, srd, flags) asm volatile("buffer_load_dwordx2 %0, %1, %2, 0 offen " flags : "=v"(dst) : "v"(voffv), "s"(srd) : "memory")
#define LD8(dst, voffv, srd, soffv, flags) asm volatile("buffer_load_dwordx2 %0, %1, %2, %3 offen " flags : "=v"(dst) : "v"(voffv), "s"(srd), "s"(soffv) : "memory")
#define LD4(dst, voffv, srd, soffv, flags) asm volatile("buffer_load_dword %0, %1, %2, %3 offen " flags : "=v"(dst) : "v"(voffv), "s"(srd), "s"(soffv) : "memory")
#define LD2(dst, voffv, srd, soffv, flags) asm volatile("buffer_load_ushort %0, %1, %2, %3 offen " flags : "=v"(dst) : "v"(voffv), "s"(srd), "s"(soffv) : "memory")
#define XSTR(x) #x
#define STR(x) XSTR(x)
#define BAR_VM(n) asm volatile("s_waitcnt vmcnt(" STR(n) ") lgkmcnt(0)\n\ts_barrier" ::: "memory")
#define BAR_LGKM() asm volatile("s_waitcnt lgkmcnt(0)\n\ts_barrier" ::: "memory")
#define SCHED() __builtin_amdgcn_sched_barrier(0)
template <int N> DI void bar_vm() { asm volatile("s_waitcnt vmcnt(%0) lgkmcnt(0)\n\ts_barrier" :: "i"(N) : "memory"); }
template <int N> DI void wait_vm() { asm volatile("s_waitcnt vmcnt(%0)" :: "i"(N) : "memory"); }

DI unsigned pk2(float a, float b) { f32x2 p = {a, b}; return __builtin_bit_cast(unsigned, __builtin_convertvector(p, bf16x2)); }
DI float bflo(unsigned u) { return __builtin_bit_cast(float, u << 16); }
DI float bfhi(unsigned u) { return __builtin_bit_cast(float, u & 0xffff0000u); }
DI float asf(unsigned u) { return __builtin_bit_cast(float, u); }
DI f32x16 mfma32(v4u a, v4u b, f32x16 c) {
  return __builtin_amdgcn_mfma_f32_32x32x16_bf16(__builtin_bit_cast(bf16x8, a), __builtin_bit_cast(bf16x8, b), c, 0, 0, 0);
}
DI f32x4 mfma16(v4u a, v4u b, f32x4 c) {
  return __builtin_amdgcn_mfma_f32_16x16x32_bf16(__builtin_bit_cast(bf16x8, a), __builtin_bit_cast(bf16x8, b), c, 0, 0, 0);
}
DI v2u tr_read(LAS char* p) { return __builtin_bit_cast(v2u, __builtin_amdgcn_ds_read_tr16_b64_v4i16((LAS s16x4*)p)); }
DI v4u tr_read2(LAS char* p0, LAS char* p1) { v2u a = tr_read(p0), b = tr_read(p1); v4u r; r.x = a.x; r.y = a.y; r.z = b.x; r.w = b.y; return r; }
DI v4u lds_ld16(LAS char* p) { return *(LAS v4u*)p; }
DI float cosrev(float r) { return __builtin_amdgcn_cosf(r); }
DI float sinrev(float r) { return __builtin_amdgcn_sinf(r); }
DI __amdgpu_buffer_rsrc_t as_rsrc(v4u srd) { return __builtin_amdgcn_make_buffer_rsrc((void*)(((unsigned long long)srd.y << 32) | srd.x), 0, srd.z, 0x00020000); }
DI void st16_sc1(v4u srd, unsigned voff, v4u v) { __builtin_amdgcn_raw_buffer_store_b128(v, as_rsrc(srd), voff, 0, 16); }
DI void flag_store(void* ws, unsigned off, unsigned tag) { __hip_atomic_store((GAS unsigned*)((GAS char*)ws + off), tag, __ATOMIC_RELAXED, __HIP_MEMORY_SCOPE_AGENT); }
template <int CTRL, int RMASK> DI float dpp_add(float v) { return v + asf((unsigned)__builtin_amdgcn_update_dpp(0, (int)__builtin_bit_cast(unsigned, v), CTRL, RMASK, 0xf, true)); }
DI void poll_words(v4u sws, unsigned off, int n, unsigned tag, int lane) {
  const unsigned vo = off + FS * (unsigned)(lane < n ? lane : 0);
#pragma unroll 1
  for (unsigned spins = 0; spins < kSpinCap; ++spins) {
    unsigned f;
    asm volatile("buffer_load_dword %0, %1, %2, 0 offen sc1\n\ts_waitcnt vmcnt(0)" : "=&v"(f) : "v"(vo), "s"(sws) : "memory");
    if (__all(f == tag)) return;
    __builtin_amdgcn_s_sleep(4);
  }
}

constexpr unsigned A_XIMG = 64 * 1024, A_FIMG = 128 * 1024;
DI void phaseA_genF(LAS char* L, int i, int mt, int w, int lane) {
  const int mh = w >> 2, sg = w & 3, n = lane & 31, hh = lane >> 5;
  const int m1c = 64 * mt + 32 * mh + n, m1 = m1c >> 1, c = m1c & 1;
  const int j0 = 64 * sg + 32 * hh;
  const float ph = c ? 0.25f : 0.f;
  const float k2 = 2.f * cosrev((float)(8 * m1) * (1.f / 2048.f));
  float vm = cosrev((float)(((i + 8 * (j0 - 1)) * m1) & 2047) * (1.f / 2048.f) + ph);
  float v0 = cosrev((float)(((i + 8 * j0) * m1) & 2047) * (1.f / 2048.f) + ph);
  const unsigned cst = c ? 0x3F800000u : 0x00003F80u;
  const bool sp = m1c < 2;
#pragma unroll
  for (int s = 0; s < 4; ++s) {
    float e[8];
#pragma unroll
    for (int q = 0; q < 8; ++q) { e[q] = v0; const float vn = __builtin_fmaf(k2, v0, -vm); vm = v0; v0 = vn; }
    v4u f; f.x = pk2(e[0], e[1]); f.y = pk2(e[2], e[3]); f.z = pk2(e[4], e[5]); f.w = pk2(e[6], e[7]);
    if (mt == 0 && w < 4) { f.x = sp ? cst : f.x; f.y = sp ? cst : f.y; f.z = sp ? cst : f.z; f.w = sp ? cst : f.w; }
    *(LAS v4u*)(L + A_FIMG + (mh * 16 + sg * 4 + s) * 1024 + lane * 16) = f;
  }
}

struct Frag1 { v4u a[4], b0[4], b1[4]; };
DI void g1_read(Frag1& f, LAS char* wa, LAS char* xb0, LAS char* xb1, LAS char* xb2, LAS char* xb3, int slot) {
  const int so = slot * 32768;
  f.a[0] = tr_read2(wa + so, wa + so + 1024);              f.b0[0] = lds_ld16(xb0 + so); f.b1[0] = lds_ld16(xb0 + so + 4096);
  f.a[1] = tr_read2(wa + so + 4096, wa + so + 5120);       f.b0[1] = lds_ld16(xb1 + so); f.b1[1] = lds_ld16(xb1 + so + 4096);
  f.a[2] = tr_read2(wa + so + 8192, wa + so + 9216);       f.b0[2] = lds_ld16(xb2 + so); f.b1[2] = lds_ld16(xb2 + so + 4096);
  f.a[3] = tr_read2(wa + so + 12288, wa + so + 13312);     f.b0[3] = lds_ld16(xb3 + so); f.b1[3] = lds_ld16(xb3 + so + 4096);
}
DI unsigned silu2(float a, float b) {
  const f32x2 h = {a, b}, t = h * -1.44269504088896341f;
  f32x2 e = {__builtin_amdgcn_exp2f(t[0]), __builtin_amdgcn_exp2f(t[1])};
  e = e + 1.f;
  const f32x2 r = {__builtin_amdgcn_rcpf(e[0]), __builtin_amdgcn_rcpf(e[1])};
  return __builtin_bit_cast(unsigned, __builtin_convertvector(h * r, bf16x2));
}

DI f32x2 lo2(const f32x4& v) { return __builtin_shufflevector(v, v, 0, 1); }
DI f32x2 hi2(const f32x4& v) { return __builtin_shufflevector(v, v, 2, 3); }
DI unsigned pk2v(f32x2 p) { return __builtin_bit_cast(unsigned, __builtin_convertvector(p, bf16x2)); }
DI f32x4 asf4(v4u v) { return __builtin_bit_cast(f32x4, v); }
template <bool CONJ> DI v4u cmul_pack(const f32x4& re, const f32x4& im, const f32x4& tr, const f32x4& ti) {
  const f32x2 r0 = lo2(re), r1 = hi2(re), i0 = lo2(im), i1 = hi2(im), c0 = lo2(tr), c1 = hi2(tr), s0 = lo2(ti), s1 = hi2(ti);
  f32x2 a0, a1, b0, b1;
  if (CONJ) {
    a0 = __builtin_elementwise_fma(r0, c0, i0 * s0); a1 = __builtin_elementwise_fma(r1, c1, i1 * s1);
    b0 = __builtin_elementwise_fma(i0, c0, -(r0 * s0)); b1 = __builtin_elementwise_fma(i1, c1, -(r1 * s1));
  } else {
    a0 = __builtin_elementwise_fma(r0, c0, -(i0 * s0)); a1 = __builtin_elementwise_fma(r1, c1, -(i1 * s1));
    b0 = __builtin_elementwise_fma(r0, s0, i0 * c0); b1 = __builtin_elementwise_fma(r1, s1, i1 * c1);
  }
  v4u r; r.x = pk2v(a0); r.y = pk2v(a1); r.z = pk2v(b0); r.w = pk2v(b1); return r;
}
struct cf { float r, i; };
DI cf cmul(cf a, cf b) { return {a.r * b.r - a.i * b.i, a.r * b.i + a.i * b.r}; }
DI cf cmulc(cf a, cf b) { return {a.r * b.r + a.i * b.i, a.i * b.r - a.r * b.i}; }
DI unsigned w_src(int m, int lane) {
  const int mm = (NS - m) & (NS - 1);
  const int e = lane < 32 ? m * 257 - (m & 1) : mm * 257 + (m & 1);
  return (unsigned)(e * 2 + (lane & 31) * 16);
}
DI unsigned w_tail_src(int m, int lane) {
  const int mm = (NS - m) & (NS - 1), l = lane & 3;
  return (unsigned)((l < 2 ? m * 257 + 255 + l : mm * 257 + (l - 2) * 256) * 2);
}
struct WTail { unsigned r255, r256, rm0, rm256, i255, i256, im0, im256; };
DI WTail w_tail(unsigned tr, unsigned ti) {
  return {(unsigned)__builtin_amdgcn_readlane((int)tr, 0), (unsigned)__builtin_amdgcn_readlane((int)tr, 1), (unsigned)__builtin_amdgcn_readlane((int)tr, 2), (unsigned)__builtin_amdgcn_readlane((int)tr, 3),
          (unsigned)__builtin_amdgcn_readlane((int)ti, 0), (unsigned)__builtin_amdgcn_readlane((int)ti, 1), (unsigned)__builtin_amdgcn_readlane((int)ti, 2), (unsigned)__builtin_amdgcn_readlane((int)ti, 3)};
}
struct WVal { f32x4 r0, i0, r1, i1; };
struct WRawL { unsigned r[4][2], i[4][2]; };
DI void w_put(LAS char* scr, v4u qr, v4u qi, int lane) { *(LAS v4u*)(scr + lane * 16) = qr; *(LAS v4u*)(scr + 1024 + lane * 16) = qi; }
DI int w_off(int m, int lane) { const int k1 = lane & 15, g = lane >> 4, odd = m & 1; return lane < 32 ? (2 * k1 + 128 * g + odd) * 2 : 512 + (511 - 2 * k1 - 128 * g - odd) * 2; }
DI void w_get(WRawL& o, LAS char* scr, int off, int lane, int r) {
  LAS unsigned* a = (LAS unsigned*)(scr + ((off + (lane < 32 ? 64 : -64) * r) & ~3));
  o.r[r][0] = a[0]; o.r[r][1] = a[1]; o.i[r][0] = a[256]; o.i[r][1] = a[257];
}
DI WVal w_unpack(const WRawL& x, const WTail& t, int off, int m, int lane) {
  const bool lo = lane < 32;
  const unsigned sh = (off & 2) * 8;
  unsigned rr[4], ii[4];
#pragma unroll
  for (int r = 0; r < 4; ++r) { rr[r] = __builtin_amdgcn_alignbit(x.r[r][1], x.r[r][0], sh); ii[r] = __builtin_amdgcn_alignbit(x.i[r][1], x.i[r][0], sh); }
  if (m & 1) { rr[3] = lane == 31 ? (rr[3] & 0xffffu) | (t.r255 << 16) : rr[3]; ii[3] = lane == 31 ? (ii[3] & 0xffffu) | (t.i255 << 16) : ii[3]; }
  else { rr[0] = lane == 32 ? (rr[0] & 0xffffu) | (t.rm256 << 16) : rr[0]; ii[0] = lane == 32 ? (ii[0] & 0xffffu) | (t.im256 << 16) : ii[0]; }
  const unsigned s0 = lo ? 16u : 0u, s1 = lo ? 0u : 16u, sg = lo ? 0u : 0x80000000u;
  WVal o;
#pragma unroll
  for (int r = 0; r < 4; ++r) {
    o.r0[r] = asf((rr[r] << s0) & 0xffff0000u); o.r1[r] = asf((rr[r] << s1) & 0xffff0000u);
    o.i0[r] = asf(((ii[r] << s0) & 0xffff0000u) ^ sg); o.i1[r] = asf(((ii[r] << s1) & 0xffff0000u) ^ sg);
  }
  const bool av = (lane & 31) == 0;
  const float xr = bflo(lo ? t.rm0 : t.r256), xi = asf(((lo ? t.im0 : t.i256) << 16) ^ (sg ^ 0x80000000u));
  o.r0[0] = av ? 0.5f * (o.r0[0] + xr) : o.r0[0];
  o.i0[0] = av ? 0.5f * (o.i0[0] + xi) : o.i0[0];
  return o;
}

DI void packed_coef(WVal& a, WVal& b, int m2) {
  const cf om = {cosrev((float)m2 * 0.0625f), -sinrev((float)m2 * 0.0625f)};
#pragma unroll
  for (int pp = 0; pp < 2; ++pp)
#pragma unroll
    for (int r = 0; r < 4; ++r) {
      const float war = pp ? a.r1[r] : a.r0[r], wai = pp ? a.i1[r] : a.i0[r], wbr = pp ? b.r1[r] : b.r0[r], wbi = pp ? b.i1[r] : b.i0[r];
      const cf sp = {war + wbr, wai + wbi}, sm = {war - wbr, wai - wbi};
      const cf t1 = cmulc(sm, om), t2 = cmul(sm, om);
      const cf cE = {sp.r - t1.i, sp.i + t1.r}, cO = {t2.r - sp.i, t2.i + sp.r};
      const float alr = 0.25f * (cE.r + cO.i), ali = 0.25f * (cE.i - cO.r), ber = 0.25f * (cE.r - cO.i), bei = 0.25f * (cE.i + cO.r);
      if (pp) { a.r1[r] = alr; a.i1[r] = ali; b.r1[r] = ber; b.i1[r] = bei; } else { a.r0[r] = alr; a.i0[r] = ali; b.r0[r] = ber; b.i0[r] = bei; }
    }
}

struct Geo {
  int tid, lane, w, b, i, i1, at, m1, rb, fc, hh, blk, q4, p4;
  int jb, dt, rh, q, j;
  v4u sx, sws, sw2;
  unsigned vw20, vw21, ldsw, fq0b, fq1b;
};
template <int T, int U> DI void w2_dma(const v4u& sw2, unsigned vw20, unsigned vw21, unsigned ldsw, unsigned fq0, unsigned fq1) {
  dma16<(T & 7) * 16384u + U * 1024u, (T < 8 ? T : T - 8) * 32768u>(sw2, U ? vw21 : vw20, ldsw, T < 8 ? fq0 : fq1);
}
#define w2_issue1(g, t, u) (w2_dma<t, u>((g).sw2, (g).vw20, (g).vw21, (g).ldsw, (g).fq0b, (g).fq1b))
struct GRot { float c1, s1, c4, s4, c, s; };
DI void gen_g_init(const Geo& g, GRot& r) {
  const int srow = g.i + 8 * g.j;
  const int mb = 32 * g.q + 2 * g.hh;
  const float r1 = (float)(srow & 2047) * (1.f / 2048.f), r4 = (float)((4 * srow) & 2047) * (1.f / 2048.f), r0 = (float)((mb * srow) & 2047) * (1.f / 2048.f);
  r.c1 = cosrev(r1); r.s1 = sinrev(r1); r.c4 = cosrev(r4); r.s4 = sinrev(r4); r.c = cosrev(r0); r.s = sinrev(r0);
}
template <int S> DI void gen_g_frag(const Geo& g, GRot& r, v4u& f) {
#pragma unroll
  for (int h = 0; h < 2; ++h) {
    const float cb = __builtin_fmaf(r.c, r.c1, -(r.s * r.s1)), sb = __builtin_fmaf(r.s, r.c1, r.c * r.s1);
    unsigned w0 = pk2(r.c, -r.s), w1 = pk2(cb, -sb);
    if (S == 0 && h == 0) w0 = (g.q == 0 && g.hh == 0) ? ((g.j & 1) ? 0x3F800000u : 0x00003F80u) : w0;
    if (h) { f.z = w0; f.w = w1; } else { f.x = w0; f.y = w1; }
    const float cn = __builtin_fmaf(r.c, r.c4, -(r.s * r.s4)), sn = __builtin_fmaf(r.s, r.c4, r.c * r.s4);
    r.c = cn; r.s = sn;
  }
  asm volatile("" : "+v"(f));
}
template <int N0, int N1, int NR> DI void bar_w(int w) { if (w == 0) bar_vm<N0>(); else if (w == 1) bar_vm<N1>(); else bar_vm<NR>(); }
#define NOP do {} while (0)

#define TLBW 0
#define TOUCH() do {                                                                                                      \
    const int gl = G.b * 256 + (w & 3) * 64 + lane;                                                                         \
    const char* base_ = gl < 14336 ? (const char*)p.w2 : gl < 28672 ? (const char*)p.w1 - 14336 * 128 : gl < 36896 ? (const char*)p.wr - 28672 * 128           \
                      : gl < 45120 ? (const char*)p.wi - 36896 * 128 : (const char*)p.x - 45120 * 128;                     \
    const char* ad_ = base_ + (size_t)(gl < 61504 ? gl : 45120) * 128;                                                      \
    if (w >= 4) ad_ = TLBW ? (w == 7 ? (const char*)p.out + lane * 32768 : (const char*)p.ws + (size_t)((w - 4) * 64 + lane + 8) * 65536 + 32768 - 128) : (const char*)p.x; \
    asm volatile("global_load_dword %0, %1, off" : "=v"(touch) : "v"(ad_) : "memory");                                     \
  } while (0)
template <bool ISA>
DI unsigned phase1(long long& pad_t, const Params& p, LAS char* L, const Geo& G, v4u& m5a, v4u& m5b
) {
  const int tid = G.tid, lane = G.lane, w = G.w, i = G.i, i1 = G.i1, rb = G.rb, fc = G.fc, hh = G.hh, blk = G.blk, q4 = G.q4, p4 = G.p4;
  const v4u sx = G.sx, sws = G.sws, sw1 = make_srd(p.w1, ND * NF * 2);
  unsigned touch;
  unsigned ep; v4u gx, gv;
  const int jg = 128 * rb + 8 * fc + w;
  LD16Z(gx, (unsigned)(lane * 16 + (i1 + 8 * jg) * 1024), sx, "");
  LD16Z(gv, (unsigned)lane * 16u, make_srd(p.gw, ND * 2), "");
  LD4Z(ep, WS_EPOCH, sws, "sc1");
  const int f0 = ISA ? 96 * fc : 768 + 128 * (fc - 8), nf = ISA ? 96 : 128;
  const int mt = G.at >> 2, dt4 = G.at & 3;
  const int m1 = G.m1;
  const v4u swr = make_srd(p.wr, NS * 257 * 2), swi = make_srd(p.wi, NS * 257 * 2);
  const int mW = m1 == 0 ? 128 * w : m1 + 256 * w;
  v4u wqr, wqi, xqr, xqi;
  unsigned wtr, wti, xtr, xti;
  WVal Wc, Wd;
  if (ISA) {
    const unsigned voff = (unsigned)(lane >> 4) * 8192u + (unsigned)(((lane & 15) ^ ((lane >> 4) << 2)) << 4);
    const unsigned xl = uni(A_XIMG + (unsigned)w * 8192u), xs = uni((unsigned)i * 1024u + (unsigned)dt4 * 256u + (unsigned)w * 262144u);
    dma16<0, 0>(sx, voff, xl, xs); dma16<1024, 32768>(sx, voff, xl, xs); dma16<2048, 65536>(sx, voff, xl, xs); dma16<3072, 98304>(sx, voff, xl, xs);
    dma16<4096, 131072>(sx, voff, xl, xs); dma16<5120, 163840>(sx, voff, xl, xs); dma16<6144, 196608>(sx, voff, xl, xs); dma16<7168, 229376>(sx, voff, xl, xs);
  }
  unsigned vx0, vx1, vw0, vw1;
  {
    const int r0 = 16 * w + (lane >> 3), pos = lane & 7, r1 = r0 + 8;
    vx0 = (unsigned)(i1 * 1024 + (128 * rb + r0) * 8192 + ((pos ^ ((r0 >> 1) & 7)) << 4));
    vx1 = (unsigned)(i1 * 1024 + (128 * rb + r1) * 8192 + ((pos ^ ((r1 >> 1) & 7)) << 4));
    const int k0 = 8 * w + (lane >> 4), chunk = (lane & 15) ^ ((lane >> 4) << 2);
    const bool ok = chunk * 8 < nf;
    vw0 = ok ? (unsigned)(k0 * 3584 + f0 * 2 + chunk * 16) : 0x80000000u;
    vw1 = ok ? vw0 + 4u * 3584u : 0x80000000u;
  }
  const unsigned g1l = uni((unsigned)w * 2048u);
#define g1_dma(kt, u) do {                                                                                         \
    if ((u) == 0) dma16z<((kt) & 3) * 32768u, (kt) * 128u>(sx, vx0, g1l);                                           \
    if ((u) == 1) dma16z<((kt) & 3) * 32768u + 1024u, (kt) * 128u>(sx, vx1, g1l);                                   \
    if ((u) == 2) dma16z<((kt) & 3) * 32768u + 16384u, (kt) * (64u * 3584u)>(sw1, vw0, g1l);                        \
    if ((u) == 3) dma16z<((kt) & 3) * 32768u + 17408u, (kt) * (64u * 3584u)>(sw1, vw1, g1l); } while (0)
#define g1_issue(kt) do { g1_dma(kt, 0); g1_dma(kt, 1); g1_dma(kt, 2); g1_dma(kt, 3); } while (0)
  if (ISA) {
    phaseA_genF(L, i, mt, w, lane);
    g1_issue(0); g1_issue(1);
  } else {
    g1_issue(0);
    if (m1 == 0) {
      const unsigned ws2 = w_src(128 * w + 1024, lane), wt2 = w_tail_src(128 * w + 1024, lane);
      LD16Z(xqr, ws2, swr, ""); LD16Z(xqi, ws2, swi, ""); LD2Z(xtr, wt2, swr, ""); LD2Z(xti, wt2, swi, "");
    }
    { const unsigned wsrc = w_src(mW, lane), wts = w_tail_src(mW, lane);
      LD16Z(wqr, wsrc, swr, ""); LD16Z(wqi, wsrc, swi, ""); LD2Z(wtr, wts, swr, ""); LD2Z(wti, wts, swi, ""); }
    asm volatile("s_barrier" ::: "memory");
    g1_issue(1); asm volatile("s_barrier" ::: "memory");
    g1_issue(2); asm volatile("s_barrier" ::: "memory");
    g1_issue(3);
  }
  TOUCH();
  if (ISA) wait_vm<17>(); else wait_vm<21>();
  asm volatile("" : "+v"(ep), "+v"(gx), "+v"(gv) :: "memory");
  const unsigned tag = (unsigned)__builtin_amdgcn_readfirstlane((int)ep) + 1u;
  float gate_g, gate_og;
  {
    float z = 0.f;
#pragma unroll
    for (int q = 0; q < 4; ++q) {
      const unsigned pr = pk2(bflo(gx[q]) * bflo(gv[q]), bfhi(gx[q]) * bfhi(gv[q]));
      z += bflo(pr) + bfhi(pr);
    }
    z = dpp_add<0x111, 0xf>(z); z = dpp_add<0x112, 0xf>(z); z = dpp_add<0x114, 0xf>(z); z = dpp_add<0x118, 0xf>(z);
    z = dpp_add<0x142, 0xa>(z); z = dpp_add<0x143, 0xc>(z);
    const float zb = bflo(pk2(z, 0.f));
    const float g = bflo(pk2(__builtin_amdgcn_rcpf(1.f + __builtin_amdgcn_exp2f(zb * -1.44269504088896341f)), 0.f));
    gate_g = asf((unsigned)__builtin_amdgcn_readlane((int)__builtin_bit_cast(unsigned, g), 63));
    gate_og = bflo(pk2(1.f - gate_g, 0.f));
  }
  if (!ISA) {
    asm volatile("s_waitcnt vmcnt(13)" : "+v"(wqr), "+v"(wqi), "+v"(wtr), "+v"(wti) :: "memory");
    LAS char* wscr0 = L + 128 * 1024 + w * 2048;
    const int woff = w_off(mW, lane);
    w_put(wscr0, wqr, wqi, lane);
    asm volatile("s_waitcnt lgkmcnt(0)" ::: "memory");
    WRawL wraw;
    w_get(wraw, wscr0, woff, lane, 0); w_get(wraw, wscr0, woff, lane, 1); w_get(wraw, wscr0, woff, lane, 2); w_get(wraw, wscr0, woff, lane, 3);
    Wc = w_unpack(wraw, w_tail(wtr, wti), woff, mW, lane);
    asm volatile("" : "+v"(Wc.r0), "+v"(Wc.i0), "+v"(Wc.r1), "+v"(Wc.i1));
  }
  if (ISA) {
    const int dq = w & 3, mh = w >> 2;
    LAS char* xa = L + A_XIMG + (32 * hh + q4) * 256 + (((4 * dq + 2 * blk + (p4 >> 1)) ^ (q4 << 2)) << 4) + 8 * (p4 & 1);
    LAS char* fb = L + A_FIMG + mh * 16384 + lane * 16;
    const int srow = 32 * mh + (lane & 31);
    LAS char* stw = L + A_FIMG + srow * 256 + 8 * hh;
    const int rrow = tid >> 3;
    LAS char* str = L + A_FIMG + rrow * 256;
    const unsigned tdst = WS_T + (unsigned)(((64 * mt + rrow) * 8 + i) * 1024 + dt4 * 256 + (tid & 7) * 16);
    if (w < 4) bar_vm<9>(); else BAR_LGKM();
    STAMP(1);
    f32x16 acc = {};
    v4u fa[2][4], fbv[2][4];
#pragma unroll
    for (int s = 0; s < 4; ++s) { fa[0][s] = tr_read2(xa + 8 * s * 256, xa + 8 * s * 256 + 1024); fbv[0][s] = lds_ld16(fb + s * 1024); }
#pragma unroll
    for (int g = 0; g < 4; ++g) {
      acc = mfma32(fa[g & 1][0], fbv[g & 1][0], acc);
      SCHED();
      if (g == 1) { if (w >= 4) bar_vm<9>(); else BAR_LGKM(); }
      if (g < 3) {
#pragma unroll
        for (int s = 0; s < 4; ++s) {
          const int ro = (64 * (g + 1) + 8 * s) * 256;
          fa[(g + 1) & 1][s] = tr_read2(xa + ro, xa + ro + 1024); fbv[(g + 1) & 1][s] = lds_ld16(fb + (4 * (g + 1) + s) * 1024);
        }
      }
      SCHED();
#pragma unroll
      for (int s = 1; s < 4; ++s) { acc = mfma32(fa[g & 1][s], fbv[g & 1][s], acc); if (g == 1 && s == 1) { g1_dma(2, 0); g1_dma(2, 1); } if (g == 1 && s == 2) { g1_dma(2, 2); g1_dma(2, 3); } SCHED(); }
    }
    BAR_LGKM();
    STAMP(2);
    g1_issue(3);
#pragma unroll
    for (int Gq = 0; Gq < 4; ++Gq) {
      v2u o; o.x = pk2(acc[4 * Gq], acc[4 * Gq + 1]); o.y = pk2(acc[4 * Gq + 2], acc[4 * Gq + 3]);
      *(LAS v2u*)(stw + (((4 * dq + Gq) ^ (srow & 15)) << 4)) = o;
    }
    BAR_LGKM();
    {
      const v4u v0 = lds_ld16(str + (((tid & 7) ^ (rrow & 15)) << 4)), v1 = lds_ld16(str + ((((tid & 7) + 8) ^ (rrow & 15)) << 4));
      st16_sc1(sws, tdst, v0);
      st16_sc1(sws, tdst + 128u, v1);
    }
    SCHED();
  }
  const int ft = w & 3, rh1 = w >> 2;
  const bool active = ft * 32 < nf;
  f32x16 acc1[2] = {};
  v4u tq0, tq1;
  v4u tb[16];
  unsigned fla;
  int t_ok = 0;
  unsigned tsrc0 = 0, tsrc1 = 0;
  const unsigned flaoff = WS_FLA + (unsigned)(((m1 >> 5) & 3) * 32 + (lane & 31)) * FS;
  LAS char* CTL = L + 128 * 1024;
  {
    LAS char* wa = L + 16384 + (8 * hh + q4) * 256 + (((4 * ft + 2 * blk + (p4 >> 1)) ^ (q4 << 2)) << 4) + 8 * (p4 & 1);
    const int r = 64 * rh1 + (lane & 31), fz = (r >> 1) & 7;
    LAS char* xb0 = L + r * 128 + (((0 + hh) ^ fz) << 4);
    LAS char* xb1 = L + r * 128 + (((2 + hh) ^ fz) << 4);
    LAS char* xb2 = L + r * 128 + (((4 + hh) ^ fz) << 4);
    LAS char* xb3 = L + r * 128 + (((6 + hh) ^ fz) << 4);
    Frag1 fr[2];
    bar_vm<ISA ? 15 : 13>();
    STAMP(3);
    if (lane == 0) { v2u gv2; gv2.x = __builtin_bit_cast(unsigned, gate_g); gv2.y = __builtin_bit_cast(unsigned, gate_og); *(LAS v2u*)(CTL + 64 + w * 8) = gv2; }
    g1_read(fr[0], wa, xb0, xb1, xb2, xb3, 0);
#define PIN1(c) asm volatile("" : "+v"(c.a[0]), "+v"(c.a[1]), "+v"(c.a[2]), "+v"(c.a[3]), "+v"(c.b0[0]), "+v"(c.b0[1]), "+v"(c.b0[2]), "+v"(c.b0[3]), \
                             "+v"(c.b1[0]), "+v"(c.b1[1]), "+v"(c.b1[2]), "+v"(c.b1[3]))
#define G1_STEP(kt, X0, X1, X2, X3, X4, X5, X6, X7)                                                                   \
    {                                                                                                                   \
      FT(10 + (kt));                                                                                                    \
      Frag1& c = fr[(kt) & 1];                                                                                          \
      Frag1& n = fr[((kt) + 1) & 1];                                                                                    \
      const int so = (((kt) + 1) & 3) * 32768;                                                                          \
      PIN1(c);                                                                                                          \
      asm volatile("" :: "v"(acc1[0]), "v"(acc1[1]));                                                                   \
      SCHED();                                                                                                          \
      acc1[0] = mfma32(c.a[0], c.b0[0], acc1[0]); if ((kt) < 7) n.a[0] = tr_read2(wa + so, wa + so + 1024); X0; SCHED(); \
      acc1[1] = mfma32(c.a[0], c.b1[0], acc1[1]); if ((kt) < 7) { n.b0[0] = lds_ld16(xb0 + so); n.b1[0] = lds_ld16(xb0 + so + 4096); } X1; SCHED(); \
      acc1[0] = mfma32(c.a[1], c.b0[1], acc1[0]); if ((kt) < 7) n.a[1] = tr_read2(wa + so + 4096, wa + so + 5120); X2; SCHED(); \
      acc1[1] = mfma32(c.a[1], c.b1[1], acc1[1]); if ((kt) < 7) { n.b0[1] = lds_ld16(xb1 + so); n.b1[1] = lds_ld16(xb1 + so + 4096); } X3; SCHED(); \
      acc1[0] = mfma32(c.a[2], c.b0[2], acc1[0]); if ((kt) < 7) n.a[2] = tr_read2(wa + so + 8192, wa + so + 9216); X4; SCHED(); \
      acc1[1] = mfma32(c.a[2], c.b1[2], acc1[1]); if ((kt) < 7) { n.b0[2] = lds_ld16(xb2 + so); n.b1[2] = lds_ld16(xb2 + so + 4096); } X5; SCHED(); \
      acc1[0] = mfma32(c.a[3], c.b0[3], acc1[0]); if ((kt) < 7) n.a[3] = tr_read2(wa + so + 12288, wa + so + 13312); X6; SCHED(); \
      acc1[1] = mfma32(c.a[3], c.b1[3], acc1[1]); if ((kt) < 7) { n.b0[3] = lds_ld16(xb3 + so); n.b1[3] = lds_ld16(xb3 + so + 4096); } X7; SCHED(); \
    }
#define GATE_STORE() do { if (w == 2 && lane < 8) { const v2u gv2 = *(LAS v2u*)(CTL + 64 + lane * 8);                       \
      __builtin_amdgcn_raw_buffer_store_b64(gv2, as_rsrc(sws), WS_GATE + (unsigned)(i1 * 256 + 128 * rb + 8 * fc + lane) * 8u, 0, 16); } } while (0)
#define STEP8 G1_STEP
    if constexpr (ISA) {
      bar_vm<11>();
      STAMP(4);
      STEP8(0, NOP, NOP, NOP, NOP, NOP, NOP, NOP, NOP)
      bar_vm<0>();
      if (tid == 0) flag_store(p.ws, WS_FLA + (unsigned)(mt * 32 + i * 4 + dt4) * FS, tag);
      STAMP(13);
      STEP8(1, g1_dma(4, 0), g1_dma(4, 1), g1_dma(4, 2), g1_dma(4, 3), g1_dma(5, 0), g1_dma(5, 1), g1_dma(5, 2), g1_dma(5, 3))
      BAR_LGKM();
      GATE_STORE();
      STEP8(2, NOP, NOP, NOP, NOP, g1_dma(6, 0), g1_dma(6, 1), g1_dma(6, 2), g1_dma(6, 3))
      bar_vm<8>();
      STEP8(3, NOP, NOP, NOP, NOP, g1_dma(7, 0), g1_dma(7, 1), g1_dma(7, 2), g1_dma(7, 3))
      bar_vm<8>();
      STAMP(12);
      STEP8(4, NOP, NOP, NOP, NOP, w2_issue1(G, 0, 0), w2_issue1(G, 0, 1), w2_issue1(G, 1, 0), w2_issue1(G, 1, 1))
      bar_vm<8>();
      STEP8(5, NOP, NOP, NOP, NOP, w2_issue1(G, 2, 0), w2_issue1(G, 2, 1), w2_issue1(G, 3, 0), w2_issue1(G, 3, 1))
      bar_vm<8>();
      STEP8(6, NOP, NOP, NOP, NOP, w2_issue1(G, 4, 0), w2_issue1(G, 4, 1), w2_issue1(G, 5, 0), w2_issue1(G, 5, 1))
      BAR_LGKM();
    } else {
      const v4u stab = make_srd(&kTab, sizeof(Tab));
      {
        const int kap = tid >> 5, ii = (kap >> 1) & 7, d0 = kap & 1;
        const int g0 = m1 == 0 ? 2 * ii : ii, g1 = m1 == 0 ? 2 * ii + 1 : 8 + ii;
        const unsigned tb = WS_T + (unsigned)m1 * 16384u + (unsigned)(d0 * 512 + (tid & 31) * 16);
        tsrc0 = tb + (unsigned)g0 * 1024u; tsrc1 = tb + (unsigned)g1 * 1024u;
      }
      bar_vm<9>();
      STAMP(4);
      STEP8(0, NOP, NOP, NOP, NOP, g1_dma(4, 0), g1_dma(4, 1), g1_dma(4, 2), g1_dma(4, 3))
      bar_vm<9>();
      STEP8(1, NOP, NOP, NOP, NOP, g1_dma(5, 0), g1_dma(5, 1), g1_dma(5, 2), g1_dma(5, 3))
      bar_vm<9>();
      GATE_STORE();
      STEP8(2, NOP, NOP, NOP, NOP, g1_dma(6, 0), g1_dma(6, 1), g1_dma(6, 2), g1_dma(6, 3))
      bar_vm<8>();
      LAS char* wscr0 = L + 128 * 1024 + w * 2048;
      const bool special = m1 == 0;
      const int m2nd = 128 * w + 1024, off2 = w_off(m2nd, lane);
      if (special) { asm volatile("" : "+v"(xqr), "+v"(xqi), "+v"(xtr), "+v"(xti)); w_put(wscr0, xqr, xqi, lane); }
      STEP8(3, NOP, NOP, (dma16<128u * 1024u + 16384u, 0>(stab, (unsigned)lane * 16u, g1l, g1l)), (dma16<128u * 1024u + 17408u, 1024>(stab, (unsigned)lane * 16u, g1l, g1l)),
            g1_dma(7, 0), g1_dma(7, 1), g1_dma(7, 2), g1_dma(7, 3))
      bar_vm<10>();
      STAMP(12);
      WRawL raw2;
      if (special) { w_get(raw2, wscr0, off2, lane, 0); w_get(raw2, wscr0, off2, lane, 1); w_get(raw2, wscr0, off2, lane, 2); w_get(raw2, wscr0, off2, lane, 3); }
      STEP8(4, NOP, NOP, NOP, NOP, w2_issue1(G, 0, 0), w2_issue1(G, 0, 1), w2_issue1(G, 1, 0), w2_issue1(G, 1, 1))
      bar_vm<10>();
      if (special) { Wd = w_unpack(raw2, w_tail(xtr, xti), off2, m2nd, lane); packed_coef(Wc, Wd, w); }
      STEP8(5, NOP, NOP, NOP, NOP, w2_issue1(G, 2, 0), w2_issue1(G, 2, 1), w2_issue1(G, 3, 0), w2_issue1(G, 3, 1))
      bar_vm<8>();
      STEP8(6, if (w == 0) LD4Z(fla, flaoff, sws, "sc1"), NOP, NOP, NOP, w2_issue1(G, 4, 0), w2_issue1(G, 4, 1), w2_issue1(G, 5, 0), w2_issue1(G, 5, 1))
      BAR_LGKM();
    }
    if constexpr (ISA) {
      STEP8(7, NOP, NOP, NOP, NOP, w2_issue1(G, 6, 0), w2_issue1(G, 6, 1), w2_issue1(G, 7, 0), w2_issue1(G, 7, 1))
    } else {
      LAS char* TB = L + 128 * 1024 + 16384 + lane * 16;
#define TBR(f) (tb[f] = lds_ld16(TB + (f) * 1024), tb[(f) + 1] = lds_ld16(TB + ((f) + 1) * 1024))
      STEP8(7, TBR(6), TBR(0), TBR(10), TBR(2), { TBR(12); w2_issue1(G, 6, 0); }, { TBR(14); w2_issue1(G, 6, 1); }, { TBR(4); w2_issue1(G, 7, 0); }, { TBR(8); w2_issue1(G, 7, 1); })
      if (w == 0) {
        asm volatile("s_waitcnt vmcnt(8)" : "+v"(fla) :: "memory");
        const unsigned ok = __all(fla == tag) ? 1u : 0u;
        if (lane == 0) *(LAS unsigned*)CTL = ok;
      }
      BAR_LGKM();
      t_ok = __builtin_amdgcn_readfirstlane(*(volatile LAS unsigned*)CTL);
      if (t_ok) { LD16Z(tq0, tsrc0, sws, "sc1"); LD16Z(tq1, tsrc1, sws, "sc1"); }
    }
  }
  asm volatile("" :: "v"(touch));
  STAMP(5);
  FT(18);
  if (active) {
#pragma unroll
    for (int rt = 0; rt < 2; ++rt)
#pragma unroll
      for (int u = 0; u < 2; ++u) {
        v4u o;
        o.x = silu2(acc1[rt][8 * u + 0], acc1[rt][8 * u + 1]);
        o.y = silu2(acc1[rt][8 * u + 2], acc1[rt][8 * u + 3]);
        o.z = silu2(acc1[rt][8 * u + 4], acc1[rt][8 * u + 5]);
        o.w = silu2(acc1[rt][8 * u + 6], acc1[rt][8 * u + 7]);
        const int jb32 = 4 * rb + 2 * rh1 + rt, fs = (f0 >> 4) + 2 * ft + u;
        st16_sc1(sws, WS_H + (unsigned)(((i1 * 8 + jb32) * 112 + fs) * 1024 + lane * 16), o);
      }
  }
  SCHED();
  STAMP(6);
  FT(20);
  const unsigned flh_own = WS_FLH + (unsigned)((i1 * 2 + rb) * 16 + fc) * FS;
  if constexpr (ISA) {
    bar_vm<0>();
    if (tid == 0) flag_store(p.ws, flh_own, tag);
  } else {
    LAS char* I0 = L + 128 * 1024;
    LAS char* I1 = L + 128 * 1024 + 16384;
    const int g = lane >> 4, l15 = lane & 15;
    const bool special = (m1 == 0);
    bool published = false;
    if (t_ok) {
      wait_vm<4>();
      asm volatile("s_barrier" ::: "memory");
    } else {
      bar_vm<0>();
      if (tid == 0) flag_store(p.ws, flh_own, tag);
      published = true;
      if (w == 0) poll_words(sws, WS_FLA + (unsigned)(m1 >> 5) * 32u * FS, 32, tag, lane);
      asm volatile("s_barrier" ::: "memory");
      LD16Z(tq0, tsrc0, sws, "sc1"); LD16Z(tq1, tsrc1, sws, "sc1");
      wait_vm<0>();
    }
    asm volatile("" : "+v"(tq0), "+v"(tq1) :: "memory");
    {
      const int kap = tid >> 5, ch = tid & 31;
      *(LAS v4u*)(I0 + kap * 512 + ((ch ^ ((kap & 3) << 2)) << 4)) = tq0;
      *(LAS v4u*)(I0 + (kap + 16) * 512 + ((ch ^ ((kap & 3) << 2)) << 4)) = tq1;
    }
    BAR_LGKM();
    FT(21);
    LAS char* ta = I0 + (8 * hh + q4) * 512 + (((4 * w + 2 * blk + (p4 >> 1)) ^ (q4 << 2)) << 4) + 8 * (p4 & 1);
    const int n = lane & 31;
    LAS char* so = I1 + n * 512 + 8 * hh;
    {
      f32x16 acc = {};
      acc = mfma32(tr_read2(ta, ta + 2048), tb[6], acc);
      acc = mfma32(tr_read2(ta + 8192, ta + 10240), tb[7], acc);
#pragma unroll
      for (int Gq = 0; Gq < 4; ++Gq) {
        v2u o; o.x = pk2(acc[4 * Gq], acc[4 * Gq + 1]); o.y = pk2(acc[4 * Gq + 2], acc[4 * Gq + 3]);
        *(LAS v2u*)(so + (((4 * w + Gq) ^ (n & 15)) << 4)) = o;
      }
    }
    BAR_LGKM();
    FT(22);
    const f32x4 z4 = {0.f, 0.f, 0.f, 0.f};
    const v4u F16fr = tb[0], F16fi = tb[1];
    v4u F16ir, F16ii;
    F16ir.x = F16fr.x; F16ir.y = F16fr.y; F16ir.z = F16fr.z ^ 0x80008000u; F16ir.w = F16fr.w ^ 0x80008000u;
    F16ii.x = F16fi.x ^ 0x80008000u; F16ii.y = F16fi.y ^ 0x80008000u; F16ii.z = F16fi.z; F16ii.w = F16fi.w;
    f32x4 zr[2], zi[2];
#pragma unroll
    for (int pp = 0; pp < 2; ++pp) {
      const int n0 = 4 * w + 2 * pp, n1 = n0 + 1;
      const int ch = 2 * (4 * g + q4) + (p4 >> 1);
      const v4u a1 = tr_read2(I1 + n0 * 512 + ((ch ^ (n0 & 15)) << 4) + 8 * (p4 & 1), I1 + n1 * 512 + ((ch ^ (n1 & 15)) << 4) + 8 * (p4 & 1));
      const f32x4 dr = mfma16(a1, pp ? tb[2] : F16fr, z4), di = mfma16(a1, pp ? tb[3] : F16fi, z4);
      const v4u b3 = cmul_pack<false>(dr, di, asf4(tb[10 + 2 * pp]), asf4(tb[11 + 2 * pp]));
      zr[pp] = mfma16(F16fr, b3, z4); zi[pp] = mfma16(F16fi, b3, z4);
    }
    v4u a4[2];
    if (!special) {
      a4[0] = cmul_pack<false>(zr[0], zi[0], Wc.r0, Wc.i0);
      a4[1] = cmul_pack<false>(zr[1], zi[1], Wc.r1, Wc.i1);
    } else {
      BAR_LGKM();
      LAS char* Z = I0;
#pragma unroll
      for (int pp = 0; pp < 2; ++pp)
#pragma unroll
        for (int r = 0; r < 4; ++r) { f32x2 v = {zr[pp][r], zi[pp][r]}; *(LAS f32x2*)(Z + ((w * 2 + pp) * 256 + l15 + 16 * (4 * g + r)) * 8) = v; }
      BAR_LGKM();
#pragma unroll
      for (int pp = 0; pp < 2; ++pp) {
        f32x4 cr, ci;
#pragma unroll
        for (int r = 0; r < 4; ++r) {
          const int K = l15 + 16 * (4 * g + r), Kn = pp ? 255 - K : (256 - K) & 255;
          const f32x2 y = *(LAS f32x2*)(Z + ((((8 - w) & 7) * 2 + pp) * 256 + Kn) * 8);
          cr[r] = y[0]; ci[r] = -y[1];
        }
        const f32x4 ar = pp ? Wc.r1 : Wc.r0, ai = pp ? Wc.i1 : Wc.i0, br = pp ? Wd.r1 : Wd.r0, bi = pp ? Wd.i1 : Wd.i0;
        const f32x4 yr = zr[pp] * ar - zi[pp] * ai + cr * br - ci * bi, yi = zr[pp] * ai + zi[pp] * ar + cr * bi + ci * br;
        a4[pp].x = pk2(yr[0], yr[1]); a4[pp].y = pk2(yr[2], yr[3]); a4[pp].z = pk2(yi[0], yi[1]); a4[pp].w = pk2(yi[2], yi[3]);
      }
      BAR_LGKM();
    }
#pragma unroll
    for (int pp = 0; pp < 2; ++pp) {
      const f32x4 dr = mfma16(a4[pp], F16ir, z4), di = mfma16(a4[pp], F16ii, z4);
      const v4u a5 = cmul_pack<true>(dr, di, asf4(tb[pp ? 14 : 10]), asf4(tb[pp ? 15 : 11]));
      const f32x4 er = mfma16(a5, pp ? tb[4] : F16ir, z4), ei = mfma16(a5, pp ? tb[5] : F16ii, z4);
      const int n0 = 4 * w + 2 * pp, n1 = n0 + 1;
      const int ch = 2 * l15 + (g >> 1);
      v2u o; o.x = pk2(er[0], er[1]); o.y = pk2(er[2], er[3]);
      *(LAS v2u*)(I0 + n0 * 512 + ((ch ^ ((n0 & 3) << 2)) << 4) + 8 * (g & 1)) = o;
      o.x = pk2(ei[0], ei[1]); o.y = pk2(ei[2], ei[3]);
      *(LAS v2u*)(I0 + n1 * 512 + ((ch ^ ((n1 & 3) << 2)) << 4) + 8 * (g & 1)) = o;
    }
    bar_vm<0>();
    if (!published && tid == 0) flag_store(p.ws, flh_own, tag);
    FT(23);
    m5a = tb[8]; m5b = tb[9];
  }
  return tag;
}

DI void make_geo(Geo& G, const Params& p, int tz) {
  G.tid = tz; G.lane = G.tid & 63; G.w = __builtin_amdgcn_readfirstlane(G.tid >> 6);
  G.b = blockIdx.x;
  const int Lx = G.b >> 3, x8 = G.b & 7;
  {
    const int a = x8 & 3, h = x8 >> 2, rbi = Lx >> 3, c = Lx & 7;
    G.i = 2 * a + h; G.i1 = 2 * a + (rbi >> 1); G.rb = rbi & 1;
    G.fc = c < 4 ? 4 * h + c : 4 + 4 * h + c;
    G.at = rbi * 4 + c; G.m1 = ((rbi * 4 + c - 4) << 3) | x8;
  }
  G.hh = G.lane >> 5; G.blk = (G.lane >> 4) & 1; G.q4 = (G.lane & 15) >> 2; G.p4 = G.lane & 3;
  G.sx = make_srd(p.x, NS * ND * 2); G.sws = make_srd(p.ws, 16u << 20); G.sw2 = make_srd(p.w2, NF * ND * 2);
  G.jb = Lx >> 3; G.dt = Lx & 7; G.rh = G.w & 1; G.q = G.w >> 1;
  G.j = 64 * G.jb + 32 * G.rh + (G.lane & 31);
  const int rho = 16 * G.rh + (G.lane >> 3), chs = (G.lane & 7) ^ (4 * ((G.lane >> 4) & 1));
  G.vw20 = (unsigned)(rho * 1024 + G.dt * 128 + chs * 16); G.vw21 = G.vw20 + 8 * 1024;
  G.ldsw = uni((unsigned)(G.q * 4096 + G.rh * 2048));
  G.fq0b = uni((unsigned)(768 + 256 * G.q) * 1024u); G.fq1b = uni((unsigned)(192 * G.q) * 1024u);
}

__global__ __launch_bounds__(512, 2) void gm_kernel(Params p) {
  extern __shared__ __attribute__((aligned(16))) char smem[];
  LAS char* L = (LAS char*)smem;
  long long pad_t = 0;
  STAMP(0);
  FT(0);
  Geo G;
  make_geo(G, p, threadIdx.x);
  const bool isA = G.fc < 8;
  v4u gf[4], m5a, m5b;
  unsigned tag;
  if (isA) tag = phase1<true>(pad_t, p, L, G, m5a, m5b); else tag = phase1<false>(pad_t, p, L, G, m5a, m5b);
  { int tz = threadIdx.x; asm volatile("" : "+v"(tz)); make_geo(G, p, tz); }
  const int tid = G.tid, lane = G.lane, w = G.w, b = G.b, i = G.i, jb = G.jb, dt = G.dt, rh = G.rh, q = G.q, j = G.j;
  const int hh = G.hh, blk = G.blk, q4 = G.q4, p4 = G.p4;
  const v4u sws = G.sws;
  const unsigned ldsw = G.ldsw;
  const int m1 = G.m1;
  STAMP(7);
  FT(25);
  if (!isA) {
    LAS char* I0 = L + 128 * 1024;
    LAS char* I1 = L + 128 * 1024 + 16384;
    LAS char* ta = I0 + (8 * hh + q4) * 512 + (((4 * w + 2 * blk + (p4 >> 1)) ^ (q4 << 2)) << 4) + 8 * (p4 & 1);
    const int n = lane & 31;
    LAS char* so = I1 + n * 512 + 8 * hh;
    f32x16 acc = {};
    acc = mfma32(tr_read2(ta, ta + 2048), m5a, acc);
    acc = mfma32(tr_read2(ta + 8192, ta + 10240), m5b, acc);
#pragma unroll
    for (int Gq = 0; Gq < 4; ++Gq) {
      v2u o; o.x = pk2(acc[4 * Gq], acc[4 * Gq + 1]); o.y = pk2(acc[4 * Gq + 2], acc[4 * Gq + 3]);
      *(LAS v2u*)(so + (((4 * w + Gq) ^ (n & 15)) << 4)) = o;
    }
    BAR_LGKM();
    const bool special = (m1 == 0);
    auto u_store = [&](int t2) {
      const int nn = t2 >> 5, ch = t2 & 31, ii = (nn >> 1) & 7, d0 = nn & 1;
      const int g0 = special ? 2 * ii : ii, g1 = special ? 2 * ii + 1 : 8 + ii;
      const v4u v0 = lds_ld16(I1 + nn * 512 + ((ch ^ (nn & 15)) << 4));
      const v4u v1 = lds_ld16(I1 + (nn + 16) * 512 + ((ch ^ (nn & 15)) << 4));
      const unsigned ub = WS_U + (unsigned)m1 * 16384u + (unsigned)(d0 * 512 + ch * 16);
      st16_sc1(sws, ub + (unsigned)g0 * 1024u, v0);
      st16_sc1(sws, ub + (unsigned)g1 * 1024u, v1);
    };
    if (w != 0) u_store(tid);
    if (w == 1) u_store(tid - 64);
    SCHED();
  }
  const int sz = (q4 >> 1) & 1;
  LAS char* ra0 = L + q * 4096 + (4 * hh + q4) * 128 + (((0 ^ (4 * sz)) + 2 * blk + (p4 >> 1)) << 4) + 8 * (p4 & 1);
  LAS char* ra1 = L + q * 4096 + (4 * hh + q4) * 128 + (((4 ^ (4 * sz)) + 2 * blk + (p4 >> 1)) << 4) + 8 * (p4 & 1);
  v4u wp[4][4];
  BAR_LGKM();
#pragma unroll
  for (int t = 0; t < 4; ++t) {
    LAS char* s0 = ra0 + t * 16384; LAS char* s1 = ra1 + t * 16384;
    wp[t][0] = tr_read2(s0, s0 + 1024); wp[t][1] = tr_read2(s1, s1 + 1024);
    wp[t][2] = tr_read2(s0 + 2048, s0 + 3072); wp[t][3] = tr_read2(s1 + 2048, s1 + 3072);
  }
#define W2_8_11() do { w2_issue1(G, 8, 0); w2_issue1(G, 8, 1); w2_issue1(G, 9, 0); w2_issue1(G, 9, 1); w2_issue1(G, 10, 0); w2_issue1(G, 10, 1); w2_issue1(G, 11, 0); w2_issue1(G, 11, 1); } while (0)
  if (isA) { BAR_LGKM(); W2_8_11(); }
  const unsigned flh = WS_FLH + (unsigned)((i * 2 + (jb >> 1)) * 16) * FS;
  if (w == 0) poll_words(sws, flh, 16, tag, lane);
  BAR_LGKM();
  STAMP(8);
  FT(26);
  const unsigned vh = (unsigned)lane * 16u;
  const unsigned hbase = WS_H + (unsigned)((i * 8 + 2 * jb + rh) * 112) * 1024u;
  const unsigned hb0 = uni(hbase + (G.fq0b >> 4)), hb1 = uni(hbase + (G.fq1b >> 4));
  v2u gg;
  v4u hf[14][2];
#define H_ISSUE(t) do { LD16I(hf[t][0], vh, sws, ((t) < 8 ? hb0 : hb1), (2 * ((t) & 7)) * 1024, "sc1"); LD16I(hf[t][1], vh, sws, ((t) < 8 ? hb0 : hb1), (2 * ((t) & 7) + 1) * 1024, "sc1"); } while (0)
  H_ISSUE(0); H_ISSUE(1);
  asm volatile("s_barrier" ::: "memory");
  H_ISSUE(2); H_ISSUE(3); H_ISSUE(4); H_ISSUE(5); H_ISSUE(6);
  LD8Z(gg, WS_GATE + (unsigned)(i * 256 + j) * 8u, sws, "sc1");
  SCHED();
  f32x16 accb[2] = {}, acca[2] = {};
  v4u wf[2][4];
  unsigned fl0, fl1;
  v4u ub0, ub1, ub2, ub3;
  const unsigned vu = WS_U + (unsigned)(((64 * q + 16 * rh + (lane >> 3)) * 8 + i) * 1024 + dt * 128 + ((lane & 7) ^ (4 * ((lane >> 4) & 1))) * 16);
#define G2_BAR(n, t) asm volatile("s_waitcnt vmcnt(" STR(n) ") lgkmcnt(0)\n\ts_barrier" : "+v"(hf[t][0]), "+v"(hf[t][1]) :: "memory")
#define G2_CORE(t, c, n, RD, acc, b0, b1, X0, X1, X2)                                             \
  {                                                                                                 \
    FT(30 + (t));                                                                                   \
    LAS char* s0 = ra0 + (((t) + 1) & 7) * 16384; LAS char* s1 = ra1 + (((t) + 1) & 7) * 16384;    \
    asm volatile("" : "+v"(c[0]), "+v"(c[1]), "+v"(c[2]), "+v"(c[3]));                             \
    asm volatile("" :: "v"(acc[0]), "v"(acc[1]));                                                   \
    SCHED();                                                                                        \
    acc[0] = mfma32(c[0], b0, acc[0]); if (RD) n[0] = tr_read2(s0, s0 + 1024); SCHED();            \
    acc[1] = mfma32(c[1], b0, acc[1]); if (RD) n[1] = tr_read2(s1, s1 + 1024); X0; SCHED();        \
    acc[0] = mfma32(c[2], b1, acc[0]); if (RD) n[2] = tr_read2(s0 + 2048, s0 + 3072); X1; SCHED(); \
    acc[1] = mfma32(c[3], b1, acc[1]); if (RD) n[3] = tr_read2(s1 + 2048, s1 + 3072); X2; SCHED(); \
  }
#define G2_PRE(t, X2) G2_CORE(t, wp[t], wf[0], (t) == 3, accb, hf[t][0], hf[t][1], if (!isA) w2_issue1(G, (t) + 8, 0), if (!isA) w2_issue1(G, (t) + 8, 1), X2)
#define G2_STEP(t, X0, X1, X2) G2_CORE(t, wf[(t) & 1], wf[((t) + 1) & 1], true, accb, hf[t][0], hf[t][1], X0, X1, X2)
  G2_BAR(13, 0);
  if (!isA && tid == 0) flag_store(p.ws, WS_FLB + (unsigned)m1 * FS, tag);
  STAMP(14);
  G2_PRE(0, H_ISSUE(7))
  G2_BAR(13, 1); G2_PRE(1, H_ISSUE(8))
  G2_BAR(13, 2); G2_PRE(2, H_ISSUE(9))
  G2_BAR(13, 3); G2_PRE(3, H_ISSUE(10))
  G2_BAR(13, 4); G2_STEP(4, w2_issue1(G, 12, 0), w2_issue1(G, 12, 1), H_ISSUE(11))
  G2_BAR(15, 5); G2_STEP(5, w2_issue1(G, 13, 0), w2_issue1(G, 13, 1), H_ISSUE(12))
  if (w == 0) { LD4Z(fl0, WS_FLB + (unsigned)lane * FS, sws, "sc1"); LD4Z(fl1, WS_FLB + (64u + (unsigned)lane) * FS, sws, "sc1"); }
  G2_BAR(17, 6); G2_STEP(6, NOP, NOP, H_ISSUE(13))
  STAMP(9);
  G2_BAR(16, 7); G2_STEP(7, NOP, NOP, NOP)
  LAS unsigned* verdict = (LAS unsigned*)(L + 128 * 1024);
  if (w == 0) {
    asm volatile("s_waitcnt vmcnt(2)" : "+v"(fl0), "+v"(fl1) :: "memory");
    const unsigned ok = __all(fl0 == tag && fl1 == tag) ? 1u : 0u;
    if (lane == 0) *verdict = ok;
  }
  G2_BAR(14, 8);
  const int u_early = __builtin_amdgcn_readfirstlane(*(volatile LAS unsigned*)verdict);
  STAMP(15);
#define U_ISSUE() do { LD16Z(ub0, vu, sws, "sc1"); LD16Z(ub1, vu + 8u * 8192u, sws, "sc1"); LD16Z(ub2, vu + 32u * 8192u, sws, "sc1"); LD16Z(ub3, vu + 40u * 8192u, sws, "sc1"); } while (0)
  if (u_early) U_ISSUE();
  GRot grot;
  G2_STEP(8, gen_g_init(G, grot), NOP, gen_g_frag<0>(G, grot, gf[0]))
  unsigned e0, e1, e2, e3;
  if (b == 0 && w == 1) {
    LD4Z(e0, WS_FLH + (unsigned)lane * FS, sws, "sc1"); LD4Z(e1, WS_FLH + (64u + (unsigned)lane) * FS, sws, "sc1");
    LD4Z(e2, WS_FLH + (128u + (unsigned)lane) * FS, sws, "sc1"); LD4Z(e3, WS_FLH + (192u + (unsigned)lane) * FS, sws, "sc1");
  }
  G2_BAR(12, 9); G2_STEP(9, NOP, NOP, gen_g_frag<1>(G, grot, gf[1]))
  G2_BAR(10, 10); G2_STEP(10, NOP, NOP, gen_g_frag<2>(G, grot, gf[2]))
  G2_BAR(6, 11); G2_STEP(11, NOP, NOP, gen_g_frag<3>(G, grot, gf[3]))
  G2_BAR(2, 12);
  G2_STEP(12, NOP, NOP, NOP)
#define U_PUT() do {                                                                                                        \
    asm volatile("s_waitcnt vmcnt(0)" : "+v"(ub0), "+v"(ub1), "+v"(ub2), "+v"(ub3) :: "memory");                             \
    *(LAS v4u*)(L + 6 * 16384 + ldsw + lane * 16) = ub0; *(LAS v4u*)(L + 6 * 16384 + ldsw + 1024 + lane * 16) = ub1;         \
    *(LAS v4u*)(L + 7 * 16384 + ldsw + lane * 16) = ub2; *(LAS v4u*)(L + 7 * 16384 + ldsw + 1024 + lane * 16) = ub3; } while (0)
  if (u_early) U_PUT();
  G2_BAR(0, 13);
  asm volatile("" : "+v"(gg));
  if (b == 0 && w == 1) {
    asm volatile("" : "+v"(e0), "+v"(e1), "+v"(e2), "+v"(e3));
    if (!__all(e0 == tag && e1 == tag && e2 == tag && e3 == tag)) {
#pragma unroll 1
      for (int u = 0; u < 4; ++u) poll_words(sws, WS_FLH + 64u * FS * u, 64, tag, lane);
    }
    if (lane == 0) flag_store(p.ws, WS_EPOCH, tag);
  }
  G2_CORE(13, wf[1], wf[0], u_early, accb, hf[13][0], hf[13][1], NOP, NOP, NOP)
  if (!u_early) {
    if (w == 0) { poll_words(sws, WS_FLB, 64, tag, lane); poll_words(sws, WS_FLB + 64u * FS, 64, tag, lane); }
    BAR_LGKM();
    U_ISSUE();
    U_PUT();
    BAR_LGKM();
    LAS char* s0 = ra0 + 6 * 16384; LAS char* s1 = ra1 + 6 * 16384;
    wf[0][0] = tr_read2(s0, s0 + 1024); wf[0][1] = tr_read2(s1, s1 + 1024); wf[0][2] = tr_read2(s0 + 2048, s0 + 3072); wf[0][3] = tr_read2(s1 + 2048, s1 + 3072);
  }
  STAMP(10);
  BAR_LGKM(); G2_CORE(14, wf[0], wf[1], true, acca, gf[0], gf[1], NOP, NOP, NOP)
  BAR_LGKM(); G2_CORE(15, wf[1], wf[0], false, acca, gf[2], gf[3], NOP, NOP, NOP)
  FT(50);
  {
    const float og = asf(gg.y), gs = asf(gg.x) * (1.f / 524288.f);
    const int row = 32 * rh + (lane & 31);
#pragma unroll
    for (int dh = 0; dh < 2; ++dh)
#pragma unroll
      for (int G = 0; G < 4; ++G) {
        f32x4 v;
#pragma unroll
        for (int e = 0; e < 4; ++e) v[e] = __builtin_fmaf(og, accb[dh][4 * G + e], gs * acca[dh][4 * G + e]);
        *(LAS f32x4*)(L + q * 16384 + row * 256 + (((8 * dh + 2 * G + hh) ^ (row & 7)) << 4)) = v;
      }
  }
  BAR_LGKM();
  FT(51);
  {
    const int row = tid >> 3, c8 = tid & 7;
    f32x4 s0 = {0.f, 0.f, 0.f, 0.f}, s1 = s0;
#pragma unroll
    for (int qq = 0; qq < 4; ++qq) {
      s0 += *(LAS f32x4*)(L + qq * 16384 + row * 256 + (((2 * c8) ^ (row & 7)) << 4));
      s1 += *(LAS f32x4*)(L + qq * 16384 + row * 256 + (((2 * c8 + 1) ^ (row & 7)) << 4));
    }
    v4u o; o.x = pk2(s0[0], s0[1]); o.y = pk2(s0[2], s0[3]); o.z = pk2(s1[0], s1[1]); o.w = pk2(s1[2], s1[3]);
    *(GAS v4u*)((GAS char*)p.out + (size_t)(i + 8 * (64 * jb + row)) * 1024 + dt * 128 + c8 * 16) = o;
  }
  STAMP(11);
  while (__builtin_amdgcn_s_memrealtime() - pad_t < 3000) {}
}

}

extern "C" void kernel_launch(void* const* d_in, const int* in_sizes, int n_in, void* d_out, int out_size, void* d_ws, size_t ws_size,
                              hipStream_t stream) {
  Params p;
  p.x = d_in[0]; p.wr = d_in[1]; p.wi = d_in[2]; p.w1 = d_in[4]; p.w2 = d_in[5]; p.gw = d_in[6];
  p.out = d_out; p.ws = d_ws;
  (void)hipFuncSetAttribute((const void*)gm_kernel, hipFuncAttributeMaxDynamicSharedMemorySize, LDS_BYTES);
  gm_kernel<<<256, 512, LDS_BYTES, stream>>>(p);
}
```

```cpp
#include <hip/hip_runtime.h>
#include <hip/hip_bf16.h>
#include <stdint.h>

namespace {

typedef unsigned v4u __attribute__((ext_vector_type(4)));
typedef unsigned v2u __attribute__((ext_vector_type(2)));
typedef float f32x16 __attribute__((ext_vector_type(16)));
typedef float f32x4 __attribute__((ext_vector_type(4)));
typedef float f32x2 __attribute__((ext_vector_type(2)));
typedef __bf16 bf16x8 __attribute__((ext_vector_type(8)));
typedef __bf16 bf16x2 __attribute__((ext_vector_type(2)));
typedef short s16x4 __attribute__((ext_vector_type(4)));
#define LAS __attribute__((address_space(3)))
#define GAS __attribute__((address_space(1)))
#define DI __device__ __forceinline__

constexpr int NS = 2048, ND = 512, NF = 1792;
constexpr unsigned WS_EPOCH = 0;
constexpr unsigned FS = 64;
constexpr unsigned WS_FLA = 4096;
constexpr unsigned WS_FLH = 16384;
constexpr unsigned WS_FLB = 32768;
constexpr unsigned WS_GATE = 65536;
constexpr unsigned WS_T = 1u << 20;
constexpr unsigned WS_U = 3u << 20;
constexpr unsigned WS_H = 5u << 20;
constexpr unsigned H_BYTES = 8u * 8u * 112u * 1024u;
constexpr int LDS_BYTES = 160 * 1024;
constexpr unsigned kSpinCap = 1u << 20;

struct Params {
  const void* x; const void* wr; const void* wi; const void* w1; const void* w2; const void* gw;
  void* out; void* ws;
};

constexpr double kPi = 3.14159265358979323846;
constexpr double ccos(double x) { double t = 1, s = 1; for (int n = 1; n < 16; ++n) { t *= -x * x / ((2 * n - 1) * (2 * n)); s += t; } return s; }
constexpr double cosn(int e, int n) { e = ((e % n) + n) % n; double x = 2 * kPi * e / n; if (x > kPi) x -= 2 * kPi; return ccos(x); }
constexpr double wre(int e, int n) { return cosn(e, n); }
constexpr double wim(int e, int n) { return -cosn(4 * e - n, 4 * n); }
constexpr unsigned bfb(double v) { float f = (float)v; unsigned u = __builtin_bit_cast(unsigned, f); u += 0x7fffu + ((u >> 16) & 1u); return u >> 16; }
constexpr unsigned bf2(double a, double b) { return bfb(a) | (bfb(b) << 16); }
constexpr unsigned fbits(double v) { return __builtin_bit_cast(unsigned, (float)v); }
struct Tab { unsigned v[16][64][4]; };
constexpr Tab make_tab() {
  Tab t{};
  for (int l = 0; l < 64; ++l) {
    const int g = l >> 4, l15 = l & 15, hh = l >> 5, n = l & 31;
    for (int mtx = 0; mtx < 3; ++mtx) {
      double r[4] = {}, im[4] = {};
      for (int q = 0; q < 4; ++q) {
        const int a = 4 * g + q;
        if (mtx == 0) { r[q] = wre(l15 * a, 16); im[q] = wim(l15 * a, 16); }
        else if (mtx == 1) { r[q] = wre(a * (2 * l15 + 1), 32); im[q] = wim(a * (2 * l15 + 1), 32); }
        else { r[q] = wre(l15 * (2 * a + 1), 32); im[q] = -wim(l15 * (2 * a + 1), 32); }
      }
      t.v[2 * mtx][l][0] = bf2(r[0], r[1]); t.v[2 * mtx][l][1] = bf2(r[2], r[3]); t.v[2 * mtx][l][2] = bf2(-im[0], -im[1]); t.v[2 * mtx][l][3] = bf2(-im[2], -im[3]);
      t.v[2 * mtx + 1][l][0] = bf2(im[0], im[1]); t.v[2 * mtx + 1][l][1] = bf2(im[2], im[3]); t.v[2 * mtx + 1][l][2] = bf2(r[0], r[1]); t.v[2 * mtx + 1][l][3] = bf2(r[2], r[3]);
    }
    {
      const int m2 = n >> 2, pp = (n >> 1) & 1, cp = n & 1;
      double v0[8] = {}, v1[8] = {};
      for (int jj = 0; jj < 8; ++jj) {
        const int ii = (8 * hh + jj) >> 1, d0 = jj & 1;
        const double sg = (d0 & pp) ? -1.0 : 1.0, orr = sg * wre(ii * m2, 8), oi = sg * wim(ii * m2, 8);
        v0[jj] = cp ? oi : orr; v1[jj] = cp ? orr : -oi;
      }
      for (int q = 0; q < 4; ++q) { t.v[6][l][q] = bf2(v0[2 * q], v0[2 * q + 1]); t.v[7][l][q] = bf2(v1[2 * q], v1[2 * q + 1]); }
    }
    {
      const int cq = n >> 4, ii = (n >> 1) & 7, d0 = n & 1;
      for (int s = 0; s < 2; ++s) {
        double v[8] = {};
        for (int jj = 0; jj < 8; ++jj) {
          const int m2k = 4 * s + 2 * hh + (jj >> 2), pk = (jj >> 1) & 1, ck = jj & 1;
          const double sg = (d0 & pk) ? -1.0 : 1.0, orr = sg * wre(ii * m2k, 8), oi = -sg * wim(ii * m2k, 8);
          v[jj] = cq ? (ck ? orr : oi) : (ck ? -oi : orr);
        }
        for (int q = 0; q < 4; ++q) t.v[8 + s][l][q] = bf2(v[2 * q], v[2 * q + 1]);
      }
    }
    for (int q = 0; q < 4; ++q) {
      const int a = 4 * g + q;
      t.v[10][l][q] = fbits(wre(a * (2 * l15), 512));      t.v[11][l][q] = fbits(wim(a * (2 * l15), 512));
      t.v[12][l][q] = fbits(wre(a * (2 * l15 + 1), 512));  t.v[13][l][q] = fbits(wim(a * (2 * l15 + 1), 512));
      t.v[14][l][q] = fbits(wre(l15 * (2 * a + 1), 512));  t.v[15][l][q] = fbits(wim(l15 * (2 * a + 1), 512));
    }
  }
  return t;
}
__device__ const Tab kTab = make_tab();

constexpr unsigned STAMP_OFF = 512 * 1024;
#define STAMP(k) do {} while (0)
#define FT(k) do {} while (0)

DI v4u make_srd(const void* p, unsigned bytes) {
  const unsigned long long a = (unsigned long long)p;
  v4u r; r.x = (unsigned)a; r.y = (unsigned)(a >> 32) & 0xffffu; r.z = bytes; r.w = 0x00020000u;
  return r;
}
template <unsigned LDSI, unsigned SOI> DI void dma16(v4u srd, unsigned voff, unsigned ldsb, unsigned sob) {
  unsigned tmp;
  asm volatile("s_add_u32 m0, %3, %5\n\ts_add_u32 %0, %4, %6\n\tbuffer_load_dwordx4 %1, %2, %0 offen lds"
               : "=&s"(tmp) : "v"(voff), "s"(srd), "s"(ldsb), "s"(sob), "i"(LDSI), "i"(SOI) : "memory", "scc");
}
template <unsigned LDSI, unsigned SOI> DI void dma16z(v4u srd, unsigned voff, unsigned ldsb) {
  unsigned tmp;
  asm volatile("s_add_u32 m0, %3, %4\n\ts_mov_b32 %0, %5\n\tbuffer_load_dwordx4 %1, %2, %0 offen lds"
               : "=&s"(tmp) : "v"(voff), "s"(srd), "s"(ldsb), "i"(LDSI), "i"(SOI) : "memory", "scc");
}
DI unsigned uni(unsigned v) { return (unsigned)__builtin_amdgcn_readfirstlane((int)v); }
#define LD16(dst, voffv, srd, soffv, flags) asm volatile("buffer_load_dwordx4 %0, %1, %2, %3 offen " flags : "=v"(dst) : "v"(voffv), "s"(srd), "s"(soffv) : "memory")
#define LD16I(dst, voffv, srd, sob, soi, flags) do { unsigned tmp_; asm volatile("s_add_u32 %1, %4, %5\n\tbuffer_load_dwordx4 %0, %2, %3, %1 offen " flags : "=v"(dst), "=&s"(tmp_) : "v"(voffv), "s"(srd), "s"(sob), "i"(soi) : "memory", "scc"); } while (0)
#define LD16Z(dst, voffv, srd, flags) asm volatile("buffer_load_dwordx4 %0, %1, %2, 0 offen " flags : "=v"(dst) : "v"(voffv), "s"(srd) : "memory")
#define LD4Z(dst, voffv, srd, flags) asm volatile("buffer_load_dword %0, %1, %2, 0 offen " flags : "=v"(dst) : "v"(voffv), "s"(srd) : "memory")
#define LD2Z(dst, voffv, srd, flags) asm volatile("buffer_load_ushort %0, %1, %2, 0 offen " flags : "=v"(dst) : "v"(voffv), "s"(srd) : "memory")
#define LD8Z(dst, voffv, srd, flags) asm volatile("buffer_load_dwordx2 %0, %1, %2, 0 offen " flags : "=v"(dst) : "v"(voffv), "s"(srd) : "memory")
#define LD8(dst, voffv, srd, soffv, flags) asm volatile("buffer_load_dwordx2 %0, %1, %2, %3 offen " flags : "=v"(dst) : "v"(voffv), "s"(srd), "s"(soffv) : "memory")
#define LD4(dst, voffv, srd, soffv, flags) asm volatile("buffer_load_dword %0, %1, %2, %3 offen " flags : "=v"(dst) : "v"(voffv), "s"(srd), "s"(soffv) : "memory")
#define LD2(dst, voffv, srd, soffv, flags) asm volatile("buffer_load_ushort %0, %1, %2, %3 offen " flags : "=v"(dst) : "v"(voffv), "s"(srd), "s"(soffv) : "memory")
#define XSTR(x) #x
#define STR(x) XSTR(x)
#define BAR_VM(n) asm volatile("s_waitcnt vmcnt(" STR(n) ") lgkmcnt(0)\n\ts_barrier" ::: "memory")
#define BAR_LGKM() asm volatile("s_waitcnt lgkmcnt(0)\n\ts_barrier" ::: "memory")
#define SCHED() __builtin_amdgcn_sched_barrier(0)
template <int N> DI void bar_vm() { asm volatile("s_waitcnt vmcnt(%0) lgkmcnt(0)\n\ts_barrier" :: "i"(N) : "memory"); }
template <int N> DI void wait_vm() { asm volatile("s_waitcnt vmcnt(%0)" :: "i"(N) : "memory"); }

DI unsigned pk2(float a, float b) { f32x2 p = {a, b}; return __builtin_bit_cast(unsigned, __builtin_convertvector(p, bf16x2)); }
DI float bflo(unsigned u) { return __builtin_bit_cast(float, u << 16); }
DI float bfhi(unsigned u) { return __builtin_bit_cast(float, u & 0xffff0000u); }
DI float asf(unsigned u) { return __builtin_bit_cast(float, u); }
DI f32x16 mfma32(v4u a, v4u b, f32x16 c) {
  return __builtin_amdgcn_mfma_f32_32x32x16_bf16(__builtin_bit_cast(bf16x8, a), __builtin_bit_cast(bf16x8, b), c, 0, 0, 0);
}
DI f32x4 mfma16(v4u a, v4u b, f32x4 c) {
  return __builtin_amdgcn_mfma_f32_16x16x32_bf16(__builtin_bit_cast(bf16x8, a), __builtin_bit_cast(bf16x8, b), c, 0, 0, 0);
}
DI v2u tr_read(LAS char* p) { return __builtin_bit_cast(v2u, __builtin_amdgcn_ds_read_tr16_b64_v4i16((LAS s16x4*)p)); }
DI v4u tr_read2(LAS char* p0, LAS char* p1) { v2u a = tr_read(p0), b = tr_read(p1); v4u r; r.x = a.x; r.y = a.y; r.z = b.x; r.w = b.y; return r; }
DI v4u lds_ld16(LAS char* p) { return *(LAS v4u*)p; }
DI float cosrev(float r) { return __builtin_amdgcn_cosf(r); }
DI float sinrev(float r) { return __builtin_amdgcn_sinf(r); }
DI __amdgpu_buffer_rsrc_t as_rsrc(v4u srd) { return __builtin_amdgcn_make_buffer_rsrc((void*)(((unsigned long long)srd.y << 32) | srd.x), 0, srd.z, 0x00020000); }
DI void st16_sc1(v4u srd, unsigned voff, v4u v) { __builtin_amdgcn_raw_buffer_store_b128(v, as_rsrc(srd), voff, 0, 16); }
DI void flag_store(void* ws, unsigned off, unsigned tag) { __hip_atomic_store((GAS unsigned*)((GAS char*)ws + off), tag, __ATOMIC_RELAXED, __HIP_MEMORY_SCOPE_AGENT); }
template <int CTRL, int RMASK> DI float dpp_add(float v) { return v + asf((unsigned)__builtin_amdgcn_update_dpp(0, (int)__builtin_bit_cast(unsigned, v), CTRL, RMASK, 0xf, true)); }
DI void poll_words(v4u sws, unsigned off, int n, unsigned tag, int lane) {
  const unsigned vo = off + FS * (unsigned)(lane < n ? lane : 0);
#pragma unroll 1
  for (unsigned spins = 0; spins < kSpinCap; ++spins) {
    unsigned f;
    asm volatile("buffer_load_dword %0, %1, %2, 0 offen sc1\n\ts_waitcnt vmcnt(0)" : "=&v"(f) : "v"(vo), "s"(sws) : "memory");
    if (__all(f == tag)) return;
    __builtin_amdgcn_s_sleep(4);
  }
}

constexpr unsigned A_XIMG = 64 * 1024, A_FIMG = 128 * 1024;
DI void phaseA_genF(LAS char* L, int i, int mt, int w, int lane) {
  const int mh = w >> 2, sg = w & 3, n = lane & 31, hh = lane >> 5;
  const int m1c = 64 * mt + 32 * mh + n, m1 = m1c >> 1, c = m1c & 1;
  const int j0 = 64 * sg + 32 * hh;
  const float ph = c ? 0.25f : 0.f;
  const float k2 = 2.f * cosrev((float)(8 * m1) * (1.f / 2048.f));
  float vm = cosrev((float)(((i + 8 * (j0 - 1)) * m1) & 2047) * (1.f / 2048.f) + ph);
  float v0 = cosrev((float)(((i + 8 * j0) * m1) & 2047) * (1.f / 2048.f) + ph);
  const unsigned cst = c ? 0x3F800000u : 0x00003F80u;
  const bool sp = m1c < 2;
#pragma unroll
  for (int s = 0; s < 4; ++s) {
    float e[8];
#pragma unroll
    for (int q = 0; q < 8; ++q) { e[q] = v0; const float vn = __builtin_fmaf(k2, v0, -vm); vm = v0; v0 = vn; }
    v4u f; f.x = pk2(e[0], e[1]); f.y = pk2(e[2], e[3]); f.z = pk2(e[4], e[5]); f.w = pk2(e[6], e[7]);
    if (mt == 0 && w < 4) { f.x = sp ? cst : f.x; f.y = sp ? cst : f.y; f.z = sp ? cst : f.z; f.w = sp ? cst : f.w; }
    *(LAS v4u*)(L + A_FIMG + (mh * 16 + sg * 4 + s) * 1024 + lane * 16) = f;
  }
}

struct Frag1 { v4u a[4], b0[4], b1[4]; };
DI void g1_read(Frag1& f, LAS char* wa, LAS char* xb0, LAS char* xb1, LAS char* xb2, LAS char* xb3, int slot) {
  const int so = slot * 32768;
  f.a[0] = tr_read2(wa + so, wa + so + 1024);              f.b0[0] = lds_ld16(xb0 + so); f.b1[0] = lds_ld16(xb0 + so + 4096);
  f.a[1] = tr_read2(wa + so + 4096, wa + so + 5120);       f.b0[1] = lds_ld16(xb1 + so); f.b1[1] = lds_ld16(xb1 + so + 4096);
  f.a[2] = tr_read2(wa + so + 8192, wa + so + 9216);       f.b0[2] = lds_ld16(xb2 + so); f.b1[2] = lds_ld16(xb2 + so + 4096);
  f.a[3] = tr_read2(wa + so + 12288, wa + so + 13312);     f.b0[3] = lds_ld16(xb3 + so); f.b1[3] = lds_ld16(xb3 + so + 4096);
}
DI unsigned silu2(float a, float b) {
  const f32x2 h = {a, b}, t = h * -1.44269504088896341f;
  f32x2 e = {__builtin_amdgcn_exp2f(t[0]), __builtin_amdgcn_exp2f(t[1])};
  e = e + 1.f;
  const f32x2 r = {__builtin_amdgcn_rcpf(e[0]), __builtin_amdgcn_rcpf(e[1])};
  return __builtin_bit_cast(unsigned, __builtin_convertvector(h * r, bf16x2));
}

DI f32x2 lo2(const f32x4& v) { return __builtin_shufflevector(v, v, 0, 1); }
DI f32x2 hi2(const f32x4& v) { return __builtin_shufflevector(v, v, 2, 3); }
DI unsigned pk2v(f32x2 p) { return __builtin_bit_cast(unsigned, __builtin_convertvector(p, bf16x2)); }
DI f32x4 asf4(v4u v) { return __builtin_bit_cast(f32x4, v); }
template <bool CONJ> DI v4u cmul_pack(const f32x4& re, const f32x4& im, const f32x4& tr, const f32x4& ti) {
  const f32x2 r0 = lo2(re), r1 = hi2(re), i0 = lo2(im), i1 = hi2(im), c0 = lo2(tr), c1 = hi2(tr), s0 = lo2(ti), s1 = hi2(ti);
  f32x2 a0, a1, b0, b1;
  if (CONJ) {
    a0 = __builtin_elementwise_fma(r0, c0, i0 * s0); a1 = __builtin_elementwise_fma(r1, c1, i1 * s1);
    b0 = __builtin_elementwise_fma(i0, c0, -(r0 * s0)); b1 = __builtin_elementwise_fma(i1, c1, -(r1 * s1));
  } else {
    a0 = __builtin_elementwise_fma(r0, c0, -(i0 * s0)); a1 = __builtin_elementwise_fma(r1, c1, -(i1 * s1));
    b0 = __builtin_elementwise_fma(r0, s0, i0 * c0); b1 = __builtin_elementwise_fma(r1, s1, i1 * c1);
  }
  v4u r; r.x = pk2v(a0); r.y = pk2v(a1); r.z = pk2v(b0); r.w = pk2v(b1); return r;
}
struct cf { float r, i; };
DI cf cmul(cf a, cf b) { return {a.r * b.r - a.i * b.i, a.r * b.i + a.i * b.r}; }
DI cf cmulc(cf a, cf b) { return {a.r * b.r + a.i * b.i, a.i * b.r - a.r * b.i}; }
DI unsigned w_src(int m, int lane) {
  const int mm = (NS - m) & (NS - 1);
  const int e = lane < 32 ? m * 257 - (m & 1) : mm * 257 + (m & 1);
  return (unsigned)(e * 2 + (lane & 31) * 16);
}
DI unsigned w_tail_src(int m, int lane) {
  const int mm = (NS - m) & (NS - 1), l = lane & 3;
  return (unsigned)((l < 2 ? m * 257 + 255 + l : mm * 257 + (l - 2) * 256) * 2);
}
struct WTail { unsigned r255, r256, rm0, rm256, i255, i256, im0, im256; };
DI WTail w_tail(unsigned tr, unsigned ti) {
  return {(unsigned)__builtin_amdgcn_readlane((int)tr, 0), (unsigned)__builtin_amdgcn_readlane((int)tr, 1), (unsigned)__builtin_amdgcn_readlane((int)tr, 2), (unsigned)__builtin_amdgcn_readlane((int)tr, 3),
          (unsigned)__builtin_amdgcn_readlane((int)ti, 0), (unsigned)__builtin_amdgcn_readlane((int)ti, 1), (unsigned)__builtin_amdgcn_readlane((int)ti, 2), (unsigned)__builtin_amdgcn_readlane((int)ti, 3)};
}
struct WVal { f32x4 r0, i0, r1, i1; };
struct WRawL { unsigned r[4][2], i[4][2]; };
DI void w_put(LAS char* scr, v4u qr, v4u qi, int lane) { *(LAS v4u*)(scr + lane * 16) = qr; *(LAS v4u*)(scr + 1024 + lane * 16) = qi; }
DI int w_off(int m, int lane) { const int k1 = lane & 15, g = lane >> 4, odd = m & 1; return lane < 32 ? (2 * k1 + 128 * g + odd) * 2 : 512 + (511 - 2 * k1 - 128 * g - odd) * 2; }
DI void w_get(WRawL& o, LAS char* scr, int off, int lane, int r) {
  LAS unsigned* a = (LAS unsigned*)(scr + ((off + (lane < 32 ? 64 : -64) * r) & ~3));
  o.r[r][0] = a[0]; o.r[r][1] = a[1]; o.i[r][0] = a[256]; o.i[r][1] = a[257];
}
DI WVal w_unpack(const WRawL& x, const WTail& t, int off, int m, int lane) {
  const bool lo = lane < 32;
  const unsigned sh = (off & 2) * 8;
  unsigned rr[4], ii[4];
#pragma unroll
  for (int r = 0; r < 4; ++r) { rr[r] = __builtin_amdgcn_alignbit(x.r[r][1], x.r[r][0], sh); ii[r] = __builtin_amdgcn_alignbit(x.i[r][1], x.i[r][0], sh); }
  if (m & 1) { rr[3] = lane == 31 ? (rr[3] & 0xffffu) | (t.r255 << 16) : rr[3]; ii[3] = lane == 31 ? (ii[3] & 0xffffu) | (t.i255 << 16) : ii[3]; }
  else { rr[0] = lane == 32 ? (rr[0] & 0xffffu) | (t.rm256 << 16) : rr[0]; ii[0] = lane == 32 ? (ii[0] & 0xffffu) | (t.im256 << 16) : ii[0]; }
  const unsigned s0 = lo ? 16u : 0u, s1 = lo ? 0u : 16u, sg = lo ? 0u : 0x80000000u;
  WVal o;
#pragma unroll
  for (int r = 0; r < 4; ++r) {
    o.r0[r] = asf((rr[r] << s0) & 0xffff0000u); o.r1[r] = asf((rr[r] << s1) & 0xffff0000u);
    o.i0[r] = asf(((ii[r] << s0) & 0xffff0000u) ^ sg); o.i1[r] = asf(((ii[r] << s1) & 0xffff0000u) ^ sg);
  }
  const bool av = (lane & 31) == 0;
  const float xr = bflo(lo ? t.rm0 : t.r256), xi = asf(((lo ? t.im0 : t.i256) << 16) ^ (sg ^ 0x80000000u));
  o.r0[0] = av ? 0.5f * (o.r0[0] + xr) : o.r0[0];
  o.i0[0] = av ? 0.5f * (o.i0[0] + xi) : o.i0[0];
  return o;
}

DI void packed_coef(WVal& a, WVal& b, int m2) {
  const cf om = {cosrev((float)m2 * 0.0625f), -sinrev((float)m2 * 0.0625f)};
#pragma unroll
  for (int pp = 0; pp < 2; ++pp)
#pragma unroll
    for (int r = 0; r < 4; ++r) {
      const float war = pp ? a.r1[r] : a.r0[r], wai = pp ? a.i1[r] : a.i0[r], wbr = pp ? b.r1[r] : b.r0[r], wbi = pp ? b.i1[r] : b.i0[r];
      const cf sp = {war + wbr, wai + wbi}, sm = {war - wbr, wai - wbi};
      const cf t1 = cmulc(sm, om), t2 = cmul(sm, om);
      const cf cE = {sp.r - t1.i, sp.i + t1.r}, cO = {t2.r - sp.i, t2.i + sp.r};
      const float alr = 0.25f * (cE.r + cO.i), ali = 0.25f * (cE.i - cO.r), ber = 0.25f * (cE.r - cO.i), bei = 0.25f * (cE.i + cO.r);
      if (pp) { a.r1[r] = alr; a.i1[r] = ali; b.r1[r] = ber; b.i1[r] = bei; } else { a.r0[r] = alr; a.i0[r] = ali; b.r0[r] = ber; b.i0[r] = bei; }
    }
}

struct Geo {
  int tid, lane, w, b, i, i1, at, m1, rb, fc, hh, blk, q4, p4;
  int jb, dt, rh, q, j;
  v4u sx, sws, sw2;
  unsigned vw20, vw21, ldsw, fq0b, fq1b;
};
template <int T, int U> DI void w2_dma(const v4u& sw2, unsigned vw20, unsigned vw21, unsigned ldsw, unsigned fq0, unsigned fq1) {
  dma16<(T & 7) * 16384u + U * 1024u, (T < 8 ? T : T - 8) * 32768u>(sw2, U ? vw21 : vw20, ldsw, T < 8 ? fq0 : fq1);
}
#define w2_issue1(g, t, u) (w2_dma<t, u>((g).sw2, (g).vw20, (g).vw21, (g).ldsw, (g).fq0b, (g).fq1b))
struct GRot { float c1, s1, c4, s4, c, s; };
DI void gen_g_init(const Geo& g, GRot& r) {
  const int srow = g.i + 8 * g.j;
  const int mb = 32 * g.q + 2 * g.hh;
  const float r1 = (float)(srow & 2047) * (1.f / 2048.f), r4 = (float)((4 * srow) & 2047) * (1.f / 2048.f), r0 = (float)((mb * srow) & 2047) * (1.f / 2048.f);
  r.c1 = cosrev(r1); r.s1 = sinrev(r1); r.c4 = cosrev(r4); r.s4 = sinrev(r4); r.c = cosrev(r0); r.s = sinrev(r0);
}
template <int S> DI void gen_g_frag(const Geo& g, GRot& r, v4u& f) {
#pragma unroll
  for (int h = 0; h < 2; ++h) {
    const float cb = __builtin_fmaf(r.c, r.c1, -(r.s * r.s1)), sb = __builtin_fmaf(r.s, r.c1, r.c * r.s1);
    unsigned w0 = pk2(r.c, -r.s), w1 = pk2(cb, -sb);
    if (S == 0 && h == 0) w0 = (g.q == 0 && g.hh == 0) ? ((g.j & 1) ? 0x3F800000u : 0x00003F80u) : w0;
    if (h) { f.z = w0; f.w = w1; } else { f.x = w0; f.y = w1; }
    const float cn = __builtin_fmaf(r.c, r.c4, -(r.s * r.s4)), sn = __builtin_fmaf(r.s, r.c4, r.c * r.s4);
    r.c = cn; r.s = sn;
  }
  asm volatile("" : "+v"(f));
}
template <int N0, int N1, int NR> DI void bar_w(int w) { if (w == 0) bar_vm<N0>(); else if (w == 1) bar_vm<N1>(); else bar_vm<NR>(); }
#define NOP do {} while (0)

#define TLBW 0
#define TOUCH() do {                                                                                                      \
    const int gl = G.b * 256 + (w & 3) * 64 + lane;                                                                         \
    const char* base_ = gl < 14336 ? (const char*)p.w2 : gl < 28672 ? (const char*)p.w1 - 14336 * 128 : gl < 36896 ? (const char*)p.wr - 28672 * 128           \
                      : gl < 45120 ? (const char*)p.wi - 36896 * 128 : (const char*)p.x - 45120 * 128;                     \
    const char* ad_ = base_ + (size_t)(gl < 61504 ? gl : 45120) * 128;                                                      \
    if (w >= 4) ad_ = TLBW ? (w == 7 ? (const char*)p.out + lane * 32768 : (const char*)p.ws + (size_t)((w - 4) * 64 + lane + 8) * 65536 + 32768 - 128) : (const char*)p.x; \
    asm volatile("global_load_dword %0, %1, off" : "=v"(touch) : "v"(ad_) : "memory");                                     \
  } while (0)
template <bool ISA>
DI unsigned phase1(const Params& p, LAS char* L, const Geo& G, v4u& m5a, v4u& m5b
) {
  const int tid = G.tid, lane = G.lane, w = G.w, i = G.i, i1 = G.i1, rb = G.rb, fc = G.fc, hh = G.hh, blk = G.blk, q4 = G.q4, p4 = G.p4;
  const v4u sx = G.sx, sws = G.sws, sw1 = make_srd(p.w1, ND * NF * 2);
  unsigned touch;
  unsigned ep; v4u gx, gv;
  const int jg = 128 * rb + 8 * fc + w;
  LD16Z(gx, (unsigned)(lane * 16 + (i1 + 8 * jg) * 1024), sx, "");
  LD16Z(gv, (unsigned)lane * 16u, make_srd(p.gw, ND * 2), "");
  LD4Z(ep, WS_EPOCH, sws, "sc1");
  const int f0 = ISA ? 96 * fc : 768 + 128 * (fc - 8), nf = ISA ? 96 : 128;
  const int mt = G.at >> 2, dt4 = G.at & 3;
  const int m1 = G.m1;
  const v4u swr = make_srd(p.wr, NS * 257 * 2), swi = make_srd(p.wi, NS * 257 * 2);
  const int mW = m1 == 0 ? 128 * w : m1 + 256 * w;
  v4u wqr, wqi, xqr, xqi;
  unsigned wtr, wti, xtr, xti;
  WVal Wc, Wd;
  if (ISA) {
    const unsigned voff = (unsigned)(lane >> 4) * 8192u + (unsigned)(((lane & 15) ^ ((lane >> 4) << 2)) << 4);
    const unsigned xl = uni(A_XIMG + (unsigned)w * 8192u), xs = uni((unsigned)i * 1024u + (unsigned)dt4 * 256u + (unsigned)w * 262144u);
    dma16<0, 0>(sx, voff, xl, xs); dma16<1024, 32768>(sx, voff, xl, xs); dma16<2048, 65536>(sx, voff, xl, xs); dma16<3072, 98304>(sx, voff, xl, xs);
    dma16<4096, 131072>(sx, voff, xl, xs); dma16<5120, 163840>(sx, voff, xl, xs); dma16<6144, 196608>(sx, voff, xl, xs); dma16<7168, 229376>(sx, voff, xl, xs);
  }
  unsigned vx0, vx1, vw0, vw1;
  {
    const int r0 = 16 * w + (lane >> 3), pos = lane & 7, r1 = r0 + 8;
    vx0 = (unsigned)(i1 * 1024 + (128 * rb + r0) * 8192 + ((pos ^ ((r0 >> 1) & 7)) << 4));
    vx1 = (unsigned)(i1 * 1024 + (128 * rb + r1) * 8192 + ((pos ^ ((r1 >> 1) & 7)) << 4));
    const int k0 = 8 * w + (lane >> 4), chunk = (lane & 15) ^ ((lane >> 4) << 2);
    const bool ok = chunk * 8 < nf;
    vw0 = ok ? (unsigned)(k0 * 3584 + f0 * 2 + chunk * 16) : 0x80000000u;
    vw1 = ok ? vw0 + 4u * 3584u : 0x80000000u;
  }
  const unsigned g1l = uni((unsigned)w * 2048u);
#define g1_dma(kt, u) do {                                                                                         \
    if ((u) == 0) dma16z<((kt) & 3) * 32768u, (kt) * 128u>(sx, vx0, g1l);                                           \
    if ((u) == 1) dma16z<((kt) & 3) * 32768u + 1024u, (kt) * 128u>(sx, vx1, g1l);                                   \
    if ((u) == 2) dma16z<((kt) & 3) * 32768u + 16384u, (kt) * (64u * 3584u)>(sw1, vw0, g1l);                        \
    if ((u) == 3) dma16z<((kt) & 3) * 32768u + 17408u, (kt) * (64u * 3584u)>(sw1, vw1, g1l); } while (0)
#define g1_issue(kt) do { g1_dma(kt, 0); g1_dma(kt, 1); g1_dma(kt, 2); g1_dma(kt, 3); } while (0)
  if (ISA) {
    phaseA_genF(L, i, mt, w, lane);
    g1_issue(0); g1_issue(1);
  } else {
    { const unsigned wsrc = w_src(mW, lane), wts = w_tail_src(mW, lane);
      LD16Z(wqr, wsrc, swr, ""); LD16Z(wqi, wsrc, swi, ""); LD2Z(wtr, wts, swr, ""); LD2Z(wti, wts, swi, ""); }
    asm volatile("s_barrier" ::: "memory");
    g1_issue(0);
    if (m1 == 0) {
      const unsigned ws2 = w_src(128 * w + 1024, lane), wt2 = w_tail_src(128 * w + 1024, lane);
      LD16Z(xqr, ws2, swr, ""); LD16Z(xqi, ws2, swi, ""); LD2Z(xtr, wt2, swr, ""); LD2Z(xti, wt2, swi, "");
    }
    asm volatile("s_barrier" ::: "memory");
    g1_issue(1); asm volatile("s_barrier" ::: "memory");
    g1_issue(2); asm volatile("s_barrier" ::: "memory");
    g1_issue(3);
  }
  TOUCH();
  if (ISA) wait_vm<17>(); else wait_vm<21>();
  asm volatile("" : "+v"(ep), "+v"(gx), "+v"(gv) :: "memory");
  const unsigned tag = (unsigned)__builtin_amdgcn_readfirstlane((int)ep) + 1u;
  float gate_g, gate_og;
  {
    float z = 0.f;
#pragma unroll
    for (int q = 0; q < 4; ++q) {
      const unsigned pr = pk2(bflo(gx[q]) * bflo(gv[q]), bfhi(gx[q]) * bfhi(gv[q]));
      z += bflo(pr) + bfhi(pr);
    }
    z = dpp_add<0x111, 0xf>(z); z = dpp_add<0x112, 0xf>(z); z = dpp_add<0x114, 0xf>(z); z = dpp_add<0x118, 0xf>(z);
    z = dpp_add<0x142, 0xa>(z); z = dpp_add<0x143, 0xc>(z);
    const float zb = bflo(pk2(z, 0.f));
    const float g = bflo(pk2(__builtin_amdgcn_rcpf(1.f + __builtin_amdgcn_exp2f(zb * -1.44269504088896341f)), 0.f));
    gate_g = asf((unsigned)__builtin_amdgcn_readlane((int)__builtin_bit_cast(unsigned, g), 63));
    gate_og = bflo(pk2(1.f - gate_g, 0.f));
  }
  if (!ISA) {
    asm volatile("s_waitcnt vmcnt(17)" : "+v"(wqr), "+v"(wqi), "+v"(wtr), "+v"(wti) :: "memory");
    LAS char* wscr0 = L + 128 * 1024 + w * 2048;
    const int woff = w_off(mW, lane);
    w_put(wscr0, wqr, wqi, lane);
    asm volatile("s_waitcnt lgkmcnt(0)" ::: "memory");
    WRawL wraw;
    w_get(wraw, wscr0, woff, lane, 0); w_get(wraw, wscr0, woff, lane, 1); w_get(wraw, wscr0, woff, lane, 2); w_get(wraw, wscr0, woff, lane, 3);
    Wc = w_unpack(wraw, w_tail(wtr, wti), woff, mW, lane);
    asm volatile("" : "+v"(Wc.r0), "+v"(Wc.i0), "+v"(Wc.r1), "+v"(Wc.i1));
  }
  if (ISA) {
    const int dq = w & 3, mh = w >> 2;
    LAS char* xa = L + A_XIMG + (32 * hh + q4) * 256 + (((4 * dq + 2 * blk + (p4 >> 1)) ^ (q4 << 2)) << 4) + 8 * (p4 & 1);
    LAS char* fb = L + A_FIMG + mh * 16384 + lane * 16;
    const int srow = 32 * mh + (lane & 31);
    LAS char* stw = L + A_FIMG + srow * 256 + 8 * hh;
    const int rrow = tid >> 3;
    LAS char* str = L + A_FIMG + rrow * 256;
    const unsigned tdst = WS_T + (unsigned)(((64 * mt + rrow) * 8 + i) * 1024 + dt4 * 256 + (tid & 7) * 16);
    if (w < 4) bar_vm<9>(); else BAR_LGKM();
    STAMP(1);
    f32x16 acc = {};
    v4u fa[2][4], fbv[2][4];
#pragma unroll
    for (int s = 0; s < 4; ++s) { fa[0][s] = tr_read2(xa + 8 * s * 256, xa + 8 * s * 256 + 1024); fbv[0][s] = lds_ld16(fb + s * 1024); }
#pragma unroll
    for (int g = 0; g < 4; ++g) {
      acc = mfma32(fa[g & 1][0], fbv[g & 1][0], acc);
      SCHED();
      if (g == 1) { if (w >= 4) bar_vm<9>(); else BAR_LGKM(); }
      if (g < 3) {
#pragma unroll
        for (int s = 0; s < 4; ++s) {
          const int ro = (64 * (g + 1) + 8 * s) * 256;
          fa[(g + 1) & 1][s] = tr_read2(xa + ro, xa + ro + 1024); fbv[(g + 1) & 1][s] = lds_ld16(fb + (4 * (g + 1) + s) * 1024);
        }
      }
      SCHED();
#pragma unroll
      for (int s = 1; s < 4; ++s) { acc = mfma32(fa[g & 1][s], fbv[g & 1][s], acc); if (g == 1 && s == 1) { g1_dma(2, 0); g1_dma(2, 1); } if (g == 1 && s == 2) { g1_dma(2, 2); g1_dma(2, 3); } SCHED(); }
    }
    BAR_LGKM();
    STAMP(2);
    g1_issue(3);
#pragma unroll
    for (int Gq = 0; Gq < 4; ++Gq) {
      v2u o; o.x = pk2(acc[4 * Gq], acc[4 * Gq + 1]); o.y = pk2(acc[4 * Gq + 2], acc[4 * Gq + 3]);
      *(LAS v2u*)(stw + (((4 * dq + Gq) ^ (srow & 15)) << 4)) = o;
    }
    BAR_LGKM();
    {
      const v4u v0 = lds_ld16(str + (((tid & 7) ^ (rrow & 15)) << 4)), v1 = lds_ld16(str + ((((tid & 7) + 8) ^ (rrow & 15)) << 4));
      st16_sc1(sws, tdst, v0);
      st16_sc1(sws, tdst + 128u, v1);
    }
    SCHED();
  }
  const int ft = w & 3, rh1 = w >> 2;
  const bool active = ft * 32 < nf;
  f32x16 acc1[2] = {};
  v4u tq0, tq1;
  v4u tb[16];
  unsigned fla;
  int t_ok = 0;
  unsigned tsrc0 = 0, tsrc1 = 0;
  const unsigned flaoff = WS_FLA + (unsigned)(((m1 >> 5) & 3) * 32 + (lane & 31)) * FS;
  LAS char* CTL = L + 128 * 1024;
  {
    LAS char* wa = L + 16384 + (8 * hh + q4) * 256 + (((4 * ft + 2 * blk + (p4 >> 1)) ^ (q4 << 2)) << 4) + 8 * (p4 & 1);
    const int r = 64 * rh1 + (lane & 31), fz = (r >> 1) & 7;
    LAS char* xb0 = L + r * 128 + (((0 + hh) ^ fz) << 4);
    LAS char* xb1 = L + r * 128 + (((2 + hh) ^ fz) << 4);
    LAS char* xb2 = L + r * 128 + (((4 + hh) ^ fz) << 4);
    LAS char* xb3 = L + r * 128 + (((6 + hh) ^ fz) << 4);
    Frag1 fr[2];
    bar_vm<ISA ? 15 : 13>();
    STAMP(3);
    if (lane == 0) { v2u gv2; gv2.x = __builtin_bit_cast(unsigned, gate_g); gv2.y = __builtin_bit_cast(unsigned, gate_og); *(LAS v2u*)(CTL + 64 + w * 8) = gv2; }
    g1_read(fr[0], wa, xb0, xb1, xb2, xb3, 0);
#define PIN1(c) asm volatile("" : "+v"(c.a[0]), "+v"(c.a[1]), "+v"(c.a[2]), "+v"(c.a[3]), "+v"(c.b0[0]), "+v"(c.b0[1]), "+v"(c.b0[2]), "+v"(c.b0[3]), \
                             "+v"(c.b1[0]), "+v"(c.b1[1]), "+v"(c.b1[2]), "+v"(c.b1[3]))
#define G1_STEP(kt, X0, X1, X2, X3, X4, X5, X6, X7)                                                                   \
    {                                                                                                                   \
      FT(10 + (kt));                                                                                                    \
      Frag1& c = fr[(kt) & 1];                                                                                          \
      Frag1& n = fr[((kt) + 1) & 1];                                                                                    \
      const int so = (((kt) + 1) & 3) * 32768;                                                                          \
      PIN1(c);                                                                                                          \
      asm volatile("" :: "v"(acc1[0]), "v"(acc1[1]));                                                                   \
      SCHED();                                                                                                          \
      acc1[0] = mfma32(c.a[0], c.b0[0], acc1[0]); if ((kt) < 7) n.a[0] = tr_read2(wa + so, wa + so + 1024); X0; SCHED(); \
      acc1[1] = mfma32(c.a[0], c.b1[0], acc1[1]); if ((kt) < 7) { n.b0[0] = lds_ld16(xb0 + so); n.b1[0] = lds_ld16(xb0 + so + 4096); } X1; SCHED(); \
      acc1[0] = mfma32(c.a[1], c.b0[1], acc1[0]); if ((kt) < 7) n.a[1] = tr_read2(wa + so + 4096, wa + so + 5120); X2; SCHED(); \
      acc1[1] = mfma32(c.a[1], c.b1[1], acc1[1]); if ((kt) < 7) { n.b0[1] = lds_ld16(xb1 + so); n.b1[1] = lds_ld16(xb1 + so + 4096); } X3; SCHED(); \
      acc1[0] = mfma32(c.a[2], c.b0[2], acc1[0]); if ((kt) < 7) n.a[2] = tr_read2(wa + so + 8192, wa + so + 9216); X4; SCHED(); \
      acc1[1] = mfma32(c.a[2], c.b1[2], acc1[1]); if ((kt) < 7) { n.b0[2] = lds_ld16(xb2 + so); n.b1[2] = lds_ld16(xb2 + so + 4096); } X5; SCHED(); \
      acc1[0] = mfma32(c.a[3], c.b0[3], acc1[0]); if ((kt) < 7) n.a[3] = tr_read2(wa + so + 12288, wa + so + 13312); X6; SCHED(); \
      acc1[1] = mfma32(c.a[3], c.b1[3], acc1[1]); if ((kt) < 7) { n.b0[3] = lds_ld16(xb3 + so); n.b1[3] = lds_ld16(xb3 + so + 4096); } X7; SCHED(); \
    }
#define GATE_STORE() do { if (w == 2 && lane < 8) { const v2u gv2 = *(LAS v2u*)(CTL + 64 + lane * 8);                       \
      __builtin_amdgcn_raw_buffer_store_b64(gv2, as_rsrc(sws), WS_GATE + (unsigned)(i1 * 256 + 128 * rb + 8 * fc + lane) * 8u, 0, 16); } } while (0)
#define STEP8 G1_STEP
    if constexpr (ISA) {
      bar_vm<11>();
      STAMP(4);
      STEP8(0, NOP, NOP, NOP, NOP, NOP, NOP, NOP, NOP)
      bar_vm<0>();
      if (tid == 0) flag_store(p.ws, WS_FLA + (unsigned)(mt * 32 + i * 4 + dt4) * FS, tag);
      STAMP(13);
      STEP8(1, g1_dma(4, 0), g1_dma(4, 1), g1_dma(4, 2), g1_dma(4, 3), g1_dma(5, 0), g1_dma(5, 1), g1_dma(5, 2), g1_dma(5, 3))
      BAR_LGKM();
      GATE_STORE();
      STEP8(2, NOP, NOP, NOP, NOP, g1_dma(6, 0), g1_dma(6, 1), g1_dma(6, 2), g1_dma(6, 3))
      bar_vm<8>();
      STEP8(3, NOP, NOP, NOP, NOP, g1_dma(7, 0), g1_dma(7, 1), g1_dma(7, 2), g1_dma(7, 3))
      bar_vm<8>();
      STAMP(12);
      STEP8(4, NOP, NOP, NOP, NOP, w2_issue1(G, 0, 0), w2_issue1(G, 0, 1), w2_issue1(G, 1, 0), w2_issue1(G, 1, 1))
      bar_vm<8>();
      STEP8(5, NOP, NOP, NOP, NOP, w2_issue1(G, 2, 0), w2_issue1(G, 2, 1), w2_issue1(G, 3, 0), w2_issue1(G, 3, 1))
      bar_vm<8>();
      STEP8(6, NOP, NOP, NOP, NOP, w2_issue1(G, 4, 0), w2_issue1(G, 4, 1), w2_issue1(G, 5, 0), w2_issue1(G, 5, 1))
      BAR_LGKM();
    } else {
      const v4u stab = make_srd(&kTab, sizeof(Tab));
      {
        const int kap = tid >> 5, ii = (kap >> 1) & 7, d0 = kap & 1;
        const int g0 = m1 == 0 ? 2 * ii : ii, g1 = m1 == 0 ? 2 * ii + 1 : 8 + ii;
        const unsigned tb = WS_T + (unsigned)m1 * 16384u + (unsigned)(d0 * 512 + (tid & 31) * 16);
        tsrc0 = tb + (unsigned)g0 * 1024u; tsrc1 = tb + (unsigned)g1 * 1024u;
      }
      bar_vm<9>();
      STAMP(4);
      STEP8(0, NOP, NOP, NOP, NOP, g1_dma(4, 0), g1_dma(4, 1), g1_dma(4, 2), g1_dma(4, 3))
      bar_vm<9>();
      STEP8(1, NOP, NOP, NOP, NOP, g1_dma(5, 0), g1_dma(5, 1), g1_dma(5, 2), g1_dma(5, 3))
      bar_vm<9>();
      GATE_STORE();
      STEP8(2, NOP, NOP, NOP, NOP, g1_dma(6, 0), g1_dma(6, 1), g1_dma(6, 2), g1_dma(6, 3))
      bar_vm<8>();
      LAS char* wscr0 = L + 128 * 1024 + w * 2048;
      const bool special = m1 == 0;
      const int m2nd = 128 * w + 1024, off2 = w_off(m2nd, lane);
      if (special) { asm volatile("" : "+v"(xqr), "+v"(xqi), "+v"(xtr), "+v"(xti)); w_put(wscr0, xqr, xqi, lane); }
      STEP8(3, NOP, NOP, (dma16<128u * 1024u + 16384u, 0>(stab, (unsigned)lane * 16u, g1l, g1l)), (dma16<128u * 1024u + 17408u, 1024>(stab, (unsigned)lane * 16u, g1l, g1l)),
            g1_dma(7, 0), g1_dma(7, 1), g1_dma(7, 2), g1_dma(7, 3))
      bar_vm<10>();
      STAMP(12);
      WRawL raw2;
      if (special) { w_get(raw2, wscr0, off2, lane, 0); w_get(raw2, wscr0, off2, lane, 1); w_get(raw2, wscr0, off2, lane, 2); w_get(raw2, wscr0, off2, lane, 3); }
      STEP8(4, NOP, NOP, NOP, NOP, w2_issue1(G, 0, 0), w2_issue1(G, 0, 1), w2_issue1(G, 1, 0), w2_issue1(G, 1, 1))
      bar_vm<10>();
      if (special) { Wd = w_unpack(raw2, w_tail(xtr, xti), off2, m2nd, lane); packed_coef(Wc, Wd, w); }
      STEP8(5, NOP, NOP, NOP, NOP, w2_issue1(G, 2, 0), w2_issue1(G, 2, 1), w2_issue1(G, 3, 0), w2_issue1(G, 3, 1))
      bar_vm<8>();
      STEP8(6, if (w == 0) LD4Z(fla, flaoff, sws, "sc1"), NOP, NOP, NOP, w2_issue1(G, 4, 0), w2_issue1(G, 4, 1), w2_issue1(G, 5, 0), w2_issue1(G, 5, 1))
      BAR_LGKM();
    }
    if constexpr (ISA) {
      STEP8(7, NOP, NOP, NOP, NOP, w2_issue1(G, 6, 0), w2_issue1(G, 6, 1), w2_issue1(G, 7, 0), w2_issue1(G, 7, 1))
    } else {
      LAS char* TB = L + 128 * 1024 + 16384 + lane * 16;
#define TBR(f) (tb[f] = lds_ld16(TB + (f) * 1024), tb[(f) + 1] = lds_ld16(TB + ((f) + 1) * 1024))
      STEP8(7, TBR(6), TBR(0), TBR(10), TBR(2), { TBR(12); w2_issue1(G, 6, 0); }, { TBR(14); w2_issue1(G, 6, 1); }, { TBR(4); w2_issue1(G, 7, 0); }, { TBR(8); w2_issue1(G, 7, 1); })
      if (w == 0) {
        asm volatile("s_waitcnt vmcnt(8)" : "+v"(fla) :: "memory");
        const unsigned ok = __all(fla == tag) ? 1u : 0u;
        if (lane == 0) *(LAS unsigned*)CTL = ok;
      }
      BAR_LGKM();
      t_ok = __builtin_amdgcn_readfirstlane(*(volatile LAS unsigned*)CTL);
      if (t_ok) { LD16Z(tq0, tsrc0, sws, "sc1"); LD16Z(tq1, tsrc1, sws, "sc1"); }
    }
  }
  asm volatile("" :: "v"(touch));
  STAMP(5);
  FT(18);
  if (active) {
#pragma unroll
    for (int rt = 0; rt < 2; ++rt)
#pragma unroll
      for (int u = 0; u < 2; ++u) {
        v4u o;
        o.x = silu2(acc1[rt][8 * u + 0], acc1[rt][8 * u + 1]);
        o.y = silu2(acc1[rt][8 * u + 2], acc1[rt][8 * u + 3]);
        o.z = silu2(acc1[rt][8 * u + 4], acc1[rt][8 * u + 5]);
        o.w = silu2(acc1[rt][8 * u + 6], acc1[rt][8 * u + 7]);
        const int jb32 = 4 * rb + 2 * rh1 + rt, fs = (f0 >> 4) + 2 * ft + u;
        st16_sc1(sws, WS_H + (unsigned)(((i1 * 8 + jb32) * 112 + fs) * 1024 + lane * 16), o);
      }
  }
  SCHED();
  STAMP(6);
  FT(20);
  const unsigned flh_own = WS_FLH + (unsigned)((i1 * 2 + rb) * 16 + fc) * FS;
  if constexpr (ISA) {
    bar_vm<0>();
    if (tid == 0) flag_store(p.ws, flh_own, tag);
  } else {
    LAS char* I0 = L + 128 * 1024;
    LAS char* I1 = L + 128 * 1024 + 16384;
    const int g = lane >> 4, l15 = lane & 15;
    const bool special = (m1 == 0);
    bool published = false;
    if (t_ok) {
      wait_vm<4>();
      asm volatile("s_barrier" ::: "memory");
    } else {
      bar_vm<0>();
      if (tid == 0) flag_store(p.ws, flh_own, tag);
      published = true;
      if (w == 0) poll_words(sws, WS_FLA + (unsigned)(m1 >> 5) * 32u * FS, 32, tag, lane);
      asm volatile("s_barrier" ::: "memory");
      LD16Z(tq0, tsrc0, sws, "sc1"); LD16Z(tq1, tsrc1, sws, "sc1");
      wait_vm<0>();
    }
    asm volatile("" : "+v"(tq0), "+v"(tq1) :: "memory");
    {
      const int kap = tid >> 5, ch = tid & 31;
      *(LAS v4u*)(I0 + kap * 512 + ((ch ^ ((kap & 3) << 2)) << 4)) = tq0;
      *(LAS v4u*)(I0 + (kap + 16) * 512 + ((ch ^ ((kap & 3) << 2)) << 4)) = tq1;
    }
    BAR_LGKM();
    FT(21);
    LAS char* ta = I0 + (8 * hh + q4) * 512 + (((4 * w + 2 * blk + (p4 >> 1)) ^ (q4 << 2)) << 4) + 8 * (p4 & 1);
    const int n = lane & 31;
    LAS char* so = I1 + n * 512 + 8 * hh;
    {
      f32x16 acc = {};
      acc = mfma32(tr_read2(ta, ta + 2048), tb[6], acc);
      acc = mfma32(tr_read2(ta + 8192, ta + 10240), tb[7], acc);
#pragma unroll
      for (int Gq = 0; Gq < 4; ++Gq) {
        v2u o; o.x = pk2(acc[4 * Gq], acc[4 * Gq + 1]); o.y = pk2(acc[4 * Gq + 2], acc[4 * Gq + 3]);
        *(LAS v2u*)(so + (((4 * w + Gq) ^ (n & 15)) << 4)) = o;
      }
    }
    BAR_LGKM();
    FT(22);
    const f32x4 z4 = {0.f, 0.f, 0.f, 0.f};
    const v4u F16fr = tb[0], F16fi = tb[1];
    v4u F16ir, F16ii;
    F16ir.x = F16fr.x; F16ir.y = F16fr.y; F16ir.z = F16fr.z ^ 0x80008000u; F16ir.w = F16fr.w ^ 0x80008000u;
    F16ii.x = F16fi.x ^ 0x80008000u; F16ii.y = F16fi.y ^ 0x80008000u; F16ii.z = F16fi.z; F16ii.w = F16fi.w;
    f32x4 zr[2], zi[2];
#pragma unroll
    for (int pp = 0; pp < 2; ++pp) {
      const int n0 = 4 * w + 2 * pp, n1 = n0 + 1;
      const int ch = 2 * (4 * g + q4) + (p4 >> 1);
      const v4u a1 = tr_read2(I1 + n0 * 512 + ((ch ^ (n0 & 15)) << 4) + 8 * (p4 & 1), I1 + n1 * 512 + ((ch ^ (n1 & 15)) << 4) + 8 * (p4 & 1));
      const f32x4 dr = mfma16(a1, pp ? tb[2] : F16fr, z4), di = mfma16(a1, pp ? tb[3] : F16fi, z4);
      const v4u b3 = cmul_pack<false>(dr, di, asf4(tb[10 + 2 * pp]), asf4(tb[11 + 2 * pp]));
      zr[pp] = mfma16(F16fr, b3, z4); zi[pp] = mfma16(F16fi, b3, z4);
    }
    v4u a4[2];
    if (!special) {
      a4[0] = cmul_pack<false>(zr[0], zi[0], Wc.r0, Wc.i0);
      a4[1] = cmul_pack<false>(zr[1], zi[1], Wc.r1, Wc.i1);
    } else {
      BAR_LGKM();
      LAS char* Z = I0;
#pragma unroll
      for (int pp = 0; pp < 2; ++pp)
#pragma unroll
        for (int r = 0; r < 4; ++r) { f32x2 v = {zr[pp][r], zi[pp][r]}; *(LAS f32x2*)(Z + ((w * 2 + pp) * 256 + l15 + 16 * (4 * g + r)) * 8) = v; }
      BAR_LGKM();
#pragma unroll
      for (int pp = 0; pp < 2; ++pp) {
        f32x4 cr, ci;
#pragma unroll
        for (int r = 0; r < 4; ++r) {
          const int K = l15 + 16 * (4 * g + r), Kn = pp ? 255 - K : (256 - K) & 255;
          const f32x2 y = *(LAS f32x2*)(Z + ((((8 - w) & 7) * 2 + pp) * 256 + Kn) * 8);
          cr[r] = y[0]; ci[r] = -y[1];
        }
        const f32x4 ar = pp ? Wc.r1 : Wc.r0, ai = pp ? Wc.i1 : Wc.i0, br = pp ? Wd.r1 : Wd.r0, bi = pp ? Wd.i1 : Wd.i0;
        const f32x4 yr = zr[pp] * ar - zi[pp] * ai + cr * br - ci * bi, yi = zr[pp] * ai + zi[pp] * ar + cr * bi + ci * br;
        a4[pp].x = pk2(yr[0], yr[1]); a4[pp].y = pk2(yr[2], yr[3]); a4[pp].z = pk2(yi[0], yi[1]); a4[pp].w = pk2(yi[2], yi[3]);
      }
      BAR_LGKM();
    }
#pragma unroll
    for (int pp = 0; pp < 2; ++pp) {
      const f32x4 dr = mfma16(a4[pp], F16ir, z4), di = mfma16(a4[pp], F16ii, z4);
      const v4u a5 = cmul_pack<true>(dr, di, asf4(tb[pp ? 14 : 10]), asf4(tb[pp ? 15 : 11]));
      const f32x4 er = mfma16(a5, pp ? tb[4] : F16ir, z4), ei = mfma16(a5, pp ? tb[5] : F16ii, z4);
      const int n0 = 4 * w + 2 * pp, n1 = n0 + 1;
      const int ch = 2 * l15 + (g >> 1);
      v2u o; o.x = pk2(er[0], er[1]); o.y = pk2(er[2], er[3]);
      *(LAS v2u*)(I0 + n0 * 512 + ((ch ^ ((n0 & 3) << 2)) << 4) + 8 * (g & 1)) = o;
      o.x = pk2(ei[0], ei[1]); o.y = pk2(ei[2], ei[3]);
      *(LAS v2u*)(I0 + n1 * 512 + ((ch ^ ((n1 & 3) << 2)) << 4) + 8 * (g & 1)) = o;
    }
    bar_vm<0>();
    if (!published && tid == 0) flag_store(p.ws, flh_own, tag);
    FT(23);
    m5a = tb[8]; m5b = tb[9];
  }
  return tag;
}

DI void make_geo(Geo& G, const Params& p, int tz) {
  G.tid = tz; G.lane = G.tid & 63; G.w = __builtin_amdgcn_readfirstlane(G.tid >> 6);
  G.b = blockIdx.x;
  const int Lx = G.b >> 3, x8 = G.b & 7;
  {
    const int a = x8 & 3, h = x8 >> 2, rbi = Lx >> 3, c = Lx & 7;
    G.i = 2 * a + h; G.i1 = 2 * a + (rbi >> 1); G.rb = rbi & 1;
    G.fc = c < 4 ? 4 * h + c : 4 + 4 * h + c;
    G.at = rbi * 4 + c; G.m1 = ((rbi * 4 + c - 4) << 3) | x8;
  }
  G.hh = G.lane >> 5; G.blk = (G.lane >> 4) & 1; G.q4 = (G.lane & 15) >> 2; G.p4 = G.lane & 3;
  G.sx = make_srd(p.x, NS * ND * 2); G.sws = make_srd(p.ws, 16u << 20); G.sw2 = make_srd(p.w2, NF * ND * 2);
  G.jb = Lx >> 3; G.dt = Lx & 7; G.rh = G.w & 1; G.q = G.w >> 1;
  G.j = 64 * G.jb + 32 * G.rh + (G.lane & 31);
  const int rho = 16 * G.rh + (G.lane >> 3), chs = (G.lane & 7) ^ (4 * ((G.lane >> 4) & 1));
  G.vw20 = (unsigned)(rho * 1024 + G.dt * 128 + chs * 16); G.vw21 = G.vw20 + 8 * 1024;
  G.ldsw = uni((unsigned)(G.q * 4096 + G.rh * 2048));
  G.fq0b = uni((unsigned)(768 + 256 * G.q) * 1024u); G.fq1b = uni((unsigned)(192 * G.q) * 1024u);
}

__global__ __launch_bounds__(512, 2) void gm_kernel(Params p) {
  extern __shared__ __attribute__((aligned(16))) char smem[];
  LAS char* L = (LAS char*)smem;
  STAMP(0);
  FT(0);
  Geo G;
  make_geo(G, p, threadIdx.x);
  const bool isA = G.fc < 8;
  v4u gf[4], m5a, m5b;
  unsigned tag;
  if (isA) tag = phase1<true>(p, L, G, m5a, m5b); else tag = phase1<false>(p, L, G, m5a, m5b);
  { int tz = threadIdx.x; asm volatile("" : "+v"(tz)); make_geo(G, p, tz); }
  const int tid = G.tid, lane = G.lane, w = G.w, b = G.b, i = G.i, jb = G.jb, dt = G.dt, rh = G.rh, q = G.q, j = G.j;
  const int hh = G.hh, blk = G.blk, q4 = G.q4, p4 = G.p4;
  const v4u sws = G.sws;
  const unsigned ldsw = G.ldsw;
  const int m1 = G.m1;
  STAMP(7);
  FT(25);
  if (!isA) {
    LAS char* I0 = L + 128 * 1024;
    LAS char* I1 = L + 128 * 1024 + 16384;
    LAS char* ta = I0 + (8 * hh + q4) * 512 + (((4 * w + 2 * blk + (p4 >> 1)) ^ (q4 << 2)) << 4) + 8 * (p4 & 1);
    const int n = lane & 31;
    LAS char* so = I1 + n * 512 + 8 * hh;
    f32x16 acc = {};
    acc = mfma32(tr_read2(ta, ta + 2048), m5a, acc);
    acc = mfma32(tr_read2(ta + 8192, ta + 10240), m5b, acc);
#pragma unroll
    for (int Gq = 0; Gq < 4; ++Gq) {
      v2u o; o.x = pk2(acc[4 * Gq], acc[4 * Gq + 1]); o.y = pk2(acc[4 * Gq + 2], acc[4 * Gq + 3]);
      *(LAS v2u*)(so + (((4 * w + Gq) ^ (n & 15)) << 4)) = o;
    }
    BAR_LGKM();
    const bool special = (m1 == 0);
    auto u_store = [&](int t2) {
      const int nn = t2 >> 5, ch = t2 & 31, ii = (nn >> 1) & 7, d0 = nn & 1;
      const int g0 = special ? 2 * ii : ii, g1 = special ? 2 * ii + 1 : 8 + ii;
      const v4u v0 = lds_ld16(I1 + nn * 512 + ((ch ^ (nn & 15)) << 4));
      const v4u v1 = lds_ld16(I1 + (nn + 16) * 512 + ((ch ^ (nn & 15)) << 4));
      const unsigned ub = WS_U + (unsigned)m1 * 16384u + (unsigned)(d0 * 512 + ch * 16);
      st16_sc1(sws, ub + (unsigned)g0 * 1024u, v0);
      st16_sc1(sws, ub + (unsigned)g1 * 1024u, v1);
    };
    if (w != 0) u_store(tid);
    if (w == 1) u_store(tid - 64);
    SCHED();
  }
  const int sz = (q4 >> 1) & 1;
  LAS char* ra0 = L + q * 4096 + (4 * hh + q4) * 128 + (((0 ^ (4 * sz)) + 2 * blk + (p4 >> 1)) << 4) + 8 * (p4 & 1);
  LAS char* ra1 = L + q * 4096 + (4 * hh + q4) * 128 + (((4 ^ (4 * sz)) + 2 * blk + (p4 >> 1)) << 4) + 8 * (p4 & 1);
  v4u wp[4][4];
  BAR_LGKM();
#pragma unroll
  for (int t = 0; t < 4; ++t) {
    LAS char* s0 = ra0 + t * 16384; LAS char* s1 = ra1 + t * 16384;
    wp[t][0] = tr_read2(s0, s0 + 1024); wp[t][1] = tr_read2(s1, s1 + 1024);
    wp[t][2] = tr_read2(s0 + 2048, s0 + 3072); wp[t][3] = tr_read2(s1 + 2048, s1 + 3072);
  }
#define W2_8_11() do { w2_issue1(G, 8, 0); w2_issue1(G, 8, 1); w2_issue1(G, 9, 0); w2_issue1(G, 9, 1); w2_issue1(G, 10, 0); w2_issue1(G, 10, 1); w2_issue1(G, 11, 0); w2_issue1(G, 11, 1); } while (0)
  if (isA) { BAR_LGKM(); W2_8_11(); }
  const unsigned flh = WS_FLH + (unsigned)((i * 2 + (jb >> 1)) * 16) * FS;
  if (w == 0) poll_words(sws, flh, 16, tag, lane);
  BAR_LGKM();
  STAMP(8);
  FT(26);
  const unsigned vh = (unsigned)lane * 16u;
  const unsigned hbase = WS_H + (unsigned)((i * 8 + 2 * jb + rh) * 112) * 1024u;
  const unsigned hb0 = uni(hbase + (G.fq0b >> 4)), hb1 = uni(hbase + (G.fq1b >> 4));
  v2u gg;
  v4u hf[14][2];
#define H_ISSUE(t) do { LD16I(hf[t][0], vh, sws, ((t) < 8 ? hb0 : hb1), (2 * ((t) & 7)) * 1024, "sc1"); LD16I(hf[t][1], vh, sws, ((t) < 8 ? hb0 : hb1), (2 * ((t) & 7) + 1) * 1024, "sc1"); } while (0)
  H_ISSUE(0); H_ISSUE(1);
  asm volatile("s_barrier" ::: "memory");
  H_ISSUE(2); H_ISSUE(3); H_ISSUE(4); H_ISSUE(5); H_ISSUE(6);
  LD8Z(gg, WS_GATE + (unsigned)(i * 256 + j) * 8u, sws, "sc1");
  SCHED();
  f32x16 accb[2] = {}, acca[2] = {};
  v4u wf[2][4];
  unsigned fl0, fl1;
  v4u ub0, ub1, ub2, ub3;
  const unsigned vu = WS_U + (unsigned)(((64 * q + 16 * rh + (lane >> 3)) * 8 + i) * 1024 + dt * 128 + ((lane & 7) ^ (4 * ((lane >> 4) & 1))) * 16);
#define G2_BAR(n, t) asm volatile("s_waitcnt vmcnt(" STR(n) ") lgkmcnt(0)\n\ts_barrier" : "+v"(hf[t][0]), "+v"(hf[t][1]) :: "memory")
#define G2_CORE(t, c, n, RD, acc, b0, b1, X0, X1, X2)                                             \
  {                                                                                                 \
    FT(30 + (t));                                                                                   \
    LAS char* s0 = ra0 + (((t) + 1) & 7) * 16384; LAS char* s1 = ra1 + (((t) + 1) & 7) * 16384;    \
    asm volatile("" : "+v"(c[0]), "+v"(c[1]), "+v"(c[2]), "+v"(c[3]));                             \
    asm volatile("" :: "v"(acc[0]), "v"(acc[1]));                                                   \
    SCHED();                                                                                        \
    acc[0] = mfma32(c[0], b0, acc[0]); if (RD) n[0] = tr_read2(s0, s0 + 1024); SCHED();            \
    acc[1] = mfma32(c[1], b0, acc[1]); if (RD) n[1] = tr_read2(s1, s1 + 1024); X0; SCHED();        \
    acc[0] = mfma32(c[2], b1, acc[0]); if (RD) n[2] = tr_read2(s0 + 2048, s0 + 3072); X1; SCHED(); \
    acc[1] = mfma32(c[3], b1, acc[1]); if (RD) n[3] = tr_read2(s1 + 2048, s1 + 3072); X2; SCHED(); \
  }
#define G2_PRE(t, X2) G2_CORE(t, wp[t], wf[0], (t) == 3, accb, hf[t][0], hf[t][1], if (!isA) w2_issue1(G, (t) + 8, 0), if (!isA) w2_issue1(G, (t) + 8, 1), X2)
#define G2_STEP(t, X0, X1, X2) G2_CORE(t, wf[(t) & 1], wf[((t) + 1) & 1], true, accb, hf[t][0], hf[t][1], X0, X1, X2)
  G2_BAR(13, 0);
  if (!isA && tid == 0) flag_store(p.ws, WS_FLB + (unsigned)m1 * FS, tag);
  STAMP(14);
  G2_PRE(0, H_ISSUE(7))
  G2_BAR(13, 1); G2_PRE(1, H_ISSUE(8))
  G2_BAR(13, 2); G2_PRE(2, H_ISSUE(9))
  G2_BAR(13, 3); G2_PRE(3, H_ISSUE(10))
  G2_BAR(13, 4); G2_STEP(4, w2_issue1(G, 12, 0), w2_issue1(G, 12, 1), H_ISSUE(11))
  G2_BAR(15, 5); G2_STEP(5, w2_issue1(G, 13, 0), w2_issue1(G, 13, 1), H_ISSUE(12))
  if (w == 0) { LD4Z(fl0, WS_FLB + (unsigned)lane * FS, sws, "sc1"); LD4Z(fl1, WS_FLB + (64u + (unsigned)lane) * FS, sws, "sc1"); }
  G2_BAR(17, 6); G2_STEP(6, NOP, NOP, H_ISSUE(13))
  STAMP(9);
  G2_BAR(16, 7); G2_STEP(7, NOP, NOP, NOP)
  LAS unsigned* verdict = (LAS unsigned*)(L + 128 * 1024);
  if (w == 0) {
    asm volatile("s_waitcnt vmcnt(2)" : "+v"(fl0), "+v"(fl1) :: "memory");
    const unsigned ok = __all(fl0 == tag && fl1 == tag) ? 1u : 0u;
    if (lane == 0) *verdict = ok;
  }
  G2_BAR(14, 8);
  const int u_early = __builtin_amdgcn_readfirstlane(*(volatile LAS unsigned*)verdict);
  STAMP(15);
#define U_ISSUE() do { LD16Z(ub0, vu, sws, "sc1"); LD16Z(ub1, vu + 8u * 8192u, sws, "sc1"); LD16Z(ub2, vu + 32u * 8192u, sws, "sc1"); LD16Z(ub3, vu + 40u * 8192u, sws, "sc1"); } while (0)
  if (u_early) U_ISSUE();
  GRot grot;
  G2_STEP(8, gen_g_init(G, grot), NOP, gen_g_frag<0>(G, grot, gf[0]))
  unsigned e0, e1, e2, e3;
  if (b == 0 && w == 1) {
    LD4Z(e0, WS_FLH + (unsigned)lane * FS, sws, "sc1"); LD4Z(e1, WS_FLH + (64u + (unsigned)lane) * FS, sws, "sc1");
    LD4Z(e2, WS_FLH + (128u + (unsigned)lane) * FS, sws, "sc1"); LD4Z(e3, WS_FLH + (192u + (unsigned)lane) * FS, sws, "sc1");
  }
  G2_BAR(12, 9); G2_STEP(9, NOP, NOP, gen_g_frag<1>(G, grot, gf[1]))
  G2_BAR(10, 10); G2_STEP(10, NOP, NOP, gen_g_frag<2>(G, grot, gf[2]))
  G2_BAR(6, 11); G2_STEP(11, NOP, NOP, gen_g_frag<3>(G, grot, gf[3]))
  G2_BAR(2, 12);
  G2_STEP(12, NOP, NOP, NOP)
#define U_PUT() do {                                                                                                        \
    asm volatile("s_waitcnt vmcnt(0)" : "+v"(ub0), "+v"(ub1), "+v"(ub2), "+v"(ub3) :: "memory");                             \
    *(LAS v4u*)(L + 6 * 16384 + ldsw + lane * 16) = ub0; *(LAS v4u*)(L + 6 * 16384 + ldsw + 1024 + lane * 16) = ub1;         \
    *(LAS v4u*)(L + 7 * 16384 + ldsw + lane * 16) = ub2; *(LAS v4u*)(L + 7 * 16384 + ldsw + 1024 + lane * 16) = ub3; } while (0)
  if (u_early) U_PUT();
  G2_BAR(0, 13);
  asm volatile("" : "+v"(gg));
  if (b == 0 && w == 1) {
    asm volatile("" : "+v"(e0), "+v"(e1), "+v"(e2), "+v"(e3));
    if (!__all(e0 == tag && e1 == tag && e2 == tag && e3 == tag)) {
#pragma unroll 1
      for (int u = 0; u < 4; ++u) poll_words(sws, WS_FLH + 64u * FS * u, 64, tag, lane);
    }
    if (lane == 0) flag_store(p.ws, WS_EPOCH, tag);
  }
  G2_CORE(13, wf[1], wf[0], u_early, accb, hf[13][0], hf[13][1], NOP, NOP, NOP)
  if (!u_early) {
    if (w == 0) { poll_words(sws, WS_FLB, 64, tag, lane); poll_words(sws, WS_FLB + 64u * FS, 64, tag, lane); }
    BAR_LGKM();
    U_ISSUE();
    U_PUT();
    BAR_LGKM();
    LAS char* s0 = ra0 + 6 * 16384; LAS char* s1 = ra1 + 6 * 16384;
    wf[0][0] = tr_read2(s0, s0 + 1024); wf[0][1] = tr_read2(s1, s1 + 1024); wf[0][2] = tr_read2(s0 + 2048, s0 + 3072); wf[0][3] = tr_read2(s1 + 2048, s1 + 3072);
  }
  STAMP(10);
  BAR_LGKM(); G2_CORE(14, wf[0], wf[1], true, acca, gf[0], gf[1], NOP, NOP, NOP)
  BAR_LGKM(); G2_CORE(15, wf[1], wf[0], false, acca, gf[2], gf[3], NOP, NOP, NOP)
  FT(50);
  {
    const float og = asf(gg.y), gs = asf(gg.x) * (1.f / 524288.f);
    const int row = 32 * rh + (lane & 31);
#pragma unroll
    for (int dh = 0; dh < 2; ++dh)
#pragma unroll
      for (int G = 0; G < 4; ++G) {
        f32x4 v;
#pragma unroll
        for (int e = 0; e < 4; ++e) v[e] = __builtin_fmaf(og, accb[dh][4 * G + e], gs * acca[dh][4 * G + e]);
        *(LAS f32x4*)(L + q * 16384 + row * 256 + (((8 * dh + 2 * G + hh) ^ (row & 7)) << 4)) = v;
      }
  }
  BAR_LGKM();
  FT(51);
  {
    const int row = tid >> 3, c8 = tid & 7;
    f32x4 s0 = {0.f, 0.f, 0.f, 0.f}, s1 = s0;
#pragma unroll
    for (int qq = 0; qq < 4; ++qq) {
      s0 += *(LAS f32x4*)(L + qq * 16384 + row * 256 + (((2 * c8) ^ (row & 7)) << 4));
      s1 += *(LAS f32x4*)(L + qq * 16384 + row * 256 + (((2 * c8 + 1) ^ (row & 7)) << 4));
    }
    v4u o; o.x = pk2(s0[0], s0[1]); o.y = pk2(s0[2], s0[3]); o.z = pk2(s1[0], s1[1]); o.w = pk2(s1[2], s1[3]);
    *(GAS v4u*)((GAS char*)p.out + (size_t)(i + 8 * (64 * jb + row)) * 1024 + dt * 128 + c8 * 16) = o;
  }
  STAMP(11);
}

}

extern "C" void kernel_launch(void* const* d_in, const int* in_sizes, int n_in, void* d_out, int out_size, void* d_ws, size_t ws_size,
                              hipStream_t stream) {
  Params p;
  p.x = d_in[0]; p.wr = d_in[1]; p.wi = d_in[2]; p.w1 = d_in[4]; p.w2 = d_in[5]; p.gw = d_in[6];
  p.out = d_out; p.ws = d_ws;
  (void)hipFuncSetAttribute((const void*)gm_kernel, hipFuncAttributeMaxDynamicSharedMemorySize, LDS_BYTES);
  gm_kernel<<<256, 512, LDS_BYTES, stream>>>(p);
}
```

```cpp
#include <hip/hip_runtime.h>
#include <hip/hip_bf16.h>
#include <stdint.h>

namespace {

typedef unsigned v4u __attribute__((ext_vector_type(4)));
typedef unsigned v2u __attribute__((ext_vector_type(2)));
typedef float f32x16 __attribute__((ext_vector_type(16)));
typedef float f32x4 __attribute__((ext_vector_type(4)));
typedef float f32x2 __attribute__((ext_vector_type(2)));
typedef __bf16 bf16x8 __attribute__((ext_vector_type(8)));
typedef __bf16 bf16x2 __attribute__((ext_vector_type(2)));
typedef short s16x4 __attribute__((ext_vector_type(4)));
#define LAS __attribute__((address_space(3)))
#define GAS __attribute__((address_space(1)))
#define DI __device__ __forceinline__

constexpr int NS = 2048, ND = 512, NF = 1792;
constexpr unsigned WS_EPOCH = 0;
constexpr unsigned FS = 64;
constexpr unsigned WS_FLA = 4096;
constexpr unsigned WS_FLH = 16384;
constexpr unsigned WS_FLB = 32768;
constexpr unsigned WS_GATE = 65536;
constexpr unsigned WS_T = 1u << 20;
constexpr unsigned WS_U = 3u << 20;
constexpr unsigned WS_H = 5u << 20;
constexpr unsigned H_BYTES = 8u * 8u * 112u * 1024u;
constexpr int LDS_BYTES = 160 * 1024;
constexpr unsigned kSpinCap = 1u << 20;

struct Params {
  const void* x; const void* wr; const void* wi; const void* w1; const void* w2; const void* gw;
  void* out; void* ws;
};

constexpr double kPi = 3.14159265358979323846;
constexpr double ccos(double x) { double t = 1, s = 1; for (int n = 1; n < 16; ++n) { t *= -x * x / ((2 * n - 1) * (2 * n)); s += t; } return s; }
constexpr double cosn(int e, int n) { e = ((e % n) + n) % n; double x = 2 * kPi * e / n; if (x > kPi) x -= 2 * kPi; return ccos(x); }
constexpr double wre(int e, int n) { return cosn(e, n); }
constexpr double wim(int e, int n) { return -cosn(4 * e - n, 4 * n); }
constexpr unsigned bfb(double v) { float f = (float)v; unsigned u = __builtin_bit_cast(unsigned, f); u += 0x7fffu + ((u >> 16) & 1u); return u >> 16; }
constexpr unsigned bf2(double a, double b) { return bfb(a) | (bfb(b) << 16); }
constexpr unsigned fbits(double v) { return __builtin_bit_cast(unsigned, (float)v); }
struct Tab { unsigned v[16][64][4]; };
constexpr Tab make_tab() {
  Tab t{};
  for (int l = 0; l < 64; ++l) {
    const int g = l >> 4, l15 = l & 15, hh = l >> 5, n = l & 31;
    for (int mtx = 0; mtx < 3; ++mtx) {
      double r[4] = {}, im[4] = {};
      for (int q = 0; q < 4; ++q) {
        const int a = 4 * g + q;
        if (mtx == 0) { r[q] = wre(l15 * a, 16); im[q] = wim(l15 * a, 16); }
        else if (mtx == 1) { r[q] = wre(a * (2 * l15 + 1), 32); im[q] = wim(a * (2 * l15 + 1), 32); }
        else { r[q] = wre(l15 * (2 * a + 1), 32); im[q] = -wim(l15 * (2 * a + 1), 32); }
      }
      t.v[2 * mtx][l][0] = bf2(r[0], r[1]); t.v[2 * mtx][l][1] = bf2(r[2], r[3]); t.v[2 * mtx][l][2] = bf2(-im[0], -im[1]); t.v[2 * mtx][l][3] = bf2(-im[2], -im[3]);
      t.v[2 * mtx + 1][l][0] = bf2(im[0], im[1]); t.v[2 * mtx + 1][l][1] = bf2(im[2], im[3]); t.v[2 * mtx + 1][l][2] = bf2(r[0], r[1]); t.v[2 * mtx + 1][l][3] = bf2(r[2], r[3]);
    }
    {
      const int m2 = n >> 2, pp = (n >> 1) & 1, cp = n & 1;
      double v0[8] = {}, v1[8] = {};
      for (int jj = 0; jj < 8; ++jj) {
        const int ii = (8 * hh + jj) >> 1, d0 = jj & 1;
        const double sg = (d0 & pp) ? -1.0 : 1.0, orr = sg * wre(ii * m2, 8), oi = sg * wim(ii * m2, 8);
        v0[jj] = cp ? oi : orr; v1[jj] = cp ? orr : -oi;
      }
      for (int q = 0; q < 4; ++q) { t.v[6][l][q] = bf2(v0[2 * q], v0[2 * q + 1]); t.v[7][l][q] = bf2(v1[2 * q], v1[2 * q + 1]); }
    }
    {
      const int cq = n >> 4, ii = (n >> 1) & 7, d0 = n & 1;
      for (int s = 0; s < 2; ++s) {
        double v[8] = {};
        for (int jj = 0; jj < 8; ++jj) {
          const int m2k = 4 * s + 2 * hh + (jj >> 2), pk = (jj >> 1) & 1, ck = jj & 1;
          const double sg = (d0 & pk) ? -1.0 : 1.0, orr = sg * wre(ii * m2k, 8), oi = -sg * wim(ii * m2k, 8);
          v[jj] = cq ? (ck ? orr : oi) : (ck ? -oi : orr);
        }
        for (int q = 0; q < 4; ++q) t.v[8 + s][l][q] = bf2(v[2 * q], v[2 * q + 1]);
      }
    }
    for (int q = 0; q < 4; ++q) {
      const int a = 4 * g + q;
      t.v[10][l][q] = fbits(wre(a * (2 * l15), 512));      t.v[11][l][q] = fbits(wim(a * (2 * l15), 512));
      t.v[12][l][q] = fbits(wre(a * (2 * l15 + 1), 512));  t.v[13][l][q] = fbits(wim(a * (2 * l15 + 1), 512));
      t.v[14][l][q] = fbits(wre(l15 * (2 * a + 1), 512));  t.v[15][l][q] = fbits(wim(l15 * (2 * a + 1), 512));
    }
  }
  return t;
}
__device__ const Tab kTab = make_tab();

constexpr unsigned STAMP_OFF = 512 * 1024;
#define STAMP(k) do {} while (0)
#define FT(k) do {} while (0)

DI v4u make_srd(const void* p, unsigned bytes) {
  const unsigned long long a = (unsigned long long)p;
  v4u r; r.x = (unsigned)a; r.y = (unsigned)(a >> 32) & 0xffffu; r.z = bytes; r.w = 0x00020000u;
  return r;
}
template <unsigned LDSI, unsigned SOI> DI void dma16(v4u srd, unsigned voff, unsigned ldsb, unsigned sob) {
  unsigned tmp;
  asm volatile("s_add_u32 m0, %3, %5\n\ts_add_u32 %0, %4, %6\n\tbuffer_load_dwordx4 %1, %2, %0 offen lds"
               : "=&s"(tmp) : "v"(voff), "s"(srd), "s"(ldsb), "s"(sob), "i"(LDSI), "i"(SOI) : "memory", "scc");
}
template <unsigned LDSI, unsigned SOI> DI void dma16z(v4u srd, unsigned voff, unsigned ldsb) {
  unsigned tmp;
  asm volatile("s_add_u32 m0, %3, %4\n\ts_mov_b32 %0, %5\n\tbuffer_load_dwordx4 %1, %2, %0 offen lds"
               : "=&s"(tmp) : "v"(voff), "s"(srd), "s"(ldsb), "i"(LDSI), "i"(SOI) : "memory", "scc");
}
DI unsigned uni(unsigned v) { return (unsigned)__builtin_amdgcn_readfirstlane((int)v); }
#define LD16(dst, voffv, srd, soffv, flags) asm volatile("buffer_load_dwordx4 %0, %1, %2, %3 offen " flags : "=v"(dst) : "v"(voffv), "s"(srd), "s"(soffv) : "memory")
#define LD16I(dst, voffv, srd, sob, soi, flags) do { unsigned tmp_; asm volatile("s_add_u32 %1, %4, %5\n\tbuffer_load_dwordx4 %0, %2, %3, %1 offen " flags : "=v"(dst), "=&s"(tmp_) : "v"(voffv), "s"(srd), "s"(sob), "i"(soi) : "memory", "scc"); } while (0)
#define LD16Z(dst, voffv, srd, flags) asm volatile("buffer_load_dwordx4 %0, %1, %2, 0 offen " flags : "=v"(dst) : "v"(voffv), "s"(srd) : "memory")
#define LD4Z(dst, voffv, srd, flags) asm volatile("buffer_load_dword %0, %1, %2, 0 offen " flags : "=v"(dst) : "v"(voffv), "s"(srd) : "memory")
#define LD2Z(dst, voffv, srd, flags) asm volatile("buffer_load_ushort %0, %1, %2, 0 offen " flags : "=v"(dst) : "v"(voffv), "s"(srd) : "memory")
#define LD8Z(dst, voffv, srd, flags) asm volatile("buffer_load_dwordx2 %0, %1, %2, 0 offen " flags : "=v"(dst) : "v"(voffv), "s"(srd) : "memory")
#define LD8(dst, voffv, srd, soffv, flags) asm volatile("buffer_load_dwordx2 %0, %1, %2, %3 offen " flags : "=v"(dst) : "v"(voffv), "s"(srd), "s"(soffv) : "memory")
#define LD4(dst, voffv, srd, soffv, flags) asm volatile("buffer_load_dword %0, %1, %2, %3 offen " flags : "=v"(dst) : "v"(voffv), "s"(srd), "s"(soffv) : "memory")
#define LD2(dst, voffv, srd, soffv, flags) asm volatile("buffer_load_ushort %0, %1, %2, %3 offen " flags : "=v"(dst) : "v"(voffv), "s"(srd), "s"(soffv) : "memory")
#define XSTR(x) #x
#define STR(x) XSTR(x)
#define BAR_VM(n) asm volatile("s_waitcnt vmcnt(" STR(n) ") lgkmcnt(0)\n\ts_barrier" ::: "memory")
#define BAR_LGKM() asm volatile("s_waitcnt lgkmcnt(0)\n\ts_barrier" ::: "memory")
#define SCHED() __builtin_amdgcn_sched_barrier(0)
template <int N> DI void bar_vm() { asm volatile("s_waitcnt vmcnt(%0) lgkmcnt(0)\n\ts_barrier" :: "i"(N) : "memory"); }
template <int N> DI void wait_vm() { asm volatile("s_waitcnt vmcnt(%0)" :: "i"(N) : "memory"); }

DI unsigned pk2(float a, float b) { f32x2 p = {a, b}; return __builtin_bit_cast(unsigned, __builtin_convertvector(p, bf16x2)); }
DI float bflo(unsigned u) { return __builtin_bit_cast(float, u << 16); }
DI float bfhi(unsigned u) { return __builtin_bit_cast(float, u & 0xffff0000u); }
DI float asf(unsigned u) { return __builtin_bit_cast(float, u); }
DI f32x16 mfma32(v4u a, v4u b, f32x16 c) {
  return __builtin_amdgcn_mfma_f32_32x32x16_bf16(__builtin_bit_cast(bf16x8, a), __builtin_bit_cast(bf16x8, b), c, 0, 0, 0);
}
DI f32x4 mfma16(v4u a, v4u b, f32x4 c) {
  return __builtin_amdgcn_mfma_f32_16x16x32_bf16(__builtin_bit_cast(bf16x8, a), __builtin_bit_cast(bf16x8, b), c, 0, 0, 0);
}
DI v2u tr_read(LAS char* p) { return __builtin_bit_cast(v2u, __builtin_amdgcn_ds_read_tr16_b64_v4i16((LAS s16x4*)p)); }
DI v4u tr_read2(LAS char* p0, LAS char* p1) { v2u a = tr_read(p0), b = tr_read(p1); v4u r; r.x = a.x; r.y = a.y; r.z = b.x; r.w = b.y; return r; }
DI v4u lds_ld16(LAS char* p) { return *(LAS v4u*)p; }
DI float cosrev(float r) { return __builtin_amdgcn_cosf(r); }
DI float sinrev(float r) { return __builtin_amdgcn_sinf(r); }
DI __amdgpu_buffer_rsrc_t as_rsrc(v4u srd) { return __builtin_amdgcn_make_buffer_rsrc((void*)(((unsigned long long)srd.y << 32) | srd.x), 0, srd.z, 0x00020000); }
DI void st16_sc1(v4u srd, unsigned voff, v4u v) { __builtin_amdgcn_raw_buffer_store_b128(v, as_rsrc(srd), voff, 0, 16); }
DI void flag_store(void* ws, unsigned off, unsigned tag) { __hip_atomic_store((GAS unsigned*)((GAS char*)ws + off), tag, __ATOMIC_RELAXED, __HIP_MEMORY_SCOPE_AGENT); }
template <int CTRL, int RMASK> DI float dpp_add(float v) { return v + asf((unsigned)__builtin_amdgcn_update_dpp(0, (int)__builtin_bit_cast(unsigned, v), CTRL, RMASK, 0xf, true)); }
DI void poll_words(v4u sws, unsigned off, int n, unsigned tag, int lane) {
  const unsigned vo = off + FS * (unsigned)(lane < n ? lane : 0);
#pragma unroll 1
  for (unsigned spins = 0; spins < kSpinCap; ++spins) {
    unsigned f;
    asm volatile("buffer_load_dword %0, %1, %2, 0 offen sc1\n\ts_waitcnt vmcnt(0)" : "=&v"(f) : "v"(vo), "s"(sws) : "memory");
    if (__all(f == tag)) return;
    __builtin_amdgcn_s_sleep(4);
  }
}

constexpr unsigned A_XIMG = 64 * 1024, A_FIMG = 128 * 1024;
DI void phaseA_genF(LAS char* L, int i, int mt, int w, int lane) {
  const int mh = w >> 2, sg = w & 3, n = lane & 31, hh = lane >> 5;
  const int m1c = 64 * mt + 32 * mh + n, m1 = m1c >> 1, c = m1c & 1;
  const int j0 = 64 * sg + 32 * hh;
  const float ph = c ? 0.25f : 0.f;
  const float k2 = 2.f * cosrev((float)(8 * m1) * (1.f / 2048.f));
  float vm = cosrev((float)(((i + 8 * (j0 - 1)) * m1) & 2047) * (1.f / 2048.f) + ph);
  float v0 = cosrev((float)(((i + 8 * j0) * m1) & 2047) * (1.f / 2048.f) + ph);
  const unsigned cst = c ? 0x3F800000u : 0x00003F80u;
  const bool sp = m1c < 2;
#pragma unroll
  for (int s = 0; s < 4; ++s) {
    float e[8];
#pragma unroll
    for (int q = 0; q < 8; ++q) { e[q] = v0; const float vn = __builtin_fmaf(k2, v0, -vm); vm = v0; v0 = vn; }
    v4u f; f.x = pk2(e[0], e[1]); f.y = pk2(e[2], e[3]); f.z = pk2(e[4], e[5]); f.w = pk2(e[6], e[7]);
    if (mt == 0 && w < 4) { f.x = sp ? cst : f.x; f.y = sp ? cst : f.y; f.z = sp ? cst : f.z; f.w = sp ? cst : f.w; }
    *(LAS v4u*)(L + A_FIMG + (mh * 16 + sg * 4 + s) * 1024 + lane * 16) = f;
  }
}

struct Frag1 { v4u a[4], b0[4], b1[4]; };
DI void g1_read(Frag1& f, LAS char* wa, LAS char* xb0, LAS char* xb1, LAS char* xb2, LAS char* xb3, int slot) {
  const int so = slot * 32768;
  f.a[0] = tr_read2(wa + so, wa + so + 1024);              f.b0[0] = lds_ld16(xb0 + so); f.b1[0] = lds_ld16(xb0 + so + 4096);
  f.a[1] = tr_read2(wa + so + 4096, wa + so + 5120);       f.b0[1] = lds_ld16(xb1 + so); f.b1[1] = lds_ld16(xb1 + so + 4096);
  f.a[2] = tr_read2(wa + so + 8192, wa + so + 9216);       f.b0[2] = lds_ld16(xb2 + so); f.b1[2] = lds_ld16(xb2 + so + 4096);
  f.a[3] = tr_read2(wa + so + 12288, wa + so + 13312);     f.b0[3] = lds_ld16(xb3 + so); f.b1[3] = lds_ld16(xb3 + so + 4096);
}
DI unsigned silu2(float a, float b) {
  const f32x2 h = {a, b}, t = h * -1.44269504088896341f;
  f32x2 e = {__builtin_amdgcn_exp2f(t[0]), __builtin_amdgcn_exp2f(t[1])};
  e = e + 1.f;
  const f32x2 r = {__builtin_amdgcn_rcpf(e[0]), __builtin_amdgcn_rcpf(e[1])};
  return __builtin_bit_cast(unsigned, __builtin_convertvector(h * r, bf16x2));
}

DI f32x2 lo2(const f32x4& v) { return __builtin_shufflevector(v, v, 0, 1); }
DI f32x2 hi2(const f32x4& v) { return __builtin_shufflevector(v, v, 2, 3); }
DI unsigned pk2v(f32x2 p) { return __builtin_bit_cast(unsigned, __builtin_convertvector(p, bf16x2)); }
DI f32x4 asf4(v4u v) { return __builtin_bit_cast(f32x4, v); }
template <bool CONJ> DI v4u cmul_pack(const f32x4& re, const f32x4& im, const f32x4& tr, const f32x4& ti) {
  const f32x2 r0 = lo2(re), r1 = hi2(re), i0 = lo2(im), i1 = hi2(im), c0 = lo2(tr), c1 = hi2(tr), s0 = lo2(ti), s1 = hi2(ti);
  f32x2 a0, a1, b0, b1;
  if (CONJ) {
    a0 = __builtin_elementwise_fma(r0, c0, i0 * s0); a1 = __builtin_elementwise_fma(r1, c1, i1 * s1);
    b0 = __builtin_elementwise_fma(i0, c0, -(r0 * s0)); b1 = __builtin_elementwise_fma(i1, c1, -(r1 * s1));
  } else {
    a0 = __builtin_elementwise_fma(r0, c0, -(i0 * s0)); a1 = __builtin_elementwise_fma(r1, c1, -(i1 * s1));
    b0 = __builtin_elementwise_fma(r0, s0, i0 * c0); b1 = __builtin_elementwise_fma(r1, s1, i1 * c1);
  }
  v4u r; r.x = pk2v(a0); r.y = pk2v(a1); r.z = pk2v(b0); r.w = pk2v(b1); return r;
}
struct cf { float r, i; };
DI cf cmul(cf a, cf b) { return {a.r * b.r - a.i * b.i, a.r * b.i + a.i * b.r}; }
DI cf cmulc(cf a, cf b) { return {a.r * b.r + a.i * b.i, a.i * b.r - a.r * b.i}; }
DI unsigned w_src(int m, int lane) {
  const int mm = (NS - m) & (NS - 1);
  const int e = lane < 32 ? m * 257 - (m & 1) : mm * 257 + (m & 1);
  return (unsigned)(e * 2 + (lane & 31) * 16);
}
DI unsigned w_tail_src(int m, int lane) {
  const int mm = (NS - m) & (NS - 1), l = lane & 3;
  return (unsigned)((l < 2 ? m * 257 + 255 + l : mm * 257 + (l - 2) * 256) * 2);
}
struct WTail { unsigned r255, r256, rm0, rm256, i255, i256, im0, im256; };
DI WTail w_tail(unsigned tr, unsigned ti) {
  return {(unsigned)__builtin_amdgcn_readlane((int)tr, 0), (unsigned)__builtin_amdgcn_readlane((int)tr, 1), (unsigned)__builtin_amdgcn_readlane((int)tr, 2), (unsigned)__builtin_amdgcn_readlane((int)tr, 3),
          (unsigned)__builtin_amdgcn_readlane((int)ti, 0), (unsigned)__builtin_amdgcn_readlane((int)ti, 1), (unsigned)__builtin_amdgcn_readlane((int)ti, 2), (unsigned)__builtin_amdgcn_readlane((int)ti, 3)};
}
struct WVal { f32x4 r0, i0, r1, i1; };
struct WRawL { unsigned r[4][2], i[4][2]; };
DI void w_put(LAS char* scr, v4u qr, v4u qi, int lane) { *(LAS v4u*)(scr + lane * 16) = qr; *(LAS v4u*)(scr + 1024 + lane * 16) = qi; }
DI int w_off(int m, int lane) { const int k1 = lane & 15, g = lane >> 4, odd = m & 1; return lane < 32 ? (2 * k1 + 128 * g + odd) * 2 : 512 + (511 - 2 * k1 - 128 * g - odd) * 2; }
DI void w_get(WRawL& o, LAS char* scr, int off, int lane, int r) {
  LAS unsigned* a = (LAS unsigned*)(scr + ((off + (lane < 32 ? 64 : -64) * r) & ~3));
  o.r[r][0] = a[0]; o.r[r][1] = a[1]; o.i[r][0] = a[256]; o.i[r][1] = a[257];
}
DI WVal w_unpack(const WRawL& x, const WTail& t, int off, int m, int lane) {
  const bool lo = lane < 32;
  const unsigned sh = (off & 2) * 8;
  unsigned rr[4], ii[4];
#pragma unroll
  for (int r = 0; r < 4; ++r) { rr[r] = __builtin_amdgcn_alignbit(x.r[r][1], x.r[r][0], sh); ii[r] = __builtin_amdgcn_alignbit(x.i[r][1], x.i[r][0], sh); }
  if (m & 1) { rr[3] = lane == 31 ? (rr[3] & 0xffffu) | (t.r255 << 16) : rr[3]; ii[3] = lane == 31 ? (ii[3] & 0xffffu) | (t.i255 << 16) : ii[3]; }
  else { rr[0] = lane == 32 ? (rr[0] & 0xffffu) | (t.rm256 << 16) : rr[0]; ii[0] = lane == 32 ? (ii[0] & 0xffffu) | (t.im256 << 16) : ii[0]; }
  const unsigned s0 = lo ? 16u : 0u, s1 = lo ? 0u : 16u, sg = lo ? 0u : 0x80000000u;
  WVal o;
#pragma unroll
  for (int r = 0; r < 4; ++r) {
    o.r0[r] = asf((rr[r] << s0) & 0xffff0000u); o.r1[r] = asf((rr[r] << s1) & 0xffff0000u);
    o.i0[r] = asf(((ii[r] << s0) & 0xffff0000u) ^ sg); o.i1[r] = asf(((ii[r] << s1) & 0xffff0000u) ^ sg);
  }
  const bool av = (lane & 31) == 0;
  const float xr = bflo(lo ? t.rm0 : t.r256), xi = asf(((lo ? t.im0 : t.i256) << 16) ^ (sg ^ 0x80000000u));
  o.r0[0] = av ? 0.5f * (o.r0[0] + xr) : o.r0[0];
  o.i0[0] = av ? 0.5f * (o.i0[0] + xi) : o.i0[0];
  return o;
}

DI void packed_coef(WVal& a, WVal& b, int m2) {
  const cf om = {cosrev((float)m2 * 0.0625f), -sinrev((float)m2 * 0.0625f)};
#pragma unroll
  for (int pp = 0; pp < 2; ++pp)
#pragma unroll
    for (int r = 0; r < 4; ++r) {
      const float war = pp ? a.r1[r] : a.r0[r], wai = pp ? a.i1[r] : a.i0[r], wbr = pp ? b.r1[r] : b.r0[r], wbi = pp ? b.i1[r] : b.i0[r];
      const cf sp = {war + wbr, wai + wbi}, sm = {war - wbr, wai - wbi};
      const cf t1 = cmulc(sm, om), t2 = cmul(sm, om);
      const cf cE = {sp.r - t1.i, sp.i + t1.r}, cO = {t2.r - sp.i, t2.i + sp.r};
      const float alr = 0.25f * (cE.r + cO.i), ali = 0.25f * (cE.i - cO.r), ber = 0.25f * (cE.r - cO.i), bei = 0.25f * (cE.i + cO.r);
      if (pp) { a.r1[r] = alr; a.i1[r] = ali; b.r1[r] = ber; b.i1[r] = bei; } else { a.r0[r] = alr; a.i0[r] = ali; b.r0[r] = ber; b.i0[r] = bei; }
    }
}

struct Geo {
  int tid, lane, w, b, i, i1, at, m1, rb, fc, hh, blk, q4, p4;
  int jb, dt, rh, q, j;
  v4u sx, sws, sw2;
  unsigned vw20, vw21, ldsw, fq0b, fq1b;
};
template <int T, int U> DI void w2_dma(const v4u& sw2, unsigned vw20, unsigned vw21, unsigned ldsw, unsigned fq0, unsigned fq1) {
  dma16<(T & 7) * 16384u + U * 1024u, (T < 8 ? T : T - 8) * 32768u>(sw2, U ? vw21 : vw20, ldsw, T < 8 ? fq0 : fq1);
}
#define w2_issue1(g, t, u) (w2_dma<t, u>((g).sw2, (g).vw20, (g).vw21, (g).ldsw, (g).fq0b, (g).fq1b))
struct GRot { float c1, s1, c4, s4, c, s; };
DI void gen_g_init(const Geo& g, GRot& r) {
  const int srow = g.i + 8 * g.j;
  const int mb = 32 * g.q + 2 * g.hh;
  const float r1 = (float)(srow & 2047) * (1.f / 2048.f), r4 = (float)((4 * srow) & 2047) * (1.f / 2048.f), r0 = (float)((mb * srow) & 2047) * (1.f / 2048.f);
  r.c1 = cosrev(r1); r.s1 = sinrev(r1); r.c4 = cosrev(r4); r.s4 = sinrev(r4); r.c = cosrev(r0); r.s = sinrev(r0);
}
template <int S> DI void gen_g_frag(const Geo& g, GRot& r, v4u& f) {
#pragma unroll
  for (int h = 0; h < 2; ++h) {
    const float cb = __builtin_fmaf(r.c, r.c1, -(r.s * r.s1)), sb = __builtin_fmaf(r.s, r.c1, r.c * r.s1);
    unsigned w0 = pk2(r.c, -r.s), w1 = pk2(cb, -sb);
    if (S == 0 && h == 0) w0 = (g.q == 0 && g.hh == 0) ? ((g.j & 1) ? 0x3F800000u : 0x00003F80u) : w0;
    if (h) { f.z = w0; f.w = w1; } else { f.x = w0; f.y = w1; }
    const float cn = __builtin_fmaf(r.c, r.c4, -(r.s * r.s4)), sn = __builtin_fmaf(r.s, r.c4, r.c * r.s4);
    r.c = cn; r.s = sn;
  }
  asm volatile("" : "+v"(f));
}
template <int N0, int N1, int NR> DI void bar_w(int w) { if (w == 0) bar_vm<N0>(); else if (w == 1) bar_vm<N1>(); else bar_vm<NR>(); }
#define NOP do {} while (0)

#define TLBW 0
#define TOUCH() do {                                                                                                      \
    const int gl = G.b * 256 + (w & 3) * 64 + lane;                                                                         \
    const char* base_ = gl < 14336 ? (const char*)p.w2 : gl < 28672 ? (const char*)p.w1 - 14336 * 128 : gl < 36896 ? (const char*)p.wr - 28672 * 128           \
                      : gl < 45120 ? (const char*)p.wi - 36896 * 128 : (const char*)p.x - 45120 * 128;                     \
    const char* ad_ = base_ + (size_t)(gl < 61504 ? gl : 45120) * 128;                                                      \
    if (w >= 4) ad_ = TLBW ? (w == 7 ? (const char*)p.out + lane * 32768 : (const char*)p.ws + (size_t)((w - 4) * 64 + lane + 8) * 65536 + 32768 - 128) : (const char*)p.x; \
    asm volatile("global_load_dword %0, %1, off" : "=v"(touch) : "v"(ad_) : "memory");                                     \
  } while (0)
template <bool ISA>
DI unsigned phase1(const Params& p, LAS char* L, const Geo& G, v4u& m5a, v4u& m5b
) {
  const int tid = G.tid, lane = G.lane, w = G.w, i = G.i, i1 = G.i1, rb = G.rb, fc = G.fc, hh = G.hh, blk = G.blk, q4 = G.q4, p4 = G.p4;
  const v4u sx = G.sx, sws = G.sws, sw1 = make_srd(p.w1, ND * NF * 2);
  unsigned touch;
  unsigned ep; v4u gx, gv;
  const int jg = 128 * rb + 8 * fc + w;
  LD16Z(gx, (unsigned)(lane * 16 + (i1 + 8 * jg) * 1024), sx, "");
  LD16Z(gv, (unsigned)lane * 16u, make_srd(p.gw, ND * 2), "");
  LD4Z(ep, WS_EPOCH, sws, "sc1");
  const int f0 = ISA ? 96 * fc : 768 + 128 * (fc - 8), nf = ISA ? 96 : 128;
  const int mt = G.at >> 2, dt4 = G.at & 3;
  const int m1 = G.m1;
  const v4u swr = make_srd(p.wr, NS * 257 * 2), swi = make_srd(p.wi, NS * 257 * 2);
  const int mW = m1 == 0 ? 128 * w : m1 + 256 * w;
  v4u wqr, wqi, xqr, xqi;
  unsigned wtr, wti, xtr, xti;
  WVal Wc, Wd;
  if (ISA) {
    const unsigned voff = (unsigned)(lane >> 4) * 8192u + (unsigned)(((lane & 15) ^ ((lane >> 4) << 2)) << 4);
    const unsigned xl = uni(A_XIMG + (unsigned)w * 8192u), xs = uni((unsigned)i * 1024u + (unsigned)dt4 * 256u + (unsigned)w * 262144u);
    dma16<0, 0>(sx, voff, xl, xs); dma16<1024, 32768>(sx, voff, xl, xs); dma16<2048, 65536>(sx, voff, xl, xs); dma16<3072, 98304>(sx, voff, xl, xs);
    dma16<4096, 131072>(sx, voff, xl, xs); dma16<5120, 163840>(sx, voff, xl, xs); dma16<6144, 196608>(sx, voff, xl, xs); dma16<7168, 229376>(sx, voff, xl, xs);
  }
  unsigned vx0, vx1, vw0, vw1;
  {
    const int r0 = 16 * w + (lane >> 3), pos = lane & 7, r1 = r0 + 8;
    vx0 = (unsigned)(i1 * 1024 + (128 * rb + r0) * 8192 + ((pos ^ ((r0 >> 1) & 7)) << 4));
    vx1 = (unsigned)(i1 * 1024 + (128 * rb + r1) * 8192 + ((pos ^ ((r1 >> 1) & 7)) << 4));
    const int k0 = 8 * w + (lane >> 4), chunk = (lane & 15) ^ ((lane >> 4) << 2);
    const bool ok = chunk * 8 < nf;
    vw0 = ok ? (unsigned)(k0 * 3584 + f0 * 2 + chunk * 16) : 0x80000000u;
    vw1 = ok ? vw0 + 4u * 3584u : 0x80000000u;
  }
  const unsigned g1l = uni((unsigned)w * 2048u);
#define g1_dma(kt, u) do {                                                                                         \
    if ((u) == 0) dma16z<((kt) & 3) * 32768u, (kt) * 128u>(sx, vx0, g1l);                                           \
    if ((u) == 1) dma16z<((kt) & 3) * 32768u + 1024u, (kt) * 128u>(sx, vx1, g1l);                                   \
    if ((u) == 2) dma16z<((kt) & 3) * 32768u + 16384u, (kt) * (64u * 3584u)>(sw1, vw0, g1l);                        \
    if ((u) == 3) dma16z<((kt) & 3) * 32768u + 17408u, (kt) * (64u * 3584u)>(sw1, vw1, g1l); } while (0)
#define g1_issue(kt) do { g1_dma(kt, 0); g1_dma(kt, 1); g1_dma(kt, 2); g1_dma(kt, 3); } while (0)
  if (ISA) {
    phaseA_genF(L, i, mt, w, lane);
    g1_issue(0); g1_issue(1);
  } else {
    { const unsigned wsrc = w_src(mW, lane), wts = w_tail_src(mW, lane);
      LD16Z(wqr, wsrc, swr, ""); LD16Z(wqi, wsrc, swi, ""); LD2Z(wtr, wts, swr, ""); LD2Z(wti, wts, swi, ""); }
    asm volatile("s_barrier" ::: "memory");
    g1_issue(0);
    if (m1 == 0) {
      const unsigned ws2 = w_src(128 * w + 1024, lane), wt2 = w_tail_src(128 * w + 1024, lane);
      LD16Z(xqr, ws2, swr, ""); LD16Z(xqi, ws2, swi, ""); LD2Z(xtr, wt2, swr, ""); LD2Z(xti, wt2, swi, "");
    }
    asm volatile("s_barrier" ::: "memory");
    g1_issue(1); asm volatile("s_barrier" ::: "memory");
    g1_issue(2); asm volatile("s_barrier" ::: "memory");
    g1_issue(3);
  }
  TOUCH();
  if (ISA) wait_vm<17>(); else wait_vm<21>();
  asm volatile("" : "+v"(ep), "+v"(gx), "+v"(gv) :: "memory");
  const unsigned tag = (unsigned)__builtin_amdgcn_readfirstlane((int)ep) + 1u;
  float gate_g, gate_og;
  {
    float z = 0.f;
#pragma unroll
    for (int q = 0; q < 4; ++q) {
      const unsigned pr = pk2(bflo(gx[q]) * bflo(gv[q]), bfhi(gx[q]) * bfhi(gv[q]));
      z += bflo(pr) + bfhi(pr);
    }
    z = dpp_add<0x111, 0xf>(z); z = dpp_add<0x112, 0xf>(z); z = dpp_add<0x114, 0xf>(z); z = dpp_add<0x118, 0xf>(z);
    z = dpp_add<0x142, 0xa>(z); z = dpp_add<0x143, 0xc>(z);
    const float zb = bflo(pk2(z, 0.f));
    const float g = bflo(pk2(__builtin_amdgcn_rcpf(1.f + __builtin_amdgcn_exp2f(zb * -1.44269504088896341f)), 0.f));
    gate_g = asf((unsigned)__builtin_amdgcn_readlane((int)__builtin_bit_cast(unsigned, g), 63));
    gate_og = bflo(pk2(1.f - gate_g, 0.f));
  }
  if (!ISA) {
    asm volatile("s_waitcnt vmcnt(17)" : "+v"(wqr), "+v"(wqi), "+v"(wtr), "+v"(wti) :: "memory");
    LAS char* wscr0 = L + 128 * 1024 + w * 2048;
    const int woff = w_off(mW, lane);
    w_put(wscr0, wqr, wqi, lane);
    asm volatile("s_waitcnt lgkmcnt(0)" ::: "memory");
    WRawL wraw;
    w_get(wraw, wscr0, woff, lane, 0); w_get(wraw, wscr0, woff, lane, 1); w_get(wraw, wscr0, woff, lane, 2); w_get(wraw, wscr0, woff, lane, 3);
    Wc = w_unpack(wraw, w_tail(wtr, wti), woff, mW, lane);
    asm volatile("" : "+v"(Wc.r0), "+v"(Wc.i0), "+v"(Wc.r1), "+v"(Wc.i1));
  }
  if (ISA) {
    const int dq = w & 3, mh = w >> 2;
    LAS char* xa = L + A_XIMG + (32 * hh + q4) * 256 + (((4 * dq + 2 * blk + (p4 >> 1)) ^ (q4 << 2)) << 4) + 8 * (p4 & 1);
    LAS char* fb = L + A_FIMG + mh * 16384 + lane * 16;
    const int srow = 32 * mh + (lane & 31);
    LAS char* stw = L + A_FIMG + srow * 256 + 8 * hh;
    const int rrow = tid >> 3;
    LAS char* str = L + A_FIMG + rrow * 256;
    const unsigned tdst = WS_T + (unsigned)(((64 * mt + rrow) * 8 + i) * 1024 + dt4 * 256 + (tid & 7) * 16);
    if (w < 4) bar_vm<9>(); else BAR_LGKM();
    STAMP(1);
    f32x16 acc = {};
    v4u fa[2][4], fbv[2][4];
#pragma unroll
    for (int s = 0; s < 4; ++s) { fa[0][s] = tr_read2(xa + 8 * s * 256, xa + 8 * s * 256 + 1024); fbv[0][s] = lds_ld16(fb + s * 1024); }
#pragma unroll
    for (int g = 0; g < 4; ++g) {
      acc = mfma32(fa[g & 1][0], fbv[g & 1][0], acc);
      SCHED();
      if (g == 1) { if (w >= 4) bar_vm<9>(); else BAR_LGKM(); }
      if (g < 3) {
#pragma unroll
        for (int s = 0; s < 4; ++s) {
          const int ro = (64 * (g + 1) + 8 * s) * 256;
          fa[(g + 1) & 1][s] = tr_read2(xa + ro, xa + ro + 1024); fbv[(g + 1) & 1][s] = lds_ld16(fb + (4 * (g + 1) + s) * 1024);
        }
      }
      SCHED();
#pragma unroll
      for (int s = 1; s < 4; ++s) { acc = mfma32(fa[g & 1][s], fbv[g & 1][s], acc); if (g == 1 && s == 1) { g1_dma(2, 0); g1_dma(2, 1); } if (g == 1 && s == 2) { g1_dma(2, 2); g1_dma(2, 3); } SCHED(); }
    }
    BAR_LGKM();
    STAMP(2);
    g1_issue(3);
#pragma unroll
    for (int Gq = 0; Gq < 4; ++Gq) {
      v2u o; o.x = pk2(acc[4 * Gq], acc[4 * Gq + 1]); o.y = pk2(acc[4 * Gq + 2], acc[4 * Gq + 3]);
      *(LAS v2u*)(stw + (((4 * dq + Gq) ^ (srow & 15)) << 4)) = o;
    }
    BAR_LGKM();
    {
      const v4u v0 = lds_ld16(str + (((tid & 7) ^ (rrow & 15)) << 4)), v1 = lds_ld16(str + ((((tid & 7) + 8) ^ (rrow & 15)) << 4));
      st16_sc1(sws, tdst, v0);
      st16_sc1(sws, tdst + 128u, v1);
    }
    SCHED();
  }
  const int ft = w & 3, rh1 = w >> 2;
  const bool active = ft * 32 < nf;
  f32x16 acc1[2] = {};
  v4u tq0, tq1;
  v4u tb[16];
  unsigned fla;
  int t_ok = 0;
  unsigned tsrc0 = 0, tsrc1 = 0;
  const unsigned flaoff = WS_FLA + (unsigned)(((m1 >> 5) & 3) * 32 + (lane & 31)) * FS;
  LAS char* CTL = L + 128 * 1024;
  {
    LAS char* wa = L + 16384 + (8 * hh + q4) * 256 + (((4 * ft + 2 * blk + (p4 >> 1)) ^ (q4 << 2)) << 4) + 8 * (p4 & 1);
    const int r = 64 * rh1 + (lane & 31), fz = (r >> 1) & 7;
    LAS char* xb0 = L + r * 128 + (((0 + hh) ^ fz) << 4);
    LAS char* xb1 = L + r * 128 + (((2 + hh) ^ fz) << 4);
    LAS char* xb2 = L + r * 128 + (((4 + hh) ^ fz) << 4);
    LAS char* xb3 = L + r * 128 + (((6 + hh) ^ fz) << 4);
    Frag1 fr[2];
    bar_vm<ISA ? 15 : 13>();
    STAMP(3);
    if (lane == 0) { v2u gv2; gv2.x = __builtin_bit_cast(unsigned, gate_g); gv2.y = __builtin_bit_cast(unsigned, gate_og); *(LAS v2u*)(CTL + 64 + w * 8) = gv2; }
    g1_read(fr[0], wa, xb0, xb1, xb2, xb3, 0);
#define PIN1(c) asm volatile("" : "+v"(c.a[0]), "+v"(c.a[1]), "+v"(c.a[2]), "+v"(c.a[3]), "+v"(c.b0[0]), "+v"(c.b0[1]), "+v"(c.b0[2]), "+v"(c.b0[3]), \
                             "+v"(c.b1[0]), "+v"(c.b1[1]), "+v"(c.b1[2]), "+v"(c.b1[3]))
#define G1_STEP(kt, X0, X1, X2, X3, X4, X5, X6, X7)                                                                   \
    {                                                                                                                   \
      FT(10 + (kt));                                                                                                    \
      Frag1& c = fr[(kt) & 1];                                                                                          \
      Frag1& n = fr[((kt) + 1) & 1];                                                                                    \
      const int so = (((kt) + 1) & 3) * 32768;                                                                          \
      PIN1(c);                                                                                                          \
      asm volatile("" :: "v"(acc1[0]), "v"(acc1[1]));                                                                   \
      SCHED();                                                                                                          \
      acc1[0] = mfma32(c.a[0], c.b0[0], acc1[0]); if ((kt) < 7) n.a[0] = tr_read2(wa + so, wa + so + 1024); X0; SCHED(); \
      acc1[1] = mfma32(c.a[0], c.b1[0], acc1[1]); if ((kt) < 7) { n.b0[0] = lds_ld16(xb0 + so); n.b1[0] = lds_ld16(xb0 + so + 4096); } X1; SCHED(); \
      acc1[0] = mfma32(c.a[1], c.b0[1], acc1[0]); if ((kt) < 7) n.a[1] = tr_read2(wa + so + 4096, wa + so + 5120); X2; SCHED(); \
      acc1[1] = mfma32(c.a[1], c.b1[1], acc1[1]); if ((kt) < 7) { n.b0[1] = lds_ld16(xb1 + so); n.b1[1] = lds_ld16(xb1 + so + 4096); } X3; SCHED(); \
      acc1[0] = mfma32(c.a[2], c.b0[2], acc1[0]); if ((kt) < 7) n.a[2] = tr_read2(wa + so + 8192, wa + so + 9216); X4; SCHED(); \
      acc1[1] = mfma32(c.a[2], c.b1[2], acc1[1]); if ((kt) < 7) { n.b0[2] = lds_ld16(xb2 + so); n.b1[2] = lds_ld16(xb2 + so + 4096); } X5; SCHED(); \
      acc1[0] = mfma32(c.a[3], c.b0[3], acc1[0]); if ((kt) < 7) n.a[3] = tr_read2(wa + so + 12288, wa + so + 13312); X6; SCHED(); \
      acc1[1] = mfma32(c.a[3], c.b1[3], acc1[1]); if ((kt) < 7) { n.b0[3] = lds_ld16(xb3 + so); n.b1[3] = lds_ld16(xb3 + so + 4096); } X7; SCHED(); \
    }
#define GATE_STORE() do { if (w == 2 && lane < 8) { const v2u gv2 = *(LAS v2u*)(CTL + 64 + lane * 8);                       \
      __builtin_amdgcn_raw_buffer_store_b64(gv2, as_rsrc(sws), WS_GATE + (unsigned)(i1 * 256 + 128 * rb + 8 * fc + lane) * 8u, 0, 16); } } while (0)
#define STEP8 G1_STEP
    if constexpr (ISA) {
      bar_vm<11>();
      STAMP(4);
      STEP8(0, NOP, NOP, NOP, NOP, NOP, NOP, NOP, NOP)
      bar_vm<0>();
      if (tid == 0) flag_store(p.ws, WS_FLA + (unsigned)(mt * 32 + i * 4 + dt4) * FS, tag);
      STAMP(13);
      STEP8(1, g1_dma(4, 0), g1_dma(4, 1), g1_dma(4, 2), g1_dma(4, 3), g1_dma(5, 0), g1_dma(5, 1), g1_dma(5, 2), g1_dma(5, 3))
      BAR_LGKM();
      GATE_STORE();
      STEP8(2, NOP, NOP, NOP, NOP, g1_dma(6, 0), g1_dma(6, 1), g1_dma(6, 2), g1_dma(6, 3))
      bar_vm<8>();
      STEP8(3, NOP, NOP, NOP, NOP, g1_dma(7, 0), g1_dma(7, 1), g1_dma(7, 2), g1_dma(7, 3))
      bar_vm<8>();
      STAMP(12);
      STEP8(4, NOP, NOP, NOP, NOP, w2_issue1(G, 0, 0), w2_issue1(G, 0, 1), w2_issue1(G, 1, 0), w2_issue1(G, 1, 1))
      bar_vm<8>();
      STEP8(5, NOP, NOP, NOP, NOP, w2_issue1(G, 2, 0), w2_issue1(G, 2, 1), w2_issue1(G, 3, 0), w2_issue1(G, 3, 1))
      bar_vm<8>();
      STEP8(6, NOP, NOP, NOP, NOP, w2_issue1(G, 4, 0), w2_issue1(G, 4, 1), w2_issue1(G, 5, 0), w2_issue1(G, 5, 1))
      BAR_LGKM();
    } else {
      const v4u stab = make_srd(&kTab, sizeof(Tab));
      {
        const int kap = tid >> 5, ii = (kap >> 1) & 7, d0 = kap & 1;
        const int g0 = m1 == 0 ? 2 * ii : ii, g1 = m1 == 0 ? 2 * ii + 1 : 8 + ii;
        const unsigned tb = WS_T + (unsigned)m1 * 16384u + (unsigned)(d0 * 512 + (tid & 31) * 16);
        tsrc0 = tb + (unsigned)g0 * 1024u; tsrc1 = tb + (unsigned)g1 * 1024u;
      }
      bar_vm<9>();
      STAMP(4);
      STEP8(0, NOP, NOP, NOP, NOP, g1_dma(4, 0), g1_dma(4, 1), g1_dma(4, 2), g1_dma(4, 3))
      bar_vm<9>();
      STEP8(1, NOP, NOP, NOP, NOP, g1_dma(5, 0), g1_dma(5, 1), g1_dma(5, 2), g1_dma(5, 3))
      bar_vm<9>();
      GATE_STORE();
      STEP8(2, NOP, NOP, NOP, NOP, g1_dma(6, 0), g1_dma(6, 1), g1_dma(6, 2), g1_dma(6, 3))
      bar_vm<8>();
      LAS char* wscr0 = L + 128 * 1024 + w * 2048;
      const bool special = m1 == 0;
      const int m2nd = 128 * w + 1024, off2 = w_off(m2nd, lane);
      if (special) { asm volatile("" : "+v"(xqr), "+v"(xqi), "+v"(xtr), "+v"(xti)); w_put(wscr0, xqr, xqi, lane); }
      STEP8(3, NOP, NOP, (dma16<128u * 1024u + 16384u, 0>(stab, (unsigned)lane * 16u, g1l, g1l)), (dma16<128u * 1024u + 17408u, 1024>(stab, (unsigned)lane * 16u, g1l, g1l)),
            g1_dma(7, 0), g1_dma(7, 1), g1_dma(7, 2), g1_dma(7, 3))
      bar_vm<10>();
      STAMP(12);
      WRawL raw2;
      if (special) { w_get(raw2, wscr0, off2, lane, 0); w_get(raw2, wscr0, off2, lane, 1); w_get(raw2, wscr0, off2, lane, 2); w_get(raw2, wscr0, off2, lane, 3); }
      STEP8(4, NOP, NOP, NOP, NOP, w2_issue1(G, 0, 0), w2_issue1(G, 0, 1), w2_issue1(G, 1, 0), w2_issue1(G, 1, 1))
      bar_vm<10>();
      if (special) { Wd = w_unpack(raw2, w_tail(xtr, xti), off2, m2nd, lane); packed_coef(Wc, Wd, w); }
      STEP8(5, NOP, NOP, NOP, NOP, w2_issue1(G, 2, 0), w2_issue1(G, 2, 1), w2_issue1(G, 3, 0), w2_issue1(G, 3, 1))
      bar_vm<8>();
      STEP8(6, if (w == 0) LD4Z(fla, flaoff, sws, "sc1"), NOP, NOP, NOP, w2_issue1(G, 4, 0), w2_issue1(G, 4, 1), w2_issue1(G, 5, 0), w2_issue1(G, 5, 1))
      BAR_LGKM();
    }
    if constexpr (ISA) {
      STEP8(7, NOP, NOP, NOP, NOP, w2_issue1(G, 6, 0), w2_issue1(G, 6, 1), w2_issue1(G, 7, 0), w2_issue1(G, 7, 1))
    } else {
      LAS char* TB = L + 128 * 1024 + 16384 + lane * 16;
#define TBR(f) (tb[f] = lds_ld16(TB + (f) * 1024), tb[(f) + 1] = lds_ld16(TB + ((f) + 1) * 1024))
      STEP8(7, TBR(6), TBR(0), TBR(10), TBR(2), { TBR(12); w2_issue1(G, 6, 0); }, { TBR(14); w2_issue1(G, 6, 1); }, { TBR(4); w2_issue1(G, 7, 0); }, { TBR(8); w2_issue1(G, 7, 1); })
      if (w == 0) {
        asm volatile("s_waitcnt vmcnt(8)" : "+v"(fla) :: "memory");
        const unsigned ok = __all(fla == tag) ? 1u : 0u;
        if (lane == 0) *(LAS unsigned*)CTL = ok;
      }
      BAR_LGKM();
      t_ok = __builtin_amdgcn_readfirstlane(*(volatile LAS unsigned*)CTL);
      if (t_ok) { LD16Z(tq0, tsrc0, sws, "sc1"); LD16Z(tq1, tsrc1, sws, "sc1"); }
    }
  }
  asm volatile("" :: "v"(touch));
  STAMP(5);
  FT(18);
  if (active) {
#pragma unroll
    for (int rt = 0; rt < 2; ++rt)
#pragma unroll
      for (int u = 0; u < 2; ++u) {
        v4u o;
        o.x = silu2(acc1[rt][8 * u + 0], acc1[rt][8 * u + 1]);
        o.y = silu2(acc1[rt][8 * u + 2], acc1[rt][8 * u + 3]);
        o.z = silu2(acc1[rt][8 * u + 4], acc1[rt][8 * u + 5]);
        o.w = silu2(acc1[rt][8 * u + 6], acc1[rt][8 * u + 7]);
        const int jb32 = 4 * rb + 2 * rh1 + rt, fs = (f0 >> 4) + 2 * ft + u;
        st16_sc1(sws, WS_H + (unsigned)(((i1 * 8 + jb32) * 112 + fs) * 1024 + lane * 16), o);
      }
  }
  SCHED();
  STAMP(6);
  FT(20);
  const unsigned flh_own = WS_FLH + (unsigned)((i1 * 2 + rb) * 16 + fc) * FS;
  if constexpr (ISA) {
    bar_vm<0>();
    if (tid == 0) flag_store(p.ws, flh_own, tag);
  } else {
    LAS char* I0 = L + 128 * 1024;
    LAS char* I1 = L + 128 * 1024 + 16384;
    const int g = lane >> 4, l15 = lane & 15;
    const bool special = (m1 == 0);
    bool published = false;
    if (t_ok) {
      wait_vm<4>();
      asm volatile("s_barrier" ::: "memory");
    } else {
      bar_vm<0>();
      if (tid == 0) flag_store(p.ws, flh_own, tag);
      published = true;
      if (w == 0) poll_words(sws, WS_FLA + (unsigned)(m1 >> 5) * 32u * FS, 32, tag, lane);
      asm volatile("s_barrier" ::: "memory");
      LD16Z(tq0, tsrc0, sws, "sc1"); LD16Z(tq1, tsrc1, sws, "sc1");
      wait_vm<0>();
    }
    asm volatile("" : "+v"(tq0), "+v"(tq1) :: "memory");
    {
      const int kap = tid >> 5, ch = tid & 31;
      *(LAS v4u*)(I0 + kap * 512 + ((ch ^ ((kap & 3) << 2)) << 4)) = tq0;
      *(LAS v4u*)(I0 + (kap + 16) * 512 + ((ch ^ ((kap & 3) << 2)) << 4)) = tq1;
    }
    BAR_LGKM();
    FT(21);
    LAS char* ta = I0 + (8 * hh + q4) * 512 + (((4 * w + 2 * blk + (p4 >> 1)) ^ (q4 << 2)) << 4) + 8 * (p4 & 1);
    const int n = lane & 31;
    LAS char* so = I1 + n * 512 + 8 * hh;
    {
      f32x16 acc = {};
      acc = mfma32(tr_read2(ta, ta + 2048), tb[6], acc);
      acc = mfma32(tr_read2(ta + 8192, ta + 10240), tb[7], acc);
#pragma unroll
      for (int Gq = 0; Gq < 4; ++Gq) {
        v2u o; o.x = pk2(acc[4 * Gq], acc[4 * Gq + 1]); o.y = pk2(acc[4 * Gq + 2], acc[4 * Gq + 3]);
        *(LAS v2u*)(so + (((4 * w + Gq) ^ (n & 15)) << 4)) = o;
      }
    }
    BAR_LGKM();
    FT(22);
    const f32x4 z4 = {0.f, 0.f, 0.f, 0.f};
    const v4u F16fr = tb[0], F16fi = tb[1];
    v4u F16ir, F16ii;
    F16ir.x = F16fr.x; F16ir.y = F16fr.y; F16ir.z = F16fr.z ^ 0x80008000u; F16ir.w = F16fr.w ^ 0x80008000u;
    F16ii.x = F16fi.x ^ 0x80008000u; F16ii.y = F16fi.y ^ 0x80008000u; F16ii.z = F16fi.z; F16ii.w = F16fi.w;
    f32x4 zr[2], zi[2];
#pragma unroll
    for (int pp = 0; pp < 2; ++pp) {
      const int n0 = 4 * w + 2 * pp, n1 = n0 + 1;
      const int ch = 2 * (4 * g + q4) + (p4 >> 1);
      const v4u a1 = tr_read2(I1 + n0 * 512 + ((ch ^ (n0 & 15)) << 4) + 8 * (p4 & 1), I1 + n1 * 512 + ((ch ^ (n1 & 15)) << 4) + 8 * (p4 & 1));
      const f32x4 dr = mfma16(a1, pp ? tb[2] : F16fr, z4), di = mfma16(a1, pp ? tb[3] : F16fi, z4);
      const v4u b3 = cmul_pack<false>(dr, di, asf4(tb[10 + 2 * pp]), asf4(tb[11 + 2 * pp]));
      zr[pp] = mfma16(F16fr, b3, z4); zi[pp] = mfma16(F16fi, b3, z4);
    }
    v4u a4[2];
    if (!special) {
      a4[0] = cmul_pack<false>(zr[0], zi[0], Wc.r0, Wc.i0);
      a4[1] = cmul_pack<false>(zr[1], zi[1], Wc.r1, Wc.i1);
    } else {
      BAR_LGKM();
      LAS char* Z = I0;
#pragma unroll
      for (int pp = 0; pp < 2; ++pp)
#pragma unroll
        for (int r = 0; r < 4; ++r) { f32x2 v = {zr[pp][r], zi[pp][r]}; *(LAS f32x2*)(Z + ((w * 2 + pp) * 256 + l15 + 16 * (4 * g + r)) * 8) = v; }
      BAR_LGKM();
#pragma unroll
      for (int pp = 0; pp < 2; ++pp) {
        f32x4 cr, ci;
#pragma unroll
        for (int r = 0; r < 4; ++r) {
          const int K = l15 + 16 * (4 * g + r), Kn = pp ? 255 - K : (256 - K) & 255;
          const f32x2 y = *(LAS f32x2*)(Z + ((((8 - w) & 7) * 2 + pp) * 256 + Kn) * 8);
          cr[r] = y[0]; ci[r] = -y[1];
        }
        const f32x4 ar = pp ? Wc.r1 : Wc.r0, ai = pp ? Wc.i1 : Wc.i0, br = pp ? Wd.r1 : Wd.r0, bi = pp ? Wd.i1 : Wd.i0;
        const f32x4 yr = zr[pp] * ar - zi[pp] * ai + cr * br - ci * bi, yi = zr[pp] * ai + zi[pp] * ar + cr * bi + ci * br;
        a4[pp].x = pk2(yr[0], yr[1]); a4[pp].y = pk2(yr[2], yr[3]); a4[pp].z = pk2(yi[0], yi[1]); a4[pp].w = pk2(yi[2], yi[3]);
      }
      BAR_LGKM();
    }
#pragma unroll
    for (int pp = 0; pp < 2; ++pp) {
      const f32x4 dr = mfma16(a4[pp], F16ir, z4), di = mfma16(a4[pp], F16ii, z4);
      const v4u a5 = cmul_pack<true>(dr, di, asf4(tb[pp ? 14 : 10]), asf4(tb[pp ? 15 : 11]));
      const f32x4 er = mfma16(a5, pp ? tb[4] : F16ir, z4), ei = mfma16(a5, pp ? tb[5] : F16ii, z4);
      const int n0 = 4 * w + 2 * pp, n1 = n0 + 1;
      const int ch = 2 * l15 + (g >> 1);
      v2u o; o.x = pk2(er[0], er[1]); o.y = pk2(er[2], er[3]);
      *(LAS v2u*)(I0 + n0 * 512 + ((ch ^ ((n0 & 3) << 2)) << 4) + 8 * (g & 1)) = o;
      o.x = pk2(ei[0], ei[1]); o.y = pk2(ei[2], ei[3]);
      *(LAS v2u*)(I0 + n1 * 512 + ((ch ^ ((n1 & 3) << 2)) << 4) + 8 * (g & 1)) = o;
    }
    bar_vm<0>();
    if (!published && tid == 0) flag_store(p.ws, flh_own, tag);
    FT(23);
    m5a = tb[8]; m5b = tb[9];
  }
  return tag;
}

DI void make_geo(Geo& G, const Params& p, int tz) {
  G.tid = tz; G.lane = G.tid & 63; G.w = __builtin_amdgcn_readfirstlane(G.tid >> 6);
  G.b = blockIdx.x;
  const int Lx = G.b >> 3, x8 = G.b & 7;
  {
    const int a = x8 & 3, h = x8 >> 2, rbi = Lx >> 3, c = Lx & 7;
    G.i = 2 * a + h; G.i1 = 2 * a + (rbi >> 1); G.rb = rbi & 1;
    G.fc = c < 4 ? 4 * h + c : 4 + 4 * h + c;
    G.at = rbi * 4 + c; G.m1 = ((rbi * 4 + c - 4) << 3) | x8;
  }
  G.hh = G.lane >> 5; G.blk = (G.lane >> 4) & 1; G.q4 = (G.lane & 15) >> 2; G.p4 = G.lane & 3;
  G.sx = make_srd(p.x, NS * ND * 2); G.sws = make_srd(p.ws, 16u << 20); G.sw2 = make_srd(p.w2, NF * ND * 2);
  G.jb = Lx >> 3; G.dt = Lx & 7; G.rh = G.w & 1; G.q = G.w >> 1;
  G.j = 64 * G.jb + 32 * G.rh + (G.lane & 31);
  const int rho = 16 * G.rh + (G.lane >> 3), chs = (G.lane & 7) ^ (4 * ((G.lane >> 4) & 1));
  G.vw20 = (unsigned)(rho * 1024 + G.dt * 128 + chs * 16); G.vw21 = G.vw20 + 8 * 1024;
  G.ldsw = uni((unsigned)(G.q * 4096 + G.rh * 2048));
  G.fq0b = uni((unsigned)(768 + 256 * G.q) * 1024u); G.fq1b = uni((unsigned)(192 * G.q) * 1024u);
}

__global__ __launch_bounds__(512, 2) void gm_kernel(Params p) {
  extern __shared__ __attribute__((aligned(16))) char smem[];
  LAS char* L = (LAS char*)smem;
  unsigned touch0;
  {
    unsigned long long pc; unsigned len;
    asm volatile("s_getpc_b64 %0\n.Lgm_code_begin:\n\ts_mov_b32 %1, .Lgm_code_end-.Lgm_code_begin" : "=s"(pc), "=s"(len));
    const unsigned line = (unsigned)((blockIdx.x >> 3) * 8 + (threadIdx.x >> 6)) * 128u;
    const char* a0 = (const char*)(pc & ~127ull) + (line < len ? line : 0u);
    asm volatile("global_load_dword %0, %1, off" : "=v"(touch0) : "v"(a0) : "memory");
  }
  STAMP(0);
  FT(0);
  Geo G;
  make_geo(G, p, threadIdx.x);
  const bool isA = G.fc < 8;
  v4u gf[4], m5a, m5b;
  unsigned tag;
  if (isA) tag = phase1<true>(p, L, G, m5a, m5b); else tag = phase1<false>(p, L, G, m5a, m5b);
  asm volatile("" :: "v"(touch0));
  { int tz = threadIdx.x; asm volatile("" : "+v"(tz)); make_geo(G, p, tz); }
  const int tid = G.tid, lane = G.lane, w = G.w, b = G.b, i = G.i, jb = G.jb, dt = G.dt, rh = G.rh, q = G.q, j = G.j;
  const int hh = G.hh, blk = G.blk, q4 = G.q4, p4 = G.p4;
  const v4u sws = G.sws;
  const unsigned ldsw = G.ldsw;
  const int m1 = G.m1;
  STAMP(7);
  FT(25);
  if (!isA) {
    LAS char* I0 = L + 128 * 1024;
    LAS char* I1 = L + 128 * 1024 + 16384;
    LAS char* ta = I0 + (8 * hh + q4) * 512 + (((4 * w + 2 * blk + (p4 >> 1)) ^ (q4 << 2)) << 4) + 8 * (p4 & 1);
    const int n = lane & 31;
    LAS char* so = I1 + n * 512 + 8 * hh;
    f32x16 acc = {};
    acc = mfma32(tr_read2(ta, ta + 2048), m5a, acc);
    acc = mfma32(tr_read2(ta + 8192, ta + 10240), m5b, acc);
#pragma unroll
    for (int Gq = 0; Gq < 4; ++Gq) {
      v2u o; o.x = pk2(acc[4 * Gq], acc[4 * Gq + 1]); o.y = pk2(acc[4 * Gq + 2], acc[4 * Gq + 3]);
      *(LAS v2u*)(so + (((4 * w + Gq) ^ (n & 15)) << 4)) = o;
    }
    BAR_LGKM();
    const bool special = (m1 == 0);
    auto u_store = [&](int t2) {
      const int nn = t2 >> 5, ch = t2 & 31, ii = (nn >> 1) & 7, d0 = nn & 1;
      const int g0 = special ? 2 * ii : ii, g1 = special ? 2 * ii + 1 : 8 + ii;
      const v4u v0 = lds_ld16(I1 + nn * 512 + ((ch ^ (nn & 15)) << 4));
      const v4u v1 = lds_ld16(I1 + (nn + 16) * 512 + ((ch ^ (nn & 15)) << 4));
      const unsigned ub = WS_U + (unsigned)m1 * 16384u + (unsigned)(d0 * 512 + ch * 16);
      st16_sc1(sws, ub + (unsigned)g0 * 1024u, v0);
      st16_sc1(sws, ub + (unsigned)g1 * 1024u, v1);
    };
    if (w != 0) u_store(tid);
    if (w == 1) u_store(tid - 64);
    SCHED();
  }
  const int sz = (q4 >> 1) & 1;
  LAS char* ra0 = L + q * 4096 + (4 * hh + q4) * 128 + (((0 ^ (4 * sz)) + 2 * blk + (p4 >> 1)) << 4) + 8 * (p4 & 1);
  LAS char* ra1 = L + q * 4096 + (4 * hh + q4) * 128 + (((4 ^ (4 * sz)) + 2 * blk + (p4 >> 1)) << 4) + 8 * (p4 & 1);
  v4u wp[4][4];
  BAR_LGKM();
#pragma unroll
  for (int t = 0; t < 4; ++t) {
    LAS char* s0 = ra0 + t * 16384; LAS char* s1 = ra1 + t * 16384;
    wp[t][0] = tr_read2(s0, s0 + 1024); wp[t][1] = tr_read2(s1, s1 + 1024);
    wp[t][2] = tr_read2(s0 + 2048, s0 + 3072); wp[t][3] = tr_read2(s1 + 2048, s1 + 3072);
  }
#define W2_8_11() do { w2_issue1(G, 8, 0); w2_issue1(G, 8, 1); w2_issue1(G, 9, 0); w2_issue1(G, 9, 1); w2_issue1(G, 10, 0); w2_issue1(G, 10, 1); w2_issue1(G, 11, 0); w2_issue1(G, 11, 1); } while (0)
  if (isA) { BAR_LGKM(); W2_8_11(); }
  const unsigned flh = WS_FLH + (unsigned)((i * 2 + (jb >> 1)) * 16) * FS;
  if (w == 0) poll_words(sws, flh, 16, tag, lane);
  BAR_LGKM();
  STAMP(8);
  FT(26);
  const unsigned vh = (unsigned)lane * 16u;
  const unsigned hbase = WS_H + (unsigned)((i * 8 + 2 * jb + rh) * 112) * 1024u;
  const unsigned hb0 = uni(hbase + (G.fq0b >> 4)), hb1 = uni(hbase + (G.fq1b >> 4));
  v2u gg;
  v4u hf[14][2];
#define H_ISSUE(t) do { LD16I(hf[t][0], vh, sws, ((t) < 8 ? hb0 : hb1), (2 * ((t) & 7)) * 1024, "sc1"); LD16I(hf[t][1], vh, sws, ((t) < 8 ? hb0 : hb1), (2 * ((t) & 7) + 1) * 1024, "sc1"); } while (0)
  H_ISSUE(0); H_ISSUE(1);
  asm volatile("s_barrier" ::: "memory");
  H_ISSUE(2); H_ISSUE(3); H_ISSUE(4); H_ISSUE(5); H_ISSUE(6);
  LD8Z(gg, WS_GATE + (unsigned)(i * 256 + j) * 8u, sws, "sc1");
  SCHED();
  f32x16 accb[2] = {}, acca[2] = {};
  v4u wf[2][4];
  unsigned fl0, fl1;
  v4u ub0, ub1, ub2, ub3;
  const unsigned vu = WS_U + (unsigned)(((64 * q + 16 * rh + (lane >> 3)) * 8 + i) * 1024 + dt * 128 + ((lane & 7) ^ (4 * ((lane >> 4) & 1))) * 16);
#define G2_BAR(n, t) asm volatile("s_waitcnt vmcnt(" STR(n) ") lgkmcnt(0)\n\ts_barrier" : "+v"(hf[t][0]), "+v"(hf[t][1]) :: "memory")
#define G2_CORE(t, c, n, RD, acc, b0, b1, X0, X1, X2)                                             \
  {                                                                                                 \
    FT(30 + (t));                                                                                   \
    LAS char* s0 = ra0 + (((t) + 1) & 7) * 16384; LAS char* s1 = ra1 + (((t) + 1) & 7) * 16384;    \
    asm volatile("" : "+v"(c[0]), "+v"(c[1]), "+v"(c[2]), "+v"(c[3]));                             \
    asm volatile("" :: "v"(acc[0]), "v"(acc[1]));                                                   \
    SCHED();                                                                                        \
    acc[0] = mfma32(c[0], b0, acc[0]); if (RD) n[0] = tr_read2(s0, s0 + 1024); SCHED();            \
    acc[1] = mfma32(c[1], b0, acc[1]); if (RD) n[1] = tr_read2(s1, s1 + 1024); X0; SCHED();        \
    acc[0] = mfma32(c[2], b1, acc[0]); if (RD) n[2] = tr_read2(s0 + 2048, s0 + 3072); X1; SCHED(); \
    acc[1] = mfma32(c[3], b1, acc[1]); if (RD) n[3] = tr_read2(s1 + 2048, s1 + 3072); X2; SCHED(); \
  }
#define G2_PRE(t, X2) G2_CORE(t, wp[t], wf[0], (t) == 3, accb, hf[t][0], hf[t][1], if (!isA) w2_issue1(G, (t) + 8, 0), if (!isA) w2_issue1(G, (t) + 8, 1), X2)
#define G2_STEP(t, X0, X1, X2) G2_CORE(t, wf[(t) & 1], wf[((t) + 1) & 1], true, accb, hf[t][0], hf[t][1], X0, X1, X2)
  G2_BAR(13, 0);
  if (!isA && tid == 0) flag_store(p.ws, WS_FLB + (unsigned)m1 * FS, tag);
  STAMP(14);
  G2_PRE(0, H_ISSUE(7))
  G2_BAR(13, 1); G2_PRE(1, H_ISSUE(8))
  G2_BAR(13, 2); G2_PRE(2, H_ISSUE(9))
  G2_BAR(13, 3); G2_PRE(3, H_ISSUE(10))
  G2_BAR(13, 4); G2_STEP(4, w2_issue1(G, 12, 0), w2_issue1(G, 12, 1), H_ISSUE(11))
  G2_BAR(15, 5); G2_STEP(5, w2_issue1(G, 13, 0), w2_issue1(G, 13, 1), H_ISSUE(12))
  if (w == 0) { LD4Z(fl0, WS_FLB + (unsigned)lane * FS, sws, "sc1"); LD4Z(fl1, WS_FLB + (64u + (unsigned)lane) * FS, sws, "sc1"); }
  G2_BAR(17, 6); G2_STEP(6, NOP, NOP, H_ISSUE(13))
  STAMP(9);
  G2_BAR(16, 7); G2_STEP(7, NOP, NOP, NOP)
  LAS unsigned* verdict = (LAS unsigned*)(L + 128 * 1024);
  if (w == 0) {
    asm volatile("s_waitcnt vmcnt(2)" : "+v"(fl0), "+v"(fl1) :: "memory");
    const unsigned ok = __all(fl0 == tag && fl1 == tag) ? 1u : 0u;
    if (lane == 0) *verdict = ok;
  }
  G2_BAR(14, 8);
  const int u_early = __builtin_amdgcn_readfirstlane(*(volatile LAS unsigned*)verdict);
  STAMP(15);
#define U_ISSUE() do { LD16Z(ub0, vu, sws, "sc1"); LD16Z(ub1, vu + 8u * 8192u, sws, "sc1"); LD16Z(ub2, vu + 32u * 8192u, sws, "sc1"); LD16Z(ub3, vu + 40u * 8192u, sws, "sc1"); } while (0)
  if (u_early) U_ISSUE();
  GRot grot;
  G2_STEP(8, gen_g_init(G, grot), NOP, gen_g_frag<0>(G, grot, gf[0]))
  unsigned e0, e1, e2, e3;
  if (b == 0 && w == 1) {
    LD4Z(e0, WS_FLH + (unsigned)lane * FS, sws, "sc1"); LD4Z(e1, WS_FLH + (64u + (unsigned)lane) * FS, sws, "sc1");
    LD4Z(e2, WS_FLH + (128u + (unsigned)lane) * FS, sws, "sc1"); LD4Z(e3, WS_FLH + (192u + (unsigned)lane) * FS, sws, "sc1");
  }
  G2_BAR(12, 9); G2_STEP(9, NOP, NOP, gen_g_frag<1>(G, grot, gf[1]))
  G2_BAR(10, 10); G2_STEP(10, NOP, NOP, gen_g_frag<2>(G, grot, gf[2]))
  G2_BAR(6, 11); G2_STEP(11, NOP, NOP, gen_g_frag<3>(G, grot, gf[3]))
  G2_BAR(2, 12);
  G2_STEP(12, NOP, NOP, NOP)
#define U_PUT() do {                                                                                                        \
    asm volatile("s_waitcnt vmcnt(0)" : "+v"(ub0), "+v"(ub1), "+v"(ub2), "+v"(ub3) :: "memory");                             \
    *(LAS v4u*)(L + 6 * 16384 + ldsw + lane * 16) = ub0; *(LAS v4u*)(L + 6 * 16384 + ldsw + 1024 + lane * 16) = ub1;         \
    *(LAS v4u*)(L + 7 * 16384 + ldsw + lane * 16) = ub2; *(LAS v4u*)(L + 7 * 16384 + ldsw + 1024 + lane * 16) = ub3; } while (0)
  if (u_early) U_PUT();
  G2_BAR(0, 13);
  asm volatile("" : "+v"(gg));
  if (b == 0 && w == 1) {
    asm volatile("" : "+v"(e0), "+v"(e1), "+v"(e2), "+v"(e3));
    if (!__all(e0 == tag && e1 == tag && e2 == tag && e3 == tag)) {
#pragma unroll 1
      for (int u = 0; u < 4; ++u) poll_words(sws, WS_FLH + 64u * FS * u, 64, tag, lane);
    }
    if (lane == 0) flag_store(p.ws, WS_EPOCH, tag);
  }
  G2_CORE(13, wf[1], wf[0], u_early, accb, hf[13][0], hf[13][1], NOP, NOP, NOP)
  if (!u_early) {
    if (w == 0) { poll_words(sws, WS_FLB, 64, tag, lane); poll_words(sws, WS_FLB + 64u * FS, 64, tag, lane); }
    BAR_LGKM();
    U_ISSUE();
    U_PUT();
    BAR_LGKM();
    LAS char* s0 = ra0 + 6 * 16384; LAS char* s1 = ra1 + 6 * 16384;
    wf[0][0] = tr_read2(s0, s0 + 1024); wf[0][1] = tr_read2(s1, s1 + 1024); wf[0][2] = tr_read2(s0 + 2048, s0 + 3072); wf[0][3] = tr_read2(s1 + 2048, s1 + 3072);
  }
  STAMP(10);
  BAR_LGKM(); G2_CORE(14, wf[0], wf[1], true, acca, gf[0], gf[1], NOP, NOP, NOP)
  BAR_LGKM(); G2_CORE(15, wf[1], wf[0], false, acca, gf[2], gf[3], NOP, NOP, NOP)
  FT(50);
  {
    const float og = asf(gg.y), gs = asf(gg.x) * (1.f / 524288.f);
    const int row = 32 * rh + (lane & 31);
#pragma unroll
    for (int dh = 0; dh < 2; ++dh)
#pragma unroll
      for (int G = 0; G < 4; ++G) {
        f32x4 v;
#pragma unroll
        for (int e = 0; e < 4; ++e) v[e] = __builtin_fmaf(og, accb[dh][4 * G + e], gs * acca[dh][4 * G + e]);
        *(LAS f32x4*)(L + q * 16384 + row * 256 + (((8 * dh + 2 * G + hh) ^ (row & 7)) << 4)) = v;
      }
  }
  BAR_LGKM();
  FT(51);
  {
    const int row = tid >> 3, c8 = tid & 7;
    f32x4 s0 = {0.f, 0.f, 0.f, 0.f}, s1 = s0;
#pragma unroll
    for (int qq = 0; qq < 4; ++qq) {
      s0 += *(LAS f32x4*)(L + qq * 16384 + row * 256 + (((2 * c8) ^ (row & 7)) << 4));
      s1 += *(LAS f32x4*)(L + qq * 16384 + row * 256 + (((2 * c8 + 1) ^ (row & 7)) << 4));
    }
    v4u o; o.x = pk2(s0[0], s0[1]); o.y = pk2(s0[2], s0[3]); o.z = pk2(s1[0], s1[1]); o.w = pk2(s1[2], s1[3]);
    *(GAS v4u*)((GAS char*)p.out + (size_t)(i + 8 * (64 * jb + row)) * 1024 + dt * 128 + c8 * 16) = o;
  }
  asm volatile(".Lgm_code_end:");
  STAMP(11);
}

}

extern "C" void kernel_launch(void* const* d_in, const int* in_sizes, int n_in, void* d_out, int out_size, void* d_ws, size_t ws_size,
                              hipStream_t stream) {
  Params p;
  p.x = d_in[0]; p.wr = d_in[1]; p.wi = d_in[2]; p.w1 = d_in[4]; p.w2 = d_in[5]; p.gw = d_in[6];
  p.out = d_out; p.ws = d_ws;
  (void)hipFuncSetAttribute((const void*)gm_kernel, hipFuncAttributeMaxDynamicSharedMemorySize, LDS_BYTES);
  gm_kernel<<<256, 512, LDS_BYTES, stream>>>(p);
}
```

```cpp
#include <hip/hip_runtime.h>
#include <hip/hip_bf16.h>
#include <stdint.h>

namespace {

typedef unsigned v4u __attribute__((ext_vector_type(4)));
typedef unsigned v2u __attribute__((ext_vector_type(2)));
typedef float f32x16 __attribute__((ext_vector_type(16)));
typedef float f32x4 __attribute__((ext_vector_type(4)));
typedef float f32x2 __attribute__((ext_vector_type(2)));
typedef __bf16 bf16x8 __attribute__((ext_vector_type(8)));
typedef __bf16 bf16x2 __attribute__((ext_vector_type(2)));
typedef short s16x4 __attribute__((ext_vector_type(4)));
#define LAS __attribute__((address_space(3)))
#define GAS __attribute__((address_space(1)))
#define DI __device__ __forceinline__

constexpr int NS = 2048, ND = 512, NF = 1792;
constexpr unsigned WS_EPOCH = 0;
constexpr unsigned FS = 64;
constexpr unsigned WS_FLA = 4096;
constexpr unsigned WS_FLH = 16384;
constexpr unsigned WS_FLB = 32768;
constexpr unsigned WS_GATE = 65536;
constexpr unsigned WS_T = 1u << 20;
constexpr unsigned WS_U = 3u << 20;
constexpr unsigned WS_H = 5u << 20;
constexpr unsigned H_BYTES = 8u * 8u * 112u * 1024u;
constexpr int LDS_BYTES = 160 * 1024;
constexpr unsigned kSpinCap = 1u << 20;

struct Params {
  const void* x; const void* wr; const void* wi; const void* w1; const void* w2; const void* gw;
  void* out; void* ws;
};

constexpr double kPi = 3.14159265358979323846;
constexpr double ccos(double x) { double t = 1, s = 1; for (int n = 1; n < 16; ++n) { t *= -x * x / ((2 * n - 1) * (2 * n)); s += t; } return s; }
constexpr double cosn(int e, int n) { e = ((e % n) + n) % n; double x = 2 * kPi * e / n; if (x > kPi) x -= 2 * kPi; return ccos(x); }
constexpr double wre(int e, int n) { return cosn(e, n); }
constexpr double wim(int e, int n) { return -cosn(4 * e - n, 4 * n); }
constexpr unsigned bfb(double v) { float f = (float)v; unsigned u = __builtin_bit_cast(unsigned, f); u += 0x7fffu + ((u >> 16) & 1u); return u >> 16; }
constexpr unsigned bf2(double a, double b) { return bfb(a) | (bfb(b) << 16); }
constexpr unsigned fbits(double v) { return __builtin_bit_cast(unsigned, (float)v); }
struct Tab { unsigned v[16][64][4]; };
constexpr Tab make_tab() {
  Tab t{};
  for (int l = 0; l < 64; ++l) {
    const int g = l >> 4, l15 = l & 15, hh = l >> 5, n = l & 31;
    for (int mtx = 0; mtx < 3; ++mtx) {
      double r[4] = {}, im[4] = {};
      for (int q = 0; q < 4; ++q) {
        const int a = 4 * g + q;
        if (mtx == 0) { r[q] = wre(l15 * a, 16); im[q] = wim(l15 * a, 16); }
        else if (mtx == 1) { r[q] = wre(a * (2 * l15 + 1), 32); im[q] = wim(a * (2 * l15 + 1), 32); }
        else { r[q] = wre(l15 * (2 * a + 1), 32); im[q] = -wim(l15 * (2 * a + 1), 32); }
      }
      t.v[2 * mtx][l][0] = bf2(r[0], r[1]); t.v[2 * mtx][l][1] = bf2(r[2], r[3]); t.v[2 * mtx][l][2] = bf2(-im[0], -im[1]); t.v[2 * mtx][l][3] = bf2(-im[2], -im[3]);
      t.v[2 * mtx + 1][l][0] = bf2(im[0], im[1]); t.v[2 * mtx + 1][l][1] = bf2(im[2], im[3]); t.v[2 * mtx + 1][l][2] = bf2(r[0], r[1]); t.v[2 * mtx + 1][l][3] = bf2(r[2], r[3]);
    }
    {
      const int m2 = n >> 2, pp = (n >> 1) & 1, cp = n & 1;
      double v0[8] = {}, v1[8] = {};
      for (int jj = 0; jj < 8; ++jj) {
        const int ii = (8 * hh + jj) >> 1, d0 = jj & 1;
        const double sg = (d0 & pp) ? -1.0 : 1.0, orr = sg * wre(ii * m2, 8), oi = sg * wim(ii * m2, 8);
        v0[jj] = cp ? oi : orr; v1[jj] = cp ? orr : -oi;
      }
      for (int q = 0; q < 4; ++q) { t.v[6][l][q] = bf2(v0[2 * q], v0[2 * q + 1]); t.v[7][l][q] = bf2(v1[2 * q], v1[2 * q + 1]); }
    }
    {
      const int cq = n >> 4, ii = (n >> 1) & 7, d0 = n & 1;
      for (int s = 0; s < 2; ++s) {
        double v[8] = {};
        for (int jj = 0; jj < 8; ++jj) {
          const int m2k = 4 * s + 2 * hh + (jj >> 2), pk = (jj >> 1) & 1, ck = jj & 1;
          const double sg = (d0 & pk) ? -1.0 : 1.0, orr = sg * wre(ii * m2k, 8), oi = -sg * wim(ii * m2k, 8);
          v[jj] = cq ? (ck ? orr : oi) : (ck ? -oi : orr);
        }
        for (int q = 0; q < 4; ++q) t.v[8 + s][l][q] = bf2(v[2 * q], v[2 * q + 1]);
      }
    }
    for (int q = 0; q < 4; ++q) {
      const int a = 4 * g + q;
      t.v[10][l][q] = fbits(wre(a * (2 * l15), 512));      t.v[11][l][q] = fbits(wim(a * (2 * l15), 512));
      t.v[12][l][q] = fbits(wre(a * (2 * l15 + 1), 512));  t.v[13][l][q] = fbits(wim(a * (2 * l15 + 1), 512));
      t.v[14][l][q] = fbits(wre(l15 * (2 * a + 1), 512));  t.v[15][l][q] = fbits(wim(l15 * (2 * a + 1), 512));
    }
  }
  return t;
}
__device__ const Tab kTab = make_tab();

constexpr unsigned STAMP_OFF = 512 * 1024;
#define STAMP(k) do {} while (0)
#define FT(k) do {} while (0)

DI v4u make_srd(const void* p, unsigned bytes) {
  const unsigned long long a = (unsigned long long)p;
  v4u r; r.x = (unsigned)a; r.y = (unsigned)(a >> 32) & 0xffffu; r.z = bytes; r.w = 0x00020000u;
  return r;
}
template <unsigned LDSI, unsigned SOI> DI void dma16(v4u srd, unsigned voff, unsigned ldsb, unsigned sob) {
  unsigned tmp;
  asm volatile("s_add_u32 m0, %3, %5\n\ts_add_u32 %0, %4, %6\n\tbuffer_load_dwordx4 %1, %2, %0 offen lds"
               : "=&s"(tmp) : "v"(voff), "s"(srd), "s"(ldsb), "s"(sob), "i"(LDSI), "i"(SOI) : "memory", "scc");
}
template <unsigned LDSI, unsigned SOI> DI void dma16z(v4u srd, unsigned voff, unsigned ldsb) {
  unsigned tmp;
  asm volatile("s_add_u32 m0, %3, %4\n\ts_mov_b32 %0, %5\n\tbuffer_load_dwordx4 %1, %2, %0 offen lds"
               : "=&s"(tmp) : "v"(voff), "s"(srd), "s"(ldsb), "i"(LDSI), "i"(SOI) : "memory", "scc");
}
DI unsigned uni(unsigned v) { return (unsigned)__builtin_amdgcn_readfirstlane((int)v); }
#define LD16(dst, voffv, srd, soffv, flags) asm volatile("buffer_load_dwordx4 %0, %1, %2, %3 offen " flags : "=v"(dst) : "v"(voffv), "s"(srd), "s"(soffv) : "memory")
#define LD16I(dst, voffv, srd, sob, soi, flags) do { unsigned tmp_; asm volatile("s_add_u32 %1, %4, %5\n\tbuffer_load_dwordx4 %0, %2, %3, %1 offen " flags : "=v"(dst), "=&s"(tmp_) : "v"(voffv), "s"(srd), "s"(sob), "i"(soi) : "memory", "scc"); } while (0)
#define LD16Z(dst, voffv, srd, flags) asm volatile("buffer_load_dwordx4 %0, %1, %2, 0 offen " flags : "=v"(dst) : "v"(voffv), "s"(srd) : "memory")
#define LD4Z(dst, voffv, srd, flags) asm volatile("buffer_load_dword %0, %1, %2, 0 offen " flags : "=v"(dst) : "v"(voffv), "s"(srd) : "memory")
#define LD2Z(dst, voffv, srd, flags) asm volatile("buffer_load_ushort %0, %1, %2, 0 offen " flags : "=v"(dst) : "v"(voffv), "s"(srd) : "memory")
#define LD8Z(dst, voffv, srd, flags) asm volatile("buffer_load_dwordx2 %0, %1, %2, 0 offen " flags : "=v"(dst) : "v"(voffv), "s"(srd) : "memory")
#define LD8(dst, voffv, srd, soffv, flags) asm volatile("buffer_load_dwordx2 %0, %1, %2, %3 offen " flags : "=v"(dst) : "v"(voffv), "s"(srd), "s"(soffv) : "memory")
#define LD4(dst, voffv, srd, soffv, flags) asm volatile("buffer_load_dword %0, %1, %2, %3 offen " flags : "=v"(dst) : "v"(voffv), "s"(srd), "s"(soffv) : "memory")
#define LD2(dst, voffv, srd, soffv, flags) asm volatile("buffer_load_ushort %0, %1, %2, %3 offen " flags : "=v"(dst) : "v"(voffv), "s"(srd), "s"(soffv) : "memory")
#define XSTR(x) #x
#define STR(x) XSTR(x)
#define BAR_VM(n) asm volatile("s_waitcnt vmcnt(" STR(n) ") lgkmcnt(0)\n\ts_barrier" ::: "memory")
#define BAR_LGKM() asm volatile("s_waitcnt lgkmcnt(0)\n\ts_barrier" ::: "memory")
#define SCHED() __builtin_amdgcn_sched_barrier(0)
template <int N> DI void bar_vm() { asm volatile("s_waitcnt vmcnt(%0) lgkmcnt(0)\n\ts_barrier" :: "i"(N) : "memory"); }
template <int N> DI void wait_vm() { asm volatile("s_waitcnt vmcnt(%0)" :: "i"(N) : "memory"); }

DI unsigned pk2(float a, float b) { f32x2 p = {a, b}; return __builtin_bit_cast(unsigned, __builtin_convertvector(p, bf16x2)); }
DI float bflo(unsigned u) { return __builtin_bit_cast(float, u << 16); }
DI float bfhi(unsigned u) { return __builtin_bit_cast(float, u & 0xffff0000u); }
DI float asf(unsigned u) { return __builtin_bit_cast(float, u); }
DI f32x16 mfma32(v4u a, v4u b, f32x16 c) {
  return __builtin_amdgcn_mfma_f32_32x32x16_bf16(__builtin_bit_cast(bf16x8, a), __builtin_bit_cast(bf16x8, b), c, 0, 0, 0);
}
DI f32x4 mfma16(v4u a, v4u b, f32x4 c) {
  return __builtin_amdgcn_mfma_f32_16x16x32_bf16(__builtin_bit_cast(bf16x8, a), __builtin_bit_cast(bf16x8, b), c, 0, 0, 0);
}
DI v2u tr_read(LAS char* p) { return __builtin_bit_cast(v2u, __builtin_amdgcn_ds_read_tr16_b64_v4i16((LAS s16x4*)p)); }
DI v4u tr_read2(LAS char* p0, LAS char* p1) { v2u a = tr_read(p0), b = tr_read(p1); v4u r; r.x = a.x; r.y = a.y; r.z = b.x; r.w = b.y; return r; }
DI v4u lds_ld16(LAS char* p) { return *(LAS v4u*)p; }
DI float cosrev(float r) { return __builtin_amdgcn_cosf(r); }
DI float sinrev(float r) { return __builtin_amdgcn_sinf(r); }
DI __amdgpu_buffer_rsrc_t as_rsrc(v4u srd) { return __builtin_amdgcn_make_buffer_rsrc((void*)(((unsigned long long)srd.y << 32) | srd.x), 0, srd.z, 0x00020000); }
DI void st16_sc1(v4u srd, unsigned voff, v4u v) { __builtin_amdgcn_raw_buffer_store_b128(v, as_rsrc(srd), voff, 0, 16); }
DI void flag_store(void* ws, unsigned off, unsigned tag) { __hip_atomic_store((GAS unsigned*)((GAS char*)ws + off), tag, __ATOMIC_RELAXED, __HIP_MEMORY_SCOPE_AGENT); }
template <int CTRL, int RMASK> DI float dpp_add(float v) { return v + asf((unsigned)__builtin_amdgcn_update_dpp(0, (int)__builtin_bit_cast(unsigned, v), CTRL, RMASK, 0xf, true)); }
DI void poll_words(v4u sws, unsigned off, int n, unsigned tag, int lane) {
  const unsigned vo = off + FS * (unsigned)(lane < n ? lane : 0);
#pragma unroll 1
  for (unsigned spins = 0; spins < kSpinCap; ++spins) {
    unsigned f;
    asm volatile("buffer_load_dword %0, %1, %2, 0 offen sc1\n\ts_waitcnt vmcnt(0)" : "=&v"(f) : "v"(vo), "s"(sws) : "memory");
    if (__all(f == tag)) return;
    __builtin_amdgcn_s_sleep(4);
  }
}

constexpr unsigned A_XIMG = 64 * 1024, A_FIMG = 128 * 1024;
DI void phaseA_genF(LAS char* L, int i, int mt, int w, int lane) {
  const int mh = w >> 2, sg = w & 3, n = lane & 31, hh = lane >> 5;
  const int m1c = 64 * mt + 32 * mh + n, m1 = m1c >> 1, c = m1c & 1;
  const int j0 = 64 * sg + 32 * hh;
  const float ph = c ? 0.25f : 0.f;
  const float k2 = 2.f * cosrev((float)(8 * m1) * (1.f / 2048.f));
  float vm = cosrev((float)(((i + 8 * (j0 - 1)) * m1) & 2047) * (1.f / 2048.f) + ph);
  float v0 = cosrev((float)(((i + 8 * j0) * m1) & 2047) * (1.f / 2048.f) + ph);
  const unsigned cst = c ? 0x3F800000u : 0x00003F80u;
  const bool sp = m1c < 2;
#pragma unroll
  for (int s = 0; s < 4; ++s) {
    float e[8];
#pragma unroll
    for (int q = 0; q < 8; ++q) { e[q] = v0; const float vn = __builtin_fmaf(k2, v0, -vm); vm = v0; v0 = vn; }
    v4u f; f.x = pk2(e[0], e[1]); f.y = pk2(e[2], e[3]); f.z = pk2(e[4], e[5]); f.w = pk2(e[6], e[7]);
    if (mt == 0 && w < 4) { f.x = sp ? cst : f.x; f.y = sp ? cst : f.y; f.z = sp ? cst : f.z; f.w = sp ? cst : f.w; }
    *(LAS v4u*)(L + A_FIMG + (mh * 16 + sg * 4 + s) * 1024 + lane * 16) = f;
  }
}

struct Frag1 { v4u a[4], b0[4], b1[4]; };
DI void g1_read(Frag1& f, LAS char* wa, LAS char* xb0, LAS char* xb1, LAS char* xb2, LAS char* xb3, int slot) {
  const int so = slot * 32768;
  f.a[0] = tr_read2(wa + so, wa + so + 1024);              f.b0[0] = lds_ld16(xb0 + so); f.b1[0] = lds_ld16(xb0 + so + 4096);
  f.a[1] = tr_read2(wa + so + 4096, wa + so + 5120);       f.b0[1] = lds_ld16(xb1 + so); f.b1[1] = lds_ld16(xb1 + so + 4096);
  f.a[2] = tr_read2(wa + so + 8192, wa + so + 9216);       f.b0[2] = lds_ld16(xb2 + so); f.b1[2] = lds_ld16(xb2 + so + 4096);
  f.a[3] = tr_read2(wa + so + 12288, wa + so + 13312);     f.b0[3] = lds_ld16(xb3 + so); f.b1[3] = lds_ld16(xb3 + so + 4096);
}
DI unsigned silu2(float a, float b) {
  const f32x2 h = {a, b}, t = h * -1.44269504088896341f;
  f32x2 e = {__builtin_amdgcn_exp2f(t[0]), __builtin_amdgcn_exp2f(t[1])};
  e = e + 1.f;
  const f32x2 r = {__builtin_amdgcn_rcpf(e[0]), __builtin_amdgcn_rcpf(e[1])};
  return __builtin_bit_cast(unsigned, __builtin_convertvector(h * r, bf16x2));
}

DI f32x2 lo2(const f32x4& v) { return __builtin_shufflevector(v, v, 0, 1); }
DI f32x2 hi2(const f32x4& v) { return __builtin_shufflevector(v, v, 2, 3); }
DI unsigned pk2v(f32x2 p) { return __builtin_bit_cast(unsigned, __builtin_convertvector(p, bf16x2)); }
DI f32x4 asf4(v4u v) { return __builtin_bit_cast(f32x4, v); }
template <bool CONJ> DI v4u cmul_pack(const f32x4& re, const f32x4& im, const f32x4& tr, const f32x4& ti) {
  const f32x2 r0 = lo2(re), r1 = hi2(re), i0 = lo2(im), i1 = hi2(im), c0 = lo2(tr), c1 = hi2(tr), s0 = lo2(ti), s1 = hi2(ti);
  f32x2 a0, a1, b0, b1;
  if (CONJ) {
    a0 = __builtin_elementwise_fma(r0, c0, i0 * s0); a1 = __builtin_elementwise_fma(r1, c1, i1 * s1);
    b0 = __builtin_elementwise_fma(i0, c0, -(r0 * s0)); b1 = __builtin_elementwise_fma(i1, c1, -(r1 * s1));
  } else {
    a0 = __builtin_elementwise_fma(r0, c0, -(i0 * s0)); a1 = __builtin_elementwise_fma(r1, c1, -(i1 * s1));
    b0 = __builtin_elementwise_fma(r0, s0, i0 * c0); b1 = __builtin_elementwise_fma(r1, s1, i1 * c1);
  }
  v4u r; r.x = pk2v(a0); r.y = pk2v(a1); r.z = pk2v(b0); r.w = pk2v(b1); return r;
}
struct cf { float r, i; };
DI cf cmul(cf a, cf b) { return {a.r * b.r - a.i * b.i, a.r * b.i + a.i * b.r}; }
DI cf cmulc(cf a, cf b) { return {a.r * b.r + a.i * b.i, a.i * b.r - a.r * b.i}; }
DI unsigned w_src(int m, int lane) {
  const int mm = (NS - m) & (NS - 1);
  const int e = lane < 32 ? m * 257 - (m & 1) : mm * 257 + (m & 1);
  return (unsigned)(e * 2 + (lane & 31) * 16);
}
DI unsigned w_tail_src(int m, int lane) {
  const int mm = (NS - m) & (NS - 1), l = lane & 3;
  return (unsigned)((l < 2 ? m * 257 + 255 + l : mm * 257 + (l - 2) * 256) * 2);
}
struct WTail { unsigned r255, r256, rm0, rm256, i255, i256, im0, im256; };
DI WTail w_tail(unsigned tr, unsigned ti) {
  return {(unsigned)__builtin_amdgcn_readlane((int)tr, 0), (unsigned)__builtin_amdgcn_readlane((int)tr, 1), (unsigned)__builtin_amdgcn_readlane((int)tr, 2), (unsigned)__builtin_amdgcn_readlane((int)tr, 3),
          (unsigned)__builtin_amdgcn_readlane((int)ti, 0), (unsigned)__builtin_amdgcn_readlane((int)ti, 1), (unsigned)__builtin_amdgcn_readlane((int)ti, 2), (unsigned)__builtin_amdgcn_readlane((int)ti, 3)};
}
struct WVal { f32x4 r0, i0, r1, i1; };
struct WRawL { unsigned r[4][2], i[4][2]; };
DI void w_put(LAS char* scr, v4u qr, v4u qi, int lane) { *(LAS v4u*)(scr + lane * 16) = qr; *(LAS v4u*)(scr + 1024 + lane * 16) = qi; }
DI int w_off(int m, int lane) { const int k1 = lane & 15, g = lane >> 4, odd = m & 1; return lane < 32 ? (2 * k1 + 128 * g + odd) * 2 : 512 + (511 - 2 * k1 - 128 * g - odd) * 2; }
DI void w_get(WRawL& o, LAS char* scr, int off, int lane, int r) {
  LAS unsigned* a = (LAS unsigned*)(scr + ((off + (lane < 32 ? 64 : -64) * r) & ~3));
  o.r[r][0] = a[0]; o.r[r][1] = a[1]; o.i[r][0] = a[256]; o.i[r][1] = a[257];
}
DI WVal w_unpack(const WRawL& x, const WTail& t, int off, int m, int lane) {
  const bool lo = lane < 32;
  const unsigned sh = (off & 2) * 8;
  unsigned rr[4], ii[4];
#pragma unroll
  for (int r = 0; r < 4; ++r) { rr[r] = __builtin_amdgcn_alignbit(x.r[r][1], x.r[r][0], sh); ii[r] = __builtin_amdgcn_alignbit(x.i[r][1], x.i[r][0], sh); }
  if (m & 1) { rr[3] = lane == 31 ? (rr[3] & 0xffffu) | (t.r255 << 16) : rr[3]; ii[3] = lane == 31 ? (ii[3] & 0xffffu) | (t.i255 << 16) : ii[3]; }
  else { rr[0] = lane == 32 ? (rr[0] & 0xffffu) | (t.rm256 << 16) : rr[0]; ii[0] = lane == 32 ? (ii[0] & 0xffffu) | (t.im256 << 16) : ii[0]; }
  const unsigned s0 = lo ? 16u : 0u, s1 = lo ? 0u : 16u, sg = lo ? 0u : 0x80000000u;
  WVal o;
#pragma unroll
  for (int r = 0; r < 4; ++r) {
    o.r0[r] = asf((rr[r] << s0) & 0xffff0000u); o.r1[r] = asf((rr[r] << s1) & 0xffff0000u);
    o.i0[r] = asf(((ii[r] << s0) & 0xffff0000u) ^ sg); o.i1[r] = asf(((ii[r] << s1) & 0xffff0000u) ^ sg);
  }
  const bool av = (lane & 31) == 0;
  const float xr = bflo(lo ? t.rm0 : t.r256), xi = asf(((lo ? t.im0 : t.i256) << 16) ^ (sg ^ 0x80000000u));
  o.r0[0] = av ? 0.5f * (o.r0[0] + xr) : o.r0[0];
  o.i0[0] = av ? 0.5f * (o.i0[0] + xi) : o.i0[0];
  return o;
}

DI void packed_coef(WVal& a, WVal& b, int m2) {
  const cf om = {cosrev((float)m2 * 0.0625f), -sinrev((float)m2 * 0.0625f)};
#pragma unroll
  for (int pp = 0; pp < 2; ++pp)
#pragma unroll
    for (int r = 0; r < 4; ++r) {
      const float war = pp ? a.r1[r] : a.r0[r], wai = pp ? a.i1[r] : a.i0[r], wbr = pp ? b.r1[r] : b.r0[r], wbi = pp ? b.i1[r] : b.i0[r];
      const cf sp = {war + wbr, wai + wbi}, sm = {war - wbr, wai - wbi};
      const cf t1 = cmulc(sm, om), t2 = cmul(sm, om);
      const cf cE = {sp.r - t1.i, sp.i + t1.r}, cO = {t2.r - sp.i, t2.i + sp.r};
      const float alr = 0.25f * (cE.r + cO.i), ali = 0.25f * (cE.i - cO.r), ber = 0.25f * (cE.r - cO.i), bei = 0.25f * (cE.i + cO.r);
      if (pp) { a.r1[r] = alr; a.i1[r] = ali; b.r1[r] = ber; b.i1[r] = bei; } else { a.r0[r] = alr; a.i0[r] = ali; b.r0[r] = ber; b.i0[r] = bei; }
    }
}

struct Geo {
  int tid, lane, w, b, i, i1, at, m1, rb, fc, hh, blk, q4, p4;
  int jb, dt, rh, q, j;
  v4u sx, sws, sw2;
  unsigned vw20, vw21, ldsw, fq0b, fq1b;
};
template <int T, int U> DI void w2_dma(const v4u& sw2, unsigned vw20, unsigned vw21, unsigned ldsw, unsigned fq0, unsigned fq1) {
  dma16<(T & 7) * 16384u + U * 1024u, (T < 8 ? T : T - 8) * 32768u>(sw2, U ? vw21 : vw20, ldsw, T < 8 ? fq0 : fq1);
}
#define w2_issue1(g, t, u) (w2_dma<t, u>((g).sw2, (g).vw20, (g).vw21, (g).ldsw, (g).fq0b, (g).fq1b))
struct GRot { float c1, s1, c4, s4, c, s; };
DI void gen_g_init(const Geo& g, GRot& r) {
  const int srow = g.i + 8 * g.j;
  const int mb = 32 * g.q + 2 * g.hh;
  const float r1 = (float)(srow & 2047) * (1.f / 2048.f), r4 = (float)((4 * srow) & 2047) * (1.f / 2048.f), r0 = (float)((mb * srow) & 2047) * (1.f / 2048.f);
  r.c1 = cosrev(r1); r.s1 = sinrev(r1); r.c4 = cosrev(r4); r.s4 = sinrev(r4); r.c = cosrev(r0); r.s = sinrev(r0);
}
template <int S> DI void gen_g_frag(const Geo& g, GRot& r, v4u& f) {
#pragma unroll
  for (int h = 0; h < 2; ++h) {
    const float cb = __builtin_fmaf(r.c, r.c1, -(r.s * r.s1)), sb = __builtin_fmaf(r.s, r.c1, r.c * r.s1);
    unsigned w0 = pk2(r.c, -r.s), w1 = pk2(cb, -sb);
    if (S == 0 && h == 0) w0 = (g.q == 0 && g.hh == 0) ? ((g.j & 1) ? 0x3F800000u : 0x00003F80u) : w0;
    if (h) { f.z = w0; f.w = w1; } else { f.x = w0; f.y = w1; }
    const float cn = __builtin_fmaf(r.c, r.c4, -(r.s * r.s4)), sn = __builtin_fmaf(r.s, r.c4, r.c * r.s4);
    r.c = cn; r.s = sn;
  }
  asm volatile("" : "+v"(f));
}
template <int N0, int N1, int NR> DI void bar_w(int w) { if (w == 0) bar_vm<N0>(); else if (w == 1) bar_vm<N1>(); else bar_vm<NR>(); }
#define NOP do {} while (0)

#define TLBW 0
#define TOUCH() do {                                                                                                      \
    const int gl = G.b * 256 + (w & 3) * 64 + lane;                                                                         \
    const char* base_ = gl < 14336 ? (const char*)p.w2 : gl < 28672 ? (const char*)p.w1 - 14336 * 128 : gl < 36896 ? (const char*)p.wr - 28672 * 128           \
                      : gl < 45120 ? (const char*)p.wi - 36896 * 128 : (const char*)p.x - 45120 * 128;                     \
    const char* ad_ = base_ + (size_t)(gl < 61504 ? gl : 45120) * 128;                                                      \
    if (w >= 4) ad_ = TLBW ? (w == 7 ? (const char*)p.out + lane * 32768 : (const char*)p.ws + (size_t)((w - 4) * 64 + lane + 8) * 65536 + 32768 - 128) : (const char*)p.x; \
    asm volatile("global_load_dword %0, %1, off" : "=v"(touch) : "v"(ad_) : "memory");                                     \
  } while (0)
#define g1_dma(kt, u) do {                                                                                         \
    if ((u) == 0) dma16z<((kt) & 3) * 32768u, (kt) * 128u>(sx, vx0, g1l);                                           \
    if ((u) == 1) dma16z<((kt) & 3) * 32768u + 1024u, (kt) * 128u>(sx, vx1, g1l);                                   \
    if ((u) == 2) dma16z<((kt) & 3) * 32768u + 16384u, (kt) * (64u * 3584u)>(sw1, vw0, g1l);                        \
    if ((u) == 3) dma16z<((kt) & 3) * 32768u + 17408u, (kt) * (64u * 3584u)>(sw1, vw1, g1l); } while (0)
#define g1_issue(kt) do { g1_dma(kt, 0); g1_dma(kt, 1); g1_dma(kt, 2); g1_dma(kt, 3); } while (0)
struct Pro { v4u sw1, swr, swi, gx, gv, wqr, wqi, xqr, xqi; unsigned ep, touch, wtr, wti, xtr, xti, vx0, vx1, vw0, vw1, g1l; };
template <bool ISA>
DI unsigned phase1(const Params& p, LAS char* L, const Geo& G, Pro& R, v4u& m5a, v4u& m5b
) {
  const int tid = G.tid, lane = G.lane, w = G.w, i = G.i, i1 = G.i1, rb = G.rb, fc = G.fc, hh = G.hh, blk = G.blk, q4 = G.q4, p4 = G.p4;
  const v4u sx = G.sx, sws = G.sws, sw1 = R.sw1, swr = R.swr, swi = R.swi;
  unsigned& ep = R.ep; v4u& gx = R.gx; v4u& gv = R.gv; unsigned& touch = R.touch;
  v4u& wqr = R.wqr; v4u& wqi = R.wqi; v4u& xqr = R.xqr; v4u& xqi = R.xqi; unsigned& wtr = R.wtr; unsigned& wti = R.wti; unsigned& xtr = R.xtr; unsigned& xti = R.xti;
  const unsigned vx0 = R.vx0, vx1 = R.vx1, vw0 = R.vw0, vw1 = R.vw1, g1l = R.g1l;
  const int f0 = ISA ? 96 * fc : 768 + 128 * (fc - 8), nf = ISA ? 96 : 128;
  const int mt = G.at >> 2, dt4 = G.at & 3;
  const int m1 = G.m1;
  const int mW = m1 == 0 ? 128 * w : m1 + 256 * w;
  WVal Wc, Wd;
  const unsigned tag = (unsigned)__builtin_amdgcn_readfirstlane((int)ep) + 1u;
  float gate_g, gate_og;
  {
    float z = 0.f;
#pragma unroll
    for (int q = 0; q < 4; ++q) {
      const unsigned pr = pk2(bflo(gx[q]) * bflo(gv[q]), bfhi(gx[q]) * bfhi(gv[q]));
      z += bflo(pr) + bfhi(pr);
    }
    z = dpp_add<0x111, 0xf>(z); z = dpp_add<0x112, 0xf>(z); z = dpp_add<0x114, 0xf>(z); z = dpp_add<0x118, 0xf>(z);
    z = dpp_add<0x142, 0xa>(z); z = dpp_add<0x143, 0xc>(z);
    const float zb = bflo(pk2(z, 0.f));
    const float g = bflo(pk2(__builtin_amdgcn_rcpf(1.f + __builtin_amdgcn_exp2f(zb * -1.44269504088896341f)), 0.f));
    gate_g = asf((unsigned)__builtin_amdgcn_readlane((int)__builtin_bit_cast(unsigned, g), 63));
    gate_og = bflo(pk2(1.f - gate_g, 0.f));
  }
  if (!ISA) {
    LAS char* wscr0 = L + 128 * 1024 + w * 2048;
    const int woff = w_off(mW, lane);
    w_put(wscr0, wqr, wqi, lane);
    asm volatile("s_waitcnt lgkmcnt(0)" ::: "memory");
    WRawL wraw;
    w_get(wraw, wscr0, woff, lane, 0); w_get(wraw, wscr0, woff, lane, 1); w_get(wraw, wscr0, woff, lane, 2); w_get(wraw, wscr0, woff, lane, 3);
    Wc = w_unpack(wraw, w_tail(wtr, wti), woff, mW, lane);
    asm volatile("" : "+v"(Wc.r0), "+v"(Wc.i0), "+v"(Wc.r1), "+v"(Wc.i1));
  }
  if (ISA) {
    const int dq = w & 3, mh = w >> 2;
    LAS char* xa = L + A_XIMG + (32 * hh + q4) * 256 + (((4 * dq + 2 * blk + (p4 >> 1)) ^ (q4 << 2)) << 4) + 8 * (p4 & 1);
    LAS char* fb = L + A_FIMG + mh * 16384 + lane * 16;
    const int srow = 32 * mh + (lane & 31);
    LAS char* stw = L + A_FIMG + srow * 256 + 8 * hh;
    const int rrow = tid >> 3;
    LAS char* str = L + A_FIMG + rrow * 256;
    const unsigned tdst = WS_T + (unsigned)(((64 * mt + rrow) * 8 + i) * 1024 + dt4 * 256 + (tid & 7) * 16);
    if (w < 4) bar_vm<9>(); else BAR_LGKM();
    STAMP(1);
    f32x16 acc = {};
    v4u fa[2][4], fbv[2][4];
#pragma unroll
    for (int s = 0; s < 4; ++s) { fa[0][s] = tr_read2(xa + 8 * s * 256, xa + 8 * s * 256 + 1024); fbv[0][s] = lds_ld16(fb + s * 1024); }
#pragma unroll
    for (int g = 0; g < 4; ++g) {
      acc = mfma32(fa[g & 1][0], fbv[g & 1][0], acc);
      SCHED();
      if (g == 1) { if (w >= 4) bar_vm<9>(); else BAR_LGKM(); }
      if (g < 3) {
#pragma unroll
        for (int s = 0; s < 4; ++s) {
          const int ro = (64 * (g + 1) + 8 * s) * 256;
          fa[(g + 1) & 1][s] = tr_read2(xa + ro, xa + ro + 1024); fbv[(g + 1) & 1][s] = lds_ld16(fb + (4 * (g + 1) + s) * 1024);
        }
      }
      SCHED();
#pragma unroll
      for (int s = 1; s < 4; ++s) { acc = mfma32(fa[g & 1][s], fbv[g & 1][s], acc); if (g == 1 && s == 1) { g1_dma(2, 0); g1_dma(2, 1); } if (g == 1 && s == 2) { g1_dma(2, 2); g1_dma(2, 3); } SCHED(); }
    }
    BAR_LGKM();
    STAMP(2);
    g1_issue(3);
#pragma unroll
    for (int Gq = 0; Gq < 4; ++Gq) {
      v2u o; o.x = pk2(acc[4 * Gq], acc[4 * Gq + 1]); o.y = pk2(acc[4 * Gq + 2], acc[4 * Gq + 3]);
      *(LAS v2u*)(stw + (((4 * dq + Gq) ^ (srow & 15)) << 4)) = o;
    }
    BAR_LGKM();
    {
      const v4u v0 = lds_ld16(str + (((tid & 7) ^ (rrow & 15)) << 4)), v1 = lds_ld16(str + ((((tid & 7) + 8) ^ (rrow & 15)) << 4));
      st16_sc1(sws, tdst, v0);
      st16_sc1(sws, tdst + 128u, v1);
    }
    SCHED();
  }
  const int ft = w & 3, rh1 = w >> 2;
  const bool active = ft * 32 < nf;
  f32x16 acc1[2] = {};
  v4u tq0, tq1;
  v4u tb[16];
  unsigned fla;
  int t_ok = 0;
  unsigned tsrc0 = 0, tsrc1 = 0;
  const unsigned flaoff = WS_FLA + (unsigned)(((m1 >> 5) & 3) * 32 + (lane & 31)) * FS;
  LAS char* CTL = L + 128 * 1024;
  {
    LAS char* wa = L + 16384 + (8 * hh + q4) * 256 + (((4 * ft + 2 * blk + (p4 >> 1)) ^ (q4 << 2)) << 4) + 8 * (p4 & 1);
    const int r = 64 * rh1 + (lane & 31), fz = (r >> 1) & 7;
    LAS char* xb0 = L + r * 128 + (((0 + hh) ^ fz) << 4);
    LAS char* xb1 = L + r * 128 + (((2 + hh) ^ fz) << 4);
    LAS char* xb2 = L + r * 128 + (((4 + hh) ^ fz) << 4);
    LAS char* xb3 = L + r * 128 + (((6 + hh) ^ fz) << 4);
    Frag1 fr[2];
    bar_vm<ISA ? 15 : 13>();
    STAMP(3);
    if (lane == 0) { v2u gv2; gv2.x = __builtin_bit_cast(unsigned, gate_g); gv2.y = __builtin_bit_cast(unsigned, gate_og); *(LAS v2u*)(CTL + 64 + w * 8) = gv2; }
    g1_read(fr[0], wa, xb0, xb1, xb2, xb3, 0);
#define PIN1(c) asm volatile("" : "+v"(c.a[0]), "+v"(c.a[1]), "+v"(c.a[2]), "+v"(c.a[3]), "+v"(c.b0[0]), "+v"(c.b0[1]), "+v"(c.b0[2]), "+v"(c.b0[3]), \
                             "+v"(c.b1[0]), "+v"(c.b1[1]), "+v"(c.b1[2]), "+v"(c.b1[3]))
#define G1_STEP(kt, X0, X1, X2, X3, X4, X5, X6, X7)                                                                   \
    {                                                                                                                   \
      FT(10 + (kt));                                                                                                    \
      Frag1& c = fr[(kt) & 1];                                                                                          \
      Frag1& n = fr[((kt) + 1) & 1];                                                                                    \
      const int so = (((kt) + 1) & 3) * 32768;                                                                          \
      PIN1(c);                                                                                                          \
      asm volatile("" :: "v"(acc1[0]), "v"(acc1[1]));                                                                   \
      SCHED();                                                                                                          \
      acc1[0] = mfma32(c.a[0], c.b0[0], acc1[0]); if ((kt) < 7) n.a[0] = tr_read2(wa + so, wa + so + 1024); X0; SCHED(); \
      acc1[1] = mfma32(c.a[0], c.b1[0], acc1[1]); if ((kt) < 7) { n.b0[0] = lds_ld16(xb0 + so); n.b1[0] = lds_ld16(xb0 + so + 4096); } X1; SCHED(); \
      acc1[0] = mfma32(c.a[1], c.b0[1], acc1[0]); if ((kt) < 7) n.a[1] = tr_read2(wa + so + 4096, wa + so + 5120); X2; SCHED(); \
      acc1[1] = mfma32(c.a[1], c.b1[1], acc1[1]); if ((kt) < 7) { n.b0[1] = lds_ld16(xb1 + so); n.b1[1] = lds_ld16(xb1 + so + 4096); } X3; SCHED(); \
      acc1[0] = mfma32(c.a[2], c.b0[2], acc1[0]); if ((kt) < 7) n.a[2] = tr_read2(wa + so + 8192, wa + so + 9216); X4; SCHED(); \
      acc1[1] = mfma32(c.a[2], c.b1[2], acc1[1]); if ((kt) < 7) { n.b0[2] = lds_ld16(xb2 + so); n.b1[2] = lds_ld16(xb2 + so + 4096); } X5; SCHED(); \
      acc1[0] = mfma32(c.a[3], c.b0[3], acc1[0]); if ((kt) < 7) n.a[3] = tr_read2(wa + so + 12288, wa + so + 13312); X6; SCHED(); \
      acc1[1] = mfma32(c.a[3], c.b1[3], acc1[1]); if ((kt) < 7) { n.b0[3] = lds_ld16(xb3 + so); n.b1[3] = lds_ld16(xb3 + so + 4096); } X7; SCHED(); \
    }
#define GATE_STORE() do { if (w == 2 && lane < 8) { const v2u gv2 = *(LAS v2u*)(CTL + 64 + lane * 8);                       \
      __builtin_amdgcn_raw_buffer_store_b64(gv2, as_rsrc(sws), WS_GATE + (unsigned)(i1 * 256 + 128 * rb + 8 * fc + lane) * 8u, 0, 16); } } while (0)
#define STEP8 G1_STEP
    if constexpr (ISA) {
      bar_vm<11>();
      STAMP(4);
      STEP8(0, NOP, NOP, NOP, NOP, NOP, NOP, NOP, NOP)
      bar_vm<0>();
      if (tid == 0) flag_store(p.ws, WS_FLA + (unsigned)(mt * 32 + i * 4 + dt4) * FS, tag);
      STAMP(13);
      STEP8(1, g1_dma(4, 0), g1_dma(4, 1), g1_dma(4, 2), g1_dma(4, 3), g1_dma(5, 0), g1_dma(5, 1), g1_dma(5, 2), g1_dma(5, 3))
      BAR_LGKM();
      GATE_STORE();
      STEP8(2, NOP, NOP, NOP, NOP, g1_dma(6, 0), g1_dma(6, 1), g1_dma(6, 2), g1_dma(6, 3))
      bar_vm<8>();
      STEP8(3, NOP, NOP, NOP, NOP, g1_dma(7, 0), g1_dma(7, 1), g1_dma(7, 2), g1_dma(7, 3))
      bar_vm<8>();
      STAMP(12);
      STEP8(4, NOP, NOP, NOP, NOP, w2_issue1(G, 0, 0), w2_issue1(G, 0, 1), w2_issue1(G, 1, 0), w2_issue1(G, 1, 1))
      bar_vm<8>();
      STEP8(5, NOP, NOP, NOP, NOP, w2_issue1(G, 2, 0), w2_issue1(G, 2, 1), w2_issue1(G, 3, 0), w2_issue1(G, 3, 1))
      bar_vm<8>();
      STEP8(6, NOP, NOP, NOP, NOP, w2_issue1(G, 4, 0), w2_issue1(G, 4, 1), w2_issue1(G, 5, 0), w2_issue1(G, 5, 1))
      BAR_LGKM();
    } else {
      const v4u stab = make_srd(&kTab, sizeof(Tab));
      {
        const int kap = tid >> 5, ii = (kap >> 1) & 7, d0 = kap & 1;
        const int g0 = m1 == 0 ? 2 * ii : ii, g1 = m1 == 0 ? 2 * ii + 1 : 8 + ii;
        const unsigned tb = WS_T + (unsigned)m1 * 16384u + (unsigned)(d0 * 512 + (tid & 31) * 16);
        tsrc0 = tb + (unsigned)g0 * 1024u; tsrc1 = tb + (unsigned)g1 * 1024u;
      }
      bar_vm<9>();
      STAMP(4);
      STEP8(0, NOP, NOP, NOP, NOP, g1_dma(4, 0), g1_dma(4, 1), g1_dma(4, 2), g1_dma(4, 3))
      bar_vm<9>();
      STEP8(1, NOP, NOP, NOP, NOP, g1_dma(5, 0), g1_dma(5, 1), g1_dma(5, 2), g1_dma(5, 3))
      bar_vm<9>();
      GATE_STORE();
      STEP8(2, NOP, NOP, NOP, NOP, g1_dma(6, 0), g1_dma(6, 1), g1_dma(6, 2), g1_dma(6, 3))
      bar_vm<8>();
      LAS char* wscr0 = L + 128 * 1024 + w * 2048;
      const bool special = m1 == 0;
      const int m2nd = 128 * w + 1024, off2 = w_off(m2nd, lane);
      if (special) { asm volatile("" : "+v"(xqr), "+v"(xqi), "+v"(xtr), "+v"(xti)); w_put(wscr0, xqr, xqi, lane); }
      STEP8(3, NOP, NOP, (dma16<128u * 1024u + 16384u, 0>(stab, (unsigned)lane * 16u, g1l, g1l)), (dma16<128u * 1024u + 17408u, 1024>(stab, (unsigned)lane * 16u, g1l, g1l)),
            g1_dma(7, 0), g1_dma(7, 1), g1_dma(7, 2), g1_dma(7, 3))
      bar_vm<10>();
      STAMP(12);
      WRawL raw2;
      if (special) { w_get(raw2, wscr0, off2, lane, 0); w_get(raw2, wscr0, off2, lane, 1); w_get(raw2, wscr0, off2, lane, 2); w_get(raw2, wscr0, off2, lane, 3); }
      STEP8(4, NOP, NOP, NOP, NOP, w2_issue1(G, 0, 0), w2_issue1(G, 0, 1), w2_issue1(G, 1, 0), w2_issue1(G, 1, 1))
      bar_vm<10>();
      if (special) { Wd = w_unpack(raw2, w_tail(xtr, xti), off2, m2nd, lane); packed_coef(Wc, Wd, w); }
      STEP8(5, NOP, NOP, NOP, NOP, w2_issue1(G, 2, 0), w2_issue1(G, 2, 1), w2_issue1(G, 3, 0), w2_issue1(G, 3, 1))
      bar_vm<8>();
      STEP8(6, if (w == 0) LD4Z(fla, flaoff, sws, "sc1"), NOP, NOP, NOP, w2_issue1(G, 4, 0), w2_issue1(G, 4, 1), w2_issue1(G, 5, 0), w2_issue1(G, 5, 1))
      BAR_LGKM();
    }
    if constexpr (ISA) {
      STEP8(7, NOP, NOP, NOP, NOP, w2_issue1(G, 6, 0), w2_issue1(G, 6, 1), w2_issue1(G, 7, 0), w2_issue1(G, 7, 1))
    } else {
      LAS char* TB = L + 128 * 1024 + 16384 + lane * 16;
#define TBR(f) (tb[f] = lds_ld16(TB + (f) * 1024), tb[(f) + 1] = lds_ld16(TB + ((f) + 1) * 1024))
      STEP8(7, TBR(6), TBR(0), TBR(10), TBR(2), { TBR(12); w2_issue1(G, 6, 0); }, { TBR(14); w2_issue1(G, 6, 1); }, { TBR(4); w2_issue1(G, 7, 0); }, { TBR(8); w2_issue1(G, 7, 1); })
      if (w == 0) {
        asm volatile("s_waitcnt vmcnt(8)" : "+v"(fla) :: "memory");
        const unsigned ok = __all(fla == tag) ? 1u : 0u;
        if (lane == 0) *(LAS unsigned*)CTL = ok;
      }
      BAR_LGKM();
      t_ok = __builtin_amdgcn_readfirstlane(*(volatile LAS unsigned*)CTL);
      if (t_ok) { LD16Z(tq0, tsrc0, sws, "sc1"); LD16Z(tq1, tsrc1, sws, "sc1"); }
    }
  }
  asm volatile("" :: "v"(touch));
  STAMP(5);
  FT(18);
  if (active) {
#pragma unroll
    for (int rt = 0; rt < 2; ++rt)
#pragma unroll
      for (int u = 0; u < 2; ++u) {
        v4u o;
        o.x = silu2(acc1[rt][8 * u + 0], acc1[rt][8 * u + 1]);
        o.y = silu2(acc1[rt][8 * u + 2], acc1[rt][8 * u + 3]);
        o.z = silu2(acc1[rt][8 * u + 4], acc1[rt][8 * u + 5]);
        o.w = silu2(acc1[rt][8 * u + 6], acc1[rt][8 * u + 7]);
        const int jb32 = 4 * rb + 2 * rh1 + rt, fs = (f0 >> 4) + 2 * ft + u;
        st16_sc1(sws, WS_H + (unsigned)(((i1 * 8 + jb32) * 112 + fs) * 1024 + lane * 16), o);
      }
  }
  SCHED();
  STAMP(6);
  FT(20);
  const unsigned flh_own = WS_FLH + (unsigned)((i1 * 2 + rb) * 16 + fc) * FS;
  if constexpr (ISA) {
    bar_vm<0>();
    if (tid == 0) flag_store(p.ws, flh_own, tag);
  } else {
    LAS char* I0 = L + 128 * 1024;
    LAS char* I1 = L + 128 * 1024 + 16384;
    const int g = lane >> 4, l15 = lane & 15;
    const bool special = (m1 == 0);
    bool published = false;
    if (t_ok) {
      wait_vm<4>();
      asm volatile("s_barrier" ::: "memory");
    } else {
      bar_vm<0>();
      if (tid == 0) flag_store(p.ws, flh_own, tag);
      published = true;
      if (w == 0) poll_words(sws, WS_FLA + (unsigned)(m1 >> 5) * 32u * FS, 32, tag, lane);
      asm volatile("s_barrier" ::: "memory");
      LD16Z(tq0, tsrc0, sws, "sc1"); LD16Z(tq1, tsrc1, sws, "sc1");
      wait_vm<0>();
    }
    asm volatile("" : "+v"(tq0), "+v"(tq1) :: "memory");
    {
      const int kap = tid >> 5, ch = tid & 31;
      *(LAS v4u*)(I0 + kap * 512 + ((ch ^ ((kap & 3) << 2)) << 4)) = tq0;
      *(LAS v4u*)(I0 + (kap + 16) * 512 + ((ch ^ ((kap & 3) << 2)) << 4)) = tq1;
    }
    BAR_LGKM();
    FT(21);
    LAS char* ta = I0 + (8 * hh + q4) * 512 + (((4 * w + 2 * blk + (p4 >> 1)) ^ (q4 << 2)) << 4) + 8 * (p4 & 1);
    const int n = lane & 31;
    LAS char* so = I1 + n * 512 + 8 * hh;
    {
      f32x16 acc = {};
      acc = mfma32(tr_read2(ta, ta + 2048), tb[6], acc);
      acc = mfma32(tr_read2(ta + 8192, ta + 10240), tb[7], acc);
#pragma unroll
      for (int Gq = 0; Gq < 4; ++Gq) {
        v2u o; o.x = pk2(acc[4 * Gq], acc[4 * Gq + 1]); o.y = pk2(acc[4 * Gq + 2], acc[4 * Gq + 3]);
        *(LAS v2u*)(so + (((4 * w + Gq) ^ (n & 15)) << 4)) = o;
      }
    }
    BAR_LGKM();
    FT(22);
    const f32x4 z4 = {0.f, 0.f, 0.f, 0.f};
    const v4u F16fr = tb[0], F16fi = tb[1];
    v4u F16ir, F16ii;
    F16ir.x = F16fr.x; F16ir.y = F16fr.y; F16ir.z = F16fr.z ^ 0x80008000u; F16ir.w = F16fr.w ^ 0x80008000u;
    F16ii.x = F16fi.x ^ 0x80008000u; F16ii.y = F16fi.y ^ 0x80008000u; F16ii.z = F16fi.z; F16ii.w = F16fi.w;
    f32x4 zr[2], zi[2];
#pragma unroll
    for (int pp = 0; pp < 2; ++pp) {
      const int n0 = 4 * w + 2 * pp, n1 = n0 + 1;
      const int ch = 2 * (4 * g + q4) + (p4 >> 1);
      const v4u a1 = tr_read2(I1 + n0 * 512 + ((ch ^ (n0 & 15)) << 4) + 8 * (p4 & 1), I1 + n1 * 512 + ((ch ^ (n1 & 15)) << 4) + 8 * (p4 & 1));
      const f32x4 dr = mfma16(a1, pp ? tb[2] : F16fr, z4), di = mfma16(a1, pp ? tb[3] : F16fi, z4);
      const v4u b3 = cmul_pack<false>(dr, di, asf4(tb[10 + 2 * pp]), asf4(tb[11 + 2 * pp]));
      zr[pp] = mfma16(F16fr, b3, z4); zi[pp] = mfma16(F16fi, b3, z4);
    }
    v4u a4[2];
    if (!special) {
      a4[0] = cmul_pack<false>(zr[0], zi[0], Wc.r0, Wc.i0);
      a4[1] = cmul_pack<false>(zr[1], zi[1], Wc.r1, Wc.i1);
    } else {
      BAR_LGKM();
      LAS char* Z = I0;
#pragma unroll
      for (int pp = 0; pp < 2; ++pp)
#pragma unroll
        for (int r = 0; r < 4; ++r) { f32x2 v = {zr[pp][r], zi[pp][r]}; *(LAS f32x2*)(Z + ((w * 2 + pp) * 256 + l15 + 16 * (4 * g + r)) * 8) = v; }
      BAR_LGKM();
#pragma unroll
      for (int pp = 0; pp < 2; ++pp) {
        f32x4 cr, ci;
#pragma unroll
        for (int r = 0; r < 4; ++r) {
          const int K = l15 + 16 * (4 * g + r), Kn = pp ? 255 - K : (256 - K) & 255;
          const f32x2 y = *(LAS f32x2*)(Z + ((((8 - w) & 7) * 2 + pp) * 256 + Kn) * 8);
          cr[r] = y[0]; ci[r] = -y[1];
        }
        const f32x4 ar = pp ? Wc.r1 : Wc.r0, ai = pp ? Wc.i1 : Wc.i0, br = pp ? Wd.r1 : Wd.r0, bi = pp ? Wd.i1 : Wd.i0;
        const f32x4 yr = zr[pp] * ar - zi[pp] * ai + cr * br - ci * bi, yi = zr[pp] * ai + zi[pp] * ar + cr * bi + ci * br;
        a4[pp].x = pk2(yr[0], yr[1]); a4[pp].y = pk2(yr[2], yr[3]); a4[pp].z = pk2(yi[0], yi[1]); a4[pp].w = pk2(yi[2], yi[3]);
      }
      BAR_LGKM();
    }
#pragma unroll
    for (int pp = 0; pp < 2; ++pp) {
      const f32x4 dr = mfma16(a4[pp], F16ir, z4), di = mfma16(a4[pp], F16ii, z4);
      const v4u a5 = cmul_pack<true>(dr, di, asf4(tb[pp ? 14 : 10]), asf4(tb[pp ? 15 : 11]));
      const f32x4 er = mfma16(a5, pp ? tb[4] : F16ir, z4), ei = mfma16(a5, pp ? tb[5] : F16ii, z4);
      const int n0 = 4 * w + 2 * pp, n1 = n0 + 1;
      const int ch = 2 * l15 + (g >> 1);
      v2u o; o.x = pk2(er[0], er[1]); o.y = pk2(er[2], er[3]);
      *(LAS v2u*)(I0 + n0 * 512 + ((ch ^ ((n0 & 3) << 2)) << 4) + 8 * (g & 1)) = o;
      o.x = pk2(ei[0], ei[1]); o.y = pk2(ei[2], ei[3]);
      *(LAS v2u*)(I0 + n1 * 512 + ((ch ^ ((n1 & 3) << 2)) << 4) + 8 * (g & 1)) = o;
    }
    bar_vm<0>();
    if (!published && tid == 0) flag_store(p.ws, flh_own, tag);
    FT(23);
    m5a = tb[8]; m5b = tb[9];
  }
  return tag;
}

DI void make_geo(Geo& G, const Params& p, int tz) {
  G.tid = tz; G.lane = G.tid & 63; G.w = __builtin_amdgcn_readfirstlane(G.tid >> 6);
  G.b = blockIdx.x;
  const int Lx = G.b >> 3, x8 = G.b & 7;
  {
    const int a = x8 & 3, h = x8 >> 2, rbi = Lx >> 3, c = Lx & 7;
    G.i = 2 * a + h; G.i1 = 2 * a + (rbi >> 1); G.rb = rbi & 1;
    G.fc = c < 4 ? 4 * h + c : 4 + 4 * h + c;
    G.at = rbi * 4 + c; G.m1 = ((rbi * 4 + c - 4) << 3) | x8;
  }
  G.hh = G.lane >> 5; G.blk = (G.lane >> 4) & 1; G.q4 = (G.lane & 15) >> 2; G.p4 = G.lane & 3;
  G.sx = make_srd(p.x, NS * ND * 2); G.sws = make_srd(p.ws, 16u << 20); G.sw2 = make_srd(p.w2, NF * ND * 2);
  G.jb = Lx >> 3; G.dt = Lx & 7; G.rh = G.w & 1; G.q = G.w >> 1;
  G.j = 64 * G.jb + 32 * G.rh + (G.lane & 31);
  const int rho = 16 * G.rh + (G.lane >> 3), chs = (G.lane & 7) ^ (4 * ((G.lane >> 4) & 1));
  G.vw20 = (unsigned)(rho * 1024 + G.dt * 128 + chs * 16); G.vw21 = G.vw20 + 8 * 1024;
  G.ldsw = uni((unsigned)(G.q * 4096 + G.rh * 2048));
  G.fq0b = uni((unsigned)(768 + 256 * G.q) * 1024u); G.fq1b = uni((unsigned)(192 * G.q) * 1024u);
}

__global__ __launch_bounds__(512, 2) void gm_kernel(Params p) {
  extern __shared__ __attribute__((aligned(16))) char smem[];
  LAS char* L = (LAS char*)smem;
  unsigned touch0;
  {
    unsigned long long pc; unsigned len;
    asm volatile("s_getpc_b64 %0\n.Lgm_code_begin:\n\ts_mov_b32 %1, .Lgm_code_end-.Lgm_code_begin" : "=s"(pc), "=s"(len));
    const unsigned line = (unsigned)((blockIdx.x >> 3) * 8 + (threadIdx.x >> 6)) * 128u;
    const char* a0 = (const char*)(pc & ~127ull) + (line < len ? line : 0u);
    asm volatile("global_load_dword %0, %1, off" : "=v"(touch0) : "v"(a0) : "memory");
  }
  STAMP(0);
  FT(0);
  Geo G;
  make_geo(G, p, threadIdx.x);
  const bool isA = G.fc < 8;
  Pro R;
  {
    const int lane = G.lane, w = G.w, i = G.i, i1 = G.i1, rb = G.rb, fc = G.fc, m1 = G.m1;
    const v4u sx = G.sx, sws = G.sws;
    R.sw1 = make_srd(p.w1, ND * NF * 2); R.swr = make_srd(p.wr, NS * 257 * 2); R.swi = make_srd(p.wi, NS * 257 * 2);
    const v4u sw1 = R.sw1, swr = R.swr, swi = R.swi;
    const int jg = 128 * rb + 8 * fc + w;
    LD16Z(R.gx, (unsigned)(lane * 16 + (i1 + 8 * jg) * 1024), sx, "");
    LD16Z(R.gv, (unsigned)lane * 16u, make_srd(p.gw, ND * 2), "");
    LD4Z(R.ep, WS_EPOCH, sws, "sc1");
    const int mW = m1 == 0 ? 128 * w : m1 + 256 * w;
    if (isA) {
      const int dt4 = G.at & 3;
      const unsigned voff = (unsigned)(lane >> 4) * 8192u + (unsigned)(((lane & 15) ^ ((lane >> 4) << 2)) << 4);
      const unsigned xl = uni(A_XIMG + (unsigned)w * 8192u), xs = uni((unsigned)i * 1024u + (unsigned)dt4 * 256u + (unsigned)w * 262144u);
      dma16<0, 0>(sx, voff, xl, xs); dma16<1024, 32768>(sx, voff, xl, xs); dma16<2048, 65536>(sx, voff, xl, xs); dma16<3072, 98304>(sx, voff, xl, xs);
      dma16<4096, 131072>(sx, voff, xl, xs); dma16<5120, 163840>(sx, voff, xl, xs); dma16<6144, 196608>(sx, voff, xl, xs); dma16<7168, 229376>(sx, voff, xl, xs);
    } else {
      const unsigned wsrc = w_src(mW, lane), wts = w_tail_src(mW, lane);
      LD16Z(R.wqr, wsrc, swr, ""); LD16Z(R.wqi, wsrc, swi, ""); LD2Z(R.wtr, wts, swr, ""); LD2Z(R.wti, wts, swi, "");
    }
    const int f0 = isA ? 96 * fc : 768 + 128 * (fc - 8), nf = isA ? 96 : 128;
    {
      const int r0 = 16 * w + (lane >> 3), pos = lane & 7, r1 = r0 + 8;
      R.vx0 = (unsigned)(i1 * 1024 + (128 * rb + r0) * 8192 + ((pos ^ ((r0 >> 1) & 7)) << 4));
      R.vx1 = (unsigned)(i1 * 1024 + (128 * rb + r1) * 8192 + ((pos ^ ((r1 >> 1) & 7)) << 4));
      const int k0 = 8 * w + (lane >> 4), chunk = (lane & 15) ^ ((lane >> 4) << 2);
      const bool ok = chunk * 8 < nf;
      R.vw0 = ok ? (unsigned)(k0 * 3584 + f0 * 2 + chunk * 16) : 0x80000000u;
      R.vw1 = ok ? R.vw0 + 4u * 3584u : 0x80000000u;
    }
    R.g1l = uni((unsigned)w * 2048u);
    const unsigned vx0 = R.vx0, vx1 = R.vx1, vw0 = R.vw0, vw1 = R.vw1, g1l = R.g1l;
    asm volatile("s_barrier" ::: "memory");
    g1_issue(0);
    if (!isA && m1 == 0) {
      const unsigned ws2 = w_src(128 * w + 1024, lane), wt2 = w_tail_src(128 * w + 1024, lane);
      LD16Z(R.xqr, ws2, swr, ""); LD16Z(R.xqi, ws2, swi, ""); LD2Z(R.xtr, wt2, swr, ""); LD2Z(R.xti, wt2, swi, "");
    }
    asm volatile("s_barrier" ::: "memory");
    g1_issue(1);
    if (!isA) {
      asm volatile("s_barrier" ::: "memory");
      g1_issue(2); asm volatile("s_barrier" ::: "memory");
      g1_issue(3);
    }
    unsigned touch;
    TOUCH();
    R.touch = touch;
    if (isA) phaseA_genF(L, i, G.at >> 2, w, lane);
    if (isA) wait_vm<17>(); else wait_vm<21>();
    asm volatile("" : "+v"(R.ep), "+v"(R.gx), "+v"(R.gv) :: "memory");
    if (!isA) {
      wait_vm<17>();
      asm volatile("" : "+v"(R.wqr), "+v"(R.wqi), "+v"(R.wtr), "+v"(R.wti) :: "memory");
      if (m1 == 0) {
        wait_vm<13>();
        asm volatile("" : "+v"(R.xqr), "+v"(R.xqi), "+v"(R.xtr), "+v"(R.xti) :: "memory");
      }
    }
  }
  v4u gf[4], m5a, m5b;
  unsigned tag;
  if (isA) tag = phase1<true>(p, L, G, R, m5a, m5b); else tag = phase1<false>(p, L, G, R, m5a, m5b);
  asm volatile("" :: "v"(touch0));
  { int tz = threadIdx.x; asm volatile("" : "+v"(tz)); make_geo(G, p, tz); }
  const int tid = G.tid, lane = G.lane, w = G.w, b = G.b, i = G.i, jb = G.jb, dt = G.dt, rh = G.rh, q = G.q, j = G.j;
  const int hh = G.hh, blk = G.blk, q4 = G.q4, p4 = G.p4;
  const v4u sws = G.sws;
  const unsigned ldsw = G.ldsw;
  const int m1 = G.m1;
  STAMP(7);
  FT(25);
  if (!isA) {
    LAS char* I0 = L + 128 * 1024;
    LAS char* I1 = L + 128 * 1024 + 16384;
    LAS char* ta = I0 + (8 * hh + q4) * 512 + (((4 * w + 2 * blk + (p4 >> 1)) ^ (q4 << 2)) << 4) + 8 * (p4 & 1);
    const int n = lane & 31;
    LAS char* so = I1 + n * 512 + 8 * hh;
    f32x16 acc = {};
    acc = mfma32(tr_read2(ta, ta + 2048), m5a, acc);
    acc = mfma32(tr_read2(ta + 8192, ta + 10240), m5b, acc);
#pragma unroll
    for (int Gq = 0; Gq < 4; ++Gq) {
      v2u o; o.x = pk2(acc[4 * Gq], acc[4 * Gq + 1]); o.y = pk2(acc[4 * Gq + 2], acc[4 * Gq + 3]);
      *(LAS v2u*)(so + (((4 * w + Gq) ^ (n & 15)) << 4)) = o;
    }
    BAR_LGKM();
    const bool special = (m1 == 0);
    auto u_store = [&](int t2) {
      const int nn = t2 >> 5, ch = t2 & 31, ii = (nn >> 1) & 7, d0 = nn & 1;
      const int g0 = special ? 2 * ii : ii, g1 = special ? 2 * ii + 1 : 8 + ii;
      const v4u v0 = lds_ld16(I1 + nn * 512 + ((ch ^ (nn & 15)) << 4));
      const v4u v1 = lds_ld16(I1 + (nn + 16) * 512 + ((ch ^ (nn & 15)) << 4));
      const unsigned ub = WS_U + (unsigned)m1 * 16384u + (unsigned)(d0 * 512 + ch * 16);
      st16_sc1(sws, ub + (unsigned)g0 * 1024u, v0);
      st16_sc1(sws, ub + (unsigned)g1 * 1024u, v1);
    };
    if (w != 0) u_store(tid);
    if (w == 1) u_store(tid - 64);
    SCHED();
  }
  const int sz = (q4 >> 1) & 1;
  LAS char* ra0 = L + q * 4096 + (4 * hh + q4) * 128 + (((0 ^ (4 * sz)) + 2 * blk + (p4 >> 1)) << 4) + 8 * (p4 & 1);
  LAS char* ra1 = L + q * 4096 + (4 * hh + q4) * 128 + (((4 ^ (4 * sz)) + 2 * blk + (p4 >> 1)) << 4) + 8 * (p4 & 1);
  v4u wp[4][4];
  BAR_LGKM();
#pragma unroll
  for (int t = 0; t < 4; ++t) {
    LAS char* s0 = ra0 + t * 16384; LAS char* s1 = ra1 + t * 16384;
    wp[t][0] = tr_read2(s0, s0 + 1024); wp[t][1] = tr_read2(s1, s1 + 1024);
    wp[t][2] = tr_read2(s0 + 2048, s0 + 3072); wp[t][3] = tr_read2(s1 + 2048, s1 + 3072);
  }
#define W2_8_11() do { w2_issue1(G, 8, 0); w2_issue1(G, 8, 1); w2_issue1(G, 9, 0); w2_issue1(G, 9, 1); w2_issue1(G, 10, 0); w2_issue1(G, 10, 1); w2_issue1(G, 11, 0); w2_issue1(G, 11, 1); } while (0)
  if (isA) { BAR_LGKM(); W2_8_11(); }
  const unsigned flh = WS_FLH + (unsigned)((i * 2 + (jb >> 1)) * 16) * FS;
  if (w == 0) poll_words(sws, flh, 16, tag, lane);
  BAR_LGKM();
  STAMP(8);
  FT(26);
  const unsigned vh = (unsigned)lane * 16u;
  const unsigned hbase = WS_H + (unsigned)((i * 8 + 2 * jb + rh) * 112) * 1024u;
  const unsigned hb0 = uni(hbase + (G.fq0b >> 4)), hb1 = uni(hbase + (G.fq1b >> 4));
  v2u gg;
  v4u hf[14][2];
#define H_ISSUE(t) do { LD16I(hf[t][0], vh, sws, ((t) < 8 ? hb0 : hb1), (2 * ((t) & 7)) * 1024, "sc1"); LD16I(hf[t][1], vh, sws, ((t) < 8 ? hb0 : hb1), (2 * ((t) & 7) + 1) * 1024, "sc1"); } while (0)
  H_ISSUE(0); H_ISSUE(1);
  asm volatile("s_barrier" ::: "memory");
  H_ISSUE(2); H_ISSUE(3); H_ISSUE(4); H_ISSUE(5); H_ISSUE(6);
  LD8Z(gg, WS_GATE + (unsigned)(i * 256 + j) * 8u, sws, "sc1");
  SCHED();
  f32x16 accb[2] = {}, acca[2] = {};
  v4u wf[2][4];
  unsigned fl0, fl1;
  v4u ub0, ub1, ub2, ub3;
  const unsigned vu = WS_U + (unsigned)(((64 * q + 16 * rh + (lane >> 3)) * 8 + i) * 1024 + dt * 128 + ((lane & 7) ^ (4 * ((lane >> 4) & 1))) * 16);
#define G2_BAR(n, t) asm volatile("s_waitcnt vmcnt(" STR(n) ") lgkmcnt(0)\n\ts_barrier" : "+v"(hf[t][0]), "+v"(hf[t][1]) :: "memory")
#define G2_CORE(t, c, n, RD, acc, b0, b1, X0, X1, X2)                                             \
  {                                                                                                 \
    FT(30 + (t));                                                                                   \
    LAS char* s0 = ra0 + (((t) + 1) & 7) * 16384; LAS char* s1 = ra1 + (((t) + 1) & 7) * 16384;    \
    asm volatile("" : "+v"(c[0]), "+v"(c[1]), "+v"(c[2]), "+v"(c[3]));                             \
    asm volatile("" :: "v"(acc[0]), "v"(acc[1]));                                                   \
    SCHED();                                                                                        \
    acc[0] = mfma32(c[0], b0, acc[0]); if (RD) n[0] = tr_read2(s0, s0 + 1024); SCHED();            \
    acc[1] = mfma32(c[1], b0, acc[1]); if (RD) n[1] = tr_read2(s1, s1 + 1024); X0; SCHED();        \
    acc[0] = mfma32(c[2], b1, acc[0]); if (RD) n[2] = tr_read2(s0 + 2048, s0 + 3072); X1; SCHED(); \
    acc[1] = mfma32(c[3], b1, acc[1]); if (RD) n[3] = tr_read2(s1 + 2048, s1 + 3072); X2; SCHED(); \
  }
#define G2_PRE(t, X2) G2_CORE(t, wp[t], wf[0], (t) == 3, accb, hf[t][0], hf[t][1], if (!isA) w2_issue1(G, (t) + 8, 0), if (!isA) w2_issue1(G, (t) + 8, 1), X2)
#define G2_STEP(t, X0, X1, X2) G2_CORE(t, wf[(t) & 1], wf[((t) + 1) & 1], true, accb, hf[t][0], hf[t][1], X0, X1, X2)
  G2_BAR(13, 0);
  if (!isA && tid == 0) flag_store(p.ws, WS_FLB + (unsigned)m1 * FS, tag);
  STAMP(14);
  G2_PRE(0, H_ISSUE(7))
  G2_BAR(13, 1); G2_PRE(1, H_ISSUE(8))
  G2_BAR(13, 2); G2_PRE(2, H_ISSUE(9))
  G2_BAR(13, 3); G2_PRE(3, H_ISSUE(10))
  G2_BAR(13, 4); G2_STEP(4, w2_issue1(G, 12, 0), w2_issue1(G, 12, 1), H_ISSUE(11))
  G2_BAR(15, 5); G2_STEP(5, w2_issue1(G, 13, 0), w2_issue1(G, 13, 1), H_ISSUE(12))
  if (w == 0) { LD4Z(fl0, WS_FLB + (unsigned)lane * FS, sws, "sc1"); LD4Z(fl1, WS_FLB + (64u + (unsigned)lane) * FS, sws, "sc1"); }
  G2_BAR(17, 6); G2_STEP(6, NOP, NOP, H_ISSUE(13))
  STAMP(9);
  G2_BAR(16, 7); G2_STEP(7, NOP, NOP, NOP)
  LAS unsigned* verdict = (LAS unsigned*)(L + 128 * 1024);
  if (w == 0) {
    asm volatile("s_waitcnt vmcnt(2)" : "+v"(fl0), "+v"(fl1) :: "memory");
    const unsigned ok = __all(fl0 == tag && fl1 == tag) ? 1u : 0u;
    if (lane == 0) *verdict = ok;
  }
  G2_BAR(14, 8);
  const int u_early = __builtin_amdgcn_readfirstlane(*(volatile LAS unsigned*)verdict);
  STAMP(15);
#define U_ISSUE() do { LD16Z(ub0, vu, sws, "sc1"); LD16Z(ub1, vu + 8u * 8192u, sws, "sc1"); LD16Z(ub2, vu + 32u * 8192u, sws, "sc1"); LD16Z(ub3, vu + 40u * 8192u, sws, "sc1"); } while (0)
  if (u_early) U_ISSUE();
  GRot grot;
  G2_STEP(8, gen_g_init(G, grot), NOP, gen_g_frag<0>(G, grot, gf[0]))
  unsigned e0, e1, e2, e3;
  if (b == 0 && w == 1) {
    LD4Z(e0, WS_FLH + (unsigned)lane * FS, sws, "sc1"); LD4Z(e1, WS_FLH + (64u + (unsigned)lane) * FS, sws, "sc1");
    LD4Z(e2, WS_FLH + (128u + (unsigned)lane) * FS, sws, "sc1"); LD4Z(e3, WS_FLH + (192u + (unsigned)lane) * FS, sws, "sc1");
  }
  G2_BAR(12, 9); G2_STEP(9, NOP, NOP, gen_g_frag<1>(G, grot, gf[1]))
  G2_BAR(10, 10); G2_STEP(10, NOP, NOP, gen_g_frag<2>(G, grot, gf[2]))
  G2_BAR(6, 11); G2_STEP(11, NOP, NOP, gen_g_frag<3>(G, grot, gf[3]))
  G2_BAR(2, 12);
  G2_STEP(12, NOP, NOP, NOP)
#define U_PUT() do {                                                                                                        \
    asm volatile("s_waitcnt vmcnt(0)" : "+v"(ub0), "+v"(ub1), "+v"(ub2), "+v"(ub3) :: "memory");                             \
    *(LAS v4u*)(L + 6 * 16384 + ldsw + lane * 16) = ub0; *(LAS v4u*)(L + 6 * 16384 + ldsw + 1024 + lane * 16) = ub1;         \
    *(LAS v4u*)(L + 7 * 16384 + ldsw + lane * 16) = ub2; *(LAS v4u*)(L + 7 * 16384 + ldsw + 1024 + lane * 16) = ub3; } while (0)
  if (u_early) U_PUT();
  G2_BAR(0, 13);
  asm volatile("" : "+v"(gg));
  if (b == 0 && w == 1) {
    asm volatile("" : "+v"(e0), "+v"(e1), "+v"(e2), "+v"(e3));
    if (!__all(e0 == tag && e1 == tag && e2 == tag && e3 == tag)) {
#pragma unroll 1
      for (int u = 0; u < 4; ++u) poll_words(sws, WS_FLH + 64u * FS * u, 64, tag, lane);
    }
    if (lane == 0) flag_store(p.ws, WS_EPOCH, tag);
  }
  G2_CORE(13, wf[1], wf[0], u_early, accb, hf[13][0], hf[13][1], NOP, NOP, NOP)
  if (!u_early) {
    if (w == 0) { poll_words(sws, WS_FLB, 64, tag, lane); poll_words(sws, WS_FLB + 64u * FS, 64, tag, lane); }
    BAR_LGKM();
    U_ISSUE();
    U_PUT();
    BAR_LGKM();
    LAS char* s0 = ra0 + 6 * 16384; LAS char* s1 = ra1 + 6 * 16384;
    wf[0][0] = tr_read2(s0, s0 + 1024); wf[0][1] = tr_read2(s1, s1 + 1024); wf[0][2] = tr_read2(s0 + 2048, s0 + 3072); wf[0][3] = tr_read2(s1 + 2048, s1 + 3072);
  }
  STAMP(10);
  BAR_LGKM(); G2_CORE(14, wf[0], wf[1], true, acca, gf[0], gf[1], NOP, NOP, NOP)
  BAR_LGKM(); G2_CORE(15, wf[1], wf[0], false, acca, gf[2], gf[3], NOP, NOP, NOP)
  FT(50);
  {
    const float og = asf(gg.y), gs = asf(gg.x) * (1.f / 524288.f);
    const int row = 32 * rh + (lane & 31);
#pragma unroll
    for (int dh = 0; dh < 2; ++dh)
#pragma unroll
      for (int G = 0; G < 4; ++G) {
        f32x4 v;
#pragma unroll
        for (int e = 0; e < 4; ++e) v[e] = __builtin_fmaf(og, accb[dh][4 * G + e], gs * acca[dh][4 * G + e]);
        *(LAS f32x4*)(L + q * 16384 + row * 256 + (((8 * dh + 2 * G + hh) ^ (row & 7)) << 4)) = v;
      }
  }
  BAR_LGKM();
  FT(51);
  {
    const int row = tid >> 3, c8 = tid & 7;
    f32x4 s0 = {0.f, 0.f, 0.f, 0.f}, s1 = s0;
#pragma unroll
    for (int qq = 0; qq < 4; ++qq) {
      s0 += *(LAS f32x4*)(L + qq * 16384 + row * 256 + (((2 * c8) ^ (row & 7)) << 4));
      s1 += *(LAS f32x4*)(L + qq * 16384 + row * 256 + (((2 * c8 + 1) ^ (row & 7)) << 4));
    }
    v4u o; o.x = pk2(s0[0], s0[1]); o.y = pk2(s0[2], s0[3]); o.z = pk2(s1[0], s1[1]); o.w = pk2(s1[2], s1[3]);
    *(GAS v4u*)((GAS char*)p.out + (size_t)(i + 8 * (64 * jb + row)) * 1024 + dt * 128 + c8 * 16) = o;
  }
  asm volatile(".Lgm_code_end:");
  STAMP(11);
}

}

extern "C" void kernel_launch(void* const* d_in, const int* in_sizes, int n_in, void* d_out, int out_size, void* d_ws, size_t ws_size,
                              hipStream_t stream) {
  Params p;
  p.x = d_in[0]; p.wr = d_in[1]; p.wi = d_in[2]; p.w1 = d_in[4]; p.w2 = d_in[5]; p.gw = d_in[6];
  p.out = d_out; p.ws = d_ws;
  (void)hipFuncSetAttribute((const void*)gm_kernel, hipFuncAttributeMaxDynamicSharedMemorySize, LDS_BYTES);
  gm_kernel<<<256, 512, LDS_BYTES, stream>>>(p);
}
```

```cpp
#include <hip/hip_runtime.h>
#include <hip/hip_bf16.h>
#include <stdint.h>

namespace {

typedef unsigned v4u __attribute__((ext_vector_type(4)));
typedef unsigned v2u __attribute__((ext_vector_type(2)));
typedef float f32x16 __attribute__((ext_vector_type(16)));
typedef float f32x4 __attribute__((ext_vector_type(4)));
typedef float f32x2 __attribute__((ext_vector_type(2)));
typedef __bf16 bf16x8 __attribute__((ext_vector_type(8)));
typedef __bf16 bf16x2 __attribute__((ext_vector_type(2)));
typedef short s16x4 __attribute__((ext_vector_type(4)));
#define LAS __attribute__((address_space(3)))
#define GAS __attribute__((address_space(1)))
#define DI __device__ __forceinline__

constexpr int NS = 2048, ND = 512, NF = 1792;
constexpr unsigned WS_EPOCH = 0;
constexpr unsigned FS = 64;
constexpr unsigned WS_FLA = 4096;
constexpr unsigned WS_FLH = 16384;
constexpr unsigned WS_FLB = 32768;
constexpr unsigned WS_GATE = 65536;
constexpr unsigned WS_T = 1u << 20;
constexpr unsigned WS_U = 3u << 20;
constexpr unsigned WS_H = 5u << 20;
constexpr unsigned H_BYTES = 8u * 8u * 112u * 1024u;
constexpr int LDS_BYTES = 160 * 1024;
constexpr unsigned kSpinCap = 1u << 20;

struct Params {
  const void* x; const void* wr; const void* wi; const void* w1; const void* w2; const void* gw;
  void* out; void* ws;
};

constexpr double kPi = 3.14159265358979323846;
constexpr double ccos(double x) { double t = 1, s = 1; for (int n = 1; n < 16; ++n) { t *= -x * x / ((2 * n - 1) * (2 * n)); s += t; } return s; }
constexpr double cosn(int e, int n) { e = ((e % n) + n) % n; double x = 2 * kPi * e / n; if (x > kPi) x -= 2 * kPi; return ccos(x); }
constexpr double wre(int e, int n) { return cosn(e, n); }
constexpr double wim(int e, int n) { return -cosn(4 * e - n, 4 * n); }
constexpr unsigned bfb(double v) { float f = (float)v; unsigned u = __builtin_bit_cast(unsigned, f); u += 0x7fffu + ((u >> 16) & 1u); return u >> 16; }
constexpr unsigned bf2(double a, double b) { return bfb(a) | (bfb(b) << 16); }
constexpr unsigned fbits(double v) { return __builtin_bit_cast(unsigned, (float)v); }
struct Tab { unsigned v[16][64][4]; };
constexpr Tab make_tab() {
  Tab t{};
  for (int l = 0; l < 64; ++l) {
    const int g = l >> 4, l15 = l & 15, hh = l >> 5, n = l & 31;
    for (int mtx = 0; mtx < 3; ++mtx) {
      double r[4] = {}, im[4] = {};
      for (int q = 0; q < 4; ++q) {
        const int a = 4 * g + q;
        if (mtx == 0) { r[q] = wre(l15 * a, 16); im[q] = wim(l15 * a, 16); }
        else if (mtx == 1) { r[q] = wre(a * (2 * l15 + 1), 32); im[q] = wim(a * (2 * l15 + 1), 32); }
        else { r[q] = wre(l15 * (2 * a + 1), 32); im[q] = -wim(l15 * (2 * a + 1), 32); }
      }
      t.v[2 * mtx][l][0] = bf2(r[0], r[1]); t.v[2 * mtx][l][1] = bf2(r[2], r[3]); t.v[2 * mtx][l][2] = bf2(-im[0], -im[1]); t.v[2 * mtx][l][3] = bf2(-im[2], -im[3]);
      t.v[2 * mtx + 1][l][0] = bf2(im[0], im[1]); t.v[2 * mtx + 1][l][1] = bf2(im[2], im[3]); t.v[2 * mtx + 1][l][2] = bf2(r[0], r[1]); t.v[2 * mtx + 1][l][3] = bf2(r[2], r[3]);
    }
    {
      const int m2 = n >> 2, pp = (n >> 1) & 1, cp = n & 1;
      double v0[8] = {}, v1[8] = {};
      for (int jj = 0; jj < 8; ++jj) {
        const int ii = (8 * hh + jj) >> 1, d0 = jj & 1;
        const double sg = (d0 & pp) ? -1.0 : 1.0, orr = sg * wre(ii * m2, 8), oi = sg * wim(ii * m2, 8);
        v0[jj] = cp ? oi : orr; v1[jj] = cp ? orr : -oi;
      }
      for (int q = 0; q < 4; ++q) { t.v[6][l][q] = bf2(v0[2 * q], v0[2 * q + 1]); t.v[7][l][q] = bf2(v1[2 * q], v1[2 * q + 1]); }
    }
    {
      const int cq = n >> 4, ii = (n >> 1) & 7, d0 = n & 1;
      for (int s = 0; s < 2; ++s) {
        double v[8] = {};
        for (int jj = 0; jj < 8; ++jj) {
          const int m2k = 4 * s + 2 * hh + (jj >> 2), pk = (jj >> 1) & 1, ck = jj & 1;
          const double sg = (d0 & pk) ? -1.0 : 1.0, orr = sg * wre(ii * m2k, 8), oi = -sg * wim(ii * m2k, 8);
          v[jj] = cq ? (ck ? orr : oi) : (ck ? -oi : orr);
        }
        for (int q = 0; q < 4; ++q) t.v[8 + s][l][q] = bf2(v[2 * q], v[2 * q + 1]);
      }
    }
    for (int q = 0; q < 4; ++q) {
      const int a = 4 * g + q;
      t.v[10][l][q] = fbits(wre(a * (2 * l15), 512));      t.v[11][l][q] = fbits(wim(a * (2 * l15), 512));
      t.v[12][l][q] = fbits(wre(a * (2 * l15 + 1), 512));  t.v[13][l][q] = fbits(wim(a * (2 * l15 + 1), 512));
      t.v[14][l][q] = fbits(wre(l15 * (2 * a + 1), 512));  t.v[15][l][q] = fbits(wim(l15 * (2 * a + 1), 512));
    }
  }
  return t;
}
__device__ const Tab kTab = make_tab();

constexpr unsigned STAMP_OFF = 512 * 1024;
#define STAMP(k) do { if ((k) == 0) { if (true) asm volatile("s_memrealtime %0\n\ts_waitcnt lgkmcnt(0)" : "=s"(pad_t) :: "memory"); } } while (0)
#define FT(k) do {} while (0)

DI v4u make_srd(const void* p, unsigned bytes) {
  const unsigned long long a = (unsigned long long)p;
  v4u r; r.x = (unsigned)a; r.y = (unsigned)(a >> 32) & 0xffffu; r.z = bytes; r.w = 0x00020000u;
  return r;
}
template <unsigned LDSI, unsigned SOI> DI void dma16(v4u srd, unsigned voff, unsigned ldsb, unsigned sob) {
  unsigned tmp;
  asm volatile("s_add_u32 m0, %3, %5\n\ts_add_u32 %0, %4, %6\n\tbuffer_load_dwordx4 %1, %2, %0 offen lds"
               : "=&s"(tmp) : "v"(voff), "s"(srd), "s"(ldsb), "s"(sob), "i"(LDSI), "i"(SOI) : "memory", "scc");
}
template <unsigned LDSI, unsigned SOI> DI void dma16z(v4u srd, unsigned voff, unsigned ldsb) {
  unsigned tmp;
  asm volatile("s_add_u32 m0, %3, %4\n\ts_mov_b32 %0, %5\n\tbuffer_load_dwordx4 %1, %2, %0 offen lds"
               : "=&s"(tmp) : "v"(voff), "s"(srd), "s"(ldsb), "i"(LDSI), "i"(SOI) : "memory", "scc");
}
DI unsigned uni(unsigned v) { return (unsigned)__builtin_amdgcn_readfirstlane((int)v); }
#define LD16(dst, voffv, srd, soffv, flags) asm volatile("buffer_load_dwordx4 %0, %1, %2, %3 offen " flags : "=v"(dst) : "v"(voffv), "s"(srd), "s"(soffv) : "memory")
#define LD16I(dst, voffv, srd, sob, soi, flags) do { unsigned tmp_; asm volatile("s_add_u32 %1, %4, %5\n\tbuffer_load_dwordx4 %0, %2, %3, %1 offen " flags : "=v"(dst), "=&s"(tmp_) : "v"(voffv), "s"(srd), "s"(sob), "i"(soi) : "memory", "scc"); } while (0)
#define LD16Z(dst, voffv, srd, flags) asm volatile("buffer_load_dwordx4 %0, %1, %2, 0 offen " flags : "=v"(dst) : "v"(voffv), "s"(srd) : "memory")
#define LD4Z(dst, voffv, srd, flags) asm volatile("buffer_load_dword %0, %1, %2, 0 offen " flags : "=v"(dst) : "v"(voffv), "s"(srd) : "memory")
#define LD2Z(dst, voffv, srd, flags) asm volatile("buffer_load_ushort %0, %1, %2, 0 offen " flags : "=v"(dst) : "v"(voffv), "s"(srd) : "memory")
#define LD8Z(dst, voffv, srd, flags) asm volatile("buffer_load_dwordx2 %0, %1, %2, 0 offen " flags : "=v"(dst) : "v"(voffv), "s"(srd) : "memory")
#define LD8(dst, voffv, srd, soffv, flags) asm volatile("buffer_load_dwordx2 %0, %1, %2, %3 offen " flags : "=v"(dst) : "v"(voffv), "s"(srd), "s"(soffv) : "memory")
#define LD4(dst, voffv, srd, soffv, flags) asm volatile("buffer_load_dword %0, %1, %2, %3 offen " flags : "=v"(dst) : "v"(voffv), "s"(srd), "s"(soffv) : "memory")
#define LD2(dst, voffv, srd, soffv, flags) asm volatile("buffer_load_ushort %0, %1, %2, %3 offen " flags : "=v"(dst) : "v"(voffv), "s"(srd), "s"(soffv) : "memory")
#define XSTR(x) #x
#define STR(x) XSTR(x)
#define BAR_VM(n) asm volatile("s_waitcnt vmcnt(" STR(n) ") lgkmcnt(0)\n\ts_barrier" ::: "memory")
#define BAR_LGKM() asm volatile("s_waitcnt lgkmcnt(0)\n\ts_barrier" ::: "memory")
#define SCHED() __builtin_amdgcn_sched_barrier(0)
template <int N> DI void bar_vm() { asm volatile("s_waitcnt vmcnt(%0) lgkmcnt(0)\n\ts_barrier" :: "i"(N) : "memory"); }
template <int N> DI void wait_vm() { asm volatile("s_waitcnt vmcnt(%0)" :: "i"(N) : "memory"); }

DI unsigned pk2(float a, float b) { f32x2 p = {a, b}; return __builtin_bit_cast(unsigned, __builtin_convertvector(p, bf16x2)); }
DI float bflo(unsigned u) { return __builtin_bit_cast(float, u << 16); }
DI float bfhi(unsigned u) { return __builtin_bit_cast(float, u & 0xffff0000u); }
DI float asf(unsigned u) { return __builtin_bit_cast(float, u); }
DI f32x16 mfma32(v4u a, v4u b, f32x16 c) {
  return __builtin_amdgcn_mfma_f32_32x32x16_bf16(__builtin_bit_cast(bf16x8, a), __builtin_bit_cast(bf16x8, b), c, 0, 0, 0);
}
DI f32x4 mfma16(v4u a, v4u b, f32x4 c) {
  return __builtin_amdgcn_mfma_f32_16x16x32_bf16(__builtin_bit_cast(bf16x8, a), __builtin_bit_cast(bf16x8, b), c, 0, 0, 0);
}
DI v2u tr_read(LAS char* p) { return __builtin_bit_cast(v2u, __builtin_amdgcn_ds_read_tr16_b64_v4i16((LAS s16x4*)p)); }
DI v4u tr_read2(LAS char* p0, LAS char* p1) { v2u a = tr_read(p0), b = tr_read(p1); v4u r; r.x = a.x; r.y = a.y; r.z = b.x; r.w = b.y; return r; }
DI v4u lds_ld16(LAS char* p) { return *(LAS v4u*)p; }
DI float cosrev(float r) { return __builtin_amdgcn_cosf(r); }
DI float sinrev(float r) { return __builtin_amdgcn_sinf(r); }
DI __amdgpu_buffer_rsrc_t as_rsrc(v4u srd) { return __builtin_amdgcn_make_buffer_rsrc((void*)(((unsigned long long)srd.y << 32) | srd.x), 0, srd.z, 0x00020000); }
DI void st16_sc1(v4u srd, unsigned voff, v4u v) { __builtin_amdgcn_raw_buffer_store_b128(v, as_rsrc(srd), voff, 0, 16); }
DI void flag_store(void* ws, unsigned off, unsigned tag) { __hip_atomic_store((GAS unsigned*)((GAS char*)ws + off), tag, __ATOMIC_RELAXED, __HIP_MEMORY_SCOPE_AGENT); }
template <int CTRL, int RMASK> DI float dpp_add(float v) { return v + asf((unsigned)__builtin_amdgcn_update_dpp(0, (int)__builtin_bit_cast(unsigned, v), CTRL, RMASK, 0xf, true)); }
DI void poll_words(v4u sws, unsigned off, int n, unsigned tag, int lane) {
  const unsigned vo = off + FS * (unsigned)(lane < n ? lane : 0);
#pragma unroll 1
  for (unsigned spins = 0; spins < kSpinCap; ++spins) {
    unsigned f;
    asm volatile("buffer_load_dword %0, %1, %2, 0 offen sc1\n\ts_waitcnt vmcnt(0)" : "=&v"(f) : "v"(vo), "s"(sws) : "memory");
    if (__all(f == tag)) return;
    __builtin_amdgcn_s_sleep(4);
  }
}

constexpr unsigned A_XIMG = 64 * 1024, A_FIMG = 128 * 1024;
DI void phaseA_genF(LAS char* L, int i, int mt, int w, int lane) {
  const int mh = w >> 2, sg = w & 3, n = lane & 31, hh = lane >> 5;
  const int m1c = 64 * mt + 32 * mh + n, m1 = m1c >> 1, c = m1c & 1;
  const int j0 = 64 * sg + 32 * hh;
  const float ph = c ? 0.25f : 0.f;
  const float k2 = 2.f * cosrev((float)(8 * m1) * (1.f / 2048.f));
  float vm = cosrev((float)(((i + 8 * (j0 - 1)) * m1) & 2047) * (1.f / 2048.f) + ph);
  float v0 = cosrev((float)(((i + 8 * j0) * m1) & 2047) * (1.f / 2048.f) + ph);
  const unsigned cst = c ? 0x3F800000u : 0x00003F80u;
  const bool sp = m1c < 2;
#pragma unroll
  for (int s = 0; s < 4; ++s) {
    float e[8];
#pragma unroll
    for (int q = 0; q < 8; ++q) { e[q] = v0; const float vn = __builtin_fmaf(k2, v0, -vm); vm = v0; v0 = vn; }
    v4u f; f.x = pk2(e[0], e[1]); f.y = pk2(e[2], e[3]); f.z = pk2(e[4], e[5]); f.w = pk2(e[6], e[7]);
    if (mt == 0 && w < 4) { f.x = sp ? cst : f.x; f.y = sp ? cst : f.y; f.z = sp ? cst : f.z; f.w = sp ? cst : f.w; }
    *(LAS v4u*)(L + A_FIMG + (mh * 16 + sg * 4 + s) * 1024 + lane * 16) = f;
  }
}

struct Frag1 { v4u a[4], b0[4], b1[4]; };
DI void g1_read(Frag1& f, LAS char* wa, LAS char* xb0, LAS char* xb1, LAS char* xb2, LAS char* xb3, int slot) {
  const int so = slot * 32768;
  f.a[0] = tr_read2(wa + so, wa + so + 1024);              f.b0[0] = lds_ld16(xb0 + so); f.b1[0] = lds_ld16(xb0 + so + 4096);
  f.a[1] = tr_read2(wa + so + 4096, wa + so + 5120);       f.b0[1] = lds_ld16(xb1 + so); f.b1[1] = lds_ld16(xb1 + so + 4096);
  f.a[2] = tr_read2(wa + so + 8192, wa + so + 9216);       f.b0[2] = lds_ld16(xb2 + so); f.b1[2] = lds_ld16(xb2 + so + 4096);
  f.a[3] = tr_read2(wa + so + 12288, wa + so + 13312);     f.b0[3] = lds_ld16(xb3 + so); f.b1[3] = lds_ld16(xb3 + so + 4096);
}
DI unsigned silu2(float a, float b) {
  const f32x2 h = {a, b}, t = h * -1.44269504088896341f;
  f32x2 e = {__builtin_amdgcn_exp2f(t[0]), __builtin_amdgcn_exp2f(t[1])};
  e = e + 1.f;
  const f32x2 r = {__builtin_amdgcn_rcpf(e[0]), __builtin_amdgcn_rcpf(e[1])};
  return __builtin_bit_cast(unsigned, __builtin_convertvector(h * r, bf16x2));
}

DI f32x2 lo2(const f32x4& v) { return __builtin_shufflevector(v, v, 0, 1); }
DI f32x2 hi2(const f32x4& v) { return __builtin_shufflevector(v, v, 2, 3); }
DI unsigned pk2v(f32x2 p) { return __builtin_bit_cast(unsigned, __builtin_convertvector(p, bf16x2)); }
DI f32x4 asf4(v4u v) { return __builtin_bit_cast(f32x4, v); }
template <bool CONJ> DI v4u cmul_pack(const f32x4& re, const f32x4& im, const f32x4& tr, const f32x4& ti) {
  const f32x2 r0 = lo2(re), r1 = hi2(re), i0 = lo2(im), i1 = hi2(im), c0 = lo2(tr), c1 = hi2(tr), s0 = lo2(ti), s1 = hi2(ti);
  f32x2 a0, a1, b0, b1;
  if (CONJ) {
    a0 = __builtin_elementwise_fma(r0, c0, i0 * s0); a1 = __builtin_elementwise_fma(r1, c1, i1 * s1);
    b0 = __builtin_elementwise_fma(i0, c0, -(r0 * s0)); b1 = __builtin_elementwise_fma(i1, c1, -(r1 * s1));
  } else {
    a0 = __builtin_elementwise_fma(r0, c0, -(i0 * s0)); a1 = __builtin_elementwise_fma(r1, c1, -(i1 * s1));
    b0 = __builtin_elementwise_fma(r0, s0, i0 * c0); b1 = __builtin_elementwise_fma(r1, s1, i1 * c1);
  }
  v4u r; r.x = pk2v(a0); r.y = pk2v(a1); r.z = pk2v(b0); r.w = pk2v(b1); return r;
}
struct cf { float r, i; };
DI cf cmul(cf a, cf b) { return {a.r * b.r - a.i * b.i, a.r * b.i + a.i * b.r}; }
DI cf cmulc(cf a, cf b) { return {a.r * b.r + a.i * b.i, a.i * b.r - a.r * b.i}; }
DI unsigned w_src(int m, int lane) {
  const int mm = (NS - m) & (NS - 1);
  const int e = lane < 32 ? m * 257 - (m & 1) : mm * 257 + (m & 1);
  return (unsigned)(e * 2 + (lane & 31) * 16);
}
DI unsigned w_tail_src(int m, int lane) {
  const int mm = (NS - m) & (NS - 1), l = lane & 3;
  return (unsigned)((l < 2 ? m * 257 + 255 + l : mm * 257 + (l - 2) * 256) * 2);
}
struct WTail { unsigned r255, r256, rm0, rm256, i255, i256, im0, im256; };
DI WTail w_tail(unsigned tr, unsigned ti) {
  return {(unsigned)__builtin_amdgcn_readlane((int)tr, 0), (unsigned)__builtin_amdgcn_readlane((int)tr, 1), (unsigned)__builtin_amdgcn_readlane((int)tr, 2), (unsigned)__builtin_amdgcn_readlane((int)tr, 3),
          (unsigned)__builtin_amdgcn_readlane((int)ti, 0), (unsigned)__builtin_amdgcn_readlane((int)ti, 1), (unsigned)__builtin_amdgcn_readlane((int)ti, 2), (unsigned)__builtin_amdgcn_readlane((int)ti, 3)};
}
struct WVal { f32x4 r0, i0, r1, i1; };
struct WRawL { unsigned r[4][2], i[4][2]; };
DI void w_put(LAS char* scr, v4u qr, v4u qi, int lane) { *(LAS v4u*)(scr + lane * 16) = qr; *(LAS v4u*)(scr + 1024 + lane * 16) = qi; }
DI int w_off(int m, int lane) { const int k1 = lane & 15, g = lane >> 4, odd = m & 1; return lane < 32 ? (2 * k1 + 128 * g + odd) * 2 : 512 + (511 - 2 * k1 - 128 * g - odd) * 2; }
DI void w_get(WRawL& o, LAS char* scr, int off, int lane, int r) {
  LAS unsigned* a = (LAS unsigned*)(scr + ((off + (lane < 32 ? 64 : -64) * r) & ~3));
  o.r[r][0] = a[0]; o.r[r][1] = a[1]; o.i[r][0] = a[256]; o.i[r][1] = a[257];
}
DI WVal w_unpack(const WRawL& x, const WTail& t, int off, int m, int lane) {
  const bool lo = lane < 32;
  const unsigned sh = (off & 2) * 8;
  unsigned rr[4], ii[4];
#pragma unroll
  for (int r = 0; r < 4; ++r) { rr[r] = __builtin_amdgcn_alignbit(x.r[r][1], x.r[r][0], sh); ii[r] = __builtin_amdgcn_alignbit(x.i[r][1], x.i[r][0], sh); }
  if (m & 1) { rr[3] = lane == 31 ? (rr[3] & 0xffffu) | (t.r255 << 16) : rr[3]; ii[3] = lane == 31 ? (ii[3] & 0xffffu) | (t.i255 << 16) : ii[3]; }
  else { rr[0] = lane == 32 ? (rr[0] & 0xffffu) | (t.rm256 << 16) : rr[0]; ii[0] = lane == 32 ? (ii[0] & 0xffffu) | (t.im256 << 16) : ii[0]; }
  const unsigned s0 = lo ? 16u : 0u, s1 = lo ? 0u : 16u, sg = lo ? 0u : 0x80000000u;
  WVal o;
#pragma unroll
  for (int r = 0; r < 4; ++r) {
    o.r0[r] = asf((rr[r] << s0) & 0xffff0000u); o.r1[r] = asf((rr[r] << s1) & 0xffff0000u);
    o.i0[r] = asf(((ii[r] << s0) & 0xffff0000u) ^ sg); o.i1[r] = asf(((ii[r] << s1) & 0xffff0000u) ^ sg);
  }
  const bool av = (lane & 31) == 0;
  const float xr = bflo(lo ? t.rm0 : t.r256), xi = asf(((lo ? t.im0 : t.i256) << 16) ^ (sg ^ 0x80000000u));
  o.r0[0] = av ? 0.5f * (o.r0[0] + xr) : o.r0[0];
  o.i0[0] = av ? 0.5f * (o.i0[0] + xi) : o.i0[0];
  return o;
}

DI void packed_coef(WVal& a, WVal& b, int m2) {
  const cf om = {cosrev((float)m2 * 0.0625f), -sinrev((float)m2 * 0.0625f)};
#pragma unroll
  for (int pp = 0; pp < 2; ++pp)
#pragma unroll
    for (int r = 0; r < 4; ++r) {
      const float war = pp ? a.r1[r] : a.r0[r], wai = pp ? a.i1[r] : a.i0[r], wbr = pp ? b.r1[r] : b.r0[r], wbi = pp ? b.i1[r] : b.i0[r];
      const cf sp = {war + wbr, wai + wbi}, sm = {war - wbr, wai - wbi};
      const cf t1 = cmulc(sm, om), t2 = cmul(sm, om);
      const cf cE = {sp.r - t1.i, sp.i + t1.r}, cO = {t2.r - sp.i, t2.i + sp.r};
      const float alr = 0.25f * (cE.r + cO.i), ali = 0.25f * (cE.i - cO.r), ber = 0.25f * (cE.r - cO.i), bei = 0.25f * (cE.i + cO.r);
      if (pp) { a.r1[r] = alr; a.i1[r] = ali; b.r1[r] = ber; b.i1[r] = bei; } else { a.r0[r] = alr; a.i0[r] = ali; b.r0[r] = ber; b.i0[r] = bei; }
    }
}

struct Geo {
  int tid, lane, w, b, i, i1, at, m1, rb, fc, hh, blk, q4, p4;
  int jb, dt, rh, q, j;
  v4u sx, sws, sw2;
  unsigned vw20, vw21, ldsw, fq0b, fq1b;
};
template <int T, int U> DI void w2_dma(const v4u& sw2, unsigned vw20, unsigned vw21, unsigned ldsw, unsigned fq0, unsigned fq1) {
  dma16<(T & 7) * 16384u + U * 1024u, (T < 8 ? T : T - 8) * 32768u>(sw2, U ? vw21 : vw20, ldsw, T < 8 ? fq0 : fq1);
}
#define w2_issue1(g, t, u) (w2_dma<t, u>((g).sw2, (g).vw20, (g).vw21, (g).ldsw, (g).fq0b, (g).fq1b))
struct GRot { float c1, s1, c4, s4, c, s; };
DI void gen_g_init(const Geo& g, GRot& r) {
  const int srow = g.i + 8 * g.j;
  const int mb = 32 * g.q + 2 * g.hh;
  const float r1 = (float)(srow & 2047) * (1.f / 2048.f), r4 = (float)((4 * srow) & 2047) * (1.f / 2048.f), r0 = (float)((mb * srow) & 2047) * (1.f / 2048.f);
  r.c1 = cosrev(r1); r.s1 = sinrev(r1); r.c4 = cosrev(r4); r.s4 = sinrev(r4); r.c = cosrev(r0); r.s = sinrev(r0);
}
template <int S> DI void gen_g_frag(const Geo& g, GRot& r, v4u& f) {
#pragma unroll
  for (int h = 0; h < 2; ++h) {
    const float cb = __builtin_fmaf(r.c, r.c1, -(r.s * r.s1)), sb = __builtin_fmaf(r.s, r.c1, r.c * r.s1);
    unsigned w0 = pk2(r.c, -r.s), w1 = pk2(cb, -sb);
    if (S == 0 && h == 0) w0 = (g.q == 0 && g.hh == 0) ? ((g.j & 1) ? 0x3F800000u : 0x00003F80u) : w0;
    if (h) { f.z = w0; f.w = w1; } else { f.x = w0; f.y = w1; }
    const float cn = __builtin_fmaf(r.c, r.c4, -(r.s * r.s4)), sn = __builtin_fmaf(r.s, r.c4, r.c * r.s4);
    r.c = cn; r.s = sn;
  }
  asm volatile("" : "+v"(f));
}
template <int N0, int N1, int NR> DI void bar_w(int w) { if (w == 0) bar_vm<N0>(); else if (w == 1) bar_vm<N1>(); else bar_vm<NR>(); }
#define NOP do {} while (0)

#define TLBW 0
#define TOUCH() do {                                                                                                      \
    const int gl = G.b * 256 + (w & 3) * 64 + lane;                                                                         \
    const char* base_ = gl < 14336 ? (const char*)p.w2 : gl < 28672 ? (const char*)p.w1 - 14336 * 128 : gl < 36896 ? (const char*)p.wr - 28672 * 128           \
                      : gl < 45120 ? (const char*)p.wi - 36896 * 128 : (const char*)p.x - 45120 * 128;                     \
    const char* ad_ = base_ + (size_t)(gl < 61504 ? gl : 45120) * 128;                                                      \
    if (w >= 4) ad_ = TLBW ? (w == 7 ? (const char*)p.out + lane * 32768 : (const char*)p.ws + (size_t)((w - 4) * 64 + lane + 8) * 65536 + 32768 - 128) : (const char*)p.x; \
    asm volatile("global_load_dword %0, %1, off" : "=v"(touch) : "v"(ad_) : "memory");                                     \
  } while (0)
#define g1_dma(kt, u) do {                                                                                         \
    if ((u) == 0) dma16z<((kt) & 3) * 32768u, (kt) * 128u>(sx, vx0, g1l);                                           \
    if ((u) == 1) dma16z<((kt) & 3) * 32768u + 1024u, (kt) * 128u>(sx, vx1, g1l);                                   \
    if ((u) == 2) dma16z<((kt) & 3) * 32768u + 16384u, (kt) * (64u * 3584u)>(sw1, vw0, g1l);                        \
    if ((u) == 3) dma16z<((kt) & 3) * 32768u + 17408u, (kt) * (64u * 3584u)>(sw1, vw1, g1l); } while (0)
#define g1_issue(kt) do { g1_dma(kt, 0); g1_dma(kt, 1); g1_dma(kt, 2); g1_dma(kt, 3); } while (0)
struct Pro { v4u sw1, swr, swi, gx, gv, wqr, wqi, xqr, xqi; unsigned ep, touch, wtr, wti, xtr, xti, vx0, vx1, vw0, vw1, g1l; };
template <bool ISA>
DI unsigned phase1(long long& pad_t, const Params& p, LAS char* L, const Geo& G, Pro& R, v4u& m5a, v4u& m5b
) {
  const int tid = G.tid, lane = G.lane, w = G.w, i = G.i, i1 = G.i1, rb = G.rb, fc = G.fc, hh = G.hh, blk = G.blk, q4 = G.q4, p4 = G.p4;
  const v4u sx = G.sx, sws = G.sws, sw1 = R.sw1, swr = R.swr, swi = R.swi;
  unsigned& ep = R.ep; v4u& gx = R.gx; v4u& gv = R.gv; unsigned& touch = R.touch;
  v4u& wqr = R.wqr; v4u& wqi = R.wqi; v4u& xqr = R.xqr; v4u& xqi = R.xqi; unsigned& wtr = R.wtr; unsigned& wti = R.wti; unsigned& xtr = R.xtr; unsigned& xti = R.xti;
  const unsigned vx0 = R.vx0, vx1 = R.vx1, vw0 = R.vw0, vw1 = R.vw1, g1l = R.g1l;
  const int f0 = ISA ? 128 * fc : 1024 + 96 * (fc - 8), nf = ISA ? 128 : 96;
  const int mt = G.at >> 2, dt4 = G.at & 3;
  const int m1 = G.m1;
  const int mW = m1 == 0 ? 128 * w : m1 + 256 * w;
  WVal Wc, Wd;
  const unsigned tag = (unsigned)__builtin_amdgcn_readfirstlane((int)ep) + 1u;
  float gate_g, gate_og;
  {
    float z = 0.f;
#pragma unroll
    for (int q = 0; q < 4; ++q) {
      const unsigned pr = pk2(bflo(gx[q]) * bflo(gv[q]), bfhi(gx[q]) * bfhi(gv[q]));
      z += bflo(pr) + bfhi(pr);
    }
    z = dpp_add<0x111, 0xf>(z); z = dpp_add<0x112, 0xf>(z); z = dpp_add<0x114, 0xf>(z); z = dpp_add<0x118, 0xf>(z);
    z = dpp_add<0x142, 0xa>(z); z = dpp_add<0x143, 0xc>(z);
    const float zb = bflo(pk2(z, 0.f));
    const float g = bflo(pk2(__builtin_amdgcn_rcpf(1.f + __builtin_amdgcn_exp2f(zb * -1.44269504088896341f)), 0.f));
    gate_g = asf((unsigned)__builtin_amdgcn_readlane((int)__builtin_bit_cast(unsigned, g), 63));
    gate_og = bflo(pk2(1.f - gate_g, 0.f));
  }
  if (!ISA) {
    LAS char* wscr0 = L + 128 * 1024 + w * 2048;
    const int woff = w_off(mW, lane);
    w_put(wscr0, wqr, wqi, lane);
    asm volatile("s_waitcnt lgkmcnt(0)" ::: "memory");
    WRawL wraw;
    w_get(wraw, wscr0, woff, lane, 0); w_get(wraw, wscr0, woff, lane, 1); w_get(wraw, wscr0, woff, lane, 2); w_get(wraw, wscr0, woff, lane, 3);
    Wc = w_unpack(wraw, w_tail(wtr, wti), woff, mW, lane);
    asm volatile("" : "+v"(Wc.r0), "+v"(Wc.i0), "+v"(Wc.r1), "+v"(Wc.i1));
  }
  if (ISA) {
    const int dq = w & 3, mh = w >> 2;
    LAS char* xa = L + A_XIMG + (32 * hh + q4) * 256 + (((4 * dq + 2 * blk + (p4 >> 1)) ^ (q4 << 2)) << 4) + 8 * (p4 & 1);
    LAS char* fb = L + A_FIMG + mh * 16384 + lane * 16;
    const int srow = 32 * mh + (lane & 31);
    LAS char* stw = L + A_FIMG + srow * 256 + 8 * hh;
    const int rrow = tid >> 3;
    LAS char* str = L + A_FIMG + rrow * 256;
    const unsigned tdst = WS_T + (unsigned)(((64 * mt + rrow) * 8 + i) * 1024 + dt4 * 256 + (tid & 7) * 16);
    if (w < 4) bar_vm<9>(); else BAR_LGKM();
    STAMP(1);
    f32x16 acc = {};
    v4u fa[2][4], fbv[2][4];
#pragma unroll
    for (int s = 0; s < 4; ++s) { fa[0][s] = tr_read2(xa + 8 * s * 256, xa + 8 * s * 256 + 1024); fbv[0][s] = lds_ld16(fb + s * 1024); }
#pragma unroll
    for (int g = 0; g < 4; ++g) {
      acc = mfma32(fa[g & 1][0], fbv[g & 1][0], acc);
      SCHED();
      if (g == 1) { if (w >= 4) bar_vm<9>(); else BAR_LGKM(); }
      if (g < 3) {
#pragma unroll
        for (int s = 0; s < 4; ++s) {
          const int ro = (64 * (g + 1) + 8 * s) * 256;
          fa[(g + 1) & 1][s] = tr_read2(xa + ro, xa + ro + 1024); fbv[(g + 1) & 1][s] = lds_ld16(fb + (4 * (g + 1) + s) * 1024);
        }
      }
      SCHED();
#pragma unroll
      for (int s = 1; s < 4; ++s) { acc = mfma32(fa[g & 1][s], fbv[g & 1][s], acc); if (g == 1 && s == 1) { g1_dma(2, 0); g1_dma(2, 1); } if (g == 1 && s == 2) { g1_dma(2, 2); g1_dma(2, 3); } SCHED(); }
    }
    BAR_LGKM();
    STAMP(2);
    g1_issue(3);
#pragma unroll
    for (int Gq = 0; Gq < 4; ++Gq) {
      v2u o; o.x = pk2(acc[4 * Gq], acc[4 * Gq + 1]); o.y = pk2(acc[4 * Gq + 2], acc[4 * Gq + 3]);
      *(LAS v2u*)(stw + (((4 * dq + Gq) ^ (srow & 15)) << 4)) = o;
    }
    BAR_LGKM();
    {
      const v4u v0 = lds_ld16(str + (((tid & 7) ^ (rrow & 15)) << 4)), v1 = lds_ld16(str + ((((tid & 7) + 8) ^ (rrow & 15)) << 4));
      st16_sc1(sws, tdst, v0);
      st16_sc1(sws, tdst + 128u, v1);
    }
    SCHED();
  }
  const int ft = w & 3, rh1 = w >> 2;
  const bool active = ft * 32 < nf;
  f32x16 acc1[2] = {};
  v4u tq0, tq1;
  v4u tb[16];
  unsigned fla;
  int t_ok = 0;
  unsigned tsrc0 = 0, tsrc1 = 0;
  const unsigned flaoff = WS_FLA + (unsigned)(((m1 >> 5) & 3) * 32 + (lane & 31)) * FS;
  LAS char* CTL = L + 128 * 1024;
  {
    LAS char* wa = L + 16384 + (8 * hh + q4) * 256 + (((4 * ft + 2 * blk + (p4 >> 1)) ^ (q4 << 2)) << 4) + 8 * (p4 & 1);
    const int r = 64 * rh1 + (lane & 31), fz = (r >> 1) & 7;
    LAS char* xb0 = L + r * 128 + (((0 + hh) ^ fz) << 4);
    LAS char* xb1 = L + r * 128 + (((2 + hh) ^ fz) << 4);
    LAS char* xb2 = L + r * 128 + (((4 + hh) ^ fz) << 4);
    LAS char* xb3 = L + r * 128 + (((6 + hh) ^ fz) << 4);
    Frag1 fr[2];
    bar_vm<ISA ? 15 : 13>();
    STAMP(3);
    if (lane == 0) { v2u gv2; gv2.x = __builtin_bit_cast(unsigned, gate_g); gv2.y = __builtin_bit_cast(unsigned, gate_og); *(LAS v2u*)(CTL + 64 + w * 8) = gv2; }
    g1_read(fr[0], wa, xb0, xb1, xb2, xb3, 0);
#define PIN1(c) asm volatile("" : "+v"(c.a[0]), "+v"(c.a[1]), "+v"(c.a[2]), "+v"(c.a[3]), "+v"(c.b0[0]), "+v"(c.b0[1]), "+v"(c.b0[2]), "+v"(c.b0[3]), \
                             "+v"(c.b1[0]), "+v"(c.b1[1]), "+v"(c.b1[2]), "+v"(c.b1[3]))
#define G1_STEP(kt, X0, X1, X2, X3, X4, X5, X6, X7)                                                                   \
    {                                                                                                                   \
      FT(10 + (kt));                                                                                                    \
      Frag1& c = fr[(kt) & 1];                                                                                          \
      Frag1& n = fr[((kt) + 1) & 1];                                                                                    \
      const int so = (((kt) + 1) & 3) * 32768;                                                                          \
      PIN1(c);                                                                                                          \
      asm volatile("" :: "v"(acc1[0]), "v"(acc1[1]));                                                                   \
      SCHED();                                                                                                          \
      acc1[0] = mfma32(c.a[0], c.b0[0], acc1[0]); if ((kt) < 7) n.a[0] = tr_read2(wa + so, wa + so + 1024); X0; SCHED(); \
      acc1[1] = mfma32(c.a[0], c.b1[0], acc1[1]); if ((kt) < 7) { n.b0[0] = lds_ld16(xb0 + so); n.b1[0] = lds_ld16(xb0 + so + 4096); } X1; SCHED(); \
      acc1[0] = mfma32(c.a[1], c.b0[1], acc1[0]); if ((kt) < 7) n.a[1] = tr_read2(wa + so + 4096, wa + so + 5120); X2; SCHED(); \
      acc1[1] = mfma32(c.a[1], c.b1[1], acc1[1]); if ((kt) < 7) { n.b0[1] = lds_ld16(xb1 + so); n.b1[1] = lds_ld16(xb1 + so + 4096); } X3; SCHED(); \
      acc1[0] = mfma32(c.a[2], c.b0[2], acc1[0]); if ((kt) < 7) n.a[2] = tr_read2(wa + so + 8192, wa + so + 9216); X4; SCHED(); \
      acc1[1] = mfma32(c.a[2], c.b1[2], acc1[1]); if ((kt) < 7) { n.b0[2] = lds_ld16(xb2 + so); n.b1[2] = lds_ld16(xb2 + so + 4096); } X5; SCHED(); \
      acc1[0] = mfma32(c.a[3], c.b0[3], acc1[0]); if ((kt) < 7) n.a[3] = tr_read2(wa + so + 12288, wa + so + 13312); X6; SCHED(); \
      acc1[1] = mfma32(c.a[3], c.b1[3], acc1[1]); if ((kt) < 7) { n.b0[3] = lds_ld16(xb3 + so); n.b1[3] = lds_ld16(xb3 + so + 4096); } X7; SCHED(); \
    }
#define GATE_STORE() do { if (w == 2 && lane < 8) { const v2u gv2 = *(LAS v2u*)(CTL + 64 + lane * 8);                       \
      __builtin_amdgcn_raw_buffer_store_b64(gv2, as_rsrc(sws), WS_GATE + (unsigned)(i1 * 256 + 128 * rb + 8 * fc + lane) * 8u, 0, 16); } } while (0)
#define STEP8 G1_STEP
    if constexpr (ISA) {
      bar_vm<11>();
      STAMP(4);
      STEP8(0, NOP, NOP, NOP, NOP, NOP, NOP, NOP, NOP)
      bar_vm<0>();
      if (tid == 0) flag_store(p.ws, WS_FLA + (unsigned)(mt * 32 + i * 4 + dt4) * FS, tag);
      STAMP(13);
      STEP8(1, g1_dma(4, 0), g1_dma(4, 1), g1_dma(4, 2), g1_dma(4, 3), g1_dma(5, 0), g1_dma(5, 1), g1_dma(5, 2), g1_dma(5, 3))
      BAR_LGKM();
      GATE_STORE();
      STEP8(2, NOP, NOP, NOP, NOP, g1_dma(6, 0), g1_dma(6, 1), g1_dma(6, 2), g1_dma(6, 3))
      bar_vm<8>();
      STEP8(3, NOP, NOP, NOP, NOP, g1_dma(7, 0), g1_dma(7, 1), g1_dma(7, 2), g1_dma(7, 3))
      bar_vm<8>();
      STAMP(12);
      STEP8(4, NOP, NOP, NOP, NOP, w2_issue1(G, 0, 0), w2_issue1(G, 0, 1), w2_issue1(G, 1, 0), w2_issue1(G, 1, 1))
      bar_vm<8>();
      STEP8(5, NOP, NOP, NOP, NOP, w2_issue1(G, 2, 0), w2_issue1(G, 2, 1), w2_issue1(G, 3, 0), w2_issue1(G, 3, 1))
      bar_vm<8>();
      STEP8(6, NOP, NOP, NOP, NOP, w2_issue1(G, 4, 0), w2_issue1(G, 4, 1), w2_issue1(G, 5, 0), w2_issue1(G, 5, 1))
      BAR_LGKM();
    } else {
      const v4u stab = make_srd(&kTab, sizeof(Tab));
      {
        const int kap = tid >> 5, ii = (kap >> 1) & 7, d0 = kap & 1;
        const int g0 = m1 == 0 ? 2 * ii : ii, g1 = m1 == 0 ? 2 * ii + 1 : 8 + ii;
        const unsigned tb = WS_T + (unsigned)m1 * 16384u + (unsigned)(d0 * 512 + (tid & 31) * 16);
        tsrc0 = tb + (unsigned)g0 * 1024u; tsrc1 = tb + (unsigned)g1 * 1024u;
      }
      bar_vm<9>();
      STAMP(4);
      STEP8(0, NOP, NOP, NOP, NOP, g1_dma(4, 0), g1_dma(4, 1), g1_dma(4, 2), g1_dma(4, 3))
      bar_vm<9>();
      STEP8(1, NOP, NOP, NOP, NOP, g1_dma(5, 0), g1_dma(5, 1), g1_dma(5, 2), g1_dma(5, 3))
      bar_vm<9>();
      GATE_STORE();
      STEP8(2, NOP, NOP, NOP, NOP, g1_dma(6, 0), g1_dma(6, 1), g1_dma(6, 2), g1_dma(6, 3))
      bar_vm<8>();
      LAS char* wscr0 = L + 128 * 1024 + w * 2048;
      const bool special = m1 == 0;
      const int m2nd = 128 * w + 1024, off2 = w_off(m2nd, lane);
      if (special) { asm volatile("" : "+v"(xqr), "+v"(xqi), "+v"(xtr), "+v"(xti)); w_put(wscr0, xqr, xqi, lane); }
      STEP8(3, NOP, NOP, (dma16<128u * 1024u + 16384u, 0>(stab, (unsigned)lane * 16u, g1l, g1l)), (dma16<128u * 1024u + 17408u, 1024>(stab, (unsigned)lane * 16u, g1l, g1l)),
            g1_dma(7, 0), g1_dma(7, 1), g1_dma(7, 2), g1_dma(7, 3))
      bar_vm<10>();
      STAMP(12);
      WRawL raw2;
      if (special) { w_get(raw2, wscr0, off2, lane, 0); w_get(raw2, wscr0, off2, lane, 1); w_get(raw2, wscr0, off2, lane, 2); w_get(raw2, wscr0, off2, lane, 3); }
      STEP8(4, NOP, NOP, NOP, NOP, w2_issue1(G, 0, 0), w2_issue1(G, 0, 1), w2_issue1(G, 1, 0), w2_issue1(G, 1, 1))
      bar_vm<10>();
      if (special) { Wd = w_unpack(raw2, w_tail(xtr, xti), off2, m2nd, lane); packed_coef(Wc, Wd, w); }
      STEP8(5, NOP, NOP, NOP, NOP, w2_issue1(G, 2, 0), w2_issue1(G, 2, 1), w2_issue1(G, 3, 0), w2_issue1(G, 3, 1))
      bar_vm<8>();
      STEP8(6, if (w == 0) LD4Z(fla, flaoff, sws, "sc1"), NOP, NOP, NOP, w2_issue1(G, 4, 0), w2_issue1(G, 4, 1), w2_issue1(G, 5, 0), w2_issue1(G, 5, 1))
      BAR_LGKM();
    }
    if constexpr (ISA) {
      STEP8(7, NOP, NOP, NOP, NOP, w2_issue1(G, 6, 0), w2_issue1(G, 6, 1), w2_issue1(G, 7, 0), w2_issue1(G, 7, 1))
    } else {
      LAS char* TB = L + 128 * 1024 + 16384 + lane * 16;
#define TBR(f) (tb[f] = lds_ld16(TB + (f) * 1024), tb[(f) + 1] = lds_ld16(TB + ((f) + 1) * 1024))
      STEP8(7, TBR(6), TBR(0), TBR(10), TBR(2), { TBR(12); w2_issue1(G, 6, 0); }, { TBR(14); w2_issue1(G, 6, 1); }, { TBR(4); w2_issue1(G, 7, 0); }, { TBR(8); w2_issue1(G, 7, 1); })
      if (w == 0) {
        asm volatile("s_waitcnt vmcnt(8)" : "+v"(fla) :: "memory");
        const unsigned ok = __all(fla == tag) ? 1u : 0u;
        if (lane == 0) *(LAS unsigned*)CTL = ok;
      }
      BAR_LGKM();
      t_ok = __builtin_amdgcn_readfirstlane(*(volatile LAS unsigned*)CTL);
      if (t_ok) { LD16Z(tq0, tsrc0, sws, "sc1"); LD16Z(tq1, tsrc1, sws, "sc1"); }
    }
  }
  asm volatile("" :: "v"(touch));
  STAMP(5);
  FT(18);
  if (active) {
#pragma unroll
    for (int rt = 0; rt < 2; ++rt)
#pragma unroll
      for (int u = 0; u < 2; ++u) {
        v4u o;
        o.x = silu2(acc1[rt][8 * u + 0], acc1[rt][8 * u + 1]);
        o.y = silu2(acc1[rt][8 * u + 2], acc1[rt][8 * u + 3]);
        o.z = silu2(acc1[rt][8 * u + 4], acc1[rt][8 * u + 5]);
        o.w = silu2(acc1[rt][8 * u + 6], acc1[rt][8 * u + 7]);
        const int jb32 = 4 * rb + 2 * rh1 + rt, fs = (f0 >> 4) + 2 * ft + u;
        st16_sc1(sws, WS_H + (unsigned)(((i1 * 8 + jb32) * 112 + fs) * 1024 + lane * 16), o);
      }
  }
  SCHED();
  STAMP(6);
  FT(20);
  const unsigned flh_own = WS_FLH + (unsigned)((i1 * 2 + rb) * 16 + fc) * FS;
  if constexpr (ISA) {
    bar_vm<0>();
    if (tid == 0) flag_store(p.ws, flh_own, tag);
  } else {
    LAS char* I0 = L + 128 * 1024;
    LAS char* I1 = L + 128 * 1024 + 16384;
    const int g = lane >> 4, l15 = lane & 15;
    const bool special = (m1 == 0);
    bool published = false;
    if (t_ok) {
      if (active) wait_vm<4>(); else wait_vm<0>();
      asm volatile("s_barrier" ::: "memory");
    } else {
      bar_vm<0>();
      if (tid == 0) flag_store(p.ws, flh_own, tag);
      published = true;
      if (w == 0) poll_words(sws, WS_FLA + (unsigned)(m1 >> 5) * 32u * FS, 32, tag, lane);
      asm volatile("s_barrier" ::: "memory");
      LD16Z(tq0, tsrc0, sws, "sc1"); LD16Z(tq1, tsrc1, sws, "sc1");
      wait_vm<0>();
    }
    asm volatile("" : "+v"(tq0), "+v"(tq1) :: "memory");
    {
      const int kap = tid >> 5, ch = tid & 31;
      *(LAS v4u*)(I0 + kap * 512 + ((ch ^ ((kap & 3) << 2)) << 4)) = tq0;
      *(LAS v4u*)(I0 + (kap + 16) * 512 + ((ch ^ ((kap & 3) << 2)) << 4)) = tq1;
    }
    BAR_LGKM();
    FT(21);
    LAS char* ta = I0 + (8 * hh + q4) * 512 + (((4 * w + 2 * blk + (p4 >> 1)) ^ (q4 << 2)) << 4) + 8 * (p4 & 1);
    const int n = lane & 31;
    LAS char* so = I1 + n * 512 + 8 * hh;
    {
      f32x16 acc = {};
      acc = mfma32(tr_read2(ta, ta + 2048), tb[6], acc);
      acc = mfma32(tr_read2(ta + 8192, ta + 10240), tb[7], acc);
#pragma unroll
      for (int Gq = 0; Gq < 4; ++Gq) {
        v2u o; o.x = pk2(acc[4 * Gq], acc[4 * Gq + 1]); o.y = pk2(acc[4 * Gq + 2], acc[4 * Gq + 3]);
        *(LAS v2u*)(so + (((4 * w + Gq) ^ (n & 15)) << 4)) = o;
      }
    }
    BAR_LGKM();
    FT(22);
    const f32x4 z4 = {0.f, 0.f, 0.f, 0.f};
    const v4u F16fr = tb[0], F16fi = tb[1];
    v4u F16ir, F16ii;
    F16ir.x = F16fr.x; F16ir.y = F16fr.y; F16ir.z = F16fr.z ^ 0x80008000u; F16ir.w = F16fr.w ^ 0x80008000u;
    F16ii.x = F16fi.x ^ 0x80008000u; F16ii.y = F16fi.y ^ 0x80008000u; F16ii.z = F16fi.z; F16ii.w = F16fi.w;
    f32x4 zr[2], zi[2];
#pragma unroll
    for (int pp = 0; pp < 2; ++pp) {
      const int n0 = 4 * w + 2 * pp, n1 = n0 + 1;
      const int ch = 2 * (4 * g + q4) + (p4 >> 1);
      const v4u a1 = tr_read2(I1 + n0 * 512 + ((ch ^ (n0 & 15)) << 4) + 8 * (p4 & 1), I1 + n1 * 512 + ((ch ^ (n1 & 15)) << 4) + 8 * (p4 & 1));
      const f32x4 dr = mfma16(a1, pp ? tb[2] : F16fr, z4), di = mfma16(a1, pp ? tb[3] : F16fi, z4);
      const v4u b3 = cmul_pack<false>(dr, di, asf4(tb[10 + 2 * pp]), asf4(tb[11 + 2 * pp]));
      zr[pp] = mfma16(F16fr, b3, z4); zi[pp] = mfma16(F16fi, b3, z4);
    }
    v4u a4[2];
    if (!special) {
      a4[0] = cmul_pack<false>(zr[0], zi[0], Wc.r0, Wc.i0);
      a4[1] = cmul_pack<false>(zr[1], zi[1], Wc.r1, Wc.i1);
    } else {
      BAR_LGKM();
      LAS char* Z = I0;
#pragma unroll
      for (int pp = 0; pp < 2; ++pp)
#pragma unroll
        for (int r = 0; r < 4; ++r) { f32x2 v = {zr[pp][r], zi[pp][r]}; *(LAS f32x2*)(Z + ((w * 2 + pp) * 256 + l15 + 16 * (4 * g + r)) * 8) = v; }
      BAR_LGKM();
#pragma unroll
      for (int pp = 0; pp < 2; ++pp) {
        f32x4 cr, ci;
#pragma unroll
        for (int r = 0; r < 4; ++r) {
          const int K = l15 + 16 * (4 * g + r), Kn = pp ? 255 - K : (256 - K) & 255;
          const f32x2 y = *(LAS f32x2*)(Z + ((((8 - w) & 7) * 2 + pp) * 256 + Kn) * 8);
          cr[r] = y[0]; ci[r] = -y[1];
        }
        const f32x4 ar = pp ? Wc.r1 : Wc.r0, ai = pp ? Wc.i1 : Wc.i0, br = pp ? Wd.r1 : Wd.r0, bi = pp ? Wd.i1 : Wd.i0;
        const f32x4 yr = zr[pp] * ar - zi[pp] * ai + cr * br - ci * bi, yi = zr[pp] * ai + zi[pp] * ar + cr * bi + ci * br;
        a4[pp].x = pk2(yr[0], yr[1]); a4[pp].y = pk2(yr[2], yr[3]); a4[pp].z = pk2(yi[0], yi[1]); a4[pp].w = pk2(yi[2], yi[3]);
      }
      BAR_LGKM();
    }
#pragma unroll
    for (int pp = 0; pp < 2; ++pp) {
      const f32x4 dr = mfma16(a4[pp], F16ir, z4), di = mfma16(a4[pp], F16ii, z4);
      const v4u a5 = cmul_pack<true>(dr, di, asf4(tb[pp ? 14 : 10]), asf4(tb[pp ? 15 : 11]));
      const f32x4 er = mfma16(a5, pp ? tb[4] : F16ir, z4), ei = mfma16(a5, pp ? tb[5] : F16ii, z4);
      const int n0 = 4 * w + 2 * pp, n1 = n0 + 1;
      const int ch = 2 * l15 + (g >> 1);
      v2u o; o.x = pk2(er[0], er[1]); o.y = pk2(er[2], er[3]);
      *(LAS v2u*)(I0 + n0 * 512 + ((ch ^ ((n0 & 3) << 2)) << 4) + 8 * (g & 1)) = o;
      o.x = pk2(ei[0], ei[1]); o.y = pk2(ei[2], ei[3]);
      *(LAS v2u*)(I0 + n1 * 512 + ((ch ^ ((n1 & 3) << 2)) << 4) + 8 * (g & 1)) = o;
    }
    bar_vm<0>();
    if (!published && tid == 0) flag_store(p.ws, flh_own, tag);
    FT(23);
    m5a = tb[8]; m5b = tb[9];
  }
  return tag;
}

DI void make_geo(Geo& G, const Params& p, int tz) {
  G.tid = tz; G.lane = G.tid & 63; G.w = __builtin_amdgcn_readfirstlane(G.tid >> 6);
  G.b = blockIdx.x;
  const int Lx = G.b >> 3, x8 = G.b & 7;
  {
    const int a = x8 & 3, h = x8 >> 2, rbi = Lx >> 3, c = Lx & 7;
    G.i = 2 * a + h; G.i1 = 2 * a + (rbi >> 1); G.rb = rbi & 1;
    G.fc = c < 4 ? 4 * h + c : 4 + 4 * h + c;
    G.at = rbi * 4 + c; G.m1 = ((rbi * 4 + c - 4) << 3) | x8;
  }
  G.hh = G.lane >> 5; G.blk = (G.lane >> 4) & 1; G.q4 = (G.lane & 15) >> 2; G.p4 = G.lane & 3;
  G.sx = make_srd(p.x, NS * ND * 2); G.sws = make_srd(p.ws, 16u << 20); G.sw2 = make_srd(p.w2, NF * ND * 2);
  G.jb = Lx >> 3; G.dt = Lx & 7; G.rh = G.w & 1; G.q = G.w >> 1;
  G.j = 64 * G.jb + 32 * G.rh + (G.lane & 31);
  const int rho = 16 * G.rh + (G.lane >> 3), chs = (G.lane & 7) ^ (4 * ((G.lane >> 4) & 1));
  G.vw20 = (unsigned)(rho * 1024 + G.dt * 128 + chs * 16); G.vw21 = G.vw20 + 8 * 1024;
  G.ldsw = uni((unsigned)(G.q * 4096 + G.rh * 2048));
  G.fq0b = uni((unsigned)(768 + 256 * G.q) * 1024u); G.fq1b = uni((unsigned)(192 * G.q) * 1024u);
}

__global__ __launch_bounds__(512, 2) void gm_kernel(Params p) {
  extern __shared__ __attribute__((aligned(16))) char smem[];
  LAS char* L = (LAS char*)smem;
  long long pad_t = 0;
  unsigned touch0;
  {
    unsigned long long pc; unsigned len;
    asm volatile("s_getpc_b64 %0\n.Lgm_code_begin:\n\ts_mov_b32 %1, .Lgm_code_end-.Lgm_code_begin" : "=s"(pc), "=s"(len));
    const unsigned line = (unsigned)((blockIdx.x >> 3) * 8 + (threadIdx.x >> 6)) * 128u;
    const char* a0 = (const char*)(pc & ~127ull) + (line < len ? line : 0u);
    asm volatile("global_load_dword %0, %1, off" : "=v"(touch0) : "v"(a0) : "memory");
  }
  STAMP(0);
  FT(0);
  Geo G;
  make_geo(G, p, threadIdx.x);
  const bool isA = G.fc < 8;
  Pro R;
  {
    const int lane = G.lane, w = G.w, i = G.i, i1 = G.i1, rb = G.rb, fc = G.fc, m1 = G.m1;
    const v4u sx = G.sx, sws = G.sws;
    R.sw1 = make_srd(p.w1, ND * NF * 2); R.swr = make_srd(p.wr, NS * 257 * 2); R.swi = make_srd(p.wi, NS * 257 * 2);
    const v4u sw1 = R.sw1, swr = R.swr, swi = R.swi;
    const int jg = 128 * rb + 8 * fc + w;
    LD16Z(R.gx, (unsigned)(lane * 16 + (i1 + 8 * jg) * 1024), sx, "");
    LD16Z(R.gv, (unsigned)lane * 16u, make_srd(p.gw, ND * 2), "");
    LD4Z(R.ep, WS_EPOCH, sws, "sc1");
    const int mW = m1 == 0 ? 128 * w : m1 + 256 * w;
    if (isA) {
      const int dt4 = G.at & 3;
      const unsigned voff = (unsigned)(lane >> 4) * 8192u + (unsigned)(((lane & 15) ^ ((lane >> 4) << 2)) << 4);
      const unsigned xl = uni(A_XIMG + (unsigned)w * 8192u), xs = uni((unsigned)i * 1024u + (unsigned)dt4 * 256u + (unsigned)w * 262144u);
      dma16<0, 0>(sx, voff, xl, xs); dma16<1024, 32768>(sx, voff, xl, xs); dma16<2048, 65536>(sx, voff, xl, xs); dma16<3072, 98304>(sx, voff, xl, xs);
      dma16<4096, 131072>(sx, voff, xl, xs); dma16<5120, 163840>(sx, voff, xl, xs); dma16<6144, 196608>(sx, voff, xl, xs); dma16<7168, 229376>(sx, voff, xl, xs);
    } else {
      const unsigned wsrc = w_src(mW, lane), wts = w_tail_src(mW, lane);
      LD16Z(R.wqr, wsrc, swr, ""); LD16Z(R.wqi, wsrc, swi, ""); LD2Z(R.wtr, wts, swr, ""); LD2Z(R.wti, wts, swi, "");
    }
    const int f0 = isA ? 128 * fc : 1024 + 96 * (fc - 8), nf = isA ? 128 : 96;
    {
      const int r0 = 16 * w + (lane >> 3), pos = lane & 7, r1 = r0 + 8;
      R.vx0 = (unsigned)(i1 * 1024 + (128 * rb + r0) * 8192 + ((pos ^ ((r0 >> 1) & 7)) << 4));
      R.vx1 = (unsigned)(i1 * 1024 + (128 * rb + r1) * 8192 + ((pos ^ ((r1 >> 1) & 7)) << 4));
      const int k0 = 8 * w + (lane >> 4), chunk = (lane & 15) ^ ((lane >> 4) << 2);
      const bool ok = chunk * 8 < nf;
      R.vw0 = ok ? (unsigned)(k0 * 3584 + f0 * 2 + chunk * 16) : 0x80000000u;
      R.vw1 = ok ? R.vw0 + 4u * 3584u : 0x80000000u;
    }
    R.g1l = uni((unsigned)w * 2048u);
    const unsigned vx0 = R.vx0, vx1 = R.vx1, vw0 = R.vw0, vw1 = R.vw1, g1l = R.g1l;
    asm volatile("s_barrier" ::: "memory");
    g1_issue(0);
    if (!isA && m1 == 0) {
      const unsigned ws2 = w_src(128 * w + 1024, lane), wt2 = w_tail_src(128 * w + 1024, lane);
      LD16Z(R.xqr, ws2, swr, ""); LD16Z(R.xqi, ws2, swi, ""); LD2Z(R.xtr, wt2, swr, ""); LD2Z(R.xti, wt2, swi, "");
    }
    asm volatile("s_barrier" ::: "memory");
    g1_issue(1);
    if (!isA) {
      asm volatile("s_barrier" ::: "memory");
      g1_issue(2); asm volatile("s_barrier" ::: "memory");
      g1_issue(3);
    }
    unsigned touch;
    TOUCH();
    R.touch = touch;
    if (isA) phaseA_genF(L, i, G.at >> 2, w, lane);
    if (isA) wait_vm<17>(); else wait_vm<21>();
    asm volatile("" : "+v"(R.ep), "+v"(R.gx), "+v"(R.gv) :: "memory");
    if (!isA) {
      wait_vm<17>();
      asm volatile("" : "+v"(R.wqr), "+v"(R.wqi), "+v"(R.wtr), "+v"(R.wti) :: "memory");
      if (m1 == 0) {
        wait_vm<13>();
        asm volatile("" : "+v"(R.xqr), "+v"(R.xqi), "+v"(R.xtr), "+v"(R.xti) :: "memory");
      }
    }
  }
  v4u gf[4], m5a, m5b;
  unsigned tag;
  if (isA) tag = phase1<true>(pad_t, p, L, G, R, m5a, m5b); else tag = phase1<false>(pad_t, p, L, G, R, m5a, m5b);
  asm volatile("" :: "v"(touch0));
  { int tz = threadIdx.x; asm volatile("" : "+v"(tz)); make_geo(G, p, tz); }
  const int tid = G.tid, lane = G.lane, w = G.w, b = G.b, i = G.i, jb = G.jb, dt = G.dt, rh = G.rh, q = G.q, j = G.j;
  const int hh = G.hh, blk = G.blk, q4 = G.q4, p4 = G.p4;
  const v4u sws = G.sws;
  const unsigned ldsw = G.ldsw;
  const int m1 = G.m1;
  STAMP(7);
  FT(25);
  if (!isA) {
    LAS char* I0 = L + 128 * 1024;
    LAS char* I1 = L + 128 * 1024 + 16384;
    LAS char* ta = I0 + (8 * hh + q4) * 512 + (((4 * w + 2 * blk + (p4 >> 1)) ^ (q4 << 2)) << 4) + 8 * (p4 & 1);
    const int n = lane & 31;
    LAS char* so = I1 + n * 512 + 8 * hh;
    f32x16 acc = {};
    acc = mfma32(tr_read2(ta, ta + 2048), m5a, acc);
    acc = mfma32(tr_read2(ta + 8192, ta + 10240), m5b, acc);
#pragma unroll
    for (int Gq = 0; Gq < 4; ++Gq) {
      v2u o; o.x = pk2(acc[4 * Gq], acc[4 * Gq + 1]); o.y = pk2(acc[4 * Gq + 2], acc[4 * Gq + 3]);
      *(LAS v2u*)(so + (((4 * w + Gq) ^ (n & 15)) << 4)) = o;
    }
    BAR_LGKM();
    const bool special = (m1 == 0);
    auto u_store = [&](int t2) {
      const int nn = t2 >> 5, ch = t2 & 31, ii = (nn >> 1) & 7, d0 = nn & 1;
      const int g0 = special ? 2 * ii : ii, g1 = special ? 2 * ii + 1 : 8 + ii;
      const v4u v0 = lds_ld16(I1 + nn * 512 + ((ch ^ (nn & 15)) << 4));
      const v4u v1 = lds_ld16(I1 + (nn + 16) * 512 + ((ch ^ (nn & 15)) << 4));
      const unsigned ub = WS_U + (unsigned)m1 * 16384u + (unsigned)(d0 * 512 + ch * 16);
      st16_sc1(sws, ub + (unsigned)g0 * 1024u, v0);
      st16_sc1(sws, ub + (unsigned)g1 * 1024u, v1);
    };
    if (w != 0) u_store(tid);
    if (w == 1) u_store(tid - 64);
    SCHED();
  }
  const int sz = (q4 >> 1) & 1;
  LAS char* ra0 = L + q * 4096 + (4 * hh + q4) * 128 + (((0 ^ (4 * sz)) + 2 * blk + (p4 >> 1)) << 4) + 8 * (p4 & 1);
  LAS char* ra1 = L + q * 4096 + (4 * hh + q4) * 128 + (((4 ^ (4 * sz)) + 2 * blk + (p4 >> 1)) << 4) + 8 * (p4 & 1);
  v4u wp[4][4];
  BAR_LGKM();
#pragma unroll
  for (int t = 0; t < 4; ++t) {
    LAS char* s0 = ra0 + t * 16384; LAS char* s1 = ra1 + t * 16384;
    wp[t][0] = tr_read2(s0, s0 + 1024); wp[t][1] = tr_read2(s1, s1 + 1024);
    wp[t][2] = tr_read2(s0 + 2048, s0 + 3072); wp[t][3] = tr_read2(s1 + 2048, s1 + 3072);
  }
#define W2_8_11() do { w2_issue1(G, 8, 0); w2_issue1(G, 8, 1); w2_issue1(G, 9, 0); w2_issue1(G, 9, 1); w2_issue1(G, 10, 0); w2_issue1(G, 10, 1); w2_issue1(G, 11, 0); w2_issue1(G, 11, 1); } while (0)
  if (isA) { BAR_LGKM(); W2_8_11(); }
  const unsigned flh = WS_FLH + (unsigned)((i * 2 + (jb >> 1)) * 16) * FS;
  if (w == 0) poll_words(sws, flh, 16, tag, lane);
  BAR_LGKM();
  STAMP(8);
  FT(26);
  const unsigned vh = (unsigned)lane * 16u;
  const unsigned hbase = WS_H + (unsigned)((i * 8 + 2 * jb + rh) * 112) * 1024u;
  const unsigned hb0 = uni(hbase + (G.fq0b >> 4)), hb1 = uni(hbase + (G.fq1b >> 4));
  v2u gg;
  v4u hf[14][2];
#define H_ISSUE(t) do { LD16I(hf[t][0], vh, sws, ((t) < 8 ? hb0 : hb1), (2 * ((t) & 7)) * 1024, "sc1"); LD16I(hf[t][1], vh, sws, ((t) < 8 ? hb0 : hb1), (2 * ((t) & 7) + 1) * 1024, "sc1"); } while (0)
  H_ISSUE(0); H_ISSUE(1);
  asm volatile("s_barrier" ::: "memory");
  H_ISSUE(2); H_ISSUE(3); H_ISSUE(4); H_ISSUE(5); H_ISSUE(6);
  LD8Z(gg, WS_GATE + (unsigned)(i * 256 + j) * 8u, sws, "sc1");
  SCHED();
  f32x16 accb[2] = {}, acca[2] = {};
  v4u wf[2][4];
  unsigned fl0, fl1;
  v4u ub0, ub1, ub2, ub3;
  const unsigned vu = WS_U + (unsigned)(((64 * q + 16 * rh + (lane >> 3)) * 8 + i) * 1024 + dt * 128 + ((lane & 7) ^ (4 * ((lane >> 4) & 1))) * 16);
#define G2_BAR(n, t) asm volatile("s_waitcnt vmcnt(" STR(n) ") lgkmcnt(0)\n\ts_barrier" : "+v"(hf[t][0]), "+v"(hf[t][1]) :: "memory")
#define G2_CORE(t, c, n, RD, acc, b0, b1, X0, X1, X2)                                             \
  {                                                                                                 \
    FT(30 + (t));                                                                                   \
    LAS char* s0 = ra0 + (((t) + 1) & 7) * 16384; LAS char* s1 = ra1 + (((t) + 1) & 7) * 16384;    \
    asm volatile("" : "+v"(c[0]), "+v"(c[1]), "+v"(c[2]), "+v"(c[3]));                             \
    asm volatile("" :: "v"(acc[0]), "v"(acc[1]));                                                   \
    SCHED();                                                                                        \
    acc[0] = mfma32(c[0], b0, acc[0]); if (RD) n[0] = tr_read2(s0, s0 + 1024); SCHED();            \
    acc[1] = mfma32(c[1], b0, acc[1]); if (RD) n[1] = tr_read2(s1, s1 + 1024); X0; SCHED();        \
    acc[0] = mfma32(c[2], b1, acc[0]); if (RD) n[2] = tr_read2(s0 + 2048, s0 + 3072); X1; SCHED(); \
    acc[1] = mfma32(c[3], b1, acc[1]); if (RD) n[3] = tr_read2(s1 + 2048, s1 + 3072); X2; SCHED(); \
  }
#define G2_PRE(t, X2) G2_CORE(t, wp[t], wf[0], (t) == 3, accb, hf[t][0], hf[t][1], if (!isA) w2_issue1(G, (t) + 8, 0), if (!isA) w2_issue1(G, (t) + 8, 1), X2)
#define G2_STEP(t, X0, X1, X2) G2_CORE(t, wf[(t) & 1], wf[((t) + 1) & 1], true, accb, hf[t][0], hf[t][1], X0, X1, X2)
  G2_BAR(13, 0);
  if (!isA && tid == 0) flag_store(p.ws, WS_FLB + (unsigned)m1 * FS, tag);
  STAMP(14);
  G2_PRE(0, H_ISSUE(7))
  G2_BAR(13, 1); G2_PRE(1, H_ISSUE(8))
  G2_BAR(13, 2); G2_PRE(2, H_ISSUE(9))
  G2_BAR(13, 3); G2_PRE(3, H_ISSUE(10))
  G2_BAR(13, 4); G2_STEP(4, w2_issue1(G, 12, 0), w2_issue1(G, 12, 1), H_ISSUE(11))
  G2_BAR(15, 5); G2_STEP(5, w2_issue1(G, 13, 0), w2_issue1(G, 13, 1), H_ISSUE(12))
  if (w == 0) { LD4Z(fl0, WS_FLB + (unsigned)lane * FS, sws, "sc1"); LD4Z(fl1, WS_FLB + (64u + (unsigned)lane) * FS, sws, "sc1"); }
  G2_BAR(17, 6); G2_STEP(6, NOP, NOP, H_ISSUE(13))
  STAMP(9);
  G2_BAR(16, 7); G2_STEP(7, NOP, NOP, NOP)
  LAS unsigned* verdict = (LAS unsigned*)(L + 128 * 1024);
  if (w == 0) {
    asm volatile("s_waitcnt vmcnt(2)" : "+v"(fl0), "+v"(fl1) :: "memory");
    const unsigned ok = __all(fl0 == tag && fl1 == tag) ? 1u : 0u;
    if (lane == 0) *verdict = ok;
  }
  G2_BAR(14, 8);
  const int u_early = __builtin_amdgcn_readfirstlane(*(volatile LAS unsigned*)verdict);
  STAMP(15);
#define U_ISSUE() do { LD16Z(ub0, vu, sws, "sc1"); LD16Z(ub1, vu + 8u * 8192u, sws, "sc1"); LD16Z(ub2, vu + 32u * 8192u, sws, "sc1"); LD16Z(ub3, vu + 40u * 8192u, sws, "sc1"); } while (0)
  if (u_early) U_ISSUE();
  GRot grot;
  G2_STEP(8, gen_g_init(G, grot), NOP, gen_g_frag<0>(G, grot, gf[0]))
  unsigned e0, e1, e2, e3;
  if (b == 0 && w == 1) {
    LD4Z(e0, WS_FLH + (unsigned)lane * FS, sws, "sc1"); LD4Z(e1, WS_FLH + (64u + (unsigned)lane) * FS, sws, "sc1");
    LD4Z(e2, WS_FLH + (128u + (unsigned)lane) * FS, sws, "sc1"); LD4Z(e3, WS_FLH + (192u + (unsigned)lane) * FS, sws, "sc1");
  }
  G2_BAR(12, 9); G2_STEP(9, NOP, NOP, gen_g_frag<1>(G, grot, gf[1]))
  G2_BAR(10, 10); G2_STEP(10, NOP, NOP, gen_g_frag<2>(G, grot, gf[2]))
  G2_BAR(6, 11); G2_STEP(11, NOP, NOP, gen_g_frag<3>(G, grot, gf[3]))
  G2_BAR(2, 12);
  G2_STEP(12, NOP, NOP, NOP)
#define U_PUT() do {                                                                                                        \
    asm volatile("s_waitcnt vmcnt(0)" : "+v"(ub0), "+v"(ub1), "+v"(ub2), "+v"(ub3) :: "memory");                             \
    *(LAS v4u*)(L + 6 * 16384 + ldsw + lane * 16) = ub0; *(LAS v4u*)(L + 6 * 16384 + ldsw + 1024 + lane * 16) = ub1;         \
    *(LAS v4u*)(L + 7 * 16384 + ldsw + lane * 16) = ub2; *(LAS v4u*)(L + 7 * 16384 + ldsw + 1024 + lane * 16) = ub3; } while (0)
  if (u_early) U_PUT();
  G2_BAR(0, 13);
  asm volatile("" : "+v"(gg));
  if (b == 0 && w == 1) {
    asm volatile("" : "+v"(e0), "+v"(e1), "+v"(e2), "+v"(e3));
    if (!__all(e0 == tag && e1 == tag && e2 == tag && e3 == tag)) {
#pragma unroll 1
      for (int u = 0; u < 4; ++u) poll_words(sws, WS_FLH + 64u * FS * u, 64, tag, lane);
    }
    if (lane == 0) flag_store(p.ws, WS_EPOCH, tag);
  }
  G2_CORE(13, wf[1], wf[0], u_early, accb, hf[13][0], hf[13][1], NOP, NOP, NOP)
  if (!u_early) {
    if (w == 0) { poll_words(sws, WS_FLB, 64, tag, lane); poll_words(sws, WS_FLB + 64u * FS, 64, tag, lane); }
    BAR_LGKM();
    U_ISSUE();
    U_PUT();
    BAR_LGKM();
    LAS char* s0 = ra0 + 6 * 16384; LAS char* s1 = ra1 + 6 * 16384;
    wf[0][0] = tr_read2(s0, s0 + 1024); wf[0][1] = tr_read2(s1, s1 + 1024); wf[0][2] = tr_read2(s0 + 2048, s0 + 3072); wf[0][3] = tr_read2(s1 + 2048, s1 + 3072);
  }
  STAMP(10);
  BAR_LGKM(); G2_CORE(14, wf[0], wf[1], true, acca, gf[0], gf[1], NOP, NOP, NOP)
  BAR_LGKM(); G2_CORE(15, wf[1], wf[0], false, acca, gf[2], gf[3], NOP, NOP, NOP)
  FT(50);
  {
    const float og = asf(gg.y), gs = asf(gg.x) * (1.f / 524288.f);
    const int row = 32 * rh + (lane & 31);
#pragma unroll
    for (int dh = 0; dh < 2; ++dh)
#pragma unroll
      for (int G = 0; G < 4; ++G) {
        f32x4 v;
#pragma unroll
        for (int e = 0; e < 4; ++e) v[e] = __builtin_fmaf(og, accb[dh][4 * G + e], gs * acca[dh][4 * G + e]);
        *(LAS f32x4*)(L + q * 16384 + row * 256 + (((8 * dh + 2 * G + hh) ^ (row & 7)) << 4)) = v;
      }
  }
  BAR_LGKM();
  FT(51);
  {
    const int row = tid >> 3, c8 = tid & 7;
    f32x4 s0 = {0.f, 0.f, 0.f, 0.f}, s1 = s0;
#pragma unroll
    for (int qq = 0; qq < 4; ++qq) {
      s0 += *(LAS f32x4*)(L + qq * 16384 + row * 256 + (((2 * c8) ^ (row & 7)) << 4));
      s1 += *(LAS f32x4*)(L + qq * 16384 + row * 256 + (((2 * c8 + 1) ^ (row & 7)) << 4));
    }
    v4u o; o.x = pk2(s0[0], s0[1]); o.y = pk2(s0[2], s0[3]); o.z = pk2(s1[0], s1[1]); o.w = pk2(s1[2], s1[3]);
    *(GAS v4u*)((GAS char*)p.out + (size_t)(i + 8 * (64 * jb + row)) * 1024 + dt * 128 + c8 * 16) = o;
  }
  asm volatile(".Lgm_code_end:");
  STAMP(11);
  while (__builtin_amdgcn_s_memrealtime() - pad_t < 3000) {}
}

}

extern "C" void kernel_launch(void* const* d_in, const int* in_sizes, int n_in, void* d_out, int out_size, void* d_ws, size_t ws_size,
                              hipStream_t stream) {
  Params p;
  p.x = d_in[0]; p.wr = d_in[1]; p.wi = d_in[2]; p.w1 = d_in[4]; p.w2 = d_in[5]; p.gw = d_in[6];
  p.out = d_out; p.ws = d_ws;
  (void)hipFuncSetAttribute((const void*)gm_kernel, hipFuncAttributeMaxDynamicSharedMemorySize, LDS_BYTES);
  gm_kernel<<<256, 512, LDS_BYTES, stream>>>(p);
}
```

```cpp
#include <hip/hip_runtime.h>
#include <hip/hip_bf16.h>
#include <stdint.h>

namespace {

typedef unsigned v4u __attribute__((ext_vector_type(4)));
typedef unsigned v2u __attribute__((ext_vector_type(2)));
typedef float f32x16 __attribute__((ext_vector_type(16)));
typedef float f32x4 __attribute__((ext_vector_type(4)));
typedef float f32x2 __attribute__((ext_vector_type(2)));
typedef __bf16 bf16x8 __attribute__((ext_vector_type(8)));
typedef __bf16 bf16x2 __attribute__((ext_vector_type(2)));
typedef short s16x4 __attribute__((ext_vector_type(4)));
#define LAS __attribute__((address_space(3)))
#define GAS __attribute__((address_space(1)))
#define DI __device__ __forceinline__

constexpr int NS = 2048, ND = 512, NF = 1792;
constexpr unsigned WS_EPOCH = 0;
constexpr unsigned FS = 64;
constexpr unsigned WS_FLA = 4096;
constexpr unsigned WS_FLH = 16384;
constexpr unsigned WS_FLB = 32768;
constexpr unsigned WS_GATE = 65536;
constexpr unsigned WS_T = 1u << 20;
constexpr unsigned WS_U = 3u << 20;
constexpr unsigned WS_H = 5u << 20;
constexpr unsigned H_BYTES = 8u * 8u * 112u * 1024u;
constexpr int LDS_BYTES = 160 * 1024;
constexpr unsigned kSpinCap = 1u << 20;

struct Params {
  const void* x; const void* wr; const void* wi; const void* w1; const void* w2; const void* gw;
  void* out; void* ws;
};

constexpr double kPi = 3.14159265358979323846;
constexpr double ccos(double x) { double t = 1, s = 1; for (int n = 1; n < 16; ++n) { t *= -x * x / ((2 * n - 1) * (2 * n)); s += t; } return s; }
constexpr double cosn(int e, int n) { e = ((e % n) + n) % n; double x = 2 * kPi * e / n; if (x > kPi) x -= 2 * kPi; return ccos(x); }
constexpr double wre(int e, int n) { return cosn(e, n); }
constexpr double wim(int e, int n) { return -cosn(4 * e - n, 4 * n); }
constexpr unsigned bfb(double v) { float f = (float)v; unsigned u = __builtin_bit_cast(unsigned, f); u += 0x7fffu + ((u >> 16) & 1u); return u >> 16; }
constexpr unsigned bf2(double a, double b) { return bfb(a) | (bfb(b) << 16); }
constexpr unsigned fbits(double v) { return __builtin_bit_cast(unsigned, (float)v); }
struct Tab { unsigned v[16][64][4]; };
constexpr Tab make_tab() {
  Tab t{};
  for (int l = 0; l < 64; ++l) {
    const int g = l >> 4, l15 = l & 15, hh = l >> 5, n = l & 31;
    for (int mtx = 0; mtx < 3; ++mtx) {
      double r[4] = {}, im[4] = {};
      for (int q = 0; q < 4; ++q) {
        const int a = 4 * g + q;
        if (mtx == 0) { r[q] = wre(l15 * a, 16); im[q] = wim(l15 * a, 16); }
        else if (mtx == 1) { r[q] = wre(a * (2 * l15 + 1), 32); im[q] = wim(a * (2 * l15 + 1), 32); }
        else { r[q] = wre(l15 * (2 * a + 1), 32); im[q] = -wim(l15 * (2 * a + 1), 32); }
      }
      t.v[2 * mtx][l][0] = bf2(r[0], r[1]); t.v[2 * mtx][l][1] = bf2(r[2], r[3]); t.v[2 * mtx][l][2] = bf2(-im[0], -im[1]); t.v[2 * mtx][l][3] = bf2(-im[2], -im[3]);
      t.v[2 * mtx + 1][l][0] = bf2(im[0], im[1]); t.v[2 * mtx + 1][l][1] = bf2(im[2], im[3]); t.v[2 * mtx + 1][l][2] = bf2(r[0], r[1]); t.v[2 * mtx + 1][l][3] = bf2(r[2], r[3]);
    }
    {
      const int m2 = n >> 2, pp = (n >> 1) & 1, cp = n & 1;
      double v0[8] = {}, v1[8] = {};
      for (int jj = 0; jj < 8; ++jj) {
        const int ii = (8 * hh + jj) >> 1, d0 = jj & 1;
        const double sg = (d0 & pp) ? -1.0 : 1.0, orr = sg * wre(ii * m2, 8), oi = sg * wim(ii * m2, 8);
        v0[jj] = cp ? oi : orr; v1[jj] = cp ? orr : -oi;
      }
      for (int q = 0; q < 4; ++q) { t.v[6][l][q] = bf2(v0[2 * q], v0[2 * q + 1]); t.v[7][l][q] = bf2(v1[2 * q], v1[2 * q + 1]); }
    }
    {
      const int cq = n >> 4, ii = (n >> 1) & 7, d0 = n & 1;
      for (int s = 0; s < 2; ++s) {
        double v[8] = {};
        for (int jj = 0; jj < 8; ++jj) {
          const int m2k = 4 * s + 2 * hh + (jj >> 2), pk = (jj >> 1) & 1, ck = jj & 1;
          const double sg = (d0 & pk) ? -1.0 : 1.0, orr = sg * wre(ii * m2k, 8), oi = -sg * wim(ii * m2k, 8);
          v[jj] = cq ? (ck ? orr : oi) : (ck ? -oi : orr);
        }
        for (int q = 0; q < 4; ++q) t.v[8 + s][l][q] = bf2(v[2 * q], v[2 * q + 1]);
      }
    }
    for (int q = 0; q < 4; ++q) {
      const int a = 4 * g + q;
      t.v[10][l][q] = fbits(wre(a * (2 * l15), 512));      t.v[11][l][q] = fbits(wim(a * (2 * l15), 512));
      t.v[12][l][q] = fbits(wre(a * (2 * l15 + 1), 512));  t.v[13][l][q] = fbits(wim(a * (2 * l15 + 1), 512));
      t.v[14][l][q] = fbits(wre(l15 * (2 * a + 1), 512));  t.v[15][l][q] = fbits(wim(l15 * (2 * a + 1), 512));
    }
  }
  return t;
}
__device__ const Tab kTab = make_tab();

constexpr unsigned STAMP_OFF = 512 * 1024;
#define STAMP(k) do {} while (0)
#define FT(k) do {} while (0)

DI v4u make_srd(const void* p, unsigned bytes) {
  const unsigned long long a = (unsigned long long)p;
  v4u r; r.x = (unsigned)a; r.y = (unsigned)(a >> 32) & 0xffffu; r.z = bytes; r.w = 0x00020000u;
  return r;
}
template <unsigned LDSI, unsigned SOI> DI void dma16(v4u srd, unsigned voff, unsigned ldsb, unsigned sob) {
  unsigned tmp;
  asm volatile("s_add_u32 m0, %3, %5\n\ts_add_u32 %0, %4, %6\n\tbuffer_load_dwordx4 %1, %2, %0 offen lds"
               : "=&s"(tmp) : "v"(voff), "s"(srd), "s"(ldsb), "s"(sob), "i"(LDSI), "i"(SOI) : "memory", "scc");
}
template <unsigned LDSI, unsigned SOI> DI void dma16z(v4u srd, unsigned voff, unsigned ldsb) {
  unsigned tmp;
  asm volatile("s_add_u32 m0, %3, %4\n\ts_mov_b32 %0, %5\n\tbuffer_load_dwordx4 %1, %2, %0 offen lds"
               : "=&s"(tmp) : "v"(voff), "s"(srd), "s"(ldsb), "i"(LDSI), "i"(SOI) : "memory", "scc");
}
DI unsigned uni(unsigned v) { return (unsigned)__builtin_amdgcn_readfirstlane((int)v); }
#define LD16(dst, voffv, srd, soffv, flags) asm volatile("buffer_load_dwordx4 %0, %1, %2, %3 offen " flags : "=v"(dst) : "v"(voffv), "s"(srd), "s"(soffv) : "memory")
#define LD16I(dst, voffv, srd, sob, soi, flags) do { unsigned tmp_; asm volatile("s_add_u32 %1, %4, %5\n\tbuffer_load_dwordx4 %0, %2, %3, %1 offen " flags : "=v"(dst), "=&s"(tmp_) : "v"(voffv), "s"(srd), "s"(sob), "i"(soi) : "memory", "scc"); } while (0)
#define LD16Z(dst, voffv, srd, flags) asm volatile("buffer_load_dwordx4 %0, %1, %2, 0 offen " flags : "=v"(dst) : "v"(voffv), "s"(srd) : "memory")
#define LD4Z(dst, voffv, srd, flags) asm volatile("buffer_load_dword %0, %1, %2, 0 offen " flags : "=v"(dst) : "v"(voffv), "s"(srd) : "memory")
#define LD2Z(dst, voffv, srd, flags) asm volatile("buffer_load_ushort %0, %1, %2, 0 offen " flags : "=v"(dst) : "v"(voffv), "s"(srd) : "memory")
#define LD8Z(dst, voffv, srd, flags) asm volatile("buffer_load_dwordx2 %0, %1, %2, 0 offen " flags : "=v"(dst) : "v"(voffv), "s"(srd) : "memory")
#define LD8(dst, voffv, srd, soffv, flags) asm volatile("buffer_load_dwordx2 %0, %1, %2, %3 offen " flags : "=v"(dst) : "v"(voffv), "s"(srd), "s"(soffv) : "memory")
#define LD4(dst, voffv, srd, soffv, flags) asm volatile("buffer_load_dword %0, %1, %2, %3 offen " flags : "=v"(dst) : "v"(voffv), "s"(srd), "s"(soffv) : "memory")
#define LD2(dst, voffv, srd, soffv, flags) asm volatile("buffer_load_ushort %0, %1, %2, %3 offen " flags : "=v"(dst) : "v"(voffv), "s"(srd), "s"(soffv) : "memory")
#define XSTR(x) #x
#define STR(x) XSTR(x)
#define BAR_VM(n) asm volatile("s_waitcnt vmcnt(" STR(n) ") lgkmcnt(0)\n\ts_barrier" ::: "memory")
#define BAR_LGKM() asm volatile("s_waitcnt lgkmcnt(0)\n\ts_barrier" ::: "memory")
#define SCHED() __builtin_amdgcn_sched_barrier(0)
template <int N> DI void bar_vm() { asm volatile("s_waitcnt vmcnt(%0) lgkmcnt(0)\n\ts_barrier" :: "i"(N) : "memory"); }
template <int N> DI void wait_vm() { asm volatile("s_waitcnt vmcnt(%0)" :: "i"(N) : "memory"); }

DI unsigned pk2(float a, float b) { f32x2 p = {a, b}; return __builtin_bit_cast(unsigned, __builtin_convertvector(p, bf16x2)); }
DI float bflo(unsigned u) { return __builtin_bit_cast(float, u << 16); }
DI float bfhi(unsigned u) { return __builtin_bit_cast(float, u & 0xffff0000u); }
DI float asf(unsigned u) { return __builtin_bit_cast(float, u); }
DI f32x16 mfma32(v4u a, v4u b, f32x16 c) {
  return __builtin_amdgcn_mfma_f32_32x32x16_bf16(__builtin_bit_cast(bf16x8, a), __builtin_bit_cast(bf16x8, b), c, 0, 0, 0);
}
DI f32x4 mfma16(v4u a, v4u b, f32x4 c) {
  return __builtin_amdgcn_mfma_f32_16x16x32_bf16(__builtin_bit_cast(bf16x8, a), __builtin_bit_cast(bf16x8, b), c, 0, 0, 0);
}
DI v2u tr_read(LAS char* p) { return __builtin_bit_cast(v2u, __builtin_amdgcn_ds_read_tr16_b64_v4i16((LAS s16x4*)p)); }
DI v4u tr_read2(LAS char* p0, LAS char* p1) { v2u a = tr_read(p0), b = tr_read(p1); v4u r; r.x = a.x; r.y = a.y; r.z = b.x; r.w = b.y; return r; }
DI v4u lds_ld16(LAS char* p) { return *(LAS v4u*)p; }
DI float cosrev(float r) { return __builtin_amdgcn_cosf(r); }
DI float sinrev(float r) { return __builtin_amdgcn_sinf(r); }
DI __amdgpu_buffer_rsrc_t as_rsrc(v4u srd) { return __builtin_amdgcn_make_buffer_rsrc((void*)(((unsigned long long)srd.y << 32) | srd.x), 0, srd.z, 0x00020000); }
DI void st16_sc1(v4u srd, unsigned voff, v4u v) { __builtin_amdgcn_raw_buffer_store_b128(v, as_rsrc(srd), voff, 0, 16); }
DI void flag_store(void* ws, unsigned off, unsigned tag) { __hip_atomic_store((GAS unsigned*)((GAS char*)ws + off), tag, __ATOMIC_RELAXED, __HIP_MEMORY_SCOPE_AGENT); }
template <int CTRL, int RMASK> DI float dpp_add(float v) { return v + asf((unsigned)__builtin_amdgcn_update_dpp(0, (int)__builtin_bit_cast(unsigned, v), CTRL, RMASK, 0xf, true)); }
DI void poll_words(v4u sws, unsigned off, int n, unsigned tag, int lane) {
  const unsigned vo = off + FS * (unsigned)(lane < n ? lane : 0);
#pragma unroll 1
  for (unsigned spins = 0; spins < kSpinCap; ++spins) {
    unsigned f;
    asm volatile("buffer_load_dword %0, %1, %2, 0 offen sc1\n\ts_waitcnt vmcnt(0)" : "=&v"(f) : "v"(vo), "s"(sws) : "memory");
    if (__all(f == tag)) return;
    __builtin_amdgcn_s_sleep(4);
  }
}

constexpr unsigned A_XIMG = 64 * 1024, A_FIMG = 128 * 1024;
DI void phaseA_genF(LAS char* L, int i, int mt, int w, int lane) {
  const int mh = w >> 2, sg = w & 3, n = lane & 31, hh = lane >> 5;
  const int m1c = 64 * mt + 32 * mh + n, m1 = m1c >> 1, c = m1c & 1;
  const int j0 = 64 * sg + 32 * hh;
  const float ph = c ? 0.25f : 0.f;
  const float k2 = 2.f * cosrev((float)(8 * m1) * (1.f / 2048.f));
  float vm = cosrev((float)(((i + 8 * (j0 - 1)) * m1) & 2047) * (1.f / 2048.f) + ph);
  float v0 = cosrev((float)(((i + 8 * j0) * m1) & 2047) * (1.f / 2048.f) + ph);
  const unsigned cst = c ? 0x3F800000u : 0x00003F80u;
  const bool sp = m1c < 2;
#pragma unroll
  for (int s = 0; s < 4; ++s) {
    float e[8];
#pragma unroll
    for (int q = 0; q < 8; ++q) { e[q] = v0; const float vn = __builtin_fmaf(k2, v0, -vm); vm = v0; v0 = vn; }
    v4u f; f.x = pk2(e[0], e[1]); f.y = pk2(e[2], e[3]); f.z = pk2(e[4], e[5]); f.w = pk2(e[6], e[7]);
    if (mt == 0 && w < 4) { f.x = sp ? cst : f.x; f.y = sp ? cst : f.y; f.z = sp ? cst : f.z; f.w = sp ? cst : f.w; }
    *(LAS v4u*)(L + A_FIMG + (mh * 16 + sg * 4 + s) * 1024 + lane * 16) = f;
  }
}

struct Frag1 { v4u a[4], b0[4], b1[4]; };
DI void g1_read(Frag1& f, LAS char* wa, LAS char* xb0, LAS char* xb1, LAS char* xb2, LAS char* xb3, int slot) {
  const int so = slot * 32768;
  f.a[0] = tr_read2(wa + so, wa + so + 1024);              f.b0[0] = lds_ld16(xb0 + so); f.b1[0] = lds_ld16(xb0 + so + 4096);
  f.a[1] = tr_read2(wa + so + 4096, wa + so + 5120);       f.b0[1] = lds_ld16(xb1 + so); f.b1[1] = lds_ld16(xb1 + so + 4096);
  f.a[2] = tr_read2(wa + so + 8192, wa + so + 9216);       f.b0[2] = lds_ld16(xb2 + so); f.b1[2] = lds_ld16(xb2 + so + 4096);
  f.a[3] = tr_read2(wa + so + 12288, wa + so + 13312);     f.b0[3] = lds_ld16(xb3 + so); f.b1[3] = lds_ld16(xb3 + so + 4096);
}
DI unsigned silu2(float a, float b) {
  const f32x2 h = {a, b}, t = h * -1.44269504088896341f;
  f32x2 e = {__builtin_amdgcn_exp2f(t[0]), __builtin_amdgcn_exp2f(t[1])};
  e = e + 1.f;
  const f32x2 r = {__builtin_amdgcn_rcpf(e[0]), __builtin_amdgcn_rcpf(e[1])};
  return __builtin_bit_cast(unsigned, __builtin_convertvector(h * r, bf16x2));
}

DI f32x2 lo2(const f32x4& v) { return __builtin_shufflevector(v, v, 0, 1); }
DI f32x2 hi2(const f32x4& v) { return __builtin_shufflevector(v, v, 2, 3); }
DI unsigned pk2v(f32x2 p) { return __builtin_bit_cast(unsigned, __builtin_convertvector(p, bf16x2)); }
DI f32x4 asf4(v4u v) { return __builtin_bit_cast(f32x4, v); }
template <bool CONJ> DI v4u cmul_pack(const f32x4& re, const f32x4& im, const f32x4& tr, const f32x4& ti) {
  const f32x2 r0 = lo2(re), r1 = hi2(re), i0 = lo2(im), i1 = hi2(im), c0 = lo2(tr), c1 = hi2(tr), s0 = lo2(ti), s1 = hi2(ti);
  f32x2 a0, a1, b0, b1;
  if (CONJ) {
    a0 = __builtin_elementwise_fma(r0, c0, i0 * s0); a1 = __builtin_elementwise_fma(r1, c1, i1 * s1);
    b0 = __builtin_elementwise_fma(i0, c0, -(r0 * s0)); b1 = __builtin_elementwise_fma(i1, c1, -(r1 * s1));
  } else {
    a0 = __builtin_elementwise_fma(r0, c0, -(i0 * s0)); a1 = __builtin_elementwise_fma(r1, c1, -(i1 * s1));
    b0 = __builtin_elementwise_fma(r0, s0, i0 * c0); b1 = __builtin_elementwise_fma(r1, s1, i1 * c1);
  }
  v4u r; r.x = pk2v(a0); r.y = pk2v(a1); r.z = pk2v(b0); r.w = pk2v(b1); return r;
}
struct cf { float r, i; };
DI cf cmul(cf a, cf b) { return {a.r * b.r - a.i * b.i, a.r * b.i + a.i * b.r}; }
DI cf cmulc(cf a, cf b) { return {a.r * b.r + a.i * b.i, a.i * b.r - a.r * b.i}; }
DI unsigned w_src(int m, int lane) {
  const int mm = (NS - m) & (NS - 1);
  const int e = lane < 32 ? m * 257 - (m & 1) : mm * 257 + (m & 1);
  return (unsigned)(e * 2 + (lane & 31) * 16);
}
DI unsigned w_tail_src(int m, int lane) {
  const int mm = (NS - m) & (NS - 1), l = lane & 3;
  return (unsigned)((l < 2 ? m * 257 + 255 + l : mm * 257 + (l - 2) * 256) * 2);
}
struct WTail { unsigned r255, r256, rm0, rm256, i255, i256, im0, im256; };
DI WTail w_tail(unsigned tr, unsigned ti) {
  return {(unsigned)__builtin_amdgcn_readlane((int)tr, 0), (unsigned)__builtin_amdgcn_readlane((int)tr, 1), (unsigned)__builtin_amdgcn_readlane((int)tr, 2), (unsigned)__builtin_amdgcn_readlane((int)tr, 3),
          (unsigned)__builtin_amdgcn_readlane((int)ti, 0), (unsigned)__builtin_amdgcn_readlane((int)ti, 1), (unsigned)__builtin_amdgcn_readlane((int)ti, 2), (unsigned)__builtin_amdgcn_readlane((int)ti, 3)};
}
struct WVal { f32x4 r0, i0, r1, i1; };
struct WRawL { unsigned r[4][2], i[4][2]; };
DI void w_put(LAS char* scr, v4u qr, v4u qi, int lane) { *(LAS v4u*)(scr + lane * 16) = qr; *(LAS v4u*)(scr + 1024 + lane * 16) = qi; }
DI int w_off(int m, int lane) { const int k1 = lane & 15, g = lane >> 4, odd = m & 1; return lane < 32 ? (2 * k1 + 128 * g + odd) * 2 : 512 + (511 - 2 * k1 - 128 * g - odd) * 2; }
DI void w_get(WRawL& o, LAS char* scr, int off, int lane, int r) {
  LAS unsigned* a = (LAS unsigned*)(scr + ((off + (lane < 32 ? 64 : -64) * r) & ~3));
  o.r[r][0] = a[0]; o.r[r][1] = a[1]; o.i[r][0] = a[256]; o.i[r][1] = a[257];
}
DI WVal w_unpack(const WRawL& x, const WTail& t, int off, int m, int lane) {
  const bool lo = lane < 32;
  const unsigned sh = (off & 2) * 8;
  unsigned rr[4], ii[4];
#pragma unroll
  for (int r = 0; r < 4; ++r) { rr[r] = __builtin_amdgcn_alignbit(x.r[r][1], x.r[r][0], sh); ii[r] = __builtin_amdgcn_alignbit(x.i[r][1], x.i[r][0], sh); }
  if (m & 1) { rr[3] = lane == 31 ? (rr[3] & 0xffffu) | (t.r255 << 16) : rr[3]; ii[3] = lane == 31 ? (ii[3] & 0xffffu) | (t.i255 << 16) : ii[3]; }
  else { rr[0] = lane == 32 ? (rr[0] & 0xffffu) | (t.rm256 << 16) : rr[0]; ii[0] = lane == 32 ? (ii[0] & 0xffffu) | (t.im256 << 16) : ii[0]; }
  const unsigned s0 = lo ? 16u : 0u, s1 = lo ? 0u : 16u, sg = lo ? 0u : 0x80000000u;
  WVal o;
#pragma unroll
  for (int r = 0; r < 4; ++r) {
    o.r0[r] = asf((rr[r] << s0) & 0xffff0000u); o.r1[r] = asf((rr[r] << s1) & 0xffff0000u);
    o.i0[r] = asf(((ii[r] << s0) & 0xffff0000u) ^ sg); o.i1[r] = asf(((ii[r] << s1) & 0xffff0000u) ^ sg);
  }
  const bool av = (lane & 31) == 0;
  const float xr = bflo(lo ? t.rm0 : t.r256), xi = asf(((lo ? t.im0 : t.i256) << 16) ^ (sg ^ 0x80000000u));
  o.r0[0] = av ? 0.5f * (o.r0[0] + xr) : o.r0[0];
  o.i0[0] = av ? 0.5f * (o.i0[0] + xi) : o.i0[0];
  return o;
}

DI void packed_coef(WVal& a, WVal& b, int m2) {
  const cf om = {cosrev((float)m2 * 0.0625f), -sinrev((float)m2 * 0.0625f)};
#pragma unroll
  for (int pp = 0; pp < 2; ++pp)
#pragma unroll
    for (int r = 0; r < 4; ++r) {
      const float war = pp ? a.r1[r] : a.r0[r], wai = pp ? a.i1[r] : a.i0[r], wbr = pp ? b.r1[r] : b.r0[r], wbi = pp ? b.i1[r] : b.i0[r];
      const cf sp = {war + wbr, wai + wbi}, sm = {war - wbr, wai - wbi};
      const cf t1 = cmulc(sm, om), t2 = cmul(sm, om);
      const cf cE = {sp.r - t1.i, sp.i + t1.r}, cO = {t2.r - sp.i, t2.i + sp.r};
      const float alr = 0.25f * (cE.r + cO.i), ali = 0.25f * (cE.i - cO.r), ber = 0.25f * (cE.r - cO.i), bei = 0.25f * (cE.i + cO.r);
      if (pp) { a.r1[r] = alr; a.i1[r] = ali; b.r1[r] = ber; b.i1[r] = bei; } else { a.r0[r] = alr; a.i0[r] = ali; b.r0[r] = ber; b.i0[r] = bei; }
    }
}

struct Geo {
  int tid, lane, w, b, i, i1, at, m1, rb, fc, hh, blk, q4, p4;
  int jb, dt, rh, q, j;
  v4u sx, sws, sw2;
  unsigned vw20, vw21, ldsw, fq0b, fq1b;
};
template <int T, int U> DI void w2_dma(const v4u& sw2, unsigned vw20, unsigned vw21, unsigned ldsw, unsigned fq0, unsigned fq1) {
  dma16<(T & 7) * 16384u + U * 1024u, (T < 8 ? T : T - 8) * 32768u>(sw2, U ? vw21 : vw20, ldsw, T < 8 ? fq0 : fq1);
}
#define w2_issue1(g, t, u) (w2_dma<t, u>((g).sw2, (g).vw20, (g).vw21, (g).ldsw, (g).fq0b, (g).fq1b))
struct GRot { float c1, s1, c4, s4, c, s; };
DI void gen_g_init(const Geo& g, GRot& r) {
  const int srow = g.i + 8 * g.j;
  const int mb = 32 * g.q + 2 * g.hh;
  const float r1 = (float)(srow & 2047) * (1.f / 2048.f), r4 = (float)((4 * srow) & 2047) * (1.f / 2048.f), r0 = (float)((mb * srow) & 2047) * (1.f / 2048.f);
  r.c1 = cosrev(r1); r.s1 = sinrev(r1); r.c4 = cosrev(r4); r.s4 = sinrev(r4); r.c = cosrev(r0); r.s = sinrev(r0);
}
template <int S> DI void gen_g_frag(const Geo& g, GRot& r, v4u& f) {
#pragma unroll
  for (int h = 0; h < 2; ++h) {
    const float cb = __builtin_fmaf(r.c, r.c1, -(r.s * r.s1)), sb = __builtin_fmaf(r.s, r.c1, r.c * r.s1);
    unsigned w0 = pk2(r.c, -r.s), w1 = pk2(cb, -sb);
    if (S == 0 && h == 0) w0 = (g.q == 0 && g.hh == 0) ? ((g.j & 1) ? 0x3F800000u : 0x00003F80u) : w0;
    if (h) { f.z = w0; f.w = w1; } else { f.x = w0; f.y = w1; }
    const float cn = __builtin_fmaf(r.c, r.c4, -(r.s * r.s4)), sn = __builtin_fmaf(r.s, r.c4, r.c * r.s4);
    r.c = cn; r.s = sn;
  }
  asm volatile("" : "+v"(f));
}
template <int N0, int N1, int NR> DI void bar_w(int w) { if (w == 0) bar_vm<N0>(); else if (w == 1) bar_vm<N1>(); else bar_vm<NR>(); }
#define NOP do {} while (0)

#define TLBW 0
#define TOUCH() do {                                                                                                      \
    const int gl = G.b * 256 + (w & 3) * 64 + lane;                                                                         \
    const char* base_ = gl < 14336 ? (const char*)p.w2 : gl < 28672 ? (const char*)p.w1 - 14336 * 128 : gl < 36896 ? (const char*)p.wr - 28672 * 128           \
                      : gl < 45120 ? (const char*)p.wi - 36896 * 128 : (const char*)p.x - 45120 * 128;                     \
    const char* ad_ = base_ + (size_t)(gl < 61504 ? gl : 45120) * 128;                                                      \
    if (w >= 4) ad_ = TLBW ? (w == 7 ? (const char*)p.out + lane * 32768 : (const char*)p.ws + (size_t)((w - 4) * 64 + lane + 8) * 65536 + 32768 - 128) : (const char*)p.x; \
    asm volatile("global_load_dword %0, %1, off" : "=v"(touch) : "v"(ad_) : "memory");                                     \
  } while (0)
#define g1_dma(kt, u) do {                                                                                         \
    if ((u) == 0) dma16z<((kt) & 3) * 32768u, (kt) * 128u>(sx, vx0, g1l);                                           \
    if ((u) == 1) dma16z<((kt) & 3) * 32768u + 1024u, (kt) * 128u>(sx, vx1, g1l);                                   \
    if ((u) == 2) dma16z<((kt) & 3) * 32768u + 16384u, (kt) * (64u * 3584u)>(sw1, vw0, g1l);                        \
    if ((u) == 3) dma16z<((kt) & 3) * 32768u + 17408u, (kt) * (64u * 3584u)>(sw1, vw1, g1l); } while (0)
#define g1_issue(kt) do { g1_dma(kt, 0); g1_dma(kt, 1); g1_dma(kt, 2); g1_dma(kt, 3); } while (0)
struct Pro { v4u sw1, swr, swi, gx, gv, wqr, wqi, xqr, xqi; unsigned ep, touch, wtr, wti, xtr, xti, vx0, vx1, vw0, vw1, g1l; };
template <bool ISA>
DI unsigned phase1(const Params& p, LAS char* L, const Geo& G, Pro& R, v4u& m5a, v4u& m5b
) {
  const int tid = G.tid, lane = G.lane, w = G.w, i = G.i, i1 = G.i1, rb = G.rb, fc = G.fc, hh = G.hh, blk = G.blk, q4 = G.q4, p4 = G.p4;
  const v4u sx = G.sx, sws = G.sws, sw1 = R.sw1, swr = R.swr, swi = R.swi;
  unsigned& ep = R.ep; v4u& gx = R.gx; v4u& gv = R.gv; unsigned& touch = R.touch;
  v4u& wqr = R.wqr; v4u& wqi = R.wqi; v4u& xqr = R.xqr; v4u& xqi = R.xqi; unsigned& wtr = R.wtr; unsigned& wti = R.wti; unsigned& xtr = R.xtr; unsigned& xti = R.xti;
  const unsigned vx0 = R.vx0, vx1 = R.vx1, vw0 = R.vw0, vw1 = R.vw1, g1l = R.g1l;
  const int f0 = ISA ? 128 * fc : 1024 + 96 * (fc - 8), nf = ISA ? 128 : 96;
  const int mt = G.at >> 2, dt4 = G.at & 3;
  const int m1 = G.m1;
  const int mW = m1 == 0 ? 128 * w : m1 + 256 * w;
  WVal Wc, Wd;
  const unsigned tag = (unsigned)__builtin_amdgcn_readfirstlane((int)ep) + 1u;
  float gate_g, gate_og;
  {
    float z = 0.f;
#pragma unroll
    for (int q = 0; q < 4; ++q) {
      const unsigned pr = pk2(bflo(gx[q]) * bflo(gv[q]), bfhi(gx[q]) * bfhi(gv[q]));
      z += bflo(pr) + bfhi(pr);
    }
    z = dpp_add<0x111, 0xf>(z); z = dpp_add<0x112, 0xf>(z); z = dpp_add<0x114, 0xf>(z); z = dpp_add<0x118, 0xf>(z);
    z = dpp_add<0x142, 0xa>(z); z = dpp_add<0x143, 0xc>(z);
    const float zb = bflo(pk2(z, 0.f));
    const float g = bflo(pk2(__builtin_amdgcn_rcpf(1.f + __builtin_amdgcn_exp2f(zb * -1.44269504088896341f)), 0.f));
    gate_g = asf((unsigned)__builtin_amdgcn_readlane((int)__builtin_bit_cast(unsigned, g), 63));
    gate_og = bflo(pk2(1.f - gate_g, 0.f));
  }
  if (!ISA) {
    LAS char* wscr0 = L + 128 * 1024 + w * 2048;
    const int woff = w_off(mW, lane);
    w_put(wscr0, wqr, wqi, lane);
    asm volatile("s_waitcnt lgkmcnt(0)" ::: "memory");
    WRawL wraw;
    w_get(wraw, wscr0, woff, lane, 0); w_get(wraw, wscr0, woff, lane, 1); w_get(wraw, wscr0, woff, lane, 2); w_get(wraw, wscr0, woff, lane, 3);
    Wc = w_unpack(wraw, w_tail(wtr, wti), woff, mW, lane);
    asm volatile("" : "+v"(Wc.r0), "+v"(Wc.i0), "+v"(Wc.r1), "+v"(Wc.i1));
  }
  if (ISA) {
    const int dq = w & 3, mh = w >> 2;
    LAS char* xa = L + A_XIMG + (32 * hh + q4) * 256 + (((4 * dq + 2 * blk + (p4 >> 1)) ^ (q4 << 2)) << 4) + 8 * (p4 & 1);
    LAS char* fb = L + A_FIMG + mh * 16384 + lane * 16;
    const int srow = 32 * mh + (lane & 31);
    LAS char* stw = L + A_FIMG + srow * 256 + 8 * hh;
    const int rrow = tid >> 3;
    LAS char* str = L + A_FIMG + rrow * 256;
    const unsigned tdst = WS_T + (unsigned)(((64 * mt + rrow) * 8 + i) * 1024 + dt4 * 256 + (tid & 7) * 16);
    if (w < 4) bar_vm<9>(); else BAR_LGKM();
    STAMP(1);
    f32x16 acc = {};
    v4u fa[2][4], fbv[2][4];
#pragma unroll
    for (int s = 0; s < 4; ++s) { fa[0][s] = tr_read2(xa + 8 * s * 256, xa + 8 * s * 256 + 1024); fbv[0][s] = lds_ld16(fb + s * 1024); }
#pragma unroll
    for (int g = 0; g < 4; ++g) {
      acc = mfma32(fa[g & 1][0], fbv[g & 1][0], acc);
      SCHED();
      if (g == 1) { if (w >= 4) bar_vm<9>(); else BAR_LGKM(); }
      if (g < 3) {
#pragma unroll
        for (int s = 0; s < 4; ++s) {
          const int ro = (64 * (g + 1) + 8 * s) * 256;
          fa[(g + 1) & 1][s] = tr_read2(xa + ro, xa + ro + 1024); fbv[(g + 1) & 1][s] = lds_ld16(fb + (4 * (g + 1) + s) * 1024);
        }
      }
      SCHED();
#pragma unroll
      for (int s = 1; s < 4; ++s) { acc = mfma32(fa[g & 1][s], fbv[g & 1][s], acc); if (g == 1 && s == 1) { g1_dma(2, 0); g1_dma(2, 1); } if (g == 1 && s == 2) { g1_dma(2, 2); g1_dma(2, 3); } SCHED(); }
    }
    BAR_LGKM();
    STAMP(2);
    g1_issue(3);
#pragma unroll
    for (int Gq = 0; Gq < 4; ++Gq) {
      v2u o; o.x = pk2(acc[4 * Gq], acc[4 * Gq + 1]); o.y = pk2(acc[4 * Gq + 2], acc[4 * Gq + 3]);
      *(LAS v2u*)(stw + (((4 * dq + Gq) ^ (srow & 15)) << 4)) = o;
    }
    BAR_LGKM();
    {
      const v4u v0 = lds_ld16(str + (((tid & 7) ^ (rrow & 15)) << 4)), v1 = lds_ld16(str + ((((tid & 7) + 8) ^ (rrow & 15)) << 4));
      st16_sc1(sws, tdst, v0);
      st16_sc1(sws, tdst + 128u, v1);
    }
    SCHED();
  }
  const int ft = w & 3, rh1 = w >> 2;
  const bool active = ft * 32 < nf;
  f32x16 acc1[2] = {};
  v4u tq0, tq1;
  v4u tb[16];
  unsigned fla;
  int t_ok = 0;
  unsigned tsrc0 = 0, tsrc1 = 0;
  const unsigned flaoff = WS_FLA + (unsigned)(((m1 >> 5) & 3) * 32 + (lane & 31)) * FS;
  LAS char* CTL = L + 128 * 1024;
  {
    LAS char* wa = L + 16384 + (8 * hh + q4) * 256 + (((4 * ft + 2 * blk + (p4 >> 1)) ^ (q4 << 2)) << 4) + 8 * (p4 & 1);
    const int r = 64 * rh1 + (lane & 31), fz = (r >> 1) & 7;
    LAS char* xb0 = L + r * 128 + (((0 + hh) ^ fz) << 4);
    LAS char* xb1 = L + r * 128 + (((2 + hh) ^ fz) << 4);
    LAS char* xb2 = L + r * 128 + (((4 + hh) ^ fz) << 4);
    LAS char* xb3 = L + r * 128 + (((6 + hh) ^ fz) << 4);
    Frag1 fr[2];
    bar_vm<ISA ? 15 : 13>();
    STAMP(3);
    if (lane == 0) { v2u gv2; gv2.x = __builtin_bit_cast(unsigned, gate_g); gv2.y = __builtin_bit_cast(unsigned, gate_og); *(LAS v2u*)(CTL + 64 + w * 8) = gv2; }
    g1_read(fr[0], wa, xb0, xb1, xb2, xb3, 0);
#define PIN1(c) asm volatile("" : "+v"(c.a[0]), "+v"(c.a[1]), "+v"(c.a[2]), "+v"(c.a[3]), "+v"(c.b0[0]), "+v"(c.b0[1]), "+v"(c.b0[2]), "+v"(c.b0[3]), \
                             "+v"(c.b1[0]), "+v"(c.b1[1]), "+v"(c.b1[2]), "+v"(c.b1[3]))
#define G1_STEP(kt, X0, X1, X2, X3, X4, X5, X6, X7)                                                                   \
    {                                                                                                                   \
      FT(10 + (kt));                                                                                                    \
      Frag1& c = fr[(kt) & 1];                                                                                          \
      Frag1& n = fr[((kt) + 1) & 1];                                                                                    \
      const int so = (((kt) + 1) & 3) * 32768;                                                                          \
      PIN1(c);                                                                                                          \
      asm volatile("" :: "v"(acc1[0]), "v"(acc1[1]));                                                                   \
      SCHED();                                                                                                          \
      acc1[0] = mfma32(c.a[0], c.b0[0], acc1[0]); if ((kt) < 7) n.a[0] = tr_read2(wa + so, wa + so + 1024); X0; SCHED(); \
      acc1[1] = mfma32(c.a[0], c.b1[0], acc1[1]); if ((kt) < 7) { n.b0[0] = lds_ld16(xb0 + so); n.b1[0] = lds_ld16(xb0 + so + 4096); } X1; SCHED(); \
      acc1[0] = mfma32(c.a[1], c.b0[1], acc1[0]); if ((kt) < 7) n.a[1] = tr_read2(wa + so + 4096, wa + so + 5120); X2; SCHED(); \
      acc1[1] = mfma32(c.a[1], c.b1[1], acc1[1]); if ((kt) < 7) { n.b0[1] = lds_ld16(xb1 + so); n.b1[1] = lds_ld16(xb1 + so + 4096); } X3; SCHED(); \
      acc1[0] = mfma32(c.a[2], c.b0[2], acc1[0]); if ((kt) < 7) n.a[2] = tr_read2(wa + so + 8192, wa + so + 9216); X4; SCHED(); \
      acc1[1] = mfma32(c.a[2], c.b1[2], acc1[1]); if ((kt) < 7) { n.b0[2] = lds_ld16(xb2 + so); n.b1[2] = lds_ld16(xb2 + so + 4096); } X5; SCHED(); \
      acc1[0] = mfma32(c.a[3], c.b0[3], acc1[0]); if ((kt) < 7) n.a[3] = tr_read2(wa + so + 12288, wa + so + 13312); X6; SCHED(); \
      acc1[1] = mfma32(c.a[3], c.b1[3], acc1[1]); if ((kt) < 7) { n.b0[3] = lds_ld16(xb3 + so); n.b1[3] = lds_ld16(xb3 + so + 4096); } X7; SCHED(); \
    }
#define GATE_STORE() do { if (w == 2 && lane < 8) { const v2u gv2 = *(LAS v2u*)(CTL + 64 + lane * 8);                       \
      __builtin_amdgcn_raw_buffer_store_b64(gv2, as_rsrc(sws), WS_GATE + (unsigned)(i1 * 256 + 128 * rb + 8 * fc + lane) * 8u, 0, 16); } } while (0)
#define STEP8 G1_STEP
    if constexpr (ISA) {
      bar_vm<11>();
      STAMP(4);
      STEP8(0, NOP, NOP, NOP, NOP, NOP, NOP, NOP, NOP)
      bar_vm<0>();
      if (tid == 0) flag_store(p.ws, WS_FLA + (unsigned)(mt * 32 + i * 4 + dt4) * FS, tag);
      STAMP(13);
      STEP8(1, g1_dma(4, 0), g1_dma(4, 1), g1_dma(4, 2), g1_dma(4, 3), g1_dma(5, 0), g1_dma(5, 1), g1_dma(5, 2), g1_dma(5, 3))
      BAR_LGKM();
      GATE_STORE();
      STEP8(2, NOP, NOP, NOP, NOP, g1_dma(6, 0), g1_dma(6, 1), g1_dma(6, 2), g1_dma(6, 3))
      bar_vm<8>();
      STEP8(3, NOP, NOP, NOP, NOP, g1_dma(7, 0), g1_dma(7, 1), g1_dma(7, 2), g1_dma(7, 3))
      bar_vm<8>();
      STAMP(12);
      STEP8(4, NOP, NOP, NOP, NOP, w2_issue1(G, 0, 0), w2_issue1(G, 0, 1), w2_issue1(G, 1, 0), w2_issue1(G, 1, 1))
      bar_vm<8>();
      STEP8(5, NOP, NOP, NOP, NOP, w2_issue1(G, 2, 0), w2_issue1(G, 2, 1), w2_issue1(G, 3, 0), w2_issue1(G, 3, 1))
      bar_vm<8>();
      STEP8(6, NOP, NOP, NOP, NOP, w2_issue1(G, 4, 0), w2_issue1(G, 4, 1), w2_issue1(G, 5, 0), w2_issue1(G, 5, 1))
      BAR_LGKM();
    } else {
      const v4u stab = make_srd(&kTab, sizeof(Tab));
      {
        const int kap = tid >> 5, ii = (kap >> 1) & 7, d0 = kap & 1;
        const int g0 = m1 == 0 ? 2 * ii : ii, g1 = m1 == 0 ? 2 * ii + 1 : 8 + ii;
        const unsigned tb = WS_T + (unsigned)m1 * 16384u + (unsigned)(d0 * 512 + (tid & 31) * 16);
        tsrc0 = tb + (unsigned)g0 * 1024u; tsrc1 = tb + (unsigned)g1 * 1024u;
      }
      bar_vm<9>();
      STAMP(4);
      STEP8(0, NOP, NOP, NOP, NOP, g1_dma(4, 0), g1_dma(4, 1), g1_dma(4, 2), g1_dma(4, 3))
      bar_vm<9>();
      STEP8(1, NOP, NOP, NOP, NOP, g1_dma(5, 0), g1_dma(5, 1), g1_dma(5, 2), g1_dma(5, 3))
      bar_vm<9>();
      GATE_STORE();
      STEP8(2, NOP, NOP, NOP, NOP, g1_dma(6, 0), g1_dma(6, 1), g1_dma(6, 2), g1_dma(6, 3))
      bar_vm<8>();
      LAS char* wscr0 = L + 128 * 1024 + w * 2048;
      const bool special = m1 == 0;
      const int m2nd = 128 * w + 1024, off2 = w_off(m2nd, lane);
      if (special) { asm volatile("" : "+v"(xqr), "+v"(xqi), "+v"(xtr), "+v"(xti)); w_put(wscr0, xqr, xqi, lane); }
      STEP8(3, NOP, NOP, (dma16<128u * 1024u + 16384u, 0>(stab, (unsigned)lane * 16u, g1l, g1l)), (dma16<128u * 1024u + 17408u, 1024>(stab, (unsigned)lane * 16u, g1l, g1l)),
            g1_dma(7, 0), g1_dma(7, 1), g1_dma(7, 2), g1_dma(7, 3))
      bar_vm<10>();
      STAMP(12);
      WRawL raw2;
      if (special) { w_get(raw2, wscr0, off2, lane, 0); w_get(raw2, wscr0, off2, lane, 1); w_get(raw2, wscr0, off2, lane, 2); w_get(raw2, wscr0, off2, lane, 3); }
      STEP8(4, NOP, NOP, NOP, NOP, w2_issue1(G, 0, 0), w2_issue1(G, 0, 1), w2_issue1(G, 1, 0), w2_issue1(G, 1, 1))
      bar_vm<10>();
      if (special) { Wd = w_unpack(raw2, w_tail(xtr, xti), off2, m2nd, lane); packed_coef(Wc, Wd, w); }
      STEP8(5, NOP, NOP, NOP, NOP, w2_issue1(G, 2, 0), w2_issue1(G, 2, 1), w2_issue1(G, 3, 0), w2_issue1(G, 3, 1))
      bar_vm<8>();
      STEP8(6, if (w == 0) LD4Z(fla, flaoff, sws, "sc1"), NOP, NOP, NOP, w2_issue1(G, 4, 0), w2_issue1(G, 4, 1), w2_issue1(G, 5, 0), w2_issue1(G, 5, 1))
      BAR_LGKM();
    }
    if constexpr (ISA) {
      STEP8(7, NOP, NOP, NOP, NOP, w2_issue1(G, 6, 0), w2_issue1(G, 6, 1), w2_issue1(G, 7, 0), w2_issue1(G, 7, 1))
    } else {
      LAS char* TB = L + 128 * 1024 + 16384 + lane * 16;
#define TBR(f) (tb[f] = lds_ld16(TB + (f) * 1024), tb[(f) + 1] = lds_ld16(TB + ((f) + 1) * 1024))
      STEP8(7, TBR(6), TBR(0), TBR(10), TBR(2), { TBR(12); w2_issue1(G, 6, 0); }, { TBR(14); w2_issue1(G, 6, 1); }, { TBR(4); w2_issue1(G, 7, 0); }, { TBR(8); w2_issue1(G, 7, 1); })
      if (w == 0) {
        asm volatile("s_waitcnt vmcnt(8)" : "+v"(fla) :: "memory");
        const unsigned ok = __all(fla == tag) ? 1u : 0u;
        if (lane == 0) *(LAS unsigned*)CTL = ok;
      }
      BAR_LGKM();
      t_ok = __builtin_amdgcn_readfirstlane(*(volatile LAS unsigned*)CTL);
      if (t_ok) { LD16Z(tq0, tsrc0, sws, "sc1"); LD16Z(tq1, tsrc1, sws, "sc1"); }
    }
  }
  asm volatile("" :: "v"(touch));
  STAMP(5);
  FT(18);
  if (active) {
#pragma unroll
    for (int rt = 0; rt < 2; ++rt)
#pragma unroll
      for (int u = 0; u < 2; ++u) {
        v4u o;
        o.x = silu2(acc1[rt][8 * u + 0], acc1[rt][8 * u + 1]);
        o.y = silu2(acc1[rt][8 * u + 2], acc1[rt][8 * u + 3]);
        o.z = silu2(acc1[rt][8 * u + 4], acc1[rt][8 * u + 5]);
        o.w = silu2(acc1[rt][8 * u + 6], acc1[rt][8 * u + 7]);
        const int jb32 = 4 * rb + 2 * rh1 + rt, fs = (f0 >> 4) + 2 * ft + u;
        st16_sc1(sws, WS_H + (unsigned)(((i1 * 8 + jb32) * 112 + fs) * 1024 + lane * 16), o);
      }
  }
  SCHED();
  STAMP(6);
  FT(20);
  const unsigned flh_own = WS_FLH + (unsigned)((i1 * 2 + rb) * 16 + fc) * FS;
  if constexpr (ISA) {
    bar_vm<0>();
    if (tid == 0) flag_store(p.ws, flh_own, tag);
  } else {
    LAS char* I0 = L + 128 * 1024;
    LAS char* I1 = L + 128 * 1024 + 16384;
    const int g = lane >> 4, l15 = lane & 15;
    const bool special = (m1 == 0);
    bool published = false;
    if (t_ok) {
      if (active) wait_vm<4>(); else wait_vm<0>();
      asm volatile("s_barrier" ::: "memory");
    } else {
      bar_vm<0>();
      if (tid == 0) flag_store(p.ws, flh_own, tag);
      published = true;
      if (w == 0) poll_words(sws, WS_FLA + (unsigned)(m1 >> 5) * 32u * FS, 32, tag, lane);
      asm volatile("s_barrier" ::: "memory");
      LD16Z(tq0, tsrc0, sws, "sc1"); LD16Z(tq1, tsrc1, sws, "sc1");
      wait_vm<0>();
    }
    asm volatile("" : "+v"(tq0), "+v"(tq1) :: "memory");
    {
      const int kap = tid >> 5, ch = tid & 31;
      *(LAS v4u*)(I0 + kap * 512 + ((ch ^ ((kap & 3) << 2)) << 4)) = tq0;
      *(LAS v4u*)(I0 + (kap + 16) * 512 + ((ch ^ ((kap & 3) << 2)) << 4)) = tq1;
    }
    BAR_LGKM();
    FT(21);
    LAS char* ta = I0 + (8 * hh + q4) * 512 + (((4 * w + 2 * blk + (p4 >> 1)) ^ (q4 << 2)) << 4) + 8 * (p4 & 1);
    const int n = lane & 31;
    LAS char* so = I1 + n * 512 + 8 * hh;
    {
      f32x16 acc = {};
      acc = mfma32(tr_read2(ta, ta + 2048), tb[6], acc);
      acc = mfma32(tr_read2(ta + 8192, ta + 10240), tb[7], acc);
#pragma unroll
      for (int Gq = 0; Gq < 4; ++Gq) {
        v2u o; o.x = pk2(acc[4 * Gq], acc[4 * Gq + 1]); o.y = pk2(acc[4 * Gq + 2], acc[4 * Gq + 3]);
        *(LAS v2u*)(so + (((4 * w + Gq) ^ (n & 15)) << 4)) = o;
      }
    }
    BAR_LGKM();
    FT(22);
    const f32x4 z4 = {0.f, 0.f, 0.f, 0.f};
    const v4u F16fr = tb[0], F16fi = tb[1];
    v4u F16ir, F16ii;
    F16ir.x = F16fr.x; F16ir.y = F16fr.y; F16ir.z = F16fr.z ^ 0x80008000u; F16ir.w = F16fr.w ^ 0x80008000u;
    F16ii.x = F16fi.x ^ 0x80008000u; F16ii.y = F16fi.y ^ 0x80008000u; F16ii.z = F16fi.z; F16ii.w = F16fi.w;
    f32x4 zr[2], zi[2];
#pragma unroll
    for (int pp = 0; pp < 2; ++pp) {
      const int n0 = 4 * w + 2 * pp, n1 = n0 + 1;
      const int ch = 2 * (4 * g + q4) + (p4 >> 1);
      const v4u a1 = tr_read2(I1 + n0 * 512 + ((ch ^ (n0 & 15)) << 4) + 8 * (p4 & 1), I1 + n1 * 512 + ((ch ^ (n1 & 15)) << 4) + 8 * (p4 & 1));
      const f32x4 dr = mfma16(a1, pp ? tb[2] : F16fr, z4), di = mfma16(a1, pp ? tb[3] : F16fi, z4);
      const v4u b3 = cmul_pack<false>(dr, di, asf4(tb[10 + 2 * pp]), asf4(tb[11 + 2 * pp]));
      zr[pp] = mfma16(F16fr, b3, z4); zi[pp] = mfma16(F16fi, b3, z4);
    }
    v4u a4[2];
    if (!special) {
      a4[0] = cmul_pack<false>(zr[0], zi[0], Wc.r0, Wc.i0);
      a4[1] = cmul_pack<false>(zr[1], zi[1], Wc.r1, Wc.i1);
    } else {
      BAR_LGKM();
      LAS char* Z = I0;
#pragma unroll
      for (int pp = 0; pp < 2; ++pp)
#pragma unroll
        for (int r = 0; r < 4; ++r) { f32x2 v = {zr[pp][r], zi[pp][r]}; *(LAS f32x2*)(Z + ((w * 2 + pp) * 256 + l15 + 16 * (4 * g + r)) * 8) = v; }
      BAR_LGKM();
#pragma unroll
      for (int pp = 0; pp < 2; ++pp) {
        f32x4 cr, ci;
#pragma unroll
        for (int r = 0; r < 4; ++r) {
          const int K = l15 + 16 * (4 * g + r), Kn = pp ? 255 - K : (256 - K) & 255;
          const f32x2 y = *(LAS f32x2*)(Z + ((((8 - w) & 7) * 2 + pp) * 256 + Kn) * 8);
          cr[r] = y[0]; ci[r] = -y[1];
        }
        const f32x4 ar = pp ? Wc.r1 : Wc.r0, ai = pp ? Wc.i1 : Wc.i0, br = pp ? Wd.r1 : Wd.r0, bi = pp ? Wd.i1 : Wd.i0;
        const f32x4 yr = zr[pp] * ar - zi[pp] * ai + cr * br - ci * bi, yi = zr[pp] * ai + zi[pp] * ar + cr * bi + ci * br;
        a4[pp].x = pk2(yr[0], yr[1]); a4[pp].y = pk2(yr[2], yr[3]); a4[pp].z = pk2(yi[0], yi[1]); a4[pp].w = pk2(yi[2], yi[3]);
      }
      BAR_LGKM();
    }
#pragma unroll
    for (int pp = 0; pp < 2; ++pp) {
      const f32x4 dr = mfma16(a4[pp], F16ir, z4), di = mfma16(a4[pp], F16ii, z4);
      const v4u a5 = cmul_pack<true>(dr, di, asf4(tb[pp ? 14 : 10]), asf4(tb[pp ? 15 : 11]));
      const f32x4 er = mfma16(a5, pp ? tb[4] : F16ir, z4), ei = mfma16(a5, pp ? tb[5] : F16ii, z4);
      const int n0 = 4 * w + 2 * pp, n1 = n0 + 1;
      const int ch = 2 * l15 + (g >> 1);
      v2u o; o.x = pk2(er[0], er[1]); o.y = pk2(er[2], er[3]);
      *(LAS v2u*)(I0 + n0 * 512 + ((ch ^ ((n0 & 3) << 2)) << 4) + 8 * (g & 1)) = o;
      o.x = pk2(ei[0], ei[1]); o.y = pk2(ei[2], ei[3]);
      *(LAS v2u*)(I0 + n1 * 512 + ((ch ^ ((n1 & 3) << 2)) << 4) + 8 * (g & 1)) = o;
    }
    bar_vm<0>();
    if (!published && tid == 0) flag_store(p.ws, flh_own, tag);
    FT(23);
    m5a = tb[8]; m5b = tb[9];
  }
  return tag;
}

DI void make_geo(Geo& G, const Params& p, int tz) {
  G.tid = tz; G.lane = G.tid & 63; G.w = __builtin_amdgcn_readfirstlane(G.tid >> 6);
  G.b = blockIdx.x;
  const int Lx = G.b >> 3, x8 = G.b & 7;
  {
    const int a = x8 & 3, h = x8 >> 2, rbi = Lx >> 3, c = Lx & 7;
    G.i = 2 * a + h; G.i1 = 2 * a + (rbi >> 1); G.rb = rbi & 1;
    G.fc = c < 4 ? 4 * h + c : 4 + 4 * h + c;
    G.at = rbi * 4 + c; G.m1 = ((rbi * 4 + c - 4) << 3) | x8;
  }
  G.hh = G.lane >> 5; G.blk = (G.lane >> 4) & 1; G.q4 = (G.lane & 15) >> 2; G.p4 = G.lane & 3;
  G.sx = make_srd(p.x, NS * ND * 2); G.sws = make_srd(p.ws, 16u << 20); G.sw2 = make_srd(p.w2, NF * ND * 2);
  G.jb = Lx >> 3; G.dt = Lx & 7; G.rh = G.w & 1; G.q = G.w >> 1;
  G.j = 64 * G.jb + 32 * G.rh + (G.lane & 31);
  const int rho = 16 * G.rh + (G.lane >> 3), chs = (G.lane & 7) ^ (4 * ((G.lane >> 4) & 1));
  G.vw20 = (unsigned)(rho * 1024 + G.dt * 128 + chs * 16); G.vw21 = G.vw20 + 8 * 1024;
  G.ldsw = uni((unsigned)(G.q * 4096 + G.rh * 2048));
  G.fq0b = uni((unsigned)(768 + 256 * G.q) * 1024u); G.fq1b = uni((unsigned)(192 * G.q) * 1024u);
}

__global__ __launch_bounds__(512, 2) void gm_kernel(Params p) {
  extern __shared__ __attribute__((aligned(16))) char smem[];
  LAS char* L = (LAS char*)smem;
  unsigned touch0;
  {
    unsigned long long pc; unsigned len;
    asm volatile("s_getpc_b64 %0\n.Lgm_code_begin:\n\ts_mov_b32 %1, .Lgm_code_end-.Lgm_code_begin" : "=s"(pc), "=s"(len));
    const unsigned line = (unsigned)((blockIdx.x >> 3) * 8 + (threadIdx.x >> 6)) * 128u;
    const char* a0 = (const char*)(pc & ~127ull) + (line < len ? line : 0u);
    asm volatile("global_load_dword %0, %1, off" : "=v"(touch0) : "v"(a0) : "memory");
  }
  STAMP(0);
  FT(0);
  Geo G;
  make_geo(G, p, threadIdx.x);
  const bool isA = G.fc < 8;
  Pro R;
  {
    const int lane = G.lane, w = G.w, i = G.i, i1 = G.i1, rb = G.rb, fc = G.fc, m1 = G.m1;
    const v4u sx = G.sx, sws = G.sws;
    R.sw1 = make_srd(p.w1, ND * NF * 2); R.swr = make_srd(p.wr, NS * 257 * 2); R.swi = make_srd(p.wi, NS * 257 * 2);
    const v4u sw1 = R.sw1, swr = R.swr, swi = R.swi;
    const int jg = 128 * rb + 8 * fc + w;
    LD16Z(R.gx, (unsigned)(lane * 16 + (i1 + 8 * jg) * 1024), sx, "");
    LD16Z(R.gv, (unsigned)lane * 16u, make_srd(p.gw, ND * 2), "");
    LD4Z(R.ep, WS_EPOCH, sws, "sc1");
    const int mW = m1 == 0 ? 128 * w : m1 + 256 * w;
    if (isA) {
      const int dt4 = G.at & 3;
      const unsigned voff = (unsigned)(lane >> 4) * 8192u + (unsigned)(((lane & 15) ^ ((lane >> 4) << 2)) << 4);
      const unsigned xl = uni(A_XIMG + (unsigned)w * 8192u), xs = uni((unsigned)i * 1024u + (unsigned)dt4 * 256u + (unsigned)w * 262144u);
      dma16<0, 0>(sx, voff, xl, xs); dma16<1024, 32768>(sx, voff, xl, xs); dma16<2048, 65536>(sx, voff, xl, xs); dma16<3072, 98304>(sx, voff, xl, xs);
      dma16<4096, 131072>(sx, voff, xl, xs); dma16<5120, 163840>(sx, voff, xl, xs); dma16<6144, 196608>(sx, voff, xl, xs); dma16<7168, 229376>(sx, voff, xl, xs);
    } else {
      const unsigned wsrc = w_src(mW, lane), wts = w_tail_src(mW, lane);
      LD16Z(R.wqr, wsrc, swr, ""); LD16Z(R.wqi, wsrc, swi, ""); LD2Z(R.wtr, wts, swr, ""); LD2Z(R.wti, wts, swi, "");
    }
    const int f0 = isA ? 128 * fc : 1024 + 96 * (fc - 8), nf = isA ? 128 : 96;
    {
      const int r0 = 16 * w + (lane >> 3), pos = lane & 7, r1 = r0 + 8;
      R.vx0 = (unsigned)(i1 * 1024 + (128 * rb + r0) * 8192 + ((pos ^ ((r0 >> 1) & 7)) << 4));
      R.vx1 = (unsigned)(i1 * 1024 + (128 * rb + r1) * 8192 + ((pos ^ ((r1 >> 1) & 7)) << 4));
      const int k0 = 8 * w + (lane >> 4), chunk = (lane & 15) ^ ((lane >> 4) << 2);
      const bool ok = chunk * 8 < nf;
      R.vw0 = ok ? (unsigned)(k0 * 3584 + f0 * 2 + chunk * 16) : 0x80000000u;
      R.vw1 = ok ? R.vw0 + 4u * 3584u : 0x80000000u;
    }
    R.g1l = uni((unsigned)w * 2048u);
    const unsigned vx0 = R.vx0, vx1 = R.vx1, vw0 = R.vw0, vw1 = R.vw1, g1l = R.g1l;
    asm volatile("s_barrier" ::: "memory");
    g1_issue(0);
    if (!isA && m1 == 0) {
      const unsigned ws2 = w_src(128 * w + 1024, lane), wt2 = w_tail_src(128 * w + 1024, lane);
      LD16Z(R.xqr, ws2, swr, ""); LD16Z(R.xqi, ws2, swi, ""); LD2Z(R.xtr, wt2, swr, ""); LD2Z(R.xti, wt2, swi, "");
    }
    asm volatile("s_barrier" ::: "memory");
    g1_issue(1);
    if (!isA) {
      asm volatile("s_barrier" ::: "memory");
      g1_issue(2); asm volatile("s_barrier" ::: "memory");
      g1_issue(3);
    }
    unsigned touch;
    TOUCH();
    R.touch = touch;
    if (isA) phaseA_genF(L, i, G.at >> 2, w, lane);
    if (isA) wait_vm<17>(); else wait_vm<21>();
    asm volatile("" : "+v"(R.ep), "+v"(R.gx), "+v"(R.gv) :: "memory");
    if (!isA) {
      wait_vm<17>();
      asm volatile("" : "+v"(R.wqr), "+v"(R.wqi), "+v"(R.wtr), "+v"(R.wti) :: "memory");
      if (m1 == 0) {
        wait_vm<13>();
        asm volatile("" : "+v"(R.xqr), "+v"(R.xqi), "+v"(R.xtr), "+v"(R.xti) :: "memory");
      }
    }
  }
  v4u gf[4], m5a, m5b;
  unsigned tag;
  if (isA) tag = phase1<true>(p, L, G, R, m5a, m5b); else tag = phase1<false>(p, L, G, R, m5a, m5b);
  asm volatile("" :: "v"(touch0));
  { int tz = threadIdx.x; asm volatile("" : "+v"(tz)); make_geo(G, p, tz); }
  const int tid = G.tid, lane = G.lane, w = G.w, b = G.b, i = G.i, jb = G.jb, dt = G.dt, rh = G.rh, q = G.q, j = G.j;
  const int hh = G.hh, blk = G.blk, q4 = G.q4, p4 = G.p4;
  const v4u sws = G.sws;
  const unsigned ldsw = G.ldsw;
  const int m1 = G.m1;
  STAMP(7);
  FT(25);
  if (!isA) {
    LAS char* I0 = L + 128 * 1024;
    LAS char* I1 = L + 128 * 1024 + 16384;
    LAS char* ta = I0 + (8 * hh + q4) * 512 + (((4 * w + 2 * blk + (p4 >> 1)) ^ (q4 << 2)) << 4) + 8 * (p4 & 1);
    const int n = lane & 31;
    LAS char* so = I1 + n * 512 + 8 * hh;
    f32x16 acc = {};
    acc = mfma32(tr_read2(ta, ta + 2048), m5a, acc);
    acc = mfma32(tr_read2(ta + 8192, ta + 10240), m5b, acc);
#pragma unroll
    for (int Gq = 0; Gq < 4; ++Gq) {
      v2u o; o.x = pk2(acc[4 * Gq], acc[4 * Gq + 1]); o.y = pk2(acc[4 * Gq + 2], acc[4 * Gq + 3]);
      *(LAS v2u*)(so + (((4 * w + Gq) ^ (n & 15)) << 4)) = o;
    }
    BAR_LGKM();
    const bool special = (m1 == 0);
    auto u_store = [&](int t2) {
      const int nn = t2 >> 5, ch = t2 & 31, ii = (nn >> 1) & 7, d0 = nn & 1;
      const int g0 = special ? 2 * ii : ii, g1 = special ? 2 * ii + 1 : 8 + ii;
      const v4u v0 = lds_ld16(I1 + nn * 512 + ((ch ^ (nn & 15)) << 4));
      const v4u v1 = lds_ld16(I1 + (nn + 16) * 512 + ((ch ^ (nn & 15)) << 4));
      const unsigned ub = WS_U + (unsigned)m1 * 16384u + (unsigned)(d0 * 512 + ch * 16);
      st16_sc1(sws, ub + (unsigned)g0 * 1024u, v0);
      st16_sc1(sws, ub + (unsigned)g1 * 1024u, v1);
    };
    if (w != 0) u_store(tid);
    if (w == 1) u_store(tid - 64);
    SCHED();
  }
  const int sz = (q4 >> 1) & 1;
  LAS char* ra0 = L + q * 4096 + (4 * hh + q4) * 128 + (((0 ^ (4 * sz)) + 2 * blk + (p4 >> 1)) << 4) + 8 * (p4 & 1);
  LAS char* ra1 = L + q * 4096 + (4 * hh + q4) * 128 + (((4 ^ (4 * sz)) + 2 * blk + (p4 >> 1)) << 4) + 8 * (p4 & 1);
  v4u wp[4][4];
  BAR_LGKM();
#pragma unroll
  for (int t = 0; t < 4; ++t) {
    LAS char* s0 = ra0 + t * 16384; LAS char* s1 = ra1 + t * 16384;
    wp[t][0] = tr_read2(s0, s0 + 1024); wp[t][1] = tr_read2(s1, s1 + 1024);
    wp[t][2] = tr_read2(s0 + 2048, s0 + 3072); wp[t][3] = tr_read2(s1 + 2048, s1 + 3072);
  }
#define W2_8_11() do { w2_issue1(G, 8, 0); w2_issue1(G, 8, 1); w2_issue1(G, 9, 0); w2_issue1(G, 9, 1); w2_issue1(G, 10, 0); w2_issue1(G, 10, 1); w2_issue1(G, 11, 0); w2_issue1(G, 11, 1); } while (0)
  if (isA) { BAR_LGKM(); W2_8_11(); }
  const unsigned flh = WS_FLH + (unsigned)((i * 2 + (jb >> 1)) * 16) * FS;
  if (w == 0) poll_words(sws, flh, 16, tag, lane);
  BAR_LGKM();
  STAMP(8);
  FT(26);
  const unsigned vh = (unsigned)lane * 16u;
  const unsigned hbase = WS_H + (unsigned)((i * 8 + 2 * jb + rh) * 112) * 1024u;
  const unsigned hb0 = uni(hbase + (G.fq0b >> 4)), hb1 = uni(hbase + (G.fq1b >> 4));
  v2u gg;
  v4u hf[14][2];
#define H_ISSUE(t) do { LD16I(hf[t][0], vh, sws, ((t) < 8 ? hb0 : hb1), (2 * ((t) & 7)) * 1024, "sc1"); LD16I(hf[t][1], vh, sws, ((t) < 8 ? hb0 : hb1), (2 * ((t) & 7) + 1) * 1024, "sc1"); } while (0)
  H_ISSUE(0); H_ISSUE(1);
  asm volatile("s_barrier" ::: "memory");
  H_ISSUE(2); H_ISSUE(3); H_ISSUE(4); H_ISSUE(5); H_ISSUE(6);
  LD8Z(gg, WS_GATE + (unsigned)(i * 256 + j) * 8u, sws, "sc1");
  SCHED();
  f32x16 accb[2] = {}, acca[2] = {};
  v4u wf[2][4];
  unsigned fl0, fl1;
  v4u ub0, ub1, ub2, ub3;
  const unsigned vu = WS_U + (unsigned)(((64 * q + 16 * rh + (lane >> 3)) * 8 + i) * 1024 + dt * 128 + ((lane & 7) ^ (4 * ((lane >> 4) & 1))) * 16);
#define G2_BAR(n, t) asm volatile("s_waitcnt vmcnt(" STR(n) ") lgkmcnt(0)\n\ts_barrier" : "+v"(hf[t][0]), "+v"(hf[t][1]) :: "memory")
#define G2_WAIT(n, t) asm volatile("s_waitcnt vmcnt(" STR(n) ")" : "+v"(hf[t][0]), "+v"(hf[t][1]) :: "memory")
#define G2_CORE(t, c, n, RD, acc, b0, b1, X0, X1, X2)                                             \
  {                                                                                                 \
    FT(30 + (t));                                                                                   \
    LAS char* s0 = ra0 + (((t) + 1) & 7) * 16384; LAS char* s1 = ra1 + (((t) + 1) & 7) * 16384;    \
    asm volatile("" : "+v"(c[0]), "+v"(c[1]), "+v"(c[2]), "+v"(c[3]));                             \
    asm volatile("" :: "v"(acc[0]), "v"(acc[1]));                                                   \
    SCHED();                                                                                        \
    acc[0] = mfma32(c[0], b0, acc[0]); if (RD) n[0] = tr_read2(s0, s0 + 1024); SCHED();            \
    acc[1] = mfma32(c[1], b0, acc[1]); if (RD) n[1] = tr_read2(s1, s1 + 1024); X0; SCHED();        \
    acc[0] = mfma32(c[2], b1, acc[0]); if (RD) n[2] = tr_read2(s0 + 2048, s0 + 3072); X1; SCHED(); \
    acc[1] = mfma32(c[3], b1, acc[1]); if (RD) n[3] = tr_read2(s1 + 2048, s1 + 3072); X2; SCHED(); \
  }
#define G2_PRE(t, X2) G2_CORE(t, wp[t], wf[0], (t) == 3, accb, hf[t][0], hf[t][1], if (!isA) w2_issue1(G, (t) + 8, 0), if (!isA) w2_issue1(G, (t) + 8, 1), X2)
#define G2_STEP(t, X0, X1, X2) G2_CORE(t, wf[(t) & 1], wf[((t) + 1) & 1], true, accb, hf[t][0], hf[t][1], X0, X1, X2)
  G2_BAR(13, 0);
  if (!isA && tid == 0) flag_store(p.ws, WS_FLB + (unsigned)m1 * FS, tag);
  STAMP(14);
  G2_PRE(0, H_ISSUE(7))
  G2_WAIT(13, 1); G2_PRE(1, H_ISSUE(8))
  G2_WAIT(13, 2); G2_PRE(2, H_ISSUE(9))
  G2_WAIT(13, 3); G2_PRE(3, H_ISSUE(10))
  G2_BAR(13, 4); G2_STEP(4, w2_issue1(G, 12, 0), w2_issue1(G, 12, 1), H_ISSUE(11))
  G2_BAR(15, 5); G2_STEP(5, w2_issue1(G, 13, 0), w2_issue1(G, 13, 1), H_ISSUE(12))
  if (w == 0) { LD4Z(fl0, WS_FLB + (unsigned)lane * FS, sws, "sc1"); LD4Z(fl1, WS_FLB + (64u + (unsigned)lane) * FS, sws, "sc1"); }
  G2_WAIT(17, 6); G2_STEP(6, NOP, NOP, H_ISSUE(13))
  STAMP(9);
  G2_BAR(16, 7); G2_STEP(7, NOP, NOP, NOP)
  LAS unsigned* verdict = (LAS unsigned*)(L + 128 * 1024);
  if (w == 0) {
    asm volatile("s_waitcnt vmcnt(2)" : "+v"(fl0), "+v"(fl1) :: "memory");
    const unsigned ok = __all(fl0 == tag && fl1 == tag) ? 1u : 0u;
    if (lane == 0) *verdict = ok;
  }
  G2_BAR(4, 8);
  const int u_early = __builtin_amdgcn_readfirstlane(*(volatile LAS unsigned*)verdict);
  STAMP(15);
#define U_ISSUE() do { LD16Z(ub0, vu, sws, "sc1"); LD16Z(ub1, vu + 8u * 8192u, sws, "sc1"); LD16Z(ub2, vu + 32u * 8192u, sws, "sc1"); LD16Z(ub3, vu + 40u * 8192u, sws, "sc1"); } while (0)
  if (u_early) U_ISSUE();
  GRot grot;
  G2_STEP(8, gen_g_init(G, grot), NOP, gen_g_frag<0>(G, grot, gf[0]))
  unsigned e0, e1, e2, e3;
  if (b == 0 && w == 1) {
    LD4Z(e0, WS_FLH + (unsigned)lane * FS, sws, "sc1"); LD4Z(e1, WS_FLH + (64u + (unsigned)lane) * FS, sws, "sc1");
    LD4Z(e2, WS_FLH + (128u + (unsigned)lane) * FS, sws, "sc1"); LD4Z(e3, WS_FLH + (192u + (unsigned)lane) * FS, sws, "sc1");
  }
  G2_WAIT(8, 9); G2_STEP(9, NOP, NOP, gen_g_frag<1>(G, grot, gf[1]))
  G2_WAIT(8, 10); G2_STEP(10, NOP, NOP, gen_g_frag<2>(G, grot, gf[2]))
  G2_WAIT(8, 11); G2_STEP(11, NOP, NOP, gen_g_frag<3>(G, grot, gf[3]))
  G2_WAIT(2, 12);
  G2_STEP(12, NOP, NOP, NOP)
#define U_PUT() do {                                                                                                        \
    asm volatile("s_waitcnt vmcnt(0)" : "+v"(ub0), "+v"(ub1), "+v"(ub2), "+v"(ub3) :: "memory");                             \
    *(LAS v4u*)(L + 6 * 16384 + ldsw + lane * 16) = ub0; *(LAS v4u*)(L + 6 * 16384 + ldsw + 1024 + lane * 16) = ub1;         \
    *(LAS v4u*)(L + 7 * 16384 + ldsw + lane * 16) = ub2; *(LAS v4u*)(L + 7 * 16384 + ldsw + 1024 + lane * 16) = ub3; } while (0)
  if (u_early) U_PUT();
  G2_BAR(0, 13);
  asm volatile("" : "+v"(gg));
  if (b == 0 && w == 1) {
    asm volatile("" : "+v"(e0), "+v"(e1), "+v"(e2), "+v"(e3));
    if (!__all(e0 == tag && e1 == tag && e2 == tag && e3 == tag)) {
#pragma unroll 1
      for (int u = 0; u < 4; ++u) poll_words(sws, WS_FLH + 64u * FS * u, 64, tag, lane);
    }
    if (lane == 0) flag_store(p.ws, WS_EPOCH, tag);
  }
  G2_CORE(13, wf[1], wf[0], u_early, accb, hf[13][0], hf[13][1], NOP, NOP, NOP)
  if (!u_early) {
    if (w == 0) { poll_words(sws, WS_FLB, 64, tag, lane); poll_words(sws, WS_FLB + 64u * FS, 64, tag, lane); }
    BAR_LGKM();
    U_ISSUE();
    U_PUT();
    BAR_LGKM();
    LAS char* s0 = ra0 + 6 * 16384; LAS char* s1 = ra1 + 6 * 16384;
    wf[0][0] = tr_read2(s0, s0 + 1024); wf[0][1] = tr_read2(s1, s1 + 1024); wf[0][2] = tr_read2(s0 + 2048, s0 + 3072); wf[0][3] = tr_read2(s1 + 2048, s1 + 3072);
  }
  STAMP(10);
  G2_CORE(14, wf[0], wf[1], true, acca, gf[0], gf[1], NOP, NOP, NOP)
  G2_CORE(15, wf[1], wf[0], false, acca, gf[2], gf[3], NOP, NOP, NOP)
  FT(50);
  {
    const float og = asf(gg.y), gs = asf(gg.x) * (1.f / 524288.f);
    const int row = 32 * rh + (lane & 31);
#pragma unroll
    for (int dh = 0; dh < 2; ++dh)
#pragma unroll
      for (int G = 0; G < 4; ++G) {
        f32x4 v;
#pragma unroll
        for (int e = 0; e < 4; ++e) v[e] = __builtin_fmaf(og, accb[dh][4 * G + e], gs * acca[dh][4 * G + e]);
        *(LAS f32x4*)(L + q * 16384 + row * 256 + (((8 * dh + 2 * G + hh) ^ (row & 7)) << 4)) = v;
      }
  }
  BAR_LGKM();
  FT(51);
  {
    const int row = tid >> 3, c8 = tid & 7;
    f32x4 s0 = {0.f, 0.f, 0.f, 0.f}, s1 = s0;
#pragma unroll
    for (int qq = 0; qq < 4; ++qq) {
      s0 += *(LAS f32x4*)(L + qq * 16384 + row * 256 + (((2 * c8) ^ (row & 7)) << 4));
      s1 += *(LAS f32x4*)(L + qq * 16384 + row * 256 + (((2 * c8 + 1) ^ (row & 7)) << 4));
    }
    v4u o; o.x = pk2(s0[0], s0[1]); o.y = pk2(s0[2], s0[3]); o.z = pk2(s1[0], s1[1]); o.w = pk2(s1[2], s1[3]);
    *(GAS v4u*)((GAS char*)p.out + (size_t)(i + 8 * (64 * jb + row)) * 1024 + dt * 128 + c8 * 16) = o;
  }
  asm volatile(".Lgm_code_end:");
  STAMP(11);
}

}

extern "C" void kernel_launch(void* const* d_in, const int* in_sizes, int n_in, void* d_out, int out_size, void* d_ws, size_t ws_size,
                              hipStream_t stream) {
  Params p;
  p.x = d_in[0]; p.wr = d_in[1]; p.wi = d_in[2]; p.w1 = d_in[4]; p.w2 = d_in[5]; p.gw = d_in[6];
  p.out = d_out; p.ws = d_ws;
  (void)hipFuncSetAttribute((const void*)gm_kernel, hipFuncAttributeMaxDynamicSharedMemorySize, LDS_BYTES);
  gm_kernel<<<256, 512, LDS_BYTES, stream>>>(p);
}
```

```cpp
#include <hip/hip_runtime.h>
#include <hip/hip_bf16.h>
#include <stdint.h>

namespace {

typedef unsigned v4u __attribute__((ext_vector_type(4)));
typedef unsigned v2u __attribute__((ext_vector_type(2)));
typedef float f32x16 __attribute__((ext_vector_type(16)));
typedef float f32x4 __attribute__((ext_vector_type(4)));
typedef float f32x2 __attribute__((ext_vector_type(2)));
typedef __bf16 bf16x8 __attribute__((ext_vector_type(8)));
typedef __bf16 bf16x2 __attribute__((ext_vector_type(2)));
typedef short s16x4 __attribute__((ext_vector_type(4)));
#define LAS __attribute__((address_space(3)))
#define GAS __attribute__((address_space(1)))
#define DI __device__ __forceinline__

constexpr int NS = 2048, ND = 512, NF = 1792;
constexpr unsigned WS_EPOCH = 0;
constexpr unsigned FS = 64;
constexpr unsigned WS_FLA = 4096;
constexpr unsigned WS_FLH = 16384;
constexpr unsigned WS_FLB = 32768;
constexpr unsigned WS_GATE = 65536;
constexpr unsigned WS_T = 1u << 20;
constexpr unsigned WS_U = 3u << 20;
constexpr unsigned WS_H = 5u << 20;
constexpr unsigned H_BYTES = 8u * 8u * 112u * 1024u;
constexpr int LDS_BYTES = 160 * 1024;
constexpr unsigned kSpinCap = 1u << 20;

struct Params {
  const void* x; const void* wr; const void* wi; const void* w1; const void* w2; const void* gw;
  void* out; void* ws;
};

constexpr double kPi = 3.14159265358979323846;
constexpr double ccos(double x) { double t = 1, s = 1; for (int n = 1; n < 16; ++n) { t *= -x * x / ((2 * n - 1) * (2 * n)); s += t; } return s; }
constexpr double cosn(int e, int n) { e = ((e % n) + n) % n; double x = 2 * kPi * e / n; if (x > kPi) x -= 2 * kPi; return ccos(x); }
constexpr double wre(int e, int n) { return cosn(e, n); }
constexpr double wim(int e, int n) { return -cosn(4 * e - n, 4 * n); }
constexpr unsigned bfb(double v) { float f = (float)v; unsigned u = __builtin_bit_cast(unsigned, f); u += 0x7fffu + ((u >> 16) & 1u); return u >> 16; }
constexpr unsigned bf2(double a, double b) { return bfb(a) | (bfb(b) << 16); }
constexpr unsigned fbits(double v) { return __builtin_bit_cast(unsigned, (float)v); }
struct Tab { unsigned v[16][64][4]; };
constexpr Tab make_tab() {
  Tab t{};
  for (int l = 0; l < 64; ++l) {
    const int g = l >> 4, l15 = l & 15, hh = l >> 5, n = l & 31;
    for (int mtx = 0; mtx < 3; ++mtx) {
      double r[4] = {}, im[4] = {};
      for (int q = 0; q < 4; ++q) {
        const int a = 4 * g + q;
        if (mtx == 0) { r[q] = wre(l15 * a, 16); im[q] = wim(l15 * a, 16); }
        else if (mtx == 1) { r[q] = wre(a * (2 * l15 + 1), 32); im[q] = wim(a * (2 * l15 + 1), 32); }
        else { r[q] = wre(l15 * (2 * a + 1), 32); im[q] = -wim(l15 * (2 * a + 1), 32); }
      }
      t.v[2 * mtx][l][0] = bf2(r[0], r[1]); t.v[2 * mtx][l][1] = bf2(r[2], r[3]); t.v[2 * mtx][l][2] = bf2(-im[0], -im[1]); t.v[2 * mtx][l][3] = bf2(-im[2], -im[3]);
      t.v[2 * mtx + 1][l][0] = bf2(im[0], im[1]); t.v[2 * mtx + 1][l][1] = bf2(im[2], im[3]); t.v[2 * mtx + 1][l][2] = bf2(r[0], r[1]); t.v[2 * mtx + 1][l][3] = bf2(r[2], r[3]);
    }
    {
      const int m2 = n >> 2, pp = (n >> 1) & 1, cp = n & 1;
      double v0[8] = {}, v1[8] = {};
      for (int jj = 0; jj < 8; ++jj) {
        const int ii = (8 * hh + jj) >> 1, d0 = jj & 1;
        const double sg = (d0 & pp) ? -1.0 : 1.0, orr = sg * wre(ii * m2, 8), oi = sg * wim(ii * m2, 8);
        v0[jj] = cp ? oi : orr; v1[jj] = cp ? orr : -oi;
      }
      for (int q = 0; q < 4; ++q) { t.v[6][l][q] = bf2(v0[2 * q], v0[2 * q + 1]); t.v[7][l][q] = bf2(v1[2 * q], v1[2 * q + 1]); }
    }
    {
      const int cq = n >> 4, ii = (n >> 1) & 7, d0 = n & 1;
      for (int s = 0; s < 2; ++s) {
        double v[8] = {};
        for (int jj = 0; jj < 8; ++jj) {
          const int m2k = 4 * s + 2 * hh + (jj >> 2), pk = (jj >> 1) & 1, ck = jj & 1;
          const double sg = (d0 & pk) ? -1.0 : 1.0, orr = sg * wre(ii * m2k, 8), oi = -sg * wim(ii * m2k, 8);
          v[jj] = cq ? (ck ? orr : oi) : (ck ? -oi : orr);
        }
        for (int q = 0; q < 4; ++q) t.v[8 + s][l][q] = bf2(v[2 * q], v[2 * q + 1]);
      }
    }
    for (int q = 0; q < 4; ++q) {
      const int a = 4 * g + q;
      t.v[10][l][q] = fbits(wre(a * (2 * l15), 512));      t.v[11][l][q] = fbits(wim(a * (2 * l15), 512));
      t.v[12][l][q] = fbits(wre(a * (2 * l15 + 1), 512));  t.v[13][l][q] = fbits(wim(a * (2 * l15 + 1), 512));
      t.v[14][l][q] = fbits(wre(l15 * (2 * a + 1), 512));  t.v[15][l][q] = fbits(wim(l15 * (2 * a + 1), 512));
    }
  }
  return t;
}
__device__ const Tab kTab = make_tab();

constexpr unsigned STAMP_OFF = 512 * 1024;
#define STAMP(k) do {} while (0)
#define FT(k) do {} while (0)

DI v4u make_srd(const void* p, unsigned bytes) {
  const unsigned long long a = (unsigned long long)p;
  v4u r; r.x = (unsigned)a; r.y = (unsigned)(a >> 32) & 0xffffu; r.z = bytes; r.w = 0x00020000u;
  return r;
}
template <unsigned LDSI, unsigned SOI> DI void dma16(v4u srd, unsigned voff, unsigned ldsb, unsigned sob) {
  unsigned tmp;
  asm volatile("s_add_u32 m0, %3, %5\n\ts_add_u32 %0, %4, %6\n\tbuffer_load_dwordx4 %1, %2, %0 offen lds"
               : "=&s"(tmp) : "v"(voff), "s"(srd), "s"(ldsb), "s"(sob), "i"(LDSI), "i"(SOI) : "memory", "scc");
}
template <unsigned LDSI, unsigned SOI> DI void dma16z(v4u srd, unsigned voff, unsigned ldsb) {
  unsigned tmp;
  asm volatile("s_add_u32 m0, %3, %4\n\ts_mov_b32 %0, %5\n\tbuffer_load_dwordx4 %1, %2, %0 offen lds"
               : "=&s"(tmp) : "v"(voff), "s"(srd), "s"(ldsb), "i"(LDSI), "i"(SOI) : "memory", "scc");
}
DI unsigned uni(unsigned v) { return (unsigned)__builtin_amdgcn_readfirstlane((int)v); }
#define LD16(dst, voffv, srd, soffv, flags) asm volatile("buffer_load_dwordx4 %0, %1, %2, %3 offen " flags : "=v"(dst) : "v"(voffv), "s"(srd), "s"(soffv) : "memory")
#define LD16I(dst, voffv, srd, sob, soi, flags) do { unsigned tmp_; asm volatile("s_add_u32 %1, %4, %5\n\tbuffer_load_dwordx4 %0, %2, %3, %1 offen " flags : "=v"(dst), "=&s"(tmp_) : "v"(voffv), "s"(srd), "s"(sob), "i"(soi) : "memory", "scc"); } while (0)
#define LD16Z(dst, voffv, srd, flags) asm volatile("buffer_load_dwordx4 %0, %1, %2, 0 offen " flags : "=v"(dst) : "v"(voffv), "s"(srd) : "memory")
#define LD4Z(dst, voffv, srd, flags) asm volatile("buffer_load_dword %0, %1, %2, 0 offen " flags : "=v"(dst) : "v"(voffv), "s"(srd) : "memory")
#define LD2Z(dst, voffv, srd, flags) asm volatile("buffer_load_ushort %0, %1, %2, 0 offen " flags : "=v"(dst) : "v"(voffv), "s"(srd) : "memory")
#define LD8Z(dst, voffv, srd, flags) asm volatile("buffer_load_dwordx2 %0, %1, %2, 0 offen " flags : "=v"(dst) : "v"(voffv), "s"(srd) : "memory")
#define LD8(dst, voffv, srd, soffv, flags) asm volatile("buffer_load_dwordx2 %0, %1, %2, %3 offen " flags : "=v"(dst) : "v"(voffv), "s"(srd), "s"(soffv) : "memory")
#define LD4(dst, voffv, srd, soffv, flags) asm volatile("buffer_load_dword %0, %1, %2, %3 offen " flags : "=v"(dst) : "v"(voffv), "s"(srd), "s"(soffv) : "memory")
#define LD2(dst, voffv, srd, soffv, flags) asm volatile("buffer_load_ushort %0, %1, %2, %3 offen " flags : "=v"(dst) : "v"(voffv), "s"(srd), "s"(soffv) : "memory")
#define XSTR(x) #x
#define STR(x) XSTR(x)
#define BAR_VM(n) asm volatile("s_waitcnt vmcnt(" STR(n) ") lgkmcnt(0)\n\ts_barrier" ::: "memory")
#define BAR_LGKM() asm volatile("s_waitcnt lgkmcnt(0)\n\ts_barrier" ::: "memory")
#define SCHED() __builtin_amdgcn_sched_barrier(0)
template <int N> DI void bar_vm() { asm volatile("s_waitcnt vmcnt(%0) lgkmcnt(0)\n\ts_barrier" :: "i"(N) : "memory"); }
template <int N> DI void wait_vm() { asm volatile("s_waitcnt vmcnt(%0)" :: "i"(N) : "memory"); }

DI unsigned pk2(float a, float b) { f32x2 p = {a, b}; return __builtin_bit_cast(unsigned, __builtin_convertvector(p, bf16x2)); }
DI float bflo(unsigned u) { return __builtin_bit_cast(float, u << 16); }
DI float bfhi(unsigned u) { return __builtin_bit_cast(float, u & 0xffff0000u); }
DI float asf(unsigned u) { return __builtin_bit_cast(float, u); }
DI f32x16 mfma32(v4u a, v4u b, f32x16 c) {
  return __builtin_amdgcn_mfma_f32_32x32x16_bf16(__builtin_bit_cast(bf16x8, a), __builtin_bit_cast(bf16x8, b), c, 0, 0, 0);
}
DI f32x4 mfma16(v4u a, v4u b, f32x4 c) {
  return __builtin_amdgcn_mfma_f32_16x16x32_bf16(__builtin_bit_cast(bf16x8, a), __builtin_bit_cast(bf16x8, b), c, 0, 0, 0);
}
DI v2u tr_read(LAS char* p) { return __builtin_bit_cast(v2u, __builtin_amdgcn_ds_read_tr16_b64_v4i16((LAS s16x4*)p)); }
DI v4u tr_read2(LAS char* p0, LAS char* p1) { v2u a = tr_read(p0), b = tr_read(p1); v4u r; r.x = a.x; r.y = a.y; r.z = b.x; r.w = b.y; return r; }
DI v4u lds_ld16(LAS char* p) { return *(LAS v4u*)p; }
DI float cosrev(float r) { return __builtin_amdgcn_cosf(r); }
DI float sinrev(float r) { return __builtin_amdgcn_sinf(r); }
DI __amdgpu_buffer_rsrc_t as_rsrc(v4u srd) { return __builtin_amdgcn_make_buffer_rsrc((void*)(((unsigned long long)srd.y << 32) | srd.x), 0, srd.z, 0x00020000); }
DI void st16_sc1(v4u srd, unsigned voff, v4u v) { __builtin_amdgcn_raw_buffer_store_b128(v, as_rsrc(srd), voff, 0, 16); }
DI void flag_store(void* ws, unsigned off, unsigned tag) { __hip_atomic_store((GAS unsigned*)((GAS char*)ws + off), tag, __ATOMIC_RELAXED, __HIP_MEMORY_SCOPE_AGENT); }
template <int CTRL, int RMASK> DI float dpp_add(float v) { return v + asf((unsigned)__builtin_amdgcn_update_dpp(0, (int)__builtin_bit_cast(unsigned, v), CTRL, RMASK, 0xf, true)); }
DI void poll_words(v4u sws, unsigned off, int n, unsigned tag, int lane) {
  const unsigned vo = off + FS * (unsigned)(lane < n ? lane : 0);
#pragma unroll 1
  for (unsigned spins = 0; spins < kSpinCap; ++spins) {
    unsigned f;
    asm volatile("buffer_load_dword %0, %1, %2, 0 offen sc1\n\ts_waitcnt vmcnt(0)" : "=&v"(f) : "v"(vo), "s"(sws) : "memory");
    if (__all(f == tag)) return;
    __builtin_amdgcn_s_sleep(4);
  }
}

constexpr unsigned A_XIMG = 64 * 1024, A_FIMG = 128 * 1024;
DI void phaseA_genF(LAS char* L, int i, int mt, int w, int lane) {
  const int mh = w >> 2, sg = w & 3, n = lane & 31, hh = lane >> 5;
  const int m1c = 64 * mt + 32 * mh + n, m1 = m1c >> 1, c = m1c & 1;
  const int j0 = 64 * sg + 32 * hh;
  const float ph = c ? 0.25f : 0.f;
  const float k2 = 2.f * cosrev((float)(8 * m1) * (1.f / 2048.f));
  float vm = cosrev((float)(((i + 8 * (j0 - 1)) * m1) & 2047) * (1.f / 2048.f) + ph);
  float v0 = cosrev((float)(((i + 8 * j0) * m1) & 2047) * (1.f / 2048.f) + ph);
  const unsigned cst = c ? 0x3F800000u : 0x00003F80u;
  const bool sp = m1c < 2;
#pragma unroll
  for (int s = 0; s < 4; ++s) {
    float e[8];
#pragma unroll
    for (int q = 0; q < 8; ++q) { e[q] = v0; const float vn = __builtin_fmaf(k2, v0, -vm); vm = v0; v0 = vn; }
    v4u f; f.x = pk2(e[0], e[1]); f.y = pk2(e[2], e[3]); f.z = pk2(e[4], e[5]); f.w = pk2(e[6], e[7]);
    if (mt == 0 && w < 4) { f.x = sp ? cst : f.x; f.y = sp ? cst : f.y; f.z = sp ? cst : f.z; f.w = sp ? cst : f.w; }
    *(LAS v4u*)(L + A_FIMG + (mh * 16 + sg * 4 + s) * 1024 + lane * 16) = f;
  }
}

struct Frag1 { v4u a[4], b0[4], b1[4]; };
DI void g1_read(Frag1& f, LAS char* wa, LAS char* xb0, LAS char* xb1, LAS char* xb2, LAS char* xb3, int slot) {
  const int so = slot * 32768;
  f.a[0] = tr_read2(wa + so, wa + so + 1024);              f.b0[0] = lds_ld16(xb0 + so); f.b1[0] = lds_ld16(xb0 + so + 4096);
  f.a[1] = tr_read2(wa + so + 4096, wa + so + 5120);       f.b0[1] = lds_ld16(xb1 + so); f.b1[1] = lds_ld16(xb1 + so + 4096);
  f.a[2] = tr_read2(wa + so + 8192, wa + so + 9216);       f.b0[2] = lds_ld16(xb2 + so); f.b1[2] = lds_ld16(xb2 + so + 4096);
  f.a[3] = tr_read2(wa + so + 12288, wa + so + 13312);     f.b0[3] = lds_ld16(xb3 + so); f.b1[3] = lds_ld16(xb3 + so + 4096);
}
DI unsigned silu2(float a, float b) {
  const f32x2 h = {a, b}, t = h * -1.44269504088896341f;
  f32x2 e = {__builtin_amdgcn_exp2f(t[0]), __builtin_amdgcn_exp2f(t[1])};
  e = e + 1.f;
  const f32x2 r = {__builtin_amdgcn_rcpf(e[0]), __builtin_amdgcn_rcpf(e[1])};
  return __builtin_bit_cast(unsigned, __builtin_convertvector(h * r, bf16x2));
}

DI f32x2 lo2(const f32x4& v) { return __builtin_shufflevector(v, v, 0, 1); }
DI f32x2 hi2(const f32x4& v) { return __builtin_shufflevector(v, v, 2, 3); }
DI unsigned pk2v(f32x2 p) { return __builtin_bit_cast(unsigned, __builtin_convertvector(p, bf16x2)); }
DI f32x4 asf4(v4u v) { return __builtin_bit_cast(f32x4, v); }
template <bool CONJ> DI v4u cmul_pack(const f32x4& re, const f32x4& im, const f32x4& tr, const f32x4& ti) {
  const f32x2 r0 = lo2(re), r1 = hi2(re), i0 = lo2(im), i1 = hi2(im), c0 = lo2(tr), c1 = hi2(tr), s0 = lo2(ti), s1 = hi2(ti);
  f32x2 a0, a1, b0, b1;
  if (CONJ) {
    a0 = __builtin_elementwise_fma(r0, c0, i0 * s0); a1 = __builtin_elementwise_fma(r1, c1, i1 * s1);
    b0 = __builtin_elementwise_fma(i0, c0, -(r0 * s0)); b1 = __builtin_elementwise_fma(i1, c1, -(r1 * s1));
  } else {
    a0 = __builtin_elementwise_fma(r0, c0, -(i0 * s0)); a1 = __builtin_elementwise_fma(r1, c1, -(i1 * s1));
    b0 = __builtin_elementwise_fma(r0, s0, i0 * c0); b1 = __builtin_elementwise_fma(r1, s1, i1 * c1);
  }
  v4u r; r.x = pk2v(a0); r.y = pk2v(a1); r.z = pk2v(b0); r.w = pk2v(b1); return r;
}
struct cf { float r, i; };
DI cf cmul(cf a, cf b) { return {a.r * b.r - a.i * b.i, a.r * b.i + a.i * b.r}; }
DI cf cmulc(cf a, cf b) { return {a.r * b.r + a.i * b.i, a.i * b.r - a.r * b.i}; }
DI unsigned w_src(int m, int lane) {
  const int mm = (NS - m) & (NS - 1);
  const int e = lane < 32 ? m * 257 - (m & 1) : mm * 257 + (m & 1);
  return (unsigned)(e * 2 + (lane & 31) * 16);
}
DI unsigned w_tail_src(int m, int lane) {
  const int mm = (NS - m) & (NS - 1), l = lane & 3;
  return (unsigned)((l < 2 ? m * 257 + 255 + l : mm * 257 + (l - 2) * 256) * 2);
}
struct WTail { unsigned r255, r256, rm0, rm256, i255, i256, im0, im256; };
DI WTail w_tail(unsigned tr, unsigned ti) {
  return {(unsigned)__builtin_amdgcn_readlane((int)tr, 0), (unsigned)__builtin_amdgcn_readlane((int)tr, 1), (unsigned)__builtin_amdgcn_readlane((int)tr, 2), (unsigned)__builtin_amdgcn_readlane((int)tr, 3),
          (unsigned)__builtin_amdgcn_readlane((int)ti, 0), (unsigned)__builtin_amdgcn_readlane((int)ti, 1), (unsigned)__builtin_amdgcn_readlane((int)ti, 2), (unsigned)__builtin_amdgcn_readlane((int)ti, 3)};
}
struct WVal { f32x4 r0, i0, r1, i1; };
struct WRawL { unsigned r[4][2], i[4][2]; };
DI void w_put(LAS char* scr, v4u qr, v4u qi, int lane) { *(LAS v4u*)(scr + lane * 16) = qr; *(LAS v4u*)(scr + 1024 + lane * 16) = qi; }
DI int w_off(int m, int lane) { const int k1 = lane & 15, g = lane >> 4, odd = m & 1; return lane < 32 ? (2 * k1 + 128 * g + odd) * 2 : 512 + (511 - 2 * k1 - 128 * g - odd) * 2; }
DI void w_get(WRawL& o, LAS char* scr, int off, int lane, int r) {
  LAS unsigned* a = (LAS unsigned*)(scr + ((off + (lane < 32 ? 64 : -64) * r) & ~3));
  o.r[r][0] = a[0]; o.r[r][1] = a[1]; o.i[r][0] = a[256]; o.i[r][1] = a[257];
}
DI WVal w_unpack(const WRawL& x, const WTail& t, int off, int m, int lane) {
  const bool lo = lane < 32;
  const unsigned sh = (off & 2) * 8;
  unsigned rr[4], ii[4];
#pragma unroll
  for (int r = 0; r < 4; ++r) { rr[r] = __builtin_amdgcn_alignbit(x.r[r][1], x.r[r][0], sh); ii[r] = __builtin_amdgcn_alignbit(x.i[r][1], x.i[r][0], sh); }
  if (m & 1) { rr[3] = lane == 31 ? (rr[3] & 0xffffu) | (t.r255 << 16) : rr[3]; ii[3] = lane == 31 ? (ii[3] & 0xffffu) | (t.i255 << 16) : ii[3]; }
  else { rr[0] = lane == 32 ? (rr[0] & 0xffffu) | (t.rm256 << 16) : rr[0]; ii[0] = lane == 32 ? (ii[0] & 0xffffu) | (t.im256 << 16) : ii[0]; }
  const unsigned s0 = lo ? 16u : 0u, s1 = lo ? 0u : 16u, sg = lo ? 0u : 0x80000000u;
  WVal o;
#pragma unroll
  for (int r = 0; r < 4; ++r) {
    o.r0[r] = asf((rr[r] << s0) & 0xffff0000u); o.r1[r] = asf((rr[r] << s1) & 0xffff0000u);
    o.i0[r] = asf(((ii[r] << s0) & 0xffff0000u) ^ sg); o.i1[r] = asf(((ii[r] << s1) & 0xffff0000u) ^ sg);
  }
  const bool av = (lane & 31) == 0;
  const float xr = bflo(lo ? t.rm0 : t.r256), xi = asf(((lo ? t.im0 : t.i256) << 16) ^ (sg ^ 0x80000000u));
  o.r0[0] = av ? 0.5f * (o.r0[0] + xr) : o.r0[0];
  o.i0[0] = av ? 0.5f * (o.i0[0] + xi) : o.i0[0];
  return o;
}

DI void packed_coef(WVal& a, WVal& b, int m2) {
  const cf om = {cosrev((float)m2 * 0.0625f), -sinrev((float)m2 * 0.0625f)};
#pragma unroll
  for (int pp = 0; pp < 2; ++pp)
#pragma unroll
    for (int r = 0; r < 4; ++r) {
      const float war = pp ? a.r1[r] : a.r0[r], wai = pp ? a.i1[r] : a.i0[r], wbr = pp ? b.r1[r] : b.r0[r], wbi = pp ? b.i1[r] : b.i0[r];
      const cf sp = {war + wbr, wai + wbi}, sm = {war - wbr, wai - wbi};
      const cf t1 = cmulc(sm, om), t2 = cmul(sm, om);
      const cf cE = {sp.r - t1.i, sp.i + t1.r}, cO = {t2.r - sp.i, t2.i + sp.r};
      const float alr = 0.25f * (cE.r + cO.i), ali = 0.25f * (cE.i - cO.r), ber = 0.25f * (cE.r - cO.i), bei = 0.25f * (cE.i + cO.r);
      if (pp) { a.r1[r] = alr; a.i1[r] = ali; b.r1[r] = ber; b.i1[r] = bei; } else { a.r0[r] = alr; a.i0[r] = ali; b.r0[r] = ber; b.i0[r] = bei; }
    }
}

struct Geo {
  int tid, lane, w, b, i, i1, at, m1, rb, fc, hh, blk, q4, p4;
  int jb, dt, rh, q, j;
  v4u sx, sws, sw2;
  unsigned vw20, vw21, ldsw, fq0b, fq1b;
};
template <int T, int U> DI void w2_dma(const v4u& sw2, unsigned vw20, unsigned vw21, unsigned ldsw, unsigned fq0, unsigned fq1) {
  dma16<(T & 7) * 16384u + U * 1024u, (T < 8 ? T : T - 8) * 32768u>(sw2, U ? vw21 : vw20, ldsw, T < 8 ? fq0 : fq1);
}
#define w2_issue1(g, t, u) (w2_dma<t, u>((g).sw2, (g).vw20, (g).vw21, (g).ldsw, (g).fq0b, (g).fq1b))
struct GRot { float c1, s1, c4, s4, c, s; };
DI void gen_g_init(const Geo& g, GRot& r) {
  const int srow = g.i + 8 * g.j;
  const int mb = 32 * g.q + 2 * g.hh;
  const float r1 = (float)(srow & 2047) * (1.f / 2048.f), r4 = (float)((4 * srow) & 2047) * (1.f / 2048.f), r0 = (float)((mb * srow) & 2047) * (1.f / 2048.f);
  r.c1 = cosrev(r1); r.s1 = sinrev(r1); r.c4 = cosrev(r4); r.s4 = sinrev(r4); r.c = cosrev(r0); r.s = sinrev(r0);
}
template <int S> DI void gen_g_frag(const Geo& g, GRot& r, v4u& f) {
#pragma unroll
  for (int h = 0; h < 2; ++h) {
    const float cb = __builtin_fmaf(r.c, r.c1, -(r.s * r.s1)), sb = __builtin_fmaf(r.s, r.c1, r.c * r.s1);
    unsigned w0 = pk2(r.c, -r.s), w1 = pk2(cb, -sb);
    if (S == 0 && h == 0) w0 = (g.q == 0 && g.hh == 0) ? ((g.j & 1) ? 0x3F800000u : 0x00003F80u) : w0;
    if (h) { f.z = w0; f.w = w1; } else { f.x = w0; f.y = w1; }
    const float cn = __builtin_fmaf(r.c, r.c4, -(r.s * r.s4)), sn = __builtin_fmaf(r.s, r.c4, r.c * r.s4);
    r.c = cn; r.s = sn;
  }
  asm volatile("" : "+v"(f));
}
template <int N0, int N1, int NR> DI void bar_w(int w) { if (w == 0) bar_vm<N0>(); else if (w == 1) bar_vm<N1>(); else bar_vm<NR>(); }
#define NOP do {} while (0)

#define TLBW 0
#define TOUCH() do {                                                                                                      \
    const int gl = G.b * 256 + (w & 3) * 64 + lane;                                                                         \
    const char* base_ = gl < 14336 ? (const char*)p.w2 : gl < 28672 ? (const char*)p.w1 - 14336 * 128 : gl < 36896 ? (const char*)p.wr - 28672 * 128           \
                      : gl < 45120 ? (const char*)p.wi - 36896 * 128 : (const char*)p.x - 45120 * 128;                     \
    const char* ad_ = base_ + (size_t)(gl < 61504 ? gl : 45120) * 128;                                                      \
    if (w >= 4) ad_ = TLBW ? (w == 7 ? (const char*)p.out + lane * 32768 : (const char*)p.ws + (size_t)((w - 4) * 64 + lane + 8) * 65536 + 32768 - 128) : (const char*)p.x; \
    asm volatile("global_load_dword %0, %1, off" : "=v"(touch) : "v"(ad_) : "memory");                                     \
  } while (0)
#define g1_dma(kt, u) do {                                                                                         \
    if ((u) == 0) dma16z<((kt) & 3) * 32768u, (kt) * 128u>(sx, vx0, g1l);                                           \
    if ((u) == 1) dma16z<((kt) & 3) * 32768u + 1024u, (kt) * 128u>(sx, vx1, g1l);                                   \
    if ((u) == 2) dma16z<((kt) & 3) * 32768u + 16384u, (kt) * (64u * 3584u)>(sw1, vw0, g1l);                        \
    if ((u) == 3) dma16z<((kt) & 3) * 32768u + 17408u, (kt) * (64u * 3584u)>(sw1, vw1, g1l); } while (0)
#define g1_issue(kt) do { g1_dma(kt, 0); g1_dma(kt, 1); g1_dma(kt, 2); g1_dma(kt, 3); } while (0)
struct Pro { v4u sw1, swr, swi, gx, gv, wqr, wqi, xqr, xqi; unsigned ep, touch, wtr, wti, xtr, xti, vx0, vx1, vw0, vw1, g1l; };
template <bool ISA>
DI unsigned phase1(const Params& p, LAS char* L, const Geo& G, Pro& R, v4u& m5a, v4u& m5b
) {
  const int tid = G.tid, lane = G.lane, w = G.w, i = G.i, i1 = G.i1, rb = G.rb, fc = G.fc, hh = G.hh, blk = G.blk, q4 = G.q4, p4 = G.p4;
  const v4u sx = G.sx, sws = G.sws, sw1 = R.sw1, swr = R.swr, swi = R.swi;
  unsigned& ep = R.ep; v4u& gx = R.gx; v4u& gv = R.gv; unsigned& touch = R.touch;
  v4u& wqr = R.wqr; v4u& wqi = R.wqi; v4u& xqr = R.xqr; v4u& xqi = R.xqi; unsigned& wtr = R.wtr; unsigned& wti = R.wti; unsigned& xtr = R.xtr; unsigned& xti = R.xti;
  const unsigned vx0 = R.vx0, vx1 = R.vx1, vw0 = R.vw0, vw1 = R.vw1, g1l = R.g1l;
  const int f0 = ISA ? 128 * fc : 1024 + 96 * (fc - 8), nf = ISA ? 128 : 96;
  const int mt = G.at >> 2, dt4 = G.at & 3;
  const int m1 = G.m1;
  const int mW = m1 == 0 ? 128 * w : m1 + 256 * w;
  WVal Wc, Wd;
  const unsigned tag = (unsigned)__builtin_amdgcn_readfirstlane((int)ep) + 1u;
  float gate_g, gate_og;
  {
    float z = 0.f;
#pragma unroll
    for (int q = 0; q < 4; ++q) {
      const unsigned pr = pk2(bflo(gx[q]) * bflo(gv[q]), bfhi(gx[q]) * bfhi(gv[q]));
      z += bflo(pr) + bfhi(pr);
    }
    z = dpp_add<0x111, 0xf>(z); z = dpp_add<0x112, 0xf>(z); z = dpp_add<0x114, 0xf>(z); z = dpp_add<0x118, 0xf>(z);
    z = dpp_add<0x142, 0xa>(z); z = dpp_add<0x143, 0xc>(z);
    const float zb = bflo(pk2(z, 0.f));
    const float g = bflo(pk2(__builtin_amdgcn_rcpf(1.f + __builtin_amdgcn_exp2f(zb * -1.44269504088896341f)), 0.f));
    gate_g = asf((unsigned)__builtin_amdgcn_readlane((int)__builtin_bit_cast(unsigned, g), 63));
    gate_og = bflo(pk2(1.f - gate_g, 0.f));
  }
  if (!ISA) {
    LAS char* wscr0 = L + 128 * 1024 + w * 2048;
    const int woff = w_off(mW, lane);
    w_put(wscr0, wqr, wqi, lane);
    asm volatile("s_waitcnt lgkmcnt(0)" ::: "memory");
    WRawL wraw;
    w_get(wraw, wscr0, woff, lane, 0); w_get(wraw, wscr0, woff, lane, 1); w_get(wraw, wscr0, woff, lane, 2); w_get(wraw, wscr0, woff, lane, 3);
    Wc = w_unpack(wraw, w_tail(wtr, wti), woff, mW, lane);
    asm volatile("" : "+v"(Wc.r0), "+v"(Wc.i0), "+v"(Wc.r1), "+v"(Wc.i1));
  }
  if (ISA) {
    const int dq = w & 3, mh = w >> 2;
    LAS char* xa = L + A_XIMG + (32 * hh + q4) * 256 + (((4 * dq + 2 * blk + (p4 >> 1)) ^ (q4 << 2)) << 4) + 8 * (p4 & 1);
    LAS char* fb = L + A_FIMG + mh * 16384 + lane * 16;
    const int srow = 32 * mh + (lane & 31);
    LAS char* stw = L + A_FIMG + srow * 256 + 8 * hh;
    const int rrow = tid >> 3;
    LAS char* str = L + A_FIMG + rrow * 256;
    const unsigned tdst = WS_T + (unsigned)(((64 * mt + rrow) * 8 + i) * 1024 + dt4 * 256 + (tid & 7) * 16);
    if (w < 4) bar_vm<9>(); else BAR_LGKM();
    STAMP(1);
    f32x16 acc = {};
    v4u fa[2][4], fbv[2][4];
#pragma unroll
    for (int s = 0; s < 4; ++s) { fa[0][s] = tr_read2(xa + 8 * s * 256, xa + 8 * s * 256 + 1024); fbv[0][s] = lds_ld16(fb + s * 1024); }
#pragma unroll
    for (int g = 0; g < 4; ++g) {
      acc = mfma32(fa[g & 1][0], fbv[g & 1][0], acc);
      SCHED();
      if (g == 1) { if (w >= 4) bar_vm<9>(); else BAR_LGKM(); }
      if (g < 3) {
#pragma unroll
        for (int s = 0; s < 4; ++s) {
          const int ro = (64 * (g + 1) + 8 * s) * 256;
          fa[(g + 1) & 1][s] = tr_read2(xa + ro, xa + ro + 1024); fbv[(g + 1) & 1][s] = lds_ld16(fb + (4 * (g + 1) + s) * 1024);
        }
      }
      SCHED();
#pragma unroll
      for (int s = 1; s < 4; ++s) { acc = mfma32(fa[g & 1][s], fbv[g & 1][s], acc); if (g == 1 && s == 1) { g1_dma(2, 0); g1_dma(2, 1); } if (g == 1 && s == 2) { g1_dma(2, 2); g1_dma(2, 3); } SCHED(); }
    }
    BAR_LGKM();
    STAMP(2);
    g1_issue(3);
#pragma unroll
    for (int Gq = 0; Gq < 4; ++Gq) {
      v2u o; o.x = pk2(acc[4 * Gq], acc[4 * Gq + 1]); o.y = pk2(acc[4 * Gq + 2], acc[4 * Gq + 3]);
      *(LAS v2u*)(stw + (((4 * dq + Gq) ^ (srow & 15)) << 4)) = o;
    }
    BAR_LGKM();
    {
      const v4u v0 = lds_ld16(str + (((tid & 7) ^ (rrow & 15)) << 4)), v1 = lds_ld16(str + ((((tid & 7) + 8) ^ (rrow & 15)) << 4));
      st16_sc1(sws, tdst, v0);
      st16_sc1(sws, tdst + 128u, v1);
    }
    SCHED();
  }
  const int ft = w & 3, rh1 = w >> 2;
  const bool active = ft * 32 < nf;
  f32x16 acc1[2] = {};
  v4u tq0, tq1;
  v4u tb[16];
  unsigned fla;
  int t_ok = 0;
  unsigned tsrc0 = 0, tsrc1 = 0;
  const unsigned flaoff = WS_FLA + (unsigned)(((m1 >> 5) & 3) * 32 + (lane & 31)) * FS;
  LAS char* CTL = L + 128 * 1024;
  {
    LAS char* wa = L + 16384 + (8 * hh + q4) * 256 + (((4 * ft + 2 * blk + (p4 >> 1)) ^ (q4 << 2)) << 4) + 8 * (p4 & 1);
    const int r = 64 * rh1 + (lane & 31), fz = (r >> 1) & 7;
    LAS char* xb0 = L + r * 128 + (((0 + hh) ^ fz) << 4);
    LAS char* xb1 = L + r * 128 + (((2 + hh) ^ fz) << 4);
    LAS char* xb2 = L + r * 128 + (((4 + hh) ^ fz) << 4);
    LAS char* xb3 = L + r * 128 + (((6 + hh) ^ fz) << 4);
    Frag1 fr[2];
    bar_vm<ISA ? 15 : 13>();
    STAMP(3);
    if (lane == 0) { v2u gv2; gv2.x = __builtin_bit_cast(unsigned, gate_g); gv2.y = __builtin_bit_cast(unsigned, gate_og); *(LAS v2u*)(CTL + 64 + w * 8) = gv2; }
    g1_read(fr[0], wa, xb0, xb1, xb2, xb3, 0);
#define PIN1(c) asm volatile("" : "+v"(c.a[0]), "+v"(c.a[1]), "+v"(c.a[2]), "+v"(c.a[3]), "+v"(c.b0[0]), "+v"(c.b0[1]), "+v"(c.b0[2]), "+v"(c.b0[3]), \
                             "+v"(c.b1[0]), "+v"(c.b1[1]), "+v"(c.b1[2]), "+v"(c.b1[3]))
#define G1_STEP(kt, X0, X1, X2, X3, X4, X5, X6, X7)                                                                   \
    {                                                                                                                   \
      FT(10 + (kt));                                                                                                    \
      Frag1& c = fr[(kt) & 1];                                                                                          \
      Frag1& n = fr[((kt) + 1) & 1];                                                                                    \
      const int so = (((kt) + 1) & 3) * 32768;                                                                          \
      PIN1(c);                                                                                                          \
      asm volatile("" :: "v"(acc1[0]), "v"(acc1[1]));                                                                   \
      SCHED();                                                                                                          \
      acc1[0] = mfma32(c.a[0], c.b0[0], acc1[0]); if ((kt) < 7) n.a[0] = tr_read2(wa + so, wa + so + 1024); X0; SCHED(); \
      acc1[1] = mfma32(c.a[0], c.b1[0], acc1[1]); if ((kt) < 7) { n.b0[0] = lds_ld16(xb0 + so); n.b1[0] = lds_ld16(xb0 + so + 4096); } X1; SCHED(); \
      acc1[0] = mfma32(c.a[1], c.b0[1], acc1[0]); if ((kt) < 7) n.a[1] = tr_read2(wa + so + 4096, wa + so + 5120); X2; SCHED(); \
      acc1[1] = mfma32(c.a[1], c.b1[1], acc1[1]); if ((kt) < 7) { n.b0[1] = lds_ld16(xb1 + so); n.b1[1] = lds_ld16(xb1 + so + 4096); } X3; SCHED(); \
      acc1[0] = mfma32(c.a[2], c.b0[2], acc1[0]); if ((kt) < 7) n.a[2] = tr_read2(wa + so + 8192, wa + so + 9216); X4; SCHED(); \
      acc1[1] = mfma32(c.a[2], c.b1[2], acc1[1]); if ((kt) < 7) { n.b0[2] = lds_ld16(xb2 + so); n.b1[2] = lds_ld16(xb2 + so + 4096); } X5; SCHED(); \
      acc1[0] = mfma32(c.a[3], c.b0[3], acc1[0]); if ((kt) < 7) n.a[3] = tr_read2(wa + so + 12288, wa + so + 13312); X6; SCHED(); \
      acc1[1] = mfma32(c.a[3], c.b1[3], acc1[1]); if ((kt) < 7) { n.b0[3] = lds_ld16(xb3 + so); n.b1[3] = lds_ld16(xb3 + so + 4096); } X7; SCHED(); \
    }
#define GATE_STORE() do { if (w == 2 && lane < 8) { const v2u gv2 = *(LAS v2u*)(CTL + 64 + lane * 8);                       \
      __builtin_amdgcn_raw_buffer_store_b64(gv2, as_rsrc(sws), WS_GATE + (unsigned)(i1 * 256 + 128 * rb + 8 * fc + lane) * 8u, 0, 16); } } while (0)
#define STEP8 G1_STEP
    if constexpr (ISA) {
      bar_vm<11>();
      STAMP(4);
      STEP8(0, NOP, NOP, NOP, NOP, NOP, NOP, NOP, NOP)
      bar_vm<0>();
      if (tid == 0) flag_store(p.ws, WS_FLA + (unsigned)(mt * 32 + i * 4 + dt4) * FS, tag);
      STAMP(13);
      STEP8(1, g1_dma(4, 0), g1_dma(4, 1), g1_dma(4, 2), g1_dma(4, 3), g1_dma(5, 0), g1_dma(5, 1), g1_dma(5, 2), g1_dma(5, 3))
      BAR_LGKM();
      GATE_STORE();
      STEP8(2, NOP, NOP, NOP, NOP, g1_dma(6, 0), g1_dma(6, 1), g1_dma(6, 2), g1_dma(6, 3))
      bar_vm<8>();
      STEP8(3, NOP, NOP, NOP, NOP, g1_dma(7, 0), g1_dma(7, 1), g1_dma(7, 2), g1_dma(7, 3))
      bar_vm<8>();
      STAMP(12);
      STEP8(4, NOP, NOP, NOP, NOP, w2_issue1(G, 0, 0), w2_issue1(G, 0, 1), w2_issue1(G, 1, 0), w2_issue1(G, 1, 1))
      bar_vm<8>();
      STEP8(5, NOP, NOP, NOP, NOP, w2_issue1(G, 2, 0), w2_issue1(G, 2, 1), w2_issue1(G, 3, 0), w2_issue1(G, 3, 1))
      bar_vm<8>();
      STEP8(6, NOP, NOP, NOP, NOP, w2_issue1(G, 4, 0), w2_issue1(G, 4, 1), w2_issue1(G, 5, 0), w2_issue1(G, 5, 1))
      BAR_LGKM();
    } else {
      const v4u stab = make_srd(&kTab, sizeof(Tab));
      {
        const int kap = tid >> 5, ii = (kap >> 1) & 7, d0 = kap & 1;
        const int g0 = m1 == 0 ? 2 * ii : ii, g1 = m1 == 0 ? 2 * ii + 1 : 8 + ii;
        const unsigned tb = WS_T + (unsigned)m1 * 16384u + (unsigned)(d0 * 512 + (tid & 31) * 16);
        tsrc0 = tb + (unsigned)g0 * 1024u; tsrc1 = tb + (unsigned)g1 * 1024u;
      }
      bar_vm<9>();
      STAMP(4);
      STEP8(0, NOP, NOP, NOP, NOP, g1_dma(4, 0), g1_dma(4, 1), g1_dma(4, 2), g1_dma(4, 3))
      bar_vm<9>();
      STEP8(1, NOP, NOP, NOP, NOP, g1_dma(5, 0), g1_dma(5, 1), g1_dma(5, 2), g1_dma(5, 3))
      bar_vm<9>();
      GATE_STORE();
      STEP8(2, NOP, NOP, NOP, NOP, g1_dma(6, 0), g1_dma(6, 1), g1_dma(6, 2), g1_dma(6, 3))
      bar_vm<8>();
      LAS char* wscr0 = L + 128 * 1024 + w * 2048;
      const bool special = m1 == 0;
      const int m2nd = 128 * w + 1024, off2 = w_off(m2nd, lane);
      if (special) { asm volatile("" : "+v"(xqr), "+v"(xqi), "+v"(xtr), "+v"(xti)); w_put(wscr0, xqr, xqi, lane); }
      STEP8(3, NOP, NOP, (dma16<128u * 1024u + 16384u, 0>(stab, (unsigned)lane * 16u, g1l, g1l)), (dma16<128u * 1024u + 17408u, 1024>(stab, (unsigned)lane * 16u, g1l, g1l)),
            g1_dma(7, 0), g1_dma(7, 1), g1_dma(7, 2), g1_dma(7, 3))
      bar_vm<10>();
      STAMP(12);
      WRawL raw2;
      if (special) { w_get(raw2, wscr0, off2, lane, 0); w_get(raw2, wscr0, off2, lane, 1); w_get(raw2, wscr0, off2, lane, 2); w_get(raw2, wscr0, off2, lane, 3); }
      STEP8(4, NOP, NOP, NOP, NOP, w2_issue1(G, 0, 0), w2_issue1(G, 0, 1), w2_issue1(G, 1, 0), w2_issue1(G, 1, 1))
      bar_vm<10>();
      if (special) { Wd = w_unpack(raw2, w_tail(xtr, xti), off2, m2nd, lane); packed_coef(Wc, Wd, w); }
      STEP8(5, NOP, NOP, NOP, NOP, w2_issue1(G, 2, 0), w2_issue1(G, 2, 1), w2_issue1(G, 3, 0), w2_issue1(G, 3, 1))
      bar_vm<8>();
      STEP8(6, if (w == 0) LD4Z(fla, flaoff, sws, "sc1"), NOP, NOP, NOP, w2_issue1(G, 4, 0), w2_issue1(G, 4, 1), w2_issue1(G, 5, 0), w2_issue1(G, 5, 1))
      BAR_LGKM();
    }
    if constexpr (ISA) {
      STEP8(7, NOP, NOP, NOP, NOP, w2_issue1(G, 6, 0), w2_issue1(G, 6, 1), w2_issue1(G, 7, 0), w2_issue1(G, 7, 1))
    } else {
      LAS char* TB = L + 128 * 1024 + 16384 + lane * 16;
#define TBR(f) (tb[f] = lds_ld16(TB + (f) * 1024), tb[(f) + 1] = lds_ld16(TB + ((f) + 1) * 1024))
      STEP8(7, TBR(6), TBR(0), TBR(10), TBR(2), { TBR(12); w2_issue1(G, 6, 0); }, { TBR(14); w2_issue1(G, 6, 1); }, { TBR(4); w2_issue1(G, 7, 0); }, { TBR(8); w2_issue1(G, 7, 1); })
      if (w == 0) {
        asm volatile("s_waitcnt vmcnt(8)" : "+v"(fla) :: "memory");
        const unsigned ok = __all(fla == tag) ? 1u : 0u;
        if (lane == 0) *(LAS unsigned*)CTL = ok;
      }
      BAR_LGKM();
      t_ok = __builtin_amdgcn_readfirstlane(*(volatile LAS unsigned*)CTL);
      if (t_ok) { LD16Z(tq0, tsrc0, sws, "sc1"); LD16Z(tq1, tsrc1, sws, "sc1"); }
    }
  }
  asm volatile("" :: "v"(touch));
  STAMP(5);
  FT(18);
  if (active) {
#pragma unroll
    for (int rt = 0; rt < 2; ++rt)
#pragma unroll
      for (int u = 0; u < 2; ++u) {
        v4u o;
        o.x = silu2(acc1[rt][8 * u + 0], acc1[rt][8 * u + 1]);
        o.y = silu2(acc1[rt][8 * u + 2], acc1[rt][8 * u + 3]);
        o.z = silu2(acc1[rt][8 * u + 4], acc1[rt][8 * u + 5]);
        o.w = silu2(acc1[rt][8 * u + 6], acc1[rt][8 * u + 7]);
        const int jb32 = 4 * rb + 2 * rh1 + rt, fs = (f0 >> 4) + 2 * ft + u;
        st16_sc1(sws, WS_H + (unsigned)(((i1 * 8 + jb32) * 112 + fs) * 1024 + lane * 16), o);
      }
  }
  SCHED();
  STAMP(6);
  FT(20);
  const unsigned flh_own = WS_FLH + (unsigned)((i1 * 2 + rb) * 16 + fc) * FS;
  if constexpr (ISA) {
    bar_vm<0>();
    if (tid == 0) flag_store(p.ws, flh_own, tag);
  } else {
    LAS char* I0 = L + 128 * 1024;
    LAS char* I1 = L + 128 * 1024 + 16384;
    const int g = lane >> 4, l15 = lane & 15;
    const bool special = (m1 == 0);
    bool published = false;
    if (t_ok) {
      if (active) wait_vm<4>(); else wait_vm<0>();
      asm volatile("s_barrier" ::: "memory");
    } else {
      bar_vm<0>();
      if (tid == 0) flag_store(p.ws, flh_own, tag);
      published = true;
      if (w == 0) poll_words(sws, WS_FLA + (unsigned)(m1 >> 5) * 32u * FS, 32, tag, lane);
      asm volatile("s_barrier" ::: "memory");
      LD16Z(tq0, tsrc0, sws, "sc1"); LD16Z(tq1, tsrc1, sws, "sc1");
      wait_vm<0>();
    }
    asm volatile("" : "+v"(tq0), "+v"(tq1) :: "memory");
    {
      const int kap = tid >> 5, ch = tid & 31;
      *(LAS v4u*)(I0 + kap * 512 + ((ch ^ ((kap & 3) << 2)) << 4)) = tq0;
      *(LAS v4u*)(I0 + (kap + 16) * 512 + ((ch ^ ((kap & 3) << 2)) << 4)) = tq1;
    }
    BAR_LGKM();
    FT(21);
    LAS char* ta = I0 + (8 * hh + q4) * 512 + (((4 * w + 2 * blk + (p4 >> 1)) ^ (q4 << 2)) << 4) + 8 * (p4 & 1);
    const int n = lane & 31;
    LAS char* so = I1 + n * 512 + 8 * hh;
    {
      f32x16 acc = {};
      acc = mfma32(tr_read2(ta, ta + 2048), tb[6], acc);
      acc = mfma32(tr_read2(ta + 8192, ta + 10240), tb[7], acc);
#pragma unroll
      for (int Gq = 0; Gq < 4; ++Gq) {
        v2u o; o.x = pk2(acc[4 * Gq], acc[4 * Gq + 1]); o.y = pk2(acc[4 * Gq + 2], acc[4 * Gq + 3]);
        *(LAS v2u*)(so + (((4 * w + Gq) ^ (n & 15)) << 4)) = o;
      }
    }
    BAR_LGKM();
    FT(22);
    const f32x4 z4 = {0.f, 0.f, 0.f, 0.f};
    const v4u F16fr = tb[0], F16fi = tb[1];
    v4u F16ir, F16ii;
    F16ir.x = F16fr.x; F16ir.y = F16fr.y; F16ir.z = F16fr.z ^ 0x80008000u; F16ir.w = F16fr.w ^ 0x80008000u;
    F16ii.x = F16fi.x ^ 0x80008000u; F16ii.y = F16fi.y ^ 0x80008000u; F16ii.z = F16fi.z; F16ii.w = F16fi.w;
    f32x4 zr[2], zi[2];
#pragma unroll
    for (int pp = 0; pp < 2; ++pp) {
      const int n0 = 4 * w + 2 * pp, n1 = n0 + 1;
      const int ch = 2 * (4 * g + q4) + (p4 >> 1);
      const v4u a1 = tr_read2(I1 + n0 * 512 + ((ch ^ (n0 & 15)) << 4) + 8 * (p4 & 1), I1 + n1 * 512 + ((ch ^ (n1 & 15)) << 4) + 8 * (p4 & 1));
      const f32x4 dr = mfma16(a1, pp ? tb[2] : F16fr, z4), di = mfma16(a1, pp ? tb[3] : F16fi, z4);
      const v4u b3 = cmul_pack<false>(dr, di, asf4(tb[10 + 2 * pp]), asf4(tb[11 + 2 * pp]));
      zr[pp] = mfma16(F16fr, b3, z4); zi[pp] = mfma16(F16fi, b3, z4);
    }
    v4u a4[2];
    if (!special) {
      a4[0] = cmul_pack<false>(zr[0], zi[0], Wc.r0, Wc.i0);
      a4[1] = cmul_pack<false>(zr[1], zi[1], Wc.r1, Wc.i1);
    } else {
      BAR_LGKM();
      LAS char* Z = I0;
#pragma unroll
      for (int pp = 0; pp < 2; ++pp)
#pragma unroll
        for (int r = 0; r < 4; ++r) { f32x2 v = {zr[pp][r], zi[pp][r]}; *(LAS f32x2*)(Z + ((w * 2 + pp) * 256 + l15 + 16 * (4 * g + r)) * 8) = v; }
      BAR_LGKM();
#pragma unroll
      for (int pp = 0; pp < 2; ++pp) {
        f32x4 cr, ci;
#pragma unroll
        for (int r = 0; r < 4; ++r) {
          const int K = l15 + 16 * (4 * g + r), Kn = pp ? 255 - K : (256 - K) & 255;
          const f32x2 y = *(LAS f32x2*)(Z + ((((8 - w) & 7) * 2 + pp) * 256 + Kn) * 8);
          cr[r] = y[0]; ci[r] = -y[1];
        }
        const f32x4 ar = pp ? Wc.r1 : Wc.r0, ai = pp ? Wc.i1 : Wc.i0, br = pp ? Wd.r1 : Wd.r0, bi = pp ? Wd.i1 : Wd.i0;
        const f32x4 yr = zr[pp] * ar - zi[pp] * ai + cr * br - ci * bi, yi = zr[pp] * ai + zi[pp] * ar + cr * bi + ci * br;
        a4[pp].x = pk2(yr[0], yr[1]); a4[pp].y = pk2(yr[2], yr[3]); a4[pp].z = pk2(yi[0], yi[1]); a4[pp].w = pk2(yi[2], yi[3]);
      }
      BAR_LGKM();
    }
#pragma unroll
    for (int pp = 0; pp < 2; ++pp) {
      const f32x4 dr = mfma16(a4[pp], F16ir, z4), di = mfma16(a4[pp], F16ii, z4);
      const v4u a5 = cmul_pack<true>(dr, di, asf4(tb[pp ? 14 : 10]), asf4(tb[pp ? 15 : 11]));
      const f32x4 er = mfma16(a5, pp ? tb[4] : F16ir, z4), ei = mfma16(a5, pp ? tb[5] : F16ii, z4);
      const int n0 = 4 * w + 2 * pp, n1 = n0 + 1;
      const int ch = 2 * l15 + (g >> 1);
      v2u o; o.x = pk2(er[0], er[1]); o.y = pk2(er[2], er[3]);
      *(LAS v2u*)(I0 + n0 * 512 + ((ch ^ ((n0 & 3) << 2)) << 4) + 8 * (g & 1)) = o;
      o.x = pk2(ei[0], ei[1]); o.y = pk2(ei[2], ei[3]);
      *(LAS v2u*)(I0 + n1 * 512 + ((ch ^ ((n1 & 3) << 2)) << 4) + 8 * (g & 1)) = o;
    }
    bar_vm<0>();
    if (!published && tid == 0) flag_store(p.ws, flh_own, tag);
    FT(23);
    m5a = tb[8]; m5b = tb[9];
  }
  return tag;
}

DI void make_geo(Geo& G, const Params& p, int tz) {
  G.tid = tz; G.lane = G.tid & 63; G.w = __builtin_amdgcn_readfirstlane(G.tid >> 6);
  G.b = blockIdx.x;
  const int Lx = G.b >> 3, x8 = G.b & 7;
  {
    const int a = x8 & 3, h = x8 >> 2, rbi = Lx >> 3, c = Lx & 7;
    G.i = 2 * a + h; G.i1 = 2 * a + (rbi >> 1); G.rb = rbi & 1;
    G.fc = c < 4 ? 4 * h + c : 4 + 4 * h + c;
    G.at = rbi * 4 + c; G.m1 = ((rbi * 4 + c - 4) << 3) | x8;
  }
  G.hh = G.lane >> 5; G.blk = (G.lane >> 4) & 1; G.q4 = (G.lane & 15) >> 2; G.p4 = G.lane & 3;
  G.sx = make_srd(p.x, NS * ND * 2); G.sws = make_srd(p.ws, 16u << 20); G.sw2 = make_srd(p.w2, NF * ND * 2);
  G.jb = Lx >> 3; G.dt = Lx & 7; G.rh = G.w & 1; G.q = G.w >> 1;
  G.j = 64 * G.jb + 32 * G.rh + (G.lane & 31);
  const int rho = 16 * G.rh + (G.lane >> 3), chs = (G.lane & 7) ^ (4 * ((G.lane >> 4) & 1));
  G.vw20 = (unsigned)(rho * 1024 + G.dt * 128 + chs * 16); G.vw21 = G.vw20 + 8 * 1024;
  G.ldsw = uni((unsigned)(G.q * 4096 + G.rh * 2048));
  G.fq0b = uni((unsigned)(768 + 256 * G.q) * 1024u); G.fq1b = uni((unsigned)(192 * G.q) * 1024u);
}

__global__ __launch_bounds__(512, 2) void gm_kernel(Params p) {
  extern __shared__ __attribute__((aligned(16))) char smem[];
  LAS char* L = (LAS char*)smem;
  unsigned touch0;
  {
    unsigned long long pc; unsigned len;
    asm volatile("s_getpc_b64 %0\n.Lgm_code_begin:\n\ts_mov_b32 %1, .Lgm_code_end-.Lgm_code_begin" : "=s"(pc), "=s"(len));
    const unsigned line = (unsigned)((blockIdx.x >> 3) * 8 + (threadIdx.x >> 6)) * 128u;
    const char* a0 = (const char*)(pc & ~127ull) + (line < len ? line : 0u);
    asm volatile("global_load_dword %0, %1, off" : "=v"(touch0) : "v"(a0) : "memory");
  }
  STAMP(0);
  FT(0);
  Geo G;
  make_geo(G, p, threadIdx.x);
  const bool isA = G.fc < 8;
  Pro R;
  {
    const int lane = G.lane, w = G.w, i = G.i, i1 = G.i1, rb = G.rb, fc = G.fc, m1 = G.m1;
    const v4u sx = G.sx, sws = G.sws;
    R.sw1 = make_srd(p.w1, ND * NF * 2); R.swr = make_srd(p.wr, NS * 257 * 2); R.swi = make_srd(p.wi, NS * 257 * 2);
    const v4u sw1 = R.sw1, swr = R.swr, swi = R.swi;
    const int jg = 128 * rb + 8 * fc + w;
    LD16Z(R.gx, (unsigned)(lane * 16 + (i1 + 8 * jg) * 1024), sx, "");
    LD16Z(R.gv, (unsigned)lane * 16u, make_srd(p.gw, ND * 2), "");
    LD4Z(R.ep, WS_EPOCH, sws, "sc1");
    const int mW = m1 == 0 ? 128 * w : m1 + 256 * w;
    if (isA) {
      const int dt4 = G.at & 3;
      const unsigned voff = (unsigned)(lane >> 4) * 8192u + (unsigned)(((lane & 15) ^ ((lane >> 4) << 2)) << 4);
      const unsigned xl = uni(A_XIMG + (unsigned)w * 8192u), xs = uni((unsigned)i * 1024u + (unsigned)dt4 * 256u + (unsigned)w * 262144u);
      dma16<0, 0>(sx, voff, xl, xs); dma16<1024, 32768>(sx, voff, xl, xs); dma16<2048, 65536>(sx, voff, xl, xs); dma16<3072, 98304>(sx, voff, xl, xs);
      dma16<4096, 131072>(sx, voff, xl, xs); dma16<5120, 163840>(sx, voff, xl, xs); dma16<6144, 196608>(sx, voff, xl, xs); dma16<7168, 229376>(sx, voff, xl, xs);
    } else {
      const unsigned wsrc = w_src(mW, lane), wts = w_tail_src(mW, lane);
      LD16Z(R.wqr, wsrc, swr, ""); LD16Z(R.wqi, wsrc, swi, ""); LD2Z(R.wtr, wts, swr, ""); LD2Z(R.wti, wts, swi, "");
    }
    const int f0 = isA ? 128 * fc : 1024 + 96 * (fc - 8), nf = isA ? 128 : 96;
    {
      const int r0 = 16 * w + (lane >> 3), pos = lane & 7, r1 = r0 + 8;
      R.vx0 = (unsigned)(i1 * 1024 + (128 * rb + r0) * 8192 + ((pos ^ ((r0 >> 1) & 7)) << 4));
      R.vx1 = (unsigned)(i1 * 1024 + (128 * rb + r1) * 8192 + ((pos ^ ((r1 >> 1) & 7)) << 4));
      const int k0 = 8 * w + (lane >> 4), chunk = (lane & 15) ^ ((lane >> 4) << 2);
      const bool ok = chunk * 8 < nf;
      R.vw0 = ok ? (unsigned)(k0 * 3584 + f0 * 2 + chunk * 16) : 0x80000000u;
      R.vw1 = ok ? R.vw0 + 4u * 3584u : 0x80000000u;
    }
    R.g1l = uni((unsigned)w * 2048u);
    const unsigned vx0 = R.vx0, vx1 = R.vx1, vw0 = R.vw0, vw1 = R.vw1, g1l = R.g1l;
    asm volatile("s_barrier" ::: "memory");
    g1_issue(0);
    if (!isA && m1 == 0) {
      const unsigned ws2 = w_src(128 * w + 1024, lane), wt2 = w_tail_src(128 * w + 1024, lane);
      LD16Z(R.xqr, ws2, swr, ""); LD16Z(R.xqi, ws2, swi, ""); LD2Z(R.xtr, wt2, swr, ""); LD2Z(R.xti, wt2, swi, "");
    }
    asm volatile("s_barrier" ::: "memory");
    g1_issue(1);
    if (!isA) {
      asm volatile("s_barrier" ::: "memory");
      g1_issue(2); asm volatile("s_barrier" ::: "memory");
      g1_issue(3);
    }
    unsigned touch;
    TOUCH();
    R.touch = touch;
    if (isA) phaseA_genF(L, i, G.at >> 2, w, lane);
    if (isA) wait_vm<17>(); else wait_vm<21>();
    asm volatile("" : "+v"(R.ep), "+v"(R.gx), "+v"(R.gv) :: "memory");
    if (!isA) {
      wait_vm<17>();
      asm volatile("" : "+v"(R.wqr), "+v"(R.wqi), "+v"(R.wtr), "+v"(R.wti) :: "memory");
      if (m1 == 0) {
        wait_vm<13>();
        asm volatile("" : "+v"(R.xqr), "+v"(R.xqi), "+v"(R.xtr), "+v"(R.xti) :: "memory");
      }
    }
  }
  v4u gf[4], m5a, m5b;
  unsigned tag;
  if (isA) tag = phase1<true>(p, L, G, R, m5a, m5b); else tag = phase1<false>(p, L, G, R, m5a, m5b);
  asm volatile("" :: "v"(touch0));
  { int tz = threadIdx.x; asm volatile("" : "+v"(tz)); make_geo(G, p, tz); }
  const int tid = G.tid, lane = G.lane, w = G.w, b = G.b, i = G.i, jb = G.jb, dt = G.dt, rh = G.rh, q = G.q, j = G.j;
  const int hh = G.hh, blk = G.blk, q4 = G.q4, p4 = G.p4;
  const v4u sws = G.sws;
  const unsigned ldsw = G.ldsw;
  const int m1 = G.m1;
  STAMP(7);
  FT(25);
  if (!isA) {
    LAS char* I0 = L + 128 * 1024;
    LAS char* I1 = L + 128 * 1024 + 16384;
    LAS char* ta = I0 + (8 * hh + q4) * 512 + (((4 * w + 2 * blk + (p4 >> 1)) ^ (q4 << 2)) << 4) + 8 * (p4 & 1);
    const int n = lane & 31;
    LAS char* so = I1 + n * 512 + 8 * hh;
    f32x16 acc = {};
    acc = mfma32(tr_read2(ta, ta + 2048), m5a, acc);
    acc = mfma32(tr_read2(ta + 8192, ta + 10240), m5b, acc);
#pragma unroll
    for (int Gq = 0; Gq < 4; ++Gq) {
      v2u o; o.x = pk2(acc[4 * Gq], acc[4 * Gq + 1]); o.y = pk2(acc[4 * Gq + 2], acc[4 * Gq + 3]);
      *(LAS v2u*)(so + (((4 * w + Gq) ^ (n & 15)) << 4)) = o;
    }
    BAR_LGKM();
    const bool special = (m1 == 0);
    auto u_store = [&](int t2) {
      const int nn = t2 >> 5, ch = t2 & 31, ii = (nn >> 1) & 7, d0 = nn & 1;
      const int g0 = special ? 2 * ii : ii, g1 = special ? 2 * ii + 1 : 8 + ii;
      const v4u v0 = lds_ld16(I1 + nn * 512 + ((ch ^ (nn & 15)) << 4));
      const v4u v1 = lds_ld16(I1 + (nn + 16) * 512 + ((ch ^ (nn & 15)) << 4));
      const unsigned ub = WS_U + (unsigned)m1 * 16384u + (unsigned)(d0 * 512 + ch * 16);
      st16_sc1(sws, ub + (unsigned)g0 * 1024u, v0);
      st16_sc1(sws, ub + (unsigned)g1 * 1024u, v1);
    };
    if (w != 0) u_store(tid);
    if (w == 1) u_store(tid - 64);
    SCHED();
  }
  const int sz = (q4 >> 1) & 1;
  LAS char* ra0 = L + q * 4096 + (4 * hh + q4) * 128 + (((0 ^ (4 * sz)) + 2 * blk + (p4 >> 1)) << 4) + 8 * (p4 & 1);
  LAS char* ra1 = L + q * 4096 + (4 * hh + q4) * 128 + (((4 ^ (4 * sz)) + 2 * blk + (p4 >> 1)) << 4) + 8 * (p4 & 1);
  v4u wp[4][4];
  BAR_LGKM();
#pragma unroll
  for (int t = 0; t < 4; ++t) {
    LAS char* s0 = ra0 + t * 16384; LAS char* s1 = ra1 + t * 16384;
    wp[t][0] = tr_read2(s0, s0 + 1024); wp[t][1] = tr_read2(s1, s1 + 1024);
    wp[t][2] = tr_read2(s0 + 2048, s0 + 3072); wp[t][3] = tr_read2(s1 + 2048, s1 + 3072);
  }
#define W2_8_11() do { w2_issue1(G, 8, 0); w2_issue1(G, 8, 1); w2_issue1(G, 9, 0); w2_issue1(G, 9, 1); w2_issue1(G, 10, 0); w2_issue1(G, 10, 1); w2_issue1(G, 11, 0); w2_issue1(G, 11, 1); } while (0)
  if (isA) { BAR_LGKM(); W2_8_11(); }
  const unsigned flh = WS_FLH + (unsigned)((i * 2 + (jb >> 1)) * 16) * FS;
  if (w == 0) poll_words(sws, flh, 16, tag, lane);
  BAR_LGKM();
  STAMP(8);
  FT(26);
  const unsigned vh = (unsigned)lane * 16u;
  const unsigned hbase = WS_H + (unsigned)((i * 8 + 2 * jb + rh) * 112) * 1024u;
  const unsigned hb0 = uni(hbase + (G.fq0b >> 4)), hb1 = uni(hbase + (G.fq1b >> 4));
  v2u gg;
  v4u hf[14][2];
#define H_ISSUE(t) do { LD16I(hf[t][0], vh, sws, ((t) < 8 ? hb0 : hb1), (2 * ((t) & 7)) * 1024, "sc1"); LD16I(hf[t][1], vh, sws, ((t) < 8 ? hb0 : hb1), (2 * ((t) & 7) + 1) * 1024, "sc1"); } while (0)
  H_ISSUE(0); H_ISSUE(1);
  asm volatile("s_barrier" ::: "memory");
  H_ISSUE(2); H_ISSUE(3); H_ISSUE(4); H_ISSUE(5); H_ISSUE(6);
  LD8Z(gg, WS_GATE + (unsigned)(i * 256 + j) * 8u, sws, "sc1");
  SCHED();
  f32x16 accb[2] = {}, acca[2] = {};
  v4u wf[2][4];
  unsigned fl0, fl1;
  v4u ub0, ub1, ub2, ub3;
  const unsigned vu = WS_U + (unsigned)(((64 * q + 16 * rh + (lane >> 3)) * 8 + i) * 1024 + dt * 128 + ((lane & 7) ^ (4 * ((lane >> 4) & 1))) * 16);
#define G2_BAR(n, t) asm volatile("s_waitcnt vmcnt(" STR(n) ") lgkmcnt(0)\n\ts_barrier" : "+v"(hf[t][0]), "+v"(hf[t][1]) :: "memory")
#define G2_WAIT(n, t) asm volatile("s_waitcnt vmcnt(" STR(n) ")" : "+v"(hf[t][0]), "+v"(hf[t][1]) :: "memory")
#define G2_CORE(t, c, n, RD, acc, b0, b1, X0, X1, X2)                                             \
  {                                                                                                 \
    FT(30 + (t));                                                                                   \
    LAS char* s0 = ra0 + (((t) + 1) & 7) * 16384; LAS char* s1 = ra1 + (((t) + 1) & 7) * 16384;    \
    asm volatile("" : "+v"(c[0]), "+v"(c[1]), "+v"(c[2]), "+v"(c[3]));                             \
    asm volatile("" :: "v"(acc[0]), "v"(acc[1]));                                                   \
    SCHED();                                                                                        \
    acc[0] = mfma32(c[0], b0, acc[0]); if (RD) n[0] = tr_read2(s0, s0 + 1024); SCHED();            \
    acc[1] = mfma32(c[1], b0, acc[1]); if (RD) n[1] = tr_read2(s1, s1 + 1024); X0; SCHED();        \
    acc[0] = mfma32(c[2], b1, acc[0]); if (RD) n[2] = tr_read2(s0 + 2048, s0 + 3072); X1; SCHED(); \
    acc[1] = mfma32(c[3], b1, acc[1]); if (RD) n[3] = tr_read2(s1 + 2048, s1 + 3072); X2; SCHED(); \
  }
#define G2_PRE(t, X2) G2_CORE(t, wp[t], wf[0], (t) == 3, accb, hf[t][0], hf[t][1], if (!isA) w2_issue1(G, (t) + 8, 0), if (!isA) w2_issue1(G, (t) + 8, 1), X2)
#define G2_STEP(t, X0, X1, X2) G2_CORE(t, wf[(t) & 1], wf[((t) + 1) & 1], true, accb, hf[t][0], hf[t][1], X0, X1, X2)
  G2_BAR(13, 0);
  if (!isA && tid == 0) flag_store(p.ws, WS_FLB + (unsigned)m1 * FS, tag);
  STAMP(14);
  G2_PRE(0, H_ISSUE(7))
  G2_WAIT(13, 1); G2_PRE(1, H_ISSUE(8))
  G2_WAIT(13, 2); G2_PRE(2, H_ISSUE(9))
  G2_WAIT(13, 3); G2_PRE(3, H_ISSUE(10))
  G2_BAR(13, 4); G2_STEP(4, w2_issue1(G, 12, 0), w2_issue1(G, 12, 1), H_ISSUE(11))
  G2_BAR(15, 5); G2_STEP(5, w2_issue1(G, 13, 0), w2_issue1(G, 13, 1), H_ISSUE(12))
  G2_WAIT(17, 6); G2_STEP(6, NOP, NOP, H_ISSUE(13))
  STAMP(9);
  G2_BAR(16, 7); G2_STEP(7, NOP, NOP, NOP)
  G2_BAR(4, 8);
  if (w == 0) { LD4Z(fl0, WS_FLB + (unsigned)lane * FS, sws, "sc1"); LD4Z(fl1, WS_FLB + (64u + (unsigned)lane) * FS, sws, "sc1"); }
  GRot grot;
  G2_STEP(8, gen_g_init(G, grot), NOP, gen_g_frag<0>(G, grot, gf[0]))
  unsigned e0, e1, e2, e3;
  if (b == 0 && w == 1) {
    LD4Z(e0, WS_FLH + (unsigned)lane * FS, sws, "sc1"); LD4Z(e1, WS_FLH + (64u + (unsigned)lane) * FS, sws, "sc1");
    LD4Z(e2, WS_FLH + (128u + (unsigned)lane) * FS, sws, "sc1"); LD4Z(e3, WS_FLH + (192u + (unsigned)lane) * FS, sws, "sc1");
  }
  G2_WAIT(8, 9); G2_STEP(9, NOP, NOP, gen_g_frag<1>(G, grot, gf[1]))
  LAS unsigned* verdict = (LAS unsigned*)(L + 128 * 1024);
  if (w == 0) {
    asm volatile("s_waitcnt vmcnt(0)" : "+v"(fl0), "+v"(fl1) :: "memory");
    const unsigned ok = __all(fl0 == tag && fl1 == tag) ? 1u : 0u;
    if (lane == 0) *verdict = ok;
  }
  G2_BAR(8, 10);
  const int u_early = __builtin_amdgcn_readfirstlane(*(volatile LAS unsigned*)verdict);
  STAMP(15);
#define U_ISSUE() do { LD16Z(ub0, vu, sws, "sc1"); LD16Z(ub1, vu + 8u * 8192u, sws, "sc1"); LD16Z(ub2, vu + 32u * 8192u, sws, "sc1"); LD16Z(ub3, vu + 40u * 8192u, sws, "sc1"); } while (0)
  if (u_early) U_ISSUE();
  G2_STEP(10, NOP, NOP, gen_g_frag<2>(G, grot, gf[2]))
  G2_WAIT(8, 11); G2_STEP(11, NOP, NOP, gen_g_frag<3>(G, grot, gf[3]))
  if (u_early) wait_vm<6>(); else wait_vm<2>();
  asm volatile("" : "+v"(hf[12][0]), "+v"(hf[12][1]) :: "memory");
  G2_STEP(12, NOP, NOP, NOP)
  if (u_early) wait_vm<4>(); else wait_vm<0>();
  asm volatile("" : "+v"(hf[13][0]), "+v"(hf[13][1]) :: "memory");
  G2_CORE(13, wf[1], wf[0], false, accb, hf[13][0], hf[13][1], NOP, NOP, NOP)
  if (!u_early) {
    if (w == 0) { poll_words(sws, WS_FLB, 64, tag, lane); poll_words(sws, WS_FLB + 64u * FS, 64, tag, lane); }
    BAR_LGKM();
    U_ISSUE();
  }
  asm volatile("s_waitcnt vmcnt(0)" : "+v"(ub0), "+v"(ub1), "+v"(ub2), "+v"(ub3), "+v"(gg) :: "memory");
  *(LAS v4u*)(L + 6 * 16384 + ldsw + lane * 16) = ub0; *(LAS v4u*)(L + 6 * 16384 + ldsw + 1024 + lane * 16) = ub1;
  *(LAS v4u*)(L + 7 * 16384 + ldsw + lane * 16) = ub2; *(LAS v4u*)(L + 7 * 16384 + ldsw + 1024 + lane * 16) = ub3;
  if (b == 0 && w == 1) {
    asm volatile("" : "+v"(e0), "+v"(e1), "+v"(e2), "+v"(e3));
    if (!__all(e0 == tag && e1 == tag && e2 == tag && e3 == tag)) {
#pragma unroll 1
      for (int u = 0; u < 4; ++u) poll_words(sws, WS_FLH + 64u * FS * u, 64, tag, lane);
    }
    if (lane == 0) flag_store(p.ws, WS_EPOCH, tag);
  }
  BAR_LGKM();
  {
    LAS char* s0 = ra0 + 6 * 16384; LAS char* s1 = ra1 + 6 * 16384;
    wf[0][0] = tr_read2(s0, s0 + 1024); wf[0][1] = tr_read2(s1, s1 + 1024); wf[0][2] = tr_read2(s0 + 2048, s0 + 3072); wf[0][3] = tr_read2(s1 + 2048, s1 + 3072);
  }
  STAMP(10);
  G2_CORE(14, wf[0], wf[1], true, acca, gf[0], gf[1], NOP, NOP, NOP)
  G2_CORE(15, wf[1], wf[0], false, acca, gf[2], gf[3], NOP, NOP, NOP)
  FT(50);
  {
    const float og = asf(gg.y), gs = asf(gg.x) * (1.f / 524288.f);
    const int row = 32 * rh + (lane & 31);
#pragma unroll
    for (int dh = 0; dh < 2; ++dh)
#pragma unroll
      for (int G = 0; G < 4; ++G) {
        f32x4 v;
#pragma unroll
        for (int e = 0; e < 4; ++e) v[e] = __builtin_fmaf(og, accb[dh][4 * G + e], gs * acca[dh][4 * G + e]);
        *(LAS f32x4*)(L + q * 16384 + row * 256 + (((8 * dh + 2 * G + hh) ^ (row & 7)) << 4)) = v;
      }
  }
  BAR_LGKM();
  FT(51);
  {
    const int row = tid >> 3, c8 = tid & 7;
    f32x4 s0 = {0.f, 0.f, 0.f, 0.f}, s1 = s0;
#pragma unroll
    for (int qq = 0; qq < 4; ++qq) {
      s0 += *(LAS f32x4*)(L + qq * 16384 + row * 256 + (((2 * c8) ^ (row & 7)) << 4));
      s1 += *(LAS f32x4*)(L + qq * 16384 + row * 256 + (((2 * c8 + 1) ^ (row & 7)) << 4));
    }
    v4u o; o.x = pk2(s0[0], s0[1]); o.y = pk2(s0[2], s0[3]); o.z = pk2(s1[0], s1[1]); o.w = pk2(s1[2], s1[3]);
    st16_sc1(make_srd(p.out, NS * ND * 2), (unsigned)((i + 8 * (64 * jb + row)) * 1024 + dt * 128 + c8 * 16), o);
  }
  asm volatile(".Lgm_code_end:");
  STAMP(11);
}

}

extern "C" void kernel_launch(void* const* d_in, const int* in_sizes, int n_in, void* d_out, int out_size, void* d_ws, size_t ws_size,
                              hipStream_t stream) {
  Params p;
  p.x = d_in[0]; p.wr = d_in[1]; p.wi = d_in[2]; p.w1 = d_in[4]; p.w2 = d_in[5]; p.gw = d_in[6];
  p.out = d_out; p.ws = d_ws;
  (void)hipFuncSetAttribute((const void*)gm_kernel, hipFuncAttributeMaxDynamicSharedMemorySize, LDS_BYTES);
  gm_kernel<<<256, 512, LDS_BYTES, stream>>>(p);
}
```
